```python
import torch

def _softmax(x, axis=-1):
    return torch.softmax(x, dim=axis)

def _gelu(x):
    return torch.nn.functional.gelu(x, approximate='tanh')

def _silu(x):
    return torch.nn.functional.silu(x)

def _sigmoid(x):
    return torch.sigmoid(x)

def _layernorm(x, axis=-1, eps=1e-05):
    m = x.mean(dim=axis, keepdim=True)
    v = x.var(dim=axis, keepdim=True, unbiased=False)
    return (x - m) / torch.sqrt(v + eps)

def _scatter_add(tgt, idx, src, axis=0):
    return tgt.clone().index_add_(axis, idx, src)

def _segment_sum(src, seg, n):
    out = torch.zeros((n,) + src.shape[1:], dtype=src.dtype)
    return out.index_add_(0, seg, src)

def _pad(x, pw):
    flat = tuple((v for pair in reversed(pw) for v in pair))
    return torch.nn.functional.pad(x, flat)
kernel_name = 'adaLN_cond__compressed_kv_cla__mixer_channel_mlp__prompt'

def setup_inputs():

    _w = lambda a: (lambda t: t.to(torch.bfloat16) if t.is_floating_point() else t)(torch.as_tensor(a))

    def _A_setup():
        B, H, Sq, Sk, D, M = (1, 32, 2048, 2048, 128, 512)
        Dc = 4 * D
        return {'x': _w((lambda *s: torch.randn(*s, dtype=torch.bfloat16) * 0.05)(B, Sq, M)), 'c_kv': _w((lambda *s: torch.randn(*s, dtype=torch.bfloat16) * 0.4)(B, Sk, Dc)), 'w_q': _w((lambda *s: torch.randn(*s, dtype=torch.bfloat16) * 3.2)(M, H, D) * M ** (-0.5)), 'w_uk': _w((lambda *s: torch.randn(*s, dtype=torch.bfloat16) * 3.2)(Dc, H, D) * Dc ** (-0.5)), 'w_uv': _w((lambda *s: torch.randn(*s, dtype=torch.bfloat16) * 0.4)(Dc, H, D) * Dc ** (-0.5)), 'w_o': _w((lambda *s: torch.randn(*s, dtype=torch.bfloat16) * 0.4)(H, D, M) * (H * D) ** (-0.5))}
    _A_in = _A_setup()

    def _B_setup():
        B, S, D, F = (1, 2048, 512, 1376)
        return {'x': _w((lambda *s: torch.randn(*s, dtype=torch.bfloat16) * 0.05)(B, S, D)), 'w1': _w((lambda *s: torch.randn(*s, dtype=torch.bfloat16) * 3.2)(D, F) * D ** (-0.5)), 'b1': _w((lambda *s: torch.randn(*s, dtype=torch.bfloat16) * 0.8)(F)), 'w2': _w((lambda *s: torch.randn(*s, dtype=torch.bfloat16) * 0.05)(F, D) * F ** (-0.5)), 'b2': _w((lambda *s: torch.randn(*s, dtype=torch.bfloat16) * 0.1)(D))}
    _B_in = _B_setup()
    return {'A_x': _A_in['x'], 'A_c_kv': _A_in['c_kv'], 'A_w_q': _A_in['w_q'], 'A_w_uk': _A_in['w_uk'], 'A_w_uv': _A_in['w_uv'], 'A_w_o': _A_in['w_o'], 'B_x': _B_in['x'], 'B_w1': _B_in['w1'], 'B_b1': _B_in['b1'], 'B_w2': _B_in['w2'], 'B_b2': _B_in['b2'], 'c': _w((lambda *s: torch.randn(*s, dtype=torch.bfloat16) * 0.1)(1, 512) * 8.0), 'w_mod': _w((lambda *s: torch.randn(*s, dtype=torch.bfloat16) * 0.1)(512, 6 * 512) * 512 ** (-0.5) * 8.0)}

def reference(A_x, A_c_kv, A_w_q, A_w_uk, A_w_uv, A_w_o, B_x, B_w1, B_b1, B_w2, B_b2, c, w_mod):

    def _A_ref(x, c_kv, w_q, w_uk, w_uv, w_o):
        B, Sq, M = (x.shape[0], x.shape[1], x.shape[2])
        Sk = c_kv.shape[1]
        H, D = (w_q.shape[1], w_q.shape[2])
        q = torch.einsum('bqm,mhd->bhqd', x, w_q)
        k = torch.einsum('bkc,chd->bhkd', c_kv, w_uk)
        v = torch.einsum('bkc,chd->bhkd', c_kv, w_uv)
        scores = torch.einsum('bhqd,bhkd->bhqk', q, k) * D ** (-0.5)
        off = Sk - Sq
        qi = torch.arange(Sq)[:, None]
        kj = torch.arange(Sk)[None, :]
        cmask = kj <= qi + off
        scores = torch.where(cmask[None, None], scores, -1000000000.0)
        p = _softmax(scores, axis=-1)
        ctx = torch.einsum('bhqk,bhkd->bhqd', p, v)
        return torch.einsum('bhqd,hdm->bqm', ctx, w_o)

    def _B_ref(x, w1, b1, w2, b2):
        y = _layernorm(x, axis=-1)
        h = _gelu(torch.einsum('bsd,df->bsf', y, w1) + b1)
        return torch.einsum('bsf,fd->bsd', h, w2) + b2
    x = A_x
    mod = (_silu(c) @ w_mod).reshape(c.shape[0], 6, 1, -1)
    sa, ca, ga, sb, cb, gb = (mod[:, 0].clone(), mod[:, 1].clone(), mod[:, 2].clone(), mod[:, 3].clone(), mod[:, 4].clone(), mod[:, 5].clone())
    h = _layernorm(x, axis=-1) * (1 + ca) + sa
    x = x + ga * _A_ref(h, A_c_kv, A_w_q, A_w_uk, A_w_uv, A_w_o)
    h = _layernorm(x, axis=-1) * (1 + cb) + sb
    x = x + gb * _B_ref(h, B_w1, B_b1, B_w2, B_b2)
    return x
```

```cpp
#include <hip/hip_runtime.h>
#include <hip/hip_bf16.h>
#include <stdint.h>

typedef __attribute__((ext_vector_type(8))) __bf16 bf16x8;
typedef __attribute__((ext_vector_type(16))) float f32x16;
typedef __attribute__((ext_vector_type(4))) float f32x4;
typedef __attribute__((ext_vector_type(4))) uint32_t u32x4;
typedef unsigned short u16;

#define S_ 2048
#define M_ 512
#define H_ 32
#define F_ 1376

__device__ __forceinline__ float bf2f(u16 v) { return __uint_as_float((uint32_t)v << 16); }
__device__ __forceinline__ uint32_t pk2(float a, float b) {
  typedef __attribute__((ext_vector_type(2))) __bf16 bf16x2;
  typedef __attribute__((ext_vector_type(2))) float f32x2;
  f32x2 f = {a, b};
  bf16x2 r = __builtin_convertvector(f, bf16x2);
  return __builtin_bit_cast(uint32_t, r);
}
__device__ __forceinline__ int pi32(int i) { return (i & ~12) | ((i & 4) << 1) | ((i & 8) >> 1); }
__device__ __forceinline__ f32x16 mfma(u32x4 a, u32x4 b, f32x16 c) {
  return __builtin_amdgcn_mfma_f32_32x32x16_bf16(__builtin_bit_cast(bf16x8, a), __builtin_bit_cast(bf16x8, b), c, 0, 0, 0);
}
__device__ __forceinline__ u32x4 pack8(const f32x16& c, int s, float sc) {
  u32x4 o;
  o[0] = pk2(c[8 * s + 0] * sc, c[8 * s + 1] * sc);
  o[1] = pk2(c[8 * s + 2] * sc, c[8 * s + 3] * sc);
  o[2] = pk2(c[8 * s + 4] * sc, c[8 * s + 5] * sc);
  o[3] = pk2(c[8 * s + 6] * sc, c[8 * s + 7] * sc);
  return o;
}

__global__ __launch_bounds__(256) void k_mod(const u16* c, const u16* wmod, float* mod) {
  __shared__ float red[256];
  int col = blockIdx.x * 16 + (threadIdx.x & 15), kg = threadIdx.x >> 4;
  float a = 0.f;
  for (int k = kg * 32; k < kg * 32 + 32; k++) {
    float cv = bf2f(c[k]);
    float s = cv / (1.f + __expf(-cv));
    a += s * bf2f(wmod[k * 3072 + col]);
  }
  red[threadIdx.x] = a;
  __syncthreads();
  if (threadIdx.x < 16) {
    float t = 0.f;
    for (int g = 0; g < 16; g++) t += red[g * 16 + threadIdx.x];
    mod[col] = t;
  }
}

__global__ __launch_bounds__(256) void k_wimg(const u16* W, u16* img, int K, int N) {
  int idx = blockIdx.x * 256 + threadIdx.x;
  int lane = idx & 63, f = idx >> 6, nks = K / 16;
  int nt = f / nks, ks = f % nks;
  if (nt * 32 >= N) return;
  int n = 32 * nt + pi32(lane & 31), k0 = 16 * ks + 8 * (lane >> 5);
  u16 t[8];
#pragma unroll
  for (int j = 0; j < 8; j++) t[j] = W[(size_t)(k0 + j) * N + n];
  u32x4 o;
#pragma unroll
  for (int j = 0; j < 4; j++) o[j] = t[2 * j] | ((uint32_t)t[2 * j + 1] << 16);
  *(u32x4*)(img + (size_t)idx * 8) = o;
}

__global__ __launch_bounds__(256) void k_cimg(const u16* ckv, u16* img) {
  int idx = blockIdx.x * 256 + threadIdx.x;
  int lane = idx & 63, f = idx >> 6, kb = f >> 5, ks = f & 31;
  int key = 32 * kb + pi32(lane & 31);
  *(u32x4*)(img + (size_t)idx * 8) = *(const u32x4*)(ckv + (size_t)key * 512 + 16 * ks + 8 * (lane >> 5));
}

__device__ __forceinline__ float rowsum32(float v) {
#pragma unroll
  for (int o = 16; o > 0; o >>= 1) v += __shfl_xor(v, o, 32);
  return v;
}

__global__ __launch_bounds__(256) void k_h(const u16* x, const float* mod, u16* himg) {
  int row = blockIdx.x * 8 + (threadIdx.x >> 5), t = threadIdx.x & 31;
  float v[16];
  u32x4 a = *(const u32x4*)(x + row * 512 + t * 16), b = *(const u32x4*)(x + row * 512 + t * 16 + 8);
  float s = 0.f;
#pragma unroll
  for (int j = 0; j < 4; j++) {
    v[2 * j] = __uint_as_float(a[j] << 16); v[2 * j + 1] = __uint_as_float(a[j] & 0xffff0000u);
    v[8 + 2 * j] = __uint_as_float(b[j] << 16); v[8 + 2 * j + 1] = __uint_as_float(b[j] & 0xffff0000u);
  }
#pragma unroll
  for (int j = 0; j < 16; j++) s += v[j];
  float mean = rowsum32(s) * (1.f / 512.f), q = 0.f;
#pragma unroll
  for (int j = 0; j < 16; j++) { v[j] -= mean; q += v[j] * v[j]; }
  float rs = 1.f / sqrtf(rowsum32(q) * (1.f / 512.f) + 1e-5f);
#pragma unroll
  for (int hh = 0; hh < 2; hh++) {
    u32x4 o;
#pragma unroll
    for (int j = 0; j < 4; j++) {
      int m = t * 16 + hh * 8 + 2 * j;
      o[j] = pk2(v[hh * 8 + 2 * j] * rs * (1.f + mod[512 + m]) + mod[m], v[hh * 8 + 2 * j + 1] * rs * (1.f + mod[512 + m + 1]) + mod[m + 1]);
    }
    *(u32x4*)(himg + ((size_t)((row >> 5) * 32 + t) * 64 + (row & 31) + 32 * hh) * 8) = o;
  }
}

template <int MODE>
__global__ __launch_bounds__(256) void k_gemm(const u16* Aimg, const u16* Bimg, int KS, int nA, int nB,
                                               u16* o16, float* o32, const u16* p0, const float* mod, const u16* p1) {
  int wv = threadIdx.x >> 6, lane = threadIdx.x & 63;
  int tilesA = (nA + 1) / 2;
  int wid = blockIdx.x * 4 + wv;
  int ta = wid % tilesA, tb = wid / tilesA;
  if (tb * 2 >= nB) return;
  int a0 = 2 * ta, a1 = min(2 * ta + 1, nA - 1), b0 = 2 * tb, b1 = 2 * tb + 1;
  const u32x4* A0 = (const u32x4*)Aimg + (size_t)a0 * KS * 64 + lane;
  const u32x4* A1 = (const u32x4*)Aimg + (size_t)a1 * KS * 64 + lane;
  const u32x4* B0 = (const u32x4*)Bimg + (size_t)b0 * KS * 64 + lane;
  const u32x4* B1 = (const u32x4*)Bimg + (size_t)b1 * KS * 64 + lane;
  f32x16 c[2][2] = {};
#pragma unroll 4
  for (int ks = 0; ks < KS; ks++) {
    u32x4 fa0 = A0[ks * 64], fa1 = A1[ks * 64], fb0 = B0[ks * 64], fb1 = B1[ks * 64];
    c[0][0] = mfma(fa0, fb0, c[0][0]); c[0][1] = mfma(fa0, fb1, c[0][1]);
    c[1][0] = mfma(fa1, fb0, c[1][0]); c[1][1] = mfma(fa1, fb1, c[1][1]);
  }
  int hh = lane >> 5;
#pragma unroll
  for (int ia = 0; ia < 2; ia++) {
    int at = 2 * ta + ia;
    if (at >= nA) continue;
#pragma unroll
    for (int ib = 0; ib < 2; ib++) {
      int bt = 2 * tb + ib;
#pragma unroll
      for (int s = 0; s < 2; s++) {
        if (MODE == 0) {
          int h = at >> 2, dt = at & 3;
          *(u32x4*)(o16 + (((size_t)(h * 64 + bt) * 8 + 2 * dt + s) * 64 + lane) * 8) = pack8(c[ia][ib], s, 0.08838834764831845f * 1.4426950408889634f);
        } else if (MODE == 1) {
          int h = at >> 2, dt = at & 3;
          *(u32x4*)(o16 + (((size_t)(h * 64 + bt) * 8 + 2 * dt + s) * 64 + lane) * 8) = pack8(c[ia][ib], s, 1.f);
        } else if (MODE == 2) {
          int h = bt >> 2, dt = bt & 3;
          *(u32x4*)(o16 + (((size_t)(h * 128 + 2 * at + s) * 4 + dt) * 64 + lane) * 8) = pack8(c[ia][ib], s, 1.f);
        } else if (MODE == 3) {
          float* d = o32 + (((size_t)bt * 32 + 2 * at + s) * 64 + lane) * 8;
#pragma unroll
          for (int j = 0; j < 8; j++) d[j] = c[ia][ib][8 * s + j];
        } else if (MODE == 4) {
          f32x16 g;
#pragma unroll
          for (int j = 0; j < 8; j++) {
            float u = c[ia][ib][8 * s + j] + bf2f(p0[32 * at + 16 * s + 8 * hh + j]);
            float z = 0.7978845608028654f * (u + 0.044715f * u * u * u);
            float e = __expf(2.f * z);
            g[8 * s + j] = u * (1.f - 1.f / (e + 1.f));
          }
          *(u32x4*)(o16 + (((size_t)bt * 86 + 2 * at + s) * 64 + lane) * 8) = pack8(g, s, 1.f);
        } else {
          f32x16 g;
          u32x4 xv = *(const u32x4*)(p1 + (((size_t)bt * 32 + 2 * at + s) * 64 + lane) * 8);
          int m0 = 32 * at + 16 * s + 8 * hh;
#pragma unroll
          for (int j = 0; j < 8; j++) {
            float x1 = (j & 1) ? __uint_as_float(xv[j >> 1] & 0xffff0000u) : __uint_as_float(xv[j >> 1] << 16);
            g[8 * s + j] = x1 + mod[2560 + m0 + j] * (c[ia][ib][8 * s + j] + bf2f(p0[m0 + j]));
          }
          *(u32x4*)(o16 + (size_t)(32 * bt + (lane & 31)) * 512 + m0) = pack8(g, s, 1.f);
        }
      }
    }
  }
}

__global__ __launch_bounds__(256) void k_attn(const u16* Qimg, const u16* Kimg, const u16* Vimg, u16* ctx) {
  int wv = threadIdx.x >> 6, lane = threadIdx.x & 63;
  int wid = blockIdx.x * 4 + wv;
  int h = wid & 31, rb = 63 - (wid >> 5);
  int i = lane & 31, hh = lane >> 5;
  u32x4 q[8];
#pragma unroll
  for (int ks = 0; ks < 8; ks++) q[ks] = *((const u32x4*)Qimg + ((size_t)(h * 64 + rb) * 8 + ks) * 64 + lane);
  f32x16 o[4] = {};
  float m = -INFINITY, l = 0.f;
  for (int kb = 0; kb <= rb; kb++) {
    f32x16 s = {};
#pragma unroll
    for (int ks = 0; ks < 8; ks++) s = mfma(*((const u32x4*)Kimg + ((size_t)(h * 64 + kb) * 8 + ks) * 64 + lane), q[ks], s);
    if (kb == rb) {
#pragma unroll
      for (int r = 0; r < 16; r++) {
        int key = 16 * (r >> 3) + 8 * hh + (r & 7);
        if (key > i) s[r] = -INFINITY;
      }
    }
    float mx = s[0];
#pragma unroll
    for (int r = 1; r < 16; r++) mx = fmaxf(mx, s[r]);
    mx = fmaxf(mx, __shfl_xor(mx, 32));
    float mn = fmaxf(m, mx), al = exp2f(m - mn);
    m = mn;
    float ps = 0.f;
#pragma unroll
    for (int r = 0; r < 16; r++) { s[r] = exp2f(s[r] - mn); ps += s[r]; }
    l = l * al + ps;
#pragma unroll
    for (int dt = 0; dt < 4; dt++)
#pragma unroll
      for (int r = 0; r < 16; r++) o[dt][r] *= al;
#pragma unroll
    for (int s2 = 0; s2 < 2; s2++) {
      u32x4 p = pack8(s, s2, 1.f);
#pragma unroll
      for (int dt = 0; dt < 4; dt++)
        o[dt] = mfma(*((const u32x4*)Vimg + ((size_t)(h * 128 + 2 * kb + s2) * 4 + dt) * 64 + lane), p, o[dt]);
    }
  }
  l += __shfl_xor(l, 32);
  float inv = 1.f / l;
#pragma unroll
  for (int dt = 0; dt < 4; dt++)
#pragma unroll
    for (int s2 = 0; s2 < 2; s2++)
      *((u32x4*)ctx + ((size_t)rb * 256 + 8 * h + 2 * dt + s2) * 64 + lane) = pack8(o[dt], s2, inv);
}

__global__ __launch_bounds__(256) void k_red(const u16* x, const float* aimg, const float* mod, u16* x1img, u16* yimg) {
  int row = blockIdx.x * 8 + (threadIdx.x >> 5), t = threadIdx.x & 31;
  float v[16];
  float s = 0.f;
#pragma unroll
  for (int hh = 0; hh < 2; hh++) {
    u32x4 a = *(const u32x4*)(x + row * 512 + t * 16 + hh * 8);
    size_t fo = ((size_t)((row >> 5) * 32 + t) * 64 + (row & 31) + 32 * hh) * 8;
    u32x4 o;
#pragma unroll
    for (int j = 0; j < 4; j++) {
      int m = t * 16 + hh * 8 + 2 * j;
      float e0 = __uint_as_float(a[j] << 16) + mod[1024 + m] * aimg[fo + 2 * j];
      float e1 = __uint_as_float(a[j] & 0xffff0000u) + mod[1024 + m + 1] * aimg[fo + 2 * j + 1];
      o[j] = pk2(e0, e1);
      v[hh * 8 + 2 * j] = __uint_as_float(o[j] << 16);
      v[hh * 8 + 2 * j + 1] = __uint_as_float(o[j] & 0xffff0000u);
    }
    *(u32x4*)(x1img + fo) = o;
  }
#pragma unroll
  for (int j = 0; j < 16; j++) s += v[j];
  float mean = rowsum32(s) * (1.f / 512.f), q = 0.f;
#pragma unroll
  for (int j = 0; j < 16; j++) { v[j] -= mean; q += v[j] * v[j]; }
  float rs = 1.f / sqrtf(rowsum32(q) * (1.f / 512.f) + 1e-5f);
  s = 0.f;
#pragma unroll
  for (int j = 0; j < 16; j++) { int m = t * 16 + j; v[j] = v[j] * rs * (1.f + mod[2048 + m]) + mod[1536 + m]; s += v[j]; }
  mean = rowsum32(s) * (1.f / 512.f); q = 0.f;
#pragma unroll
  for (int j = 0; j < 16; j++) { v[j] -= mean; q += v[j] * v[j]; }
  rs = 1.f / sqrtf(rowsum32(q) * (1.f / 512.f) + 1e-5f);
#pragma unroll
  for (int hh = 0; hh < 2; hh++) {
    u32x4 o;
#pragma unroll
    for (int j = 0; j < 4; j++) o[j] = pk2(v[hh * 8 + 2 * j] * rs, v[hh * 8 + 2 * j + 1] * rs);
    *(u32x4*)(yimg + ((size_t)((row >> 5) * 32 + t) * 64 + (row & 31) + 32 * hh) * 8) = o;
  }
}

extern "C" void kernel_launch(void* const* d_in, const int* in_sizes, int n_in, void* d_out, int out_size,
                              void* d_ws, size_t ws_size, hipStream_t stream) {
  const u16* x = (const u16*)d_in[0]; const u16* ckv = (const u16*)d_in[1];
  const u16* wq = (const u16*)d_in[2]; const u16* wk = (const u16*)d_in[3]; const u16* wv = (const u16*)d_in[4];
  const u16* wo = (const u16*)d_in[5]; const u16* w1 = (const u16*)d_in[7]; const u16* b1 = (const u16*)d_in[8];
  const u16* w2 = (const u16*)d_in[9]; const u16* b2 = (const u16*)d_in[10];
  const u16* c = (const u16*)d_in[11]; const u16* wmod = (const u16*)d_in[12];
  char* ws = (char*)d_ws;
  const size_t MB = 1u << 20;
  float* mod = (float*)(ws);
  u16* himg = (u16*)(ws + 1 * MB); u16* Kimg = (u16*)(ws + 4 * MB); u16* Vimg = (u16*)(ws + 20 * MB);
  u16* Qimg = (u16*)(ws + 36 * MB); u16* ctx = (u16*)(ws + 52 * MB); float* aimg = (float*)(ws + 68 * MB);
  u16* x1img = (u16*)(ws + 84 * MB); u16* yimg = (u16*)(ws + 86 * MB); u16* hid = (u16*)(ws + 88 * MB);
  u16* cimg = (u16*)(ws + 96 * MB);
  u16* iq = (u16*)(ws + 100 * MB); u16* ik = (u16*)(ws + 104 * MB); u16* iv = (u16*)(ws + 108 * MB);
  u16* io = (u16*)(ws + 112 * MB); u16* i1 = (u16*)(ws + 116 * MB); u16* i2 = (u16*)(ws + 118 * MB);
  k_mod<<<192, 256, 0, stream>>>(c, wmod, mod);
  k_wimg<<<1024, 256, 0, stream>>>(wq, iq, 512, 4096);
  k_wimg<<<1024, 256, 0, stream>>>(wk, ik, 512, 4096);
  k_wimg<<<1024, 256, 0, stream>>>(wv, iv, 512, 4096);
  k_wimg<<<1024, 256, 0, stream>>>(wo, io, 4096, 512);
  k_wimg<<<344, 256, 0, stream>>>(w1, i1, 512, 1376);
  k_wimg<<<344, 256, 0, stream>>>(w2, i2, 1376, 512);
  k_cimg<<<512, 256, 0, stream>>>(ckv, cimg);
  k_h<<<256, 256, 0, stream>>>(x, mod, himg);
  k_gemm<0><<<64 * 32 / 4, 256, 0, stream>>>(iq, himg, 32, 128, 64, Qimg, nullptr, nullptr, nullptr, nullptr);
  k_gemm<1><<<64 * 32 / 4, 256, 0, stream>>>(ik, cimg, 32, 128, 64, Kimg, nullptr, nullptr, nullptr, nullptr);
  k_gemm<2><<<32 * 64 / 4, 256, 0, stream>>>(cimg, iv, 32, 64, 128, Vimg, nullptr, nullptr, nullptr, nullptr);
  k_attn<<<32 * 64 / 4, 256, 0, stream>>>(Qimg, Kimg, Vimg, ctx);
  k_gemm<3><<<8 * 32 / 4, 256, 0, stream>>>(io, ctx, 256, 16, 64, nullptr, aimg, nullptr, nullptr, nullptr);
  k_red<<<256, 256, 0, stream>>>(x, aimg, mod, x1img, yimg);
  k_gemm<4><<<22 * 32 / 4, 256, 0, stream>>>(i1, yimg, 32, 43, 64, hid, nullptr, b1, nullptr, nullptr);
  k_gemm<5><<<8 * 32 / 4, 256, 0, stream>>>(i2, hid, 86, 16, 64, (u16*)d_out, nullptr, b2, mod, x1img);
}
```

```cpp
#include <hip/hip_runtime.h>
#include <hip/hip_bf16.h>
#include <stdint.h>

typedef __attribute__((ext_vector_type(8))) __bf16 bf16x8;
typedef __attribute__((ext_vector_type(16))) float f32x16;
typedef __attribute__((ext_vector_type(4))) float f32x4;
typedef __attribute__((ext_vector_type(4))) uint32_t u32x4;
typedef unsigned short u16;

#define MBY_ (1u << 20)
#define S_ 2048
#define M_ 512
#define H_ 32
#define F_ 1376

__device__ __forceinline__ float bf2f(u16 v) { return __uint_as_float((uint32_t)v << 16); }
__device__ __forceinline__ uint32_t pk2(float a, float b) {
  typedef __attribute__((ext_vector_type(2))) __bf16 bf16x2;
  typedef __attribute__((ext_vector_type(2))) float f32x2;
  f32x2 f = {a, b};
  bf16x2 r = __builtin_convertvector(f, bf16x2);
  return __builtin_bit_cast(uint32_t, r);
}
__device__ __forceinline__ int pi32(int i) { return (i & ~12) | ((i & 4) << 1) | ((i & 8) >> 1); }
__device__ __forceinline__ f32x16 mfma(u32x4 a, u32x4 b, f32x16 c) {
  return __builtin_amdgcn_mfma_f32_32x32x16_bf16(__builtin_bit_cast(bf16x8, a), __builtin_bit_cast(bf16x8, b), c, 0, 0, 0);
}
__device__ __forceinline__ u32x4 pack8(const f32x16& c, int s, float sc) {
  u32x4 o;
  o[0] = pk2(c[8 * s + 0] * sc, c[8 * s + 1] * sc);
  o[1] = pk2(c[8 * s + 2] * sc, c[8 * s + 3] * sc);
  o[2] = pk2(c[8 * s + 4] * sc, c[8 * s + 5] * sc);
  o[3] = pk2(c[8 * s + 6] * sc, c[8 * s + 7] * sc);
  return o;
}

#define PFS 128
#define TLBS 2097152
#define TLBN 48
#define PFALL 104
#define PFT (160 * 256)
template <unsigned BYTES>
__device__ __forceinline__ uint32_t touch(const u16* p, unsigned tid) {
  uint32_t acc = 0;
#pragma unroll
  for (unsigned k = 0; k < (BYTES / PFS + PFT - 1) / PFT; k++) acc ^= *(const uint32_t*)((const char*)p + min((tid + k * PFT) * PFS, BYTES - 4));
  return acc;
}
__global__ __launch_bounds__(256) void k_mod(const u16* c, const u16* wmod, float* mod, const u16* wo, const u16* w1, const u16* w2,
                                             const u16* ckv, const u16* wk, const u16* wvp, const u16* wq, const u16* x,
                                             const u16* b1, const u16* b2, const u16* out) {
  uint32_t ic = 0;
  { unsigned long long pm; int l0, l1;
    asm volatile("s_getpc_b64 %0\n.Lkm_here:\ns_mov_b32 %1, .Lkm_end-.Lkm_here\ns_mov_b32 %2, .Lkm_end2-.Lkm_here" : "=s"(pm), "=s"(l0), "=s"(l1));
    if ((int)threadIdx.x * 64 < max(l0, l1)) ic = *(const __attribute__((address_space(1))) uint32_t*)((pm & ~3ull) + threadIdx.x * 64); }
  if (blockIdx.x >= 96) {
    unsigned tid = (blockIdx.x - 96) * 256 + threadIdx.x;
    uint32_t a2 = touch<(4u << 20)>(wq, tid);
    unsigned long long pc; int d0; unsigned len;
    asm volatile("s_getpc_b64 %0\n.Lpc_here:\ns_mov_b32 %1, .Lka_begin-.Lpc_here\ns_mov_b32 %2, .Lkt_end-.Lka_begin" : "=s"(pc), "=s"(d0), "=s"(len));
    unsigned xi = threadIdx.x * 20 + ((blockIdx.x - 96) >> 3);
    uint32_t a0 = 0, a1 = 0;
    if (xi * 64 < len) a0 = *(const __attribute__((address_space(1))) uint32_t*)(((pc + d0) & ~3ull) + xi * 64);
    if (xi < TLBN) a1 = *(const uint32_t*)((const char*)mod + (size_t)xi * TLBS);
    const char* tp = nullptr; unsigned j = xi - 64;
    if (j - 0u < 32u) tp = (const char*)out + (j - 0u) * 65536u;
    if (j - 32u < 22u) tp = (const char*)b1 + min((j - 32u) * 128u, 2748u);
    if (j - 54u < 8u) tp = (const char*)b2 + min((j - 54u) * 128u, 1020u);
    uint32_t a3 = 0;
    if (tp) a3 = *(const uint32_t*)tp;
    asm volatile(".Lkm_end2:" :: "v"(a0), "v"(a1), "v"(a2), "v"(a3), "v"(ic));
    return;
  }
  __shared__ float red[64][33];
  int t = threadIdx.x, cq = t & 3, kg = t >> 2;
  if (blockIdx.x < 4) { u32x4* fl = (u32x4*)((char*)mod + 512 * 1024); fl[blockIdx.x * 256 + t] = u32x4{0, 0, 0, 0}; }
  u32x4 wv[8];
  const char* wb = (const char*)wmod + blockIdx.x * 64;
  const unsigned wo0 = (unsigned)kg * 49152u + (unsigned)cq * 16u;
#pragma unroll
  for (int r = 0; r < 8; r++) wv[r] = *(const u32x4*)(wb + (size_t)(wo0 + r * 6144u));
  u32x4 cv = *(const u32x4*)(c + kg * 8);
  __builtin_amdgcn_sched_barrier(0);
  float acc[8] = {};
#pragma unroll
  for (int r = 0; r < 8; r++) {
    float x = (r & 1) ? __uint_as_float(cv[r >> 1] & 0xffff0000u) : __uint_as_float(cv[r >> 1] << 16);
    float s = x / (1.f + __expf(-x));
#pragma unroll
    for (int j = 0; j < 4; j++) {
      acc[2 * j] += s * __uint_as_float(wv[r][j] << 16);
      acc[2 * j + 1] += s * __uint_as_float(wv[r][j] & 0xffff0000u);
    }
  }
#pragma unroll
  for (int j = 0; j < 8; j++) red[kg][cq * 8 + j] = acc[j];
  __syncthreads();
  int col = t >> 3, part = t & 7;
  float s = 0.f;
#pragma unroll
  for (int r = 0; r < 8; r++) s += red[part * 8 + r][col];
  s += __builtin_bit_cast(float, __builtin_amdgcn_update_dpp(0, __builtin_bit_cast(int, s), 0xb1, 0xf, 0xf, true));
  s += __builtin_bit_cast(float, __builtin_amdgcn_update_dpp(0, __builtin_bit_cast(int, s), 0x4e, 0xf, 0xf, true));
  s += __builtin_bit_cast(float, __builtin_amdgcn_update_dpp(0, __builtin_bit_cast(int, s), 0x141, 0xf, 0xf, true));
  if (part == 0) mod[blockIdx.x * 32 + col] = s;
  asm volatile(".Lkm_end:" :: "v"(ic));
}

__device__ __forceinline__ float rowsum32(float v) {
#pragma unroll
  for (int o = 16; o > 0; o >>= 1) v += __shfl_xor(v, o, 32);
  return v;
}

#define A_ASM \
    ".Lka_begin:\n\t" \
    "s_mov_b32 s8, %[xlo]\n\t" \
    "s_mov_b32 s9, %[xhi]\n\t" \
    "s_mov_b32 s10, 0x200000\n\t" \
    "s_mov_b32 s11, 0x00020000\n\t" \
    "s_mov_b32 s16, %[wslo]\n\t" \
    "s_mov_b32 s17, %[wshi]\n\t" \
    "s_mov_b32 s18, 0x10000000\n\t" \
    "s_mov_b32 s19, 0x00020000\n\t" \
    "s_mov_b32 s21, %[hh]\n\t" \
    "s_mov_b32 s22, %[jj]\n\t" \
    "s_mov_b32 s20, %[wv]\n\t" \
    "s_cmp_lt_u32 s20, 2\n\t" \
    "s_cselect_b32 s12, %[klo], %[vlo]\n\t" \
    "s_cselect_b32 s13, %[khi], %[vhi]\n\t" \
    "s_mov_b32 s14, 0x400000\n\t" \
    "s_mov_b32 s15, 0x00020000\n\t" \
    "v_mbcnt_lo_u32_b32 v115, -1, 0\n\t" \
    "v_mbcnt_hi_u32_b32 v115, -1, v115\n\t" \
    "s_mov_b32 s32, %[alo]\n\t" \
    "s_mov_b32 s33, %[ahi]\n\t" \
    "s_mov_b32 s34, 0x200000\n\t" \
    "s_mov_b32 s35, 0x00020000\n\t" \
    "v_lshrrev_b32 v128, 5, v115\n\t" \
    "v_lshlrev_b32 v128, 10, v128\n\t" \
    "v_and_b32 v129, 31, v115\n\t" \
    "v_lshlrev_b32 v130, 5, v129\n\t" \
    "v_add_u32 v128, v128, v130\n\t" \
    "s_lshl_b32 s23, %[bid], 13\n\t" \
    "s_lshl_b32 s24, s20, 11\n\t" \
    "s_add_u32 s23, s23, s24\n\t" \
    "v_lshlrev_b32 v129, 6, v129\n\t" \
    "buffer_load_dwordx4 v[132:135], v128, s[32:35], s23 offen\n\t" \
    "buffer_load_dwordx4 v[136:139], v128, s[32:35], s23 offen offset:16\n\t" \
    "buffer_load_dwordx4 v[140:143], v129, s[16:19], 0 offen offset:0\n\t" \
    "buffer_load_dwordx4 v[144:147], v129, s[16:19], 0 offen offset:16\n\t" \
    "buffer_load_dwordx4 v[148:151], v129, s[16:19], 0 offen offset:32\n\t" \
    "buffer_load_dwordx4 v[152:155], v129, s[16:19], 0 offen offset:48\n\t" \
    "buffer_load_dwordx4 v[156:159], v129, s[16:19], 0 offen offset:2048\n\t" \
    "buffer_load_dwordx4 v[160:163], v129, s[16:19], 0 offen offset:2064\n\t" \
    "buffer_load_dwordx4 v[164:167], v129, s[16:19], 0 offen offset:2080\n\t" \
    "buffer_load_dwordx4 v[168:171], v129, s[16:19], 0 offen offset:2096\n\t" \
    "v_lshrrev_b32 v111, 3, v115\n\t" \
    "v_lshlrev_b32 v111, 10, v111\n\t" \
    "s_lshl_b32 s23, s22, 18\n\t" \
    "s_lshl_b32 s24, s20, 16\n\t" \
    "s_add_u32 s23, s23, s24\n\t" \
    "v_add_u32 v111, s23, v111\n\t" \
    "v_and_b32 v112, 7, v115\n\t" \
    "v_bfe_u32 v113, v115, 4, 1\n\t" \
    "v_lshlrev_b32 v113, 1, v113\n\t" \
    "v_xor_b32 v112, v112, v113\n\t" \
    "v_lshlrev_b32 v112, 4, v112\n\t" \
    "v_xor_b32 v113, 0, v112\n\t" \
    "v_add_u32 v98, v111, v113\n\t" \
    "v_add_u32 v98, 0, v98\n\t" \
    "v_xor_b32 v113, 0, v112\n\t" \
    "v_add_u32 v99, v111, v113\n\t" \
    "v_add_u32 v99, 7168, v99\n\t" \
    "v_xor_b32 v113, 64, v112\n\t" \
    "v_add_u32 v100, v111, v113\n\t" \
    "v_add_u32 v100, 14336, v100\n\t" \
    "v_xor_b32 v113, 64, v112\n\t" \
    "v_add_u32 v101, v111, v113\n\t" \
    "v_add_u32 v101, 21504, v101\n\t" \
    "v_xor_b32 v113, 0, v112\n\t" \
    "v_add_u32 v102, v111, v113\n\t" \
    "v_add_u32 v102, 32768, v102\n\t" \
    "v_xor_b32 v113, 0, v112\n\t" \
    "v_add_u32 v103, v111, v113\n\t" \
    "v_add_u32 v103, 39936, v103\n\t" \
    "v_xor_b32 v113, 64, v112\n\t" \
    "v_add_u32 v104, v111, v113\n\t" \
    "v_add_u32 v104, 47104, v104\n\t" \
    "v_xor_b32 v113, 64, v112\n\t" \
    "v_add_u32 v105, v111, v113\n\t" \
    "v_add_u32 v105, 54272, v105\n\t" \
    "s_lshl_b32 s25, s20, 13\n\t" \
    "s_add_u32 s25, s25, 98304\n\t" \
    "v_lshrrev_b32 v111, 4, v115\n\t" \
    "v_bfe_u32 v112, v115, 1, 3\n\t" \
    "v_xor_b32 v112, v112, v111\n\t" \
    "v_lshlrev_b32 v112, 5, v112\n\t" \
    "v_and_b32 v113, 1, v115\n\t" \
    "v_lshlrev_b32 v113, 4, v113\n\t" \
    "v_or_b32 v112, v112, v113\n\t" \
    "v_lshlrev_b32 v111, 13, v111\n\t" \
    "s_and_b32 s23, s20, 1\n\t" \
    "s_lshl_b32 s23, s23, 17\n\t" \
    "s_lshl_b32 s24, s21, 8\n\t" \
    "s_add_u32 s23, s23, s24\n\t" \
    "v_add_u32 v111, s23, v111\n\t" \
    "v_xor_b32 v113, 0, v112\n\t" \
    "v_add_u32 v106, v111, v113\n\t" \
    "v_add_u32 v106, 0, v106\n\t" \
    "v_xor_b32 v113, 0, v112\n\t" \
    "v_add_u32 v107, v111, v113\n\t" \
    "v_add_u32 v107, 31744, v107\n\t" \
    "v_xor_b32 v113, 128, v112\n\t" \
    "v_add_u32 v108, v111, v113\n\t" \
    "v_add_u32 v108, 63488, v108\n\t" \
    "v_xor_b32 v113, 128, v112\n\t" \
    "v_add_u32 v109, v111, v113\n\t" \
    "v_add_u32 v109, 95232, v109\n\t" \
    "s_lshl_b32 s26, s20, 12\n\t" \
    "s_mov_b32 s28, 0\n\t" \
    "s_add_u32 m0, s26, 0\n\t" \
    "s_nop 0\n\t" \
    "buffer_load_dwordx4 v106, s[12:15], s28 offen offset:0 lds\n\t" \
    "buffer_load_dwordx4 v107, s[12:15], s28 offen offset:1024 lds\n\t" \
    "buffer_load_dwordx4 v108, s[12:15], s28 offen offset:2048 lds\n\t" \
    "buffer_load_dwordx4 v109, s[12:15], s28 offen offset:3072 lds\n\t" \
    "s_mov_b32 s27, 0\n\t" \
    "s_add_u32 m0, s25, 0\n\t" \
    "s_nop 0\n\t" \
    "buffer_load_dwordx4 v98, s[8:11], s27 offen offset:0 lds\n\t" \
    "buffer_load_dwordx4 v99, s[8:11], s27 offen offset:1024 lds\n\t" \
    "buffer_load_dwordx4 v100, s[8:11], s27 offen offset:2048 lds\n\t" \
    "buffer_load_dwordx4 v101, s[8:11], s27 offen offset:3072 lds\n\t" \
    "s_add_u32 m0, s25, 4096\n\t" \
    "s_nop 0\n\t" \
    "buffer_load_dwordx4 v102, s[8:11], s27 offen offset:0 lds\n\t" \
    "buffer_load_dwordx4 v103, s[8:11], s27 offen offset:1024 lds\n\t" \
    "buffer_load_dwordx4 v104, s[8:11], s27 offen offset:2048 lds\n\t" \
    "buffer_load_dwordx4 v105, s[8:11], s27 offen offset:3072 lds\n\t" \
    "s_mov_b32 s28, 262144\n\t" \
    "s_add_u32 m0, s26, 16384\n\t" \
    "s_nop 0\n\t" \
    "buffer_load_dwordx4 v106, s[12:15], s28 offen offset:0 lds\n\t" \
    "buffer_load_dwordx4 v107, s[12:15], s28 offen offset:1024 lds\n\t" \
    "buffer_load_dwordx4 v108, s[12:15], s28 offen offset:2048 lds\n\t" \
    "buffer_load_dwordx4 v109, s[12:15], s28 offen offset:3072 lds\n\t" \
    "s_mov_b32 s28, 524288\n\t" \
    "s_add_u32 m0, s26, 32768\n\t" \
    "s_nop 0\n\t" \
    "buffer_load_dwordx4 v106, s[12:15], s28 offen offset:0 lds\n\t" \
    "buffer_load_dwordx4 v107, s[12:15], s28 offen offset:1024 lds\n\t" \
    "buffer_load_dwordx4 v108, s[12:15], s28 offen offset:2048 lds\n\t" \
    "buffer_load_dwordx4 v109, s[12:15], s28 offen offset:3072 lds\n\t" \
    "s_mov_b32 s27, 128\n\t" \
    "s_add_u32 m0, s25, 32768\n\t" \
    "s_nop 0\n\t" \
    "buffer_load_dwordx4 v98, s[8:11], s27 offen offset:0 lds\n\t" \
    "buffer_load_dwordx4 v99, s[8:11], s27 offen offset:1024 lds\n\t" \
    "buffer_load_dwordx4 v100, s[8:11], s27 offen offset:2048 lds\n\t" \
    "buffer_load_dwordx4 v101, s[8:11], s27 offen offset:3072 lds\n\t" \
    "s_add_u32 m0, s25, 36864\n\t" \
    "s_nop 0\n\t" \
    "buffer_load_dwordx4 v102, s[8:11], s27 offen offset:0 lds\n\t" \
    "buffer_load_dwordx4 v103, s[8:11], s27 offen offset:1024 lds\n\t" \
    "buffer_load_dwordx4 v104, s[8:11], s27 offen offset:2048 lds\n\t" \
    "buffer_load_dwordx4 v105, s[8:11], s27 offen offset:3072 lds\n\t" \
    "s_mov_b32 s28, 786432\n\t" \
    "s_add_u32 m0, s26, 49152\n\t" \
    "s_nop 0\n\t" \
    "buffer_load_dwordx4 v106, s[12:15], s28 offen offset:0 lds\n\t" \
    "buffer_load_dwordx4 v107, s[12:15], s28 offen offset:1024 lds\n\t" \
    "buffer_load_dwordx4 v108, s[12:15], s28 offen offset:2048 lds\n\t" \
    "buffer_load_dwordx4 v109, s[12:15], s28 offen offset:3072 lds\n\t" \
    "s_mov_b32 s28, 1048576\n\t" \
    "s_add_u32 m0, s26, 65536\n\t" \
    "s_nop 0\n\t" \
    "buffer_load_dwordx4 v106, s[12:15], s28 offen offset:0 lds\n\t" \
    "buffer_load_dwordx4 v107, s[12:15], s28 offen offset:1024 lds\n\t" \
    "buffer_load_dwordx4 v108, s[12:15], s28 offen offset:2048 lds\n\t" \
    "buffer_load_dwordx4 v109, s[12:15], s28 offen offset:3072 lds\n\t" \
    "v_and_b32 v111, 3, v115\n\t" \
    "v_bfe_u32 v112, v115, 2, 2\n\t" \
    "v_lshlrev_b32 v112, 3, v112\n\t" \
    "v_add_u32 v111, v111, v112\n\t" \
    "v_lshlrev_b32 v111, 7, v111\n\t" \
    "s_and_b32 s23, s20, 1\n\t" \
    "s_lshl_b32 s23, s23, 14\n\t" \
    "s_add_u32 s23, s23, 98304\n\t" \
    "v_add_u32 v111, s23, v111\n\t" \
    "v_bfe_u32 v112, v115, 1, 1\n\t" \
    "v_lshlrev_b32 v112, 1, v112\n\t" \
    "v_bfe_u32 v113, v115, 3, 1\n\t" \
    "v_lshlrev_b32 v113, 2, v113\n\t" \
    "v_or_b32 v112, v112, v113\n\t" \
    "v_lshrrev_b32 v113, 4, v115\n\t" \
    "v_xor_b32 v112, v112, v113\n\t" \
    "v_lshlrev_b32 v112, 4, v112\n\t" \
    "v_add_u32 v80, v111, v112\n\t" \
    "v_xor_b32 v112, 64, v112\n\t" \
    "v_add_u32 v81, v111, v112\n\t" \
    "v_lshrrev_b32 v111, 4, v115\n\t" \
    "v_lshlrev_b32 v111, 3, v111\n\t" \
    "v_bfe_u32 v112, v115, 2, 2\n\t" \
    "v_add_u32 v111, v111, v112\n\t" \
    "v_lshlrev_b32 v111, 8, v111\n\t" \
    "v_and_b32 v113, 3, v115\n\t" \
    "v_lshlrev_b32 v113, 3, v113\n\t" \
    "v_add_u32 v111, v111, v113\n\t" \
    "s_lshr_b32 s23, s20, 1\n\t" \
    "s_lshl_b32 s23, s23, 13\n\t" \
    "v_add_u32 v111, s23, v111\n\t" \
    "v_bfe_u32 v113, v115, 4, 1\n\t" \
    "v_lshlrev_b32 v113, 2, v113\n\t" \
    "v_or_b32 v112, v112, v113\n\t" \
    "v_xor_b32 v113, 0, v112\n\t" \
    "v_lshlrev_b32 v113, 5, v113\n\t" \
    "v_add_u32 v82, v111, v113\n\t" \
    "v_add_u32 v90, 0x10000, v82\n\t" \
    "v_xor_b32 v113, 1, v112\n\t" \
    "v_lshlrev_b32 v113, 5, v113\n\t" \
    "v_add_u32 v83, v111, v113\n\t" \
    "v_add_u32 v91, 0x10000, v83\n\t" \
    "v_xor_b32 v113, 2, v112\n\t" \
    "v_lshlrev_b32 v113, 5, v113\n\t" \
    "v_add_u32 v84, v111, v113\n\t" \
    "v_add_u32 v92, 0x10000, v84\n\t" \
    "v_xor_b32 v113, 3, v112\n\t" \
    "v_lshlrev_b32 v113, 5, v113\n\t" \
    "v_add_u32 v85, v111, v113\n\t" \
    "v_add_u32 v93, 0x10000, v85\n\t" \
    "v_xor_b32 v113, 4, v112\n\t" \
    "v_lshlrev_b32 v113, 5, v113\n\t" \
    "v_add_u32 v86, v111, v113\n\t" \
    "v_add_u32 v94, 0x10000, v86\n\t" \
    "v_xor_b32 v113, 5, v112\n\t" \
    "v_lshlrev_b32 v113, 5, v113\n\t" \
    "v_add_u32 v87, v111, v113\n\t" \
    "v_add_u32 v95, 0x10000, v87\n\t" \
    "v_xor_b32 v113, 6, v112\n\t" \
    "v_lshlrev_b32 v113, 5, v113\n\t" \
    "v_add_u32 v88, v111, v113\n\t" \
    "v_add_u32 v96, 0x10000, v88\n\t" \
    "v_xor_b32 v113, 7, v112\n\t" \
    "v_lshlrev_b32 v113, 5, v113\n\t" \
    "v_add_u32 v89, v111, v113\n\t" \
    "v_add_u32 v97, 0x10000, v89\n\t" \
    "s_waitcnt vmcnt(45)\n\t" \
    "v_lshlrev_b32 v176, 16, v132\n\t" \
    "v_and_b32 v177, 0xffff0000, v132\n\t" \
    "v_lshlrev_b32 v178, 16, v133\n\t" \
    "v_and_b32 v179, 0xffff0000, v133\n\t" \
    "v_lshlrev_b32 v180, 16, v134\n\t" \
    "v_and_b32 v181, 0xffff0000, v134\n\t" \
    "v_lshlrev_b32 v182, 16, v135\n\t" \
    "v_and_b32 v183, 0xffff0000, v135\n\t" \
    "s_waitcnt vmcnt(44)\n\t" \
    "v_lshlrev_b32 v184, 16, v136\n\t" \
    "v_and_b32 v185, 0xffff0000, v136\n\t" \
    "v_lshlrev_b32 v186, 16, v137\n\t" \
    "v_and_b32 v187, 0xffff0000, v137\n\t" \
    "v_lshlrev_b32 v188, 16, v138\n\t" \
    "v_and_b32 v189, 0xffff0000, v138\n\t" \
    "v_lshlrev_b32 v190, 16, v139\n\t" \
    "v_and_b32 v191, 0xffff0000, v139\n\t" \
    "v_add_f32 v192, v176, v177\n\t" \
    "v_add_f32 v192, v192, v178\n\t" \
    "v_add_f32 v192, v192, v179\n\t" \
    "v_add_f32 v192, v192, v180\n\t" \
    "v_add_f32 v192, v192, v181\n\t" \
    "v_add_f32 v192, v192, v182\n\t" \
    "v_add_f32 v192, v192, v183\n\t" \
    "v_add_f32 v192, v192, v184\n\t" \
    "v_add_f32 v192, v192, v185\n\t" \
    "v_add_f32 v192, v192, v186\n\t" \
    "v_add_f32 v192, v192, v187\n\t" \
    "v_add_f32 v192, v192, v188\n\t" \
    "v_add_f32 v192, v192, v189\n\t" \
    "v_add_f32 v192, v192, v190\n\t" \
    "v_add_f32 v192, v192, v191\n\t" \
    "s_nop 1\n\t" \
    "v_add_f32_dpp v192, v192, v192 quad_perm:[1,0,3,2] row_mask:0xf bank_mask:0xf\n\t" \
    "s_nop 1\n\t" \
    "v_add_f32_dpp v192, v192, v192 quad_perm:[2,3,0,1] row_mask:0xf bank_mask:0xf\n\t" \
    "s_nop 1\n\t" \
    "v_add_f32_dpp v192, v192, v192 row_half_mirror row_mask:0xf bank_mask:0xf\n\t" \
    "s_nop 1\n\t" \
    "v_add_f32_dpp v192, v192, v192 row_mirror row_mask:0xf bank_mask:0xf\n\t" \
    "v_mov_b32 v193, v192\n\t" \
    "s_nop 1\n\t" \
    "v_permlane16_swap_b32 v193, v192\n\t" \
    "v_add_f32 v192, v192, v193\n\t" \
    "v_mul_f32 v192, 0x3b000000, v192\n\t" \
    "v_sub_f32 v176, v176, v192\n\t" \
    "v_sub_f32 v177, v177, v192\n\t" \
    "v_sub_f32 v178, v178, v192\n\t" \
    "v_sub_f32 v179, v179, v192\n\t" \
    "v_sub_f32 v180, v180, v192\n\t" \
    "v_sub_f32 v181, v181, v192\n\t" \
    "v_sub_f32 v182, v182, v192\n\t" \
    "v_sub_f32 v183, v183, v192\n\t" \
    "v_sub_f32 v184, v184, v192\n\t" \
    "v_sub_f32 v185, v185, v192\n\t" \
    "v_sub_f32 v186, v186, v192\n\t" \
    "v_sub_f32 v187, v187, v192\n\t" \
    "v_sub_f32 v188, v188, v192\n\t" \
    "v_sub_f32 v189, v189, v192\n\t" \
    "v_sub_f32 v190, v190, v192\n\t" \
    "v_sub_f32 v191, v191, v192\n\t" \
    "v_mul_f32 v194, v176, v176\n\t" \
    "v_fmac_f32 v194, v177, v177\n\t" \
    "v_fmac_f32 v194, v178, v178\n\t" \
    "v_fmac_f32 v194, v179, v179\n\t" \
    "v_fmac_f32 v194, v180, v180\n\t" \
    "v_fmac_f32 v194, v181, v181\n\t" \
    "v_fmac_f32 v194, v182, v182\n\t" \
    "v_fmac_f32 v194, v183, v183\n\t" \
    "v_fmac_f32 v194, v184, v184\n\t" \
    "v_fmac_f32 v194, v185, v185\n\t" \
    "v_fmac_f32 v194, v186, v186\n\t" \
    "v_fmac_f32 v194, v187, v187\n\t" \
    "v_fmac_f32 v194, v188, v188\n\t" \
    "v_fmac_f32 v194, v189, v189\n\t" \
    "v_fmac_f32 v194, v190, v190\n\t" \
    "v_fmac_f32 v194, v191, v191\n\t" \
    "s_nop 1\n\t" \
    "v_add_f32_dpp v194, v194, v194 quad_perm:[1,0,3,2] row_mask:0xf bank_mask:0xf\n\t" \
    "s_nop 1\n\t" \
    "v_add_f32_dpp v194, v194, v194 quad_perm:[2,3,0,1] row_mask:0xf bank_mask:0xf\n\t" \
    "s_nop 1\n\t" \
    "v_add_f32_dpp v194, v194, v194 row_half_mirror row_mask:0xf bank_mask:0xf\n\t" \
    "s_nop 1\n\t" \
    "v_add_f32_dpp v194, v194, v194 row_mirror row_mask:0xf bank_mask:0xf\n\t" \
    "v_mov_b32 v193, v194\n\t" \
    "s_nop 1\n\t" \
    "v_permlane16_swap_b32 v193, v194\n\t" \
    "v_add_f32 v194, v194, v193\n\t" \
    "v_mov_b32 v195, 0x3727c5ac\n\t" \
    "v_fmac_f32 v195, 0x3b000000, v194\n\t" \
    "v_rsq_f32 v195, v195\n\t" \
    "v_mov_b32 v196, 0x3e0293ee\n\t" \
    "v_mul_f32 v195, v195, v196\n\t" \
    "s_waitcnt vmcnt(39)\n\t" \
    "v_fma_f32 v156, v156, v195, v195\n\t" \
    "v_mul_f32 v140, v196, v140\n\t" \
    "v_fmac_f32 v140, v176, v156\n\t" \
    "v_fma_f32 v157, v157, v195, v195\n\t" \
    "v_mul_f32 v141, v196, v141\n\t" \
    "v_fmac_f32 v141, v177, v157\n\t" \
    "v_fma_f32 v158, v158, v195, v195\n\t" \
    "v_mul_f32 v142, v196, v142\n\t" \
    "v_fmac_f32 v142, v178, v158\n\t" \
    "v_fma_f32 v159, v159, v195, v195\n\t" \
    "v_mul_f32 v143, v196, v143\n\t" \
    "v_fmac_f32 v143, v179, v159\n\t" \
    "s_waitcnt vmcnt(38)\n\t" \
    "v_fma_f32 v160, v160, v195, v195\n\t" \
    "v_mul_f32 v144, v196, v144\n\t" \
    "v_fmac_f32 v144, v180, v160\n\t" \
    "v_fma_f32 v161, v161, v195, v195\n\t" \
    "v_mul_f32 v145, v196, v145\n\t" \
    "v_fmac_f32 v145, v181, v161\n\t" \
    "v_fma_f32 v162, v162, v195, v195\n\t" \
    "v_mul_f32 v146, v196, v146\n\t" \
    "v_fmac_f32 v146, v182, v162\n\t" \
    "v_fma_f32 v163, v163, v195, v195\n\t" \
    "v_mul_f32 v147, v196, v147\n\t" \
    "v_fmac_f32 v147, v183, v163\n\t" \
    "s_waitcnt vmcnt(37)\n\t" \
    "v_fma_f32 v164, v164, v195, v195\n\t" \
    "v_mul_f32 v148, v196, v148\n\t" \
    "v_fmac_f32 v148, v184, v164\n\t" \
    "v_fma_f32 v165, v165, v195, v195\n\t" \
    "v_mul_f32 v149, v196, v149\n\t" \
    "v_fmac_f32 v149, v185, v165\n\t" \
    "v_fma_f32 v166, v166, v195, v195\n\t" \
    "v_mul_f32 v150, v196, v150\n\t" \
    "v_fmac_f32 v150, v186, v166\n\t" \
    "v_fma_f32 v167, v167, v195, v195\n\t" \
    "v_mul_f32 v151, v196, v151\n\t" \
    "v_fmac_f32 v151, v187, v167\n\t" \
    "s_waitcnt vmcnt(36)\n\t" \
    "v_fma_f32 v168, v168, v195, v195\n\t" \
    "v_mul_f32 v152, v196, v152\n\t" \
    "v_fmac_f32 v152, v188, v168\n\t" \
    "v_fma_f32 v169, v169, v195, v195\n\t" \
    "v_mul_f32 v153, v196, v153\n\t" \
    "v_fmac_f32 v153, v189, v169\n\t" \
    "v_fma_f32 v170, v170, v195, v195\n\t" \
    "v_mul_f32 v154, v196, v154\n\t" \
    "v_fmac_f32 v154, v190, v170\n\t" \
    "v_fma_f32 v171, v171, v195, v195\n\t" \
    "v_mul_f32 v155, v196, v155\n\t" \
    "v_fmac_f32 v155, v191, v171\n\t" \
    "v_cvt_pk_bf16_f32 v132, v140, v141\n\t" \
    "v_cvt_pk_bf16_f32 v133, v142, v143\n\t" \
    "v_cvt_pk_bf16_f32 v134, v144, v145\n\t" \
    "v_cvt_pk_bf16_f32 v135, v146, v147\n\t" \
    "v_cvt_pk_bf16_f32 v136, v148, v149\n\t" \
    "v_cvt_pk_bf16_f32 v137, v150, v151\n\t" \
    "v_cvt_pk_bf16_f32 v138, v152, v153\n\t" \
    "v_cvt_pk_bf16_f32 v139, v154, v155\n\t" \
    "v_bfe_u32 v128, v115, 1, 4\n\t" \
    "v_lshlrev_b32 v128, 10, v128\n\t" \
    "v_and_b32 v129, 1, v115\n\t" \
    "v_lshlrev_b32 v129, 9, v129\n\t" \
    "v_add_u32 v128, v128, v129\n\t" \
    "v_lshrrev_b32 v129, 5, v115\n\t" \
    "v_lshlrev_b32 v129, 4, v129\n\t" \
    "v_add_u32 v128, v128, v129\n\t" \
    "s_lshr_b32 s23, %[bid], 1\n\t" \
    "s_lshl_b32 s23, s23, 14\n\t" \
    "s_and_b32 s24, %[bid], 1\n\t" \
    "s_lshl_b32 s24, s24, 7\n\t" \
    "s_add_u32 s23, s23, s24\n\t" \
    "s_lshl_b32 s24, s20, 5\n\t" \
    "s_add_u32 s23, s23, s24\n\t" \
    "s_add_u32 s23, s23, 0x100000\n\t" \
    "buffer_store_dwordx4 v[132:135], v128, s[16:19], s23 offen sc1\n\t" \
    "buffer_store_dwordx4 v[136:139], v128, s[16:19], s23 offen offset:256 sc1\n\t" \
    "s_waitcnt vmcnt(26)\n\t" \
    "s_barrier\n\t" \
    "ds_read_b64_tr_b16 v[64:65], v82 offset:0\n\t" \
    "ds_read_b64_tr_b16 v[66:67], v82 offset:1024\n\t" \
    "ds_read_b128 v[0:3], v80 offset:0\n\t" \
    "ds_read_b128 v[4:7], v80 offset:512\n\t" \
    "ds_read_b128 v[8:11], v80 offset:4096\n\t" \
    "ds_read_b128 v[12:15], v80 offset:4608\n\t" \
    "ds_read_b128 v[16:19], v80 offset:8192\n\t" \
    "ds_read_b128 v[20:23], v80 offset:8704\n\t" \
    "ds_read_b128 v[24:27], v80 offset:12288\n\t" \
    "ds_read_b128 v[28:31], v80 offset:12800\n\t" \
    "ds_read_b64_tr_b16 v[68:69], v83 offset:0\n\t" \
    "ds_read_b64_tr_b16 v[70:71], v83 offset:1024\n\t" \
    "s_cmp_ge_u32 s20, 2\n\t" \
    "s_cbranch_scc1 VP_%=\n\t" \
    "s_waitcnt lgkmcnt(9)\n\t" \
    "v_mfma_f32_16x16x32_bf16 v[128:131], v[64:67], v[0:3], 0\n\t" \
    "s_waitcnt lgkmcnt(8)\n\t" \
    "v_mfma_f32_16x16x32_bf16 v[132:135], v[64:67], v[4:7], 0\n\t" \
    "ds_read_b64_tr_b16 v[72:73], v84 offset:0\n\t" \
    "s_waitcnt lgkmcnt(8)\n\t" \
    "v_mfma_f32_16x16x32_bf16 v[136:139], v[64:67], v[8:11], 0\n\t" \
    "ds_read_b64_tr_b16 v[74:75], v84 offset:1024\n\t" \
    "s_waitcnt lgkmcnt(8)\n\t" \
    "v_mfma_f32_16x16x32_bf16 v[140:143], v[64:67], v[12:15], 0\n\t" \
    "ds_read_b128 v[32:35], v81 offset:0\n\t" \
    "s_waitcnt lgkmcnt(8)\n\t" \
    "v_mfma_f32_16x16x32_bf16 v[144:147], v[64:67], v[16:19], 0\n\t" \
    "ds_read_b128 v[36:39], v81 offset:512\n\t" \
    "s_waitcnt lgkmcnt(8)\n\t" \
    "v_mfma_f32_16x16x32_bf16 v[148:151], v[64:67], v[20:23], 0\n\t" \
    "ds_read_b128 v[40:43], v81 offset:4096\n\t" \
    "s_waitcnt lgkmcnt(8)\n\t" \
    "v_mfma_f32_16x16x32_bf16 v[152:155], v[64:67], v[24:27], 0\n\t" \
    "ds_read_b128 v[44:47], v81 offset:4608\n\t" \
    "s_waitcnt lgkmcnt(8)\n\t" \
    "v_mfma_f32_16x16x32_bf16 v[156:159], v[64:67], v[28:31], 0\n\t" \
    "s_waitcnt lgkmcnt(6)\n\t" \
    "v_mfma_f32_16x16x32_bf16 v[160:163], v[68:71], v[0:3], 0\n\t" \
    "v_mfma_f32_16x16x32_bf16 v[164:167], v[68:71], v[4:7], 0\n\t" \
    "ds_read_b64_tr_b16 v[76:77], v85 offset:0\n\t" \
    "v_mfma_f32_16x16x32_bf16 v[168:171], v[68:71], v[8:11], 0\n\t" \
    "ds_read_b64_tr_b16 v[78:79], v85 offset:1024\n\t" \
    "v_mfma_f32_16x16x32_bf16 v[172:175], v[68:71], v[12:15], 0\n\t" \
    "ds_read_b128 v[48:51], v81 offset:8192\n\t" \
    "v_mfma_f32_16x16x32_bf16 v[176:179], v[68:71], v[16:19], 0\n\t" \
    "ds_read_b128 v[52:55], v81 offset:8704\n\t" \
    "v_mfma_f32_16x16x32_bf16 v[180:183], v[68:71], v[20:23], 0\n\t" \
    "ds_read_b128 v[56:59], v81 offset:12288\n\t" \
    "v_mfma_f32_16x16x32_bf16 v[184:187], v[68:71], v[24:27], 0\n\t" \
    "ds_read_b128 v[60:63], v81 offset:12800\n\t" \
    "v_mfma_f32_16x16x32_bf16 v[188:191], v[68:71], v[28:31], 0\n\t" \
    "s_waitcnt lgkmcnt(10)\n\t" \
    "v_mfma_f32_16x16x32_bf16 v[192:195], v[72:75], v[0:3], 0\n\t" \
    "v_mfma_f32_16x16x32_bf16 v[196:199], v[72:75], v[4:7], 0\n\t" \
    "ds_read_b64_tr_b16 v[64:65], v86 offset:0\n\t" \
    "v_mfma_f32_16x16x32_bf16 v[200:203], v[72:75], v[8:11], 0\n\t" \
    "ds_read_b64_tr_b16 v[66:67], v86 offset:1024\n\t" \
    "v_mfma_f32_16x16x32_bf16 v[204:207], v[72:75], v[12:15], 0\n\t" \
    "s_mov_b32 s28, 1310720\n\t" \
    "s_add_u32 m0, s26, 81920\n\t" \
    "v_mfma_f32_16x16x32_bf16 v[208:211], v[72:75], v[16:19], 0\n\t" \
    "buffer_load_dwordx4 v106, s[12:15], s28 offen offset:0 lds\n\t" \
    "v_mfma_f32_16x16x32_bf16 v[212:215], v[72:75], v[20:23], 0\n\t" \
    "v_mfma_f32_16x16x32_bf16 v[216:219], v[72:75], v[24:27], 0\n\t" \
    "v_mfma_f32_16x16x32_bf16 v[220:223], v[72:75], v[28:31], 0\n\t" \
    "s_waitcnt lgkmcnt(6)\n\t" \
    "v_mfma_f32_16x16x32_bf16 v[224:227], v[76:79], v[0:3], 0\n\t" \
    "v_mfma_f32_16x16x32_bf16 v[228:231], v[76:79], v[4:7], 0\n\t" \
    "ds_read_b64_tr_b16 v[68:69], v87 offset:0\n\t" \
    "v_mfma_f32_16x16x32_bf16 v[232:235], v[76:79], v[8:11], 0\n\t" \
    "ds_read_b64_tr_b16 v[70:71], v87 offset:1024\n\t" \
    "v_mfma_f32_16x16x32_bf16 v[236:239], v[76:79], v[12:15], 0\n\t" \
    "v_mfma_f32_16x16x32_bf16 v[240:243], v[76:79], v[16:19], 0\n\t" \
    "buffer_load_dwordx4 v107, s[12:15], s28 offen offset:1024 lds\n\t" \
    "v_mfma_f32_16x16x32_bf16 v[244:247], v[76:79], v[20:23], 0\n\t" \
    "v_mfma_f32_16x16x32_bf16 v[248:251], v[76:79], v[24:27], 0\n\t" \
    "v_mfma_f32_16x16x32_bf16 v[252:255], v[76:79], v[28:31], 0\n\t" \
    "s_waitcnt lgkmcnt(2)\n\t" \
    "v_mfma_f32_16x16x32_bf16 a[0:3], v[64:67], v[0:3], 0\n\t" \
    "v_mfma_f32_16x16x32_bf16 a[4:7], v[64:67], v[4:7], 0\n\t" \
    "ds_read_b64_tr_b16 v[72:73], v88 offset:0\n\t" \
    "v_mfma_f32_16x16x32_bf16 a[8:11], v[64:67], v[8:11], 0\n\t" \
    "ds_read_b64_tr_b16 v[74:75], v88 offset:1024\n\t" \
    "v_mfma_f32_16x16x32_bf16 a[12:15], v[64:67], v[12:15], 0\n\t" \
    "v_mfma_f32_16x16x32_bf16 a[16:19], v[64:67], v[16:19], 0\n\t" \
    "buffer_load_dwordx4 v108, s[12:15], s28 offen offset:2048 lds\n\t" \
    "v_mfma_f32_16x16x32_bf16 a[20:23], v[64:67], v[20:23], 0\n\t" \
    "v_mfma_f32_16x16x32_bf16 a[24:27], v[64:67], v[24:27], 0\n\t" \
    "v_mfma_f32_16x16x32_bf16 a[28:31], v[64:67], v[28:31], 0\n\t" \
    "s_waitcnt lgkmcnt(2)\n\t" \
    "v_mfma_f32_16x16x32_bf16 a[32:35], v[68:71], v[0:3], 0\n\t" \
    "v_mfma_f32_16x16x32_bf16 a[36:39], v[68:71], v[4:7], 0\n\t" \
    "ds_read_b64_tr_b16 v[76:77], v89 offset:0\n\t" \
    "v_mfma_f32_16x16x32_bf16 a[40:43], v[68:71], v[8:11], 0\n\t" \
    "ds_read_b64_tr_b16 v[78:79], v89 offset:1024\n\t" \
    "v_mfma_f32_16x16x32_bf16 a[44:47], v[68:71], v[12:15], 0\n\t" \
    "v_mfma_f32_16x16x32_bf16 a[48:51], v[68:71], v[16:19], 0\n\t" \
    "buffer_load_dwordx4 v109, s[12:15], s28 offen offset:3072 lds\n\t" \
    "v_mfma_f32_16x16x32_bf16 a[52:55], v[68:71], v[20:23], 0\n\t" \
    "v_mfma_f32_16x16x32_bf16 a[56:59], v[68:71], v[24:27], 0\n\t" \
    "v_mfma_f32_16x16x32_bf16 a[60:63], v[68:71], v[28:31], 0\n\t" \
    "s_waitcnt vmcnt(26) lgkmcnt(0)\n\t" \
    "s_barrier\n\t" \
    "v_mfma_f32_16x16x32_bf16 a[64:67], v[72:75], v[0:3], 0\n\t" \
    "v_mfma_f32_16x16x32_bf16 a[68:71], v[72:75], v[4:7], 0\n\t" \
    "ds_read_b64_tr_b16 v[64:65], v82 offset:16384\n\t" \
    "v_mfma_f32_16x16x32_bf16 a[72:75], v[72:75], v[8:11], 0\n\t" \
    "ds_read_b64_tr_b16 v[66:67], v82 offset:17408\n\t" \
    "v_mfma_f32_16x16x32_bf16 a[76:79], v[72:75], v[12:15], 0\n\t" \
    "v_mfma_f32_16x16x32_bf16 a[80:83], v[72:75], v[16:19], 0\n\t" \
    "v_mfma_f32_16x16x32_bf16 a[84:87], v[72:75], v[20:23], 0\n\t" \
    "v_mfma_f32_16x16x32_bf16 a[88:91], v[72:75], v[24:27], 0\n\t" \
    "v_mfma_f32_16x16x32_bf16 a[92:95], v[72:75], v[28:31], 0\n\t" \
    "v_mfma_f32_16x16x32_bf16 a[96:99], v[76:79], v[0:3], 0\n\t" \
    "v_mfma_f32_16x16x32_bf16 a[100:103], v[76:79], v[4:7], 0\n\t" \
    "ds_read_b64_tr_b16 v[68:69], v83 offset:16384\n\t" \
    "v_mfma_f32_16x16x32_bf16 a[104:107], v[76:79], v[8:11], 0\n\t" \
    "ds_read_b64_tr_b16 v[70:71], v83 offset:17408\n\t" \
    "v_mfma_f32_16x16x32_bf16 a[108:111], v[76:79], v[12:15], 0\n\t" \
    "v_mfma_f32_16x16x32_bf16 a[112:115], v[76:79], v[16:19], 0\n\t" \
    "v_mfma_f32_16x16x32_bf16 a[116:119], v[76:79], v[20:23], 0\n\t" \
    "v_mfma_f32_16x16x32_bf16 a[120:123], v[76:79], v[24:27], 0\n\t" \
    "v_mfma_f32_16x16x32_bf16 a[124:127], v[76:79], v[28:31], 0\n\t" \
    "s_waitcnt lgkmcnt(2)\n\t" \
    "v_mfma_f32_16x16x32_bf16 v[128:131], v[64:67], v[32:35], v[128:131]\n\t" \
    "v_mfma_f32_16x16x32_bf16 v[132:135], v[64:67], v[36:39], v[132:135]\n\t" \
    "ds_read_b64_tr_b16 v[72:73], v84 offset:16384\n\t" \
    "v_mfma_f32_16x16x32_bf16 v[136:139], v[64:67], v[40:43], v[136:139]\n\t" \
    "ds_read_b64_tr_b16 v[74:75], v84 offset:17408\n\t" \
    "v_mfma_f32_16x16x32_bf16 v[140:143], v[64:67], v[44:47], v[140:143]\n\t" \
    "s_mov_b32 s27, 256\n\t" \
    "s_add_u32 m0, s25, 0\n\t" \
    "v_mfma_f32_16x16x32_bf16 v[144:147], v[64:67], v[48:51], v[144:147]\n\t" \
    "buffer_load_dwordx4 v98, s[8:11], s27 offen offset:0 lds\n\t" \
    "v_mfma_f32_16x16x32_bf16 v[148:151], v[64:67], v[52:55], v[148:151]\n\t" \
    "v_mfma_f32_16x16x32_bf16 v[152:155], v[64:67], v[56:59], v[152:155]\n\t" \
    "buffer_load_dwordx4 v99, s[8:11], s27 offen offset:1024 lds\n\t" \
    "v_mfma_f32_16x16x32_bf16 v[156:159], v[64:67], v[60:63], v[156:159]\n\t" \
    "s_waitcnt lgkmcnt(2)\n\t" \
    "v_mfma_f32_16x16x32_bf16 v[160:163], v[68:71], v[32:35], v[160:163]\n\t" \
    "v_mfma_f32_16x16x32_bf16 v[164:167], v[68:71], v[36:39], v[164:167]\n\t" \
    "ds_read_b64_tr_b16 v[76:77], v85 offset:16384\n\t" \
    "v_mfma_f32_16x16x32_bf16 v[168:171], v[68:71], v[40:43], v[168:171]\n\t" \
    "ds_read_b64_tr_b16 v[78:79], v85 offset:17408\n\t" \
    "v_mfma_f32_16x16x32_bf16 v[172:175], v[68:71], v[44:47], v[172:175]\n\t" \
    "v_mfma_f32_16x16x32_bf16 v[176:179], v[68:71], v[48:51], v[176:179]\n\t" \
    "buffer_load_dwordx4 v100, s[8:11], s27 offen offset:2048 lds\n\t" \
    "v_mfma_f32_16x16x32_bf16 v[180:183], v[68:71], v[52:55], v[180:183]\n\t" \
    "v_mfma_f32_16x16x32_bf16 v[184:187], v[68:71], v[56:59], v[184:187]\n\t" \
    "buffer_load_dwordx4 v101, s[8:11], s27 offen offset:3072 lds\n\t" \
    "v_mfma_f32_16x16x32_bf16 v[188:191], v[68:71], v[60:63], v[188:191]\n\t" \
    "s_waitcnt lgkmcnt(2)\n\t" \
    "v_mfma_f32_16x16x32_bf16 v[192:195], v[72:75], v[32:35], v[192:195]\n\t" \
    "v_mfma_f32_16x16x32_bf16 v[196:199], v[72:75], v[36:39], v[196:199]\n\t" \
    "ds_read_b64_tr_b16 v[64:65], v86 offset:16384\n\t" \
    "v_mfma_f32_16x16x32_bf16 v[200:203], v[72:75], v[40:43], v[200:203]\n\t" \
    "ds_read_b64_tr_b16 v[66:67], v86 offset:17408\n\t" \
    "s_add_u32 m0, s25, 4096\n\t" \
    "v_mfma_f32_16x16x32_bf16 v[204:207], v[72:75], v[44:47], v[204:207]\n\t" \
    "buffer_load_dwordx4 v102, s[8:11], s27 offen offset:0 lds\n\t" \
    "v_mfma_f32_16x16x32_bf16 v[208:211], v[72:75], v[48:51], v[208:211]\n\t" \
    "v_mfma_f32_16x16x32_bf16 v[212:215], v[72:75], v[52:55], v[212:215]\n\t" \
    "buffer_load_dwordx4 v103, s[8:11], s27 offen offset:1024 lds\n\t" \
    "v_mfma_f32_16x16x32_bf16 v[216:219], v[72:75], v[56:59], v[216:219]\n\t" \
    "v_mfma_f32_16x16x32_bf16 v[220:223], v[72:75], v[60:63], v[220:223]\n\t" \
    "s_waitcnt lgkmcnt(2)\n\t" \
    "v_mfma_f32_16x16x32_bf16 v[224:227], v[76:79], v[32:35], v[224:227]\n\t" \
    "v_mfma_f32_16x16x32_bf16 v[228:231], v[76:79], v[36:39], v[228:231]\n\t" \
    "ds_read_b64_tr_b16 v[68:69], v87 offset:16384\n\t" \
    "v_mfma_f32_16x16x32_bf16 v[232:235], v[76:79], v[40:43], v[232:235]\n\t" \
    "ds_read_b64_tr_b16 v[70:71], v87 offset:17408\n\t" \
    "v_mfma_f32_16x16x32_bf16 v[236:239], v[76:79], v[44:47], v[236:239]\n\t" \
    "buffer_load_dwordx4 v104, s[8:11], s27 offen offset:2048 lds\n\t" \
    "v_mfma_f32_16x16x32_bf16 v[240:243], v[76:79], v[48:51], v[240:243]\n\t" \
    "v_mfma_f32_16x16x32_bf16 v[244:247], v[76:79], v[52:55], v[244:247]\n\t" \
    "buffer_load_dwordx4 v105, s[8:11], s27 offen offset:3072 lds\n\t" \
    "v_mfma_f32_16x16x32_bf16 v[248:251], v[76:79], v[56:59], v[248:251]\n\t" \
    "v_mfma_f32_16x16x32_bf16 v[252:255], v[76:79], v[60:63], v[252:255]\n\t" \
    "s_waitcnt lgkmcnt(2)\n\t" \
    "v_mfma_f32_16x16x32_bf16 a[0:3], v[64:67], v[32:35], a[0:3]\n\t" \
    "v_mfma_f32_16x16x32_bf16 a[4:7], v[64:67], v[36:39], a[4:7]\n\t" \
    "ds_read_b64_tr_b16 v[72:73], v88 offset:16384\n\t" \
    "v_mfma_f32_16x16x32_bf16 a[8:11], v[64:67], v[40:43], a[8:11]\n\t" \
    "ds_read_b64_tr_b16 v[74:75], v88 offset:17408\n\t" \
    "s_mov_b32 s28, 1572864\n\t" \
    "s_add_u32 m0, s26, 0\n\t" \
    "v_mfma_f32_16x16x32_bf16 a[12:15], v[64:67], v[44:47], a[12:15]\n\t" \
    "buffer_load_dwordx4 v106, s[12:15], s28 offen offset:0 lds\n\t" \
    "v_mfma_f32_16x16x32_bf16 a[16:19], v[64:67], v[48:51], a[16:19]\n\t" \
    "v_mfma_f32_16x16x32_bf16 a[20:23], v[64:67], v[52:55], a[20:23]\n\t" \
    "buffer_load_dwordx4 v107, s[12:15], s28 offen offset:1024 lds\n\t" \
    "v_mfma_f32_16x16x32_bf16 a[24:27], v[64:67], v[56:59], a[24:27]\n\t" \
    "v_mfma_f32_16x16x32_bf16 a[28:31], v[64:67], v[60:63], a[28:31]\n\t" \
    "s_waitcnt lgkmcnt(2)\n\t" \
    "v_mfma_f32_16x16x32_bf16 a[32:35], v[68:71], v[32:35], a[32:35]\n\t" \
    "v_mfma_f32_16x16x32_bf16 a[36:39], v[68:71], v[36:39], a[36:39]\n\t" \
    "ds_read_b64_tr_b16 v[76:77], v89 offset:16384\n\t" \
    "v_mfma_f32_16x16x32_bf16 a[40:43], v[68:71], v[40:43], a[40:43]\n\t" \
    "ds_read_b64_tr_b16 v[78:79], v89 offset:17408\n\t" \
    "v_mfma_f32_16x16x32_bf16 a[44:47], v[68:71], v[44:47], a[44:47]\n\t" \
    "buffer_load_dwordx4 v108, s[12:15], s28 offen offset:2048 lds\n\t" \
    "v_mfma_f32_16x16x32_bf16 a[48:51], v[68:71], v[48:51], a[48:51]\n\t" \
    "v_mfma_f32_16x16x32_bf16 a[52:55], v[68:71], v[52:55], a[52:55]\n\t" \
    "buffer_load_dwordx4 v109, s[12:15], s28 offen offset:3072 lds\n\t" \
    "v_mfma_f32_16x16x32_bf16 a[56:59], v[68:71], v[56:59], a[56:59]\n\t" \
    "v_mfma_f32_16x16x32_bf16 a[60:63], v[68:71], v[60:63], a[60:63]\n\t" \
    "s_waitcnt vmcnt(26) lgkmcnt(0)\n\t" \
    "s_barrier\n\t" \
    "v_mfma_f32_16x16x32_bf16 a[64:67], v[72:75], v[32:35], a[64:67]\n\t" \
    "v_mfma_f32_16x16x32_bf16 a[68:71], v[72:75], v[36:39], a[68:71]\n\t" \
    "ds_read_b64_tr_b16 v[64:65], v82 offset:32768\n\t" \
    "v_mfma_f32_16x16x32_bf16 a[72:75], v[72:75], v[40:43], a[72:75]\n\t" \
    "ds_read_b64_tr_b16 v[66:67], v82 offset:33792\n\t" \
    "v_mfma_f32_16x16x32_bf16 a[76:79], v[72:75], v[44:47], a[76:79]\n\t" \
    "ds_read_b128 v[0:3], v80 offset:32768\n\t" \
    "v_mfma_f32_16x16x32_bf16 a[80:83], v[72:75], v[48:51], a[80:83]\n\t" \
    "ds_read_b128 v[4:7], v80 offset:33280\n\t" \
    "v_mfma_f32_16x16x32_bf16 a[84:87], v[72:75], v[52:55], a[84:87]\n\t" \
    "ds_read_b128 v[8:11], v80 offset:36864\n\t" \
    "v_mfma_f32_16x16x32_bf16 a[88:91], v[72:75], v[56:59], a[88:91]\n\t" \
    "ds_read_b128 v[12:15], v80 offset:37376\n\t" \
    "v_mfma_f32_16x16x32_bf16 a[92:95], v[72:75], v[60:63], a[92:95]\n\t" \
    "ds_read_b128 v[16:19], v80 offset:40960\n\t" \
    "v_mfma_f32_16x16x32_bf16 a[96:99], v[76:79], v[32:35], a[96:99]\n\t" \
    "ds_read_b128 v[20:23], v80 offset:41472\n\t" \
    "v_mfma_f32_16x16x32_bf16 a[100:103], v[76:79], v[36:39], a[100:103]\n\t" \
    "ds_read_b128 v[24:27], v80 offset:45056\n\t" \
    "v_mfma_f32_16x16x32_bf16 a[104:107], v[76:79], v[40:43], a[104:107]\n\t" \
    "ds_read_b128 v[28:31], v80 offset:45568\n\t" \
    "v_mfma_f32_16x16x32_bf16 a[108:111], v[76:79], v[44:47], a[108:111]\n\t" \
    "ds_read_b64_tr_b16 v[68:69], v83 offset:32768\n\t" \
    "v_mfma_f32_16x16x32_bf16 a[112:115], v[76:79], v[48:51], a[112:115]\n\t" \
    "ds_read_b64_tr_b16 v[70:71], v83 offset:33792\n\t" \
    "v_mfma_f32_16x16x32_bf16 a[116:119], v[76:79], v[52:55], a[116:119]\n\t" \
    "v_mfma_f32_16x16x32_bf16 a[120:123], v[76:79], v[56:59], a[120:123]\n\t" \
    "v_mfma_f32_16x16x32_bf16 a[124:127], v[76:79], v[60:63], a[124:127]\n\t" \
    "s_waitcnt lgkmcnt(9)\n\t" \
    "v_mfma_f32_16x16x32_bf16 v[128:131], v[64:67], v[0:3], v[128:131]\n\t" \
    "s_waitcnt lgkmcnt(8)\n\t" \
    "v_mfma_f32_16x16x32_bf16 v[132:135], v[64:67], v[4:7], v[132:135]\n\t" \
    "ds_read_b64_tr_b16 v[72:73], v84 offset:32768\n\t" \
    "s_waitcnt lgkmcnt(8)\n\t" \
    "v_mfma_f32_16x16x32_bf16 v[136:139], v[64:67], v[8:11], v[136:139]\n\t" \
    "ds_read_b64_tr_b16 v[74:75], v84 offset:33792\n\t" \
    "s_waitcnt lgkmcnt(8)\n\t" \
    "v_mfma_f32_16x16x32_bf16 v[140:143], v[64:67], v[12:15], v[140:143]\n\t" \
    "ds_read_b128 v[32:35], v81 offset:32768\n\t" \
    "s_waitcnt lgkmcnt(8)\n\t" \
    "v_mfma_f32_16x16x32_bf16 v[144:147], v[64:67], v[16:19], v[144:147]\n\t" \
    "ds_read_b128 v[36:39], v81 offset:33280\n\t" \
    "s_waitcnt lgkmcnt(8)\n\t" \
    "v_mfma_f32_16x16x32_bf16 v[148:151], v[64:67], v[20:23], v[148:151]\n\t" \
    "ds_read_b128 v[40:43], v81 offset:36864\n\t" \
    "s_waitcnt lgkmcnt(8)\n\t" \
    "v_mfma_f32_16x16x32_bf16 v[152:155], v[64:67], v[24:27], v[152:155]\n\t" \
    "ds_read_b128 v[44:47], v81 offset:37376\n\t" \
    "s_waitcnt lgkmcnt(8)\n\t" \
    "v_mfma_f32_16x16x32_bf16 v[156:159], v[64:67], v[28:31], v[156:159]\n\t" \
    "s_waitcnt lgkmcnt(6)\n\t" \
    "v_mfma_f32_16x16x32_bf16 v[160:163], v[68:71], v[0:3], v[160:163]\n\t" \
    "v_mfma_f32_16x16x32_bf16 v[164:167], v[68:71], v[4:7], v[164:167]\n\t" \
    "ds_read_b64_tr_b16 v[76:77], v85 offset:32768\n\t" \
    "v_mfma_f32_16x16x32_bf16 v[168:171], v[68:71], v[8:11], v[168:171]\n\t" \
    "ds_read_b64_tr_b16 v[78:79], v85 offset:33792\n\t" \
    "v_mfma_f32_16x16x32_bf16 v[172:175], v[68:71], v[12:15], v[172:175]\n\t" \
    "ds_read_b128 v[48:51], v81 offset:40960\n\t" \
    "v_mfma_f32_16x16x32_bf16 v[176:179], v[68:71], v[16:19], v[176:179]\n\t" \
    "ds_read_b128 v[52:55], v81 offset:41472\n\t" \
    "v_mfma_f32_16x16x32_bf16 v[180:183], v[68:71], v[20:23], v[180:183]\n\t" \
    "ds_read_b128 v[56:59], v81 offset:45056\n\t" \
    "v_mfma_f32_16x16x32_bf16 v[184:187], v[68:71], v[24:27], v[184:187]\n\t" \
    "ds_read_b128 v[60:63], v81 offset:45568\n\t" \
    "v_mfma_f32_16x16x32_bf16 v[188:191], v[68:71], v[28:31], v[188:191]\n\t" \
    "s_waitcnt lgkmcnt(10)\n\t" \
    "v_mfma_f32_16x16x32_bf16 v[192:195], v[72:75], v[0:3], v[192:195]\n\t" \
    "v_mfma_f32_16x16x32_bf16 v[196:199], v[72:75], v[4:7], v[196:199]\n\t" \
    "ds_read_b64_tr_b16 v[64:65], v86 offset:32768\n\t" \
    "v_mfma_f32_16x16x32_bf16 v[200:203], v[72:75], v[8:11], v[200:203]\n\t" \
    "ds_read_b64_tr_b16 v[66:67], v86 offset:33792\n\t" \
    "v_mfma_f32_16x16x32_bf16 v[204:207], v[72:75], v[12:15], v[204:207]\n\t" \
    "s_mov_b32 s28, 1835008\n\t" \
    "s_add_u32 m0, s26, 16384\n\t" \
    "v_mfma_f32_16x16x32_bf16 v[208:211], v[72:75], v[16:19], v[208:211]\n\t" \
    "buffer_load_dwordx4 v106, s[12:15], s28 offen offset:0 lds\n\t" \
    "v_mfma_f32_16x16x32_bf16 v[212:215], v[72:75], v[20:23], v[212:215]\n\t" \
    "v_mfma_f32_16x16x32_bf16 v[216:219], v[72:75], v[24:27], v[216:219]\n\t" \
    "v_mfma_f32_16x16x32_bf16 v[220:223], v[72:75], v[28:31], v[220:223]\n\t" \
    "s_waitcnt lgkmcnt(6)\n\t" \
    "v_mfma_f32_16x16x32_bf16 v[224:227], v[76:79], v[0:3], v[224:227]\n\t" \
    "v_mfma_f32_16x16x32_bf16 v[228:231], v[76:79], v[4:7], v[228:231]\n\t" \
    "ds_read_b64_tr_b16 v[68:69], v87 offset:32768\n\t" \
    "v_mfma_f32_16x16x32_bf16 v[232:235], v[76:79], v[8:11], v[232:235]\n\t" \
    "ds_read_b64_tr_b16 v[70:71], v87 offset:33792\n\t" \
    "v_mfma_f32_16x16x32_bf16 v[236:239], v[76:79], v[12:15], v[236:239]\n\t" \
    "v_mfma_f32_16x16x32_bf16 v[240:243], v[76:79], v[16:19], v[240:243]\n\t" \
    "buffer_load_dwordx4 v107, s[12:15], s28 offen offset:1024 lds\n\t" \
    "v_mfma_f32_16x16x32_bf16 v[244:247], v[76:79], v[20:23], v[244:247]\n\t" \
    "v_mfma_f32_16x16x32_bf16 v[248:251], v[76:79], v[24:27], v[248:251]\n\t" \
    "v_mfma_f32_16x16x32_bf16 v[252:255], v[76:79], v[28:31], v[252:255]\n\t" \
    "s_waitcnt lgkmcnt(2)\n\t" \
    "v_mfma_f32_16x16x32_bf16 a[0:3], v[64:67], v[0:3], a[0:3]\n\t" \
    "v_mfma_f32_16x16x32_bf16 a[4:7], v[64:67], v[4:7], a[4:7]\n\t" \
    "ds_read_b64_tr_b16 v[72:73], v88 offset:32768\n\t" \
    "v_mfma_f32_16x16x32_bf16 a[8:11], v[64:67], v[8:11], a[8:11]\n\t" \
    "ds_read_b64_tr_b16 v[74:75], v88 offset:33792\n\t" \
    "v_mfma_f32_16x16x32_bf16 a[12:15], v[64:67], v[12:15], a[12:15]\n\t" \
    "v_mfma_f32_16x16x32_bf16 a[16:19], v[64:67], v[16:19], a[16:19]\n\t" \
    "buffer_load_dwordx4 v108, s[12:15], s28 offen offset:2048 lds\n\t" \
    "v_mfma_f32_16x16x32_bf16 a[20:23], v[64:67], v[20:23], a[20:23]\n\t" \
    "v_mfma_f32_16x16x32_bf16 a[24:27], v[64:67], v[24:27], a[24:27]\n\t" \
    "v_mfma_f32_16x16x32_bf16 a[28:31], v[64:67], v[28:31], a[28:31]\n\t" \
    "s_waitcnt lgkmcnt(2)\n\t" \
    "v_mfma_f32_16x16x32_bf16 a[32:35], v[68:71], v[0:3], a[32:35]\n\t" \
    "v_mfma_f32_16x16x32_bf16 a[36:39], v[68:71], v[4:7], a[36:39]\n\t" \
    "ds_read_b64_tr_b16 v[76:77], v89 offset:32768\n\t" \
    "v_mfma_f32_16x16x32_bf16 a[40:43], v[68:71], v[8:11], a[40:43]\n\t" \
    "ds_read_b64_tr_b16 v[78:79], v89 offset:33792\n\t" \
    "v_mfma_f32_16x16x32_bf16 a[44:47], v[68:71], v[12:15], a[44:47]\n\t" \
    "v_mfma_f32_16x16x32_bf16 a[48:51], v[68:71], v[16:19], a[48:51]\n\t" \
    "buffer_load_dwordx4 v109, s[12:15], s28 offen offset:3072 lds\n\t" \
    "v_mfma_f32_16x16x32_bf16 a[52:55], v[68:71], v[20:23], a[52:55]\n\t" \
    "v_mfma_f32_16x16x32_bf16 a[56:59], v[68:71], v[24:27], a[56:59]\n\t" \
    "v_mfma_f32_16x16x32_bf16 a[60:63], v[68:71], v[28:31], a[60:63]\n\t" \
    "s_waitcnt vmcnt(26) lgkmcnt(0)\n\t" \
    "s_barrier\n\t" \
    "v_mfma_f32_16x16x32_bf16 a[64:67], v[72:75], v[0:3], a[64:67]\n\t" \
    "v_mfma_f32_16x16x32_bf16 a[68:71], v[72:75], v[4:7], a[68:71]\n\t" \
    "ds_read_b64_tr_b16 v[64:65], v82 offset:49152\n\t" \
    "v_mfma_f32_16x16x32_bf16 a[72:75], v[72:75], v[8:11], a[72:75]\n\t" \
    "ds_read_b64_tr_b16 v[66:67], v82 offset:50176\n\t" \
    "v_mfma_f32_16x16x32_bf16 a[76:79], v[72:75], v[12:15], a[76:79]\n\t" \
    "v_mfma_f32_16x16x32_bf16 a[80:83], v[72:75], v[16:19], a[80:83]\n\t" \
    "v_mfma_f32_16x16x32_bf16 a[84:87], v[72:75], v[20:23], a[84:87]\n\t" \
    "v_mfma_f32_16x16x32_bf16 a[88:91], v[72:75], v[24:27], a[88:91]\n\t" \
    "v_mfma_f32_16x16x32_bf16 a[92:95], v[72:75], v[28:31], a[92:95]\n\t" \
    "v_mfma_f32_16x16x32_bf16 a[96:99], v[76:79], v[0:3], a[96:99]\n\t" \
    "v_mfma_f32_16x16x32_bf16 a[100:103], v[76:79], v[4:7], a[100:103]\n\t" \
    "ds_read_b64_tr_b16 v[68:69], v83 offset:49152\n\t" \
    "v_mfma_f32_16x16x32_bf16 a[104:107], v[76:79], v[8:11], a[104:107]\n\t" \
    "ds_read_b64_tr_b16 v[70:71], v83 offset:50176\n\t" \
    "v_mfma_f32_16x16x32_bf16 a[108:111], v[76:79], v[12:15], a[108:111]\n\t" \
    "v_mfma_f32_16x16x32_bf16 a[112:115], v[76:79], v[16:19], a[112:115]\n\t" \
    "v_mfma_f32_16x16x32_bf16 a[116:119], v[76:79], v[20:23], a[116:119]\n\t" \
    "v_mfma_f32_16x16x32_bf16 a[120:123], v[76:79], v[24:27], a[120:123]\n\t" \
    "v_mfma_f32_16x16x32_bf16 a[124:127], v[76:79], v[28:31], a[124:127]\n\t" \
    "s_waitcnt lgkmcnt(2)\n\t" \
    "v_mfma_f32_16x16x32_bf16 v[128:131], v[64:67], v[32:35], v[128:131]\n\t" \
    "v_mfma_f32_16x16x32_bf16 v[132:135], v[64:67], v[36:39], v[132:135]\n\t" \
    "ds_read_b64_tr_b16 v[72:73], v84 offset:49152\n\t" \
    "v_mfma_f32_16x16x32_bf16 v[136:139], v[64:67], v[40:43], v[136:139]\n\t" \
    "ds_read_b64_tr_b16 v[74:75], v84 offset:50176\n\t" \
    "v_mfma_f32_16x16x32_bf16 v[140:143], v[64:67], v[44:47], v[140:143]\n\t" \
    "s_mov_b32 s27, 384\n\t" \
    "s_add_u32 m0, s25, 32768\n\t" \
    "v_mfma_f32_16x16x32_bf16 v[144:147], v[64:67], v[48:51], v[144:147]\n\t" \
    "buffer_load_dwordx4 v98, s[8:11], s27 offen offset:0 lds\n\t" \
    "v_mfma_f32_16x16x32_bf16 v[148:151], v[64:67], v[52:55], v[148:151]\n\t" \
    "v_mfma_f32_16x16x32_bf16 v[152:155], v[64:67], v[56:59], v[152:155]\n\t" \
    "buffer_load_dwordx4 v99, s[8:11], s27 offen offset:1024 lds\n\t" \
    "v_mfma_f32_16x16x32_bf16 v[156:159], v[64:67], v[60:63], v[156:159]\n\t" \
    "s_waitcnt lgkmcnt(2)\n\t" \
    "v_mfma_f32_16x16x32_bf16 v[160:163], v[68:71], v[32:35], v[160:163]\n\t" \
    "v_mfma_f32_16x16x32_bf16 v[164:167], v[68:71], v[36:39], v[164:167]\n\t" \
    "ds_read_b64_tr_b16 v[76:77], v85 offset:49152\n\t" \
    "v_mfma_f32_16x16x32_bf16 v[168:171], v[68:71], v[40:43], v[168:171]\n\t" \
    "ds_read_b64_tr_b16 v[78:79], v85 offset:50176\n\t" \
    "v_mfma_f32_16x16x32_bf16 v[172:175], v[68:71], v[44:47], v[172:175]\n\t" \
    "v_mfma_f32_16x16x32_bf16 v[176:179], v[68:71], v[48:51], v[176:179]\n\t" \
    "buffer_load_dwordx4 v100, s[8:11], s27 offen offset:2048 lds\n\t" \
    "v_mfma_f32_16x16x32_bf16 v[180:183], v[68:71], v[52:55], v[180:183]\n\t" \
    "v_mfma_f32_16x16x32_bf16 v[184:187], v[68:71], v[56:59], v[184:187]\n\t" \
    "buffer_load_dwordx4 v101, s[8:11], s27 offen offset:3072 lds\n\t" \
    "v_mfma_f32_16x16x32_bf16 v[188:191], v[68:71], v[60:63], v[188:191]\n\t" \
    "s_waitcnt lgkmcnt(2)\n\t" \
    "v_mfma_f32_16x16x32_bf16 v[192:195], v[72:75], v[32:35], v[192:195]\n\t" \
    "v_mfma_f32_16x16x32_bf16 v[196:199], v[72:75], v[36:39], v[196:199]\n\t" \
    "ds_read_b64_tr_b16 v[64:65], v86 offset:49152\n\t" \
    "v_mfma_f32_16x16x32_bf16 v[200:203], v[72:75], v[40:43], v[200:203]\n\t" \
    "ds_read_b64_tr_b16 v[66:67], v86 offset:50176\n\t" \
    "s_add_u32 m0, s25, 36864\n\t" \
    "v_mfma_f32_16x16x32_bf16 v[204:207], v[72:75], v[44:47], v[204:207]\n\t" \
    "buffer_load_dwordx4 v102, s[8:11], s27 offen offset:0 lds\n\t" \
    "v_mfma_f32_16x16x32_bf16 v[208:211], v[72:75], v[48:51], v[208:211]\n\t" \
    "v_mfma_f32_16x16x32_bf16 v[212:215], v[72:75], v[52:55], v[212:215]\n\t" \
    "buffer_load_dwordx4 v103, s[8:11], s27 offen offset:1024 lds\n\t" \
    "v_mfma_f32_16x16x32_bf16 v[216:219], v[72:75], v[56:59], v[216:219]\n\t" \
    "v_mfma_f32_16x16x32_bf16 v[220:223], v[72:75], v[60:63], v[220:223]\n\t" \
    "s_waitcnt lgkmcnt(2)\n\t" \
    "v_mfma_f32_16x16x32_bf16 v[224:227], v[76:79], v[32:35], v[224:227]\n\t" \
    "v_mfma_f32_16x16x32_bf16 v[228:231], v[76:79], v[36:39], v[228:231]\n\t" \
    "ds_read_b64_tr_b16 v[68:69], v87 offset:49152\n\t" \
    "v_mfma_f32_16x16x32_bf16 v[232:235], v[76:79], v[40:43], v[232:235]\n\t" \
    "ds_read_b64_tr_b16 v[70:71], v87 offset:50176\n\t" \
    "v_mfma_f32_16x16x32_bf16 v[236:239], v[76:79], v[44:47], v[236:239]\n\t" \
    "buffer_load_dwordx4 v104, s[8:11], s27 offen offset:2048 lds\n\t" \
    "v_mfma_f32_16x16x32_bf16 v[240:243], v[76:79], v[48:51], v[240:243]\n\t" \
    "v_mfma_f32_16x16x32_bf16 v[244:247], v[76:79], v[52:55], v[244:247]\n\t" \
    "buffer_load_dwordx4 v105, s[8:11], s27 offen offset:3072 lds\n\t" \
    "v_mfma_f32_16x16x32_bf16 v[248:251], v[76:79], v[56:59], v[248:251]\n\t" \
    "v_mfma_f32_16x16x32_bf16 v[252:255], v[76:79], v[60:63], v[252:255]\n\t" \
    "s_waitcnt lgkmcnt(2)\n\t" \
    "v_mfma_f32_16x16x32_bf16 a[0:3], v[64:67], v[32:35], a[0:3]\n\t" \
    "v_mfma_f32_16x16x32_bf16 a[4:7], v[64:67], v[36:39], a[4:7]\n\t" \
    "ds_read_b64_tr_b16 v[72:73], v88 offset:49152\n\t" \
    "v_mfma_f32_16x16x32_bf16 a[8:11], v[64:67], v[40:43], a[8:11]\n\t" \
    "ds_read_b64_tr_b16 v[74:75], v88 offset:50176\n\t" \
    "s_mov_b32 s28, 2097152\n\t" \
    "s_add_u32 m0, s26, 32768\n\t" \
    "v_mfma_f32_16x16x32_bf16 a[12:15], v[64:67], v[44:47], a[12:15]\n\t" \
    "buffer_load_dwordx4 v106, s[12:15], s28 offen offset:0 lds\n\t" \
    "v_mfma_f32_16x16x32_bf16 a[16:19], v[64:67], v[48:51], a[16:19]\n\t" \
    "v_mfma_f32_16x16x32_bf16 a[20:23], v[64:67], v[52:55], a[20:23]\n\t" \
    "buffer_load_dwordx4 v107, s[12:15], s28 offen offset:1024 lds\n\t" \
    "v_mfma_f32_16x16x32_bf16 a[24:27], v[64:67], v[56:59], a[24:27]\n\t" \
    "v_mfma_f32_16x16x32_bf16 a[28:31], v[64:67], v[60:63], a[28:31]\n\t" \
    "s_waitcnt lgkmcnt(2)\n\t" \
    "v_mfma_f32_16x16x32_bf16 a[32:35], v[68:71], v[32:35], a[32:35]\n\t" \
    "v_mfma_f32_16x16x32_bf16 a[36:39], v[68:71], v[36:39], a[36:39]\n\t" \
    "ds_read_b64_tr_b16 v[76:77], v89 offset:49152\n\t" \
    "v_mfma_f32_16x16x32_bf16 a[40:43], v[68:71], v[40:43], a[40:43]\n\t" \
    "ds_read_b64_tr_b16 v[78:79], v89 offset:50176\n\t" \
    "v_mfma_f32_16x16x32_bf16 a[44:47], v[68:71], v[44:47], a[44:47]\n\t" \
    "buffer_load_dwordx4 v108, s[12:15], s28 offen offset:2048 lds\n\t" \
    "v_mfma_f32_16x16x32_bf16 a[48:51], v[68:71], v[48:51], a[48:51]\n\t" \
    "v_mfma_f32_16x16x32_bf16 a[52:55], v[68:71], v[52:55], a[52:55]\n\t" \
    "buffer_load_dwordx4 v109, s[12:15], s28 offen offset:3072 lds\n\t" \
    "v_mfma_f32_16x16x32_bf16 a[56:59], v[68:71], v[56:59], a[56:59]\n\t" \
    "v_mfma_f32_16x16x32_bf16 a[60:63], v[68:71], v[60:63], a[60:63]\n\t" \
    "s_waitcnt vmcnt(20) lgkmcnt(0)\n\t" \
    "s_barrier\n\t" \
    "v_mfma_f32_16x16x32_bf16 a[64:67], v[72:75], v[32:35], a[64:67]\n\t" \
    "v_mfma_f32_16x16x32_bf16 a[68:71], v[72:75], v[36:39], a[68:71]\n\t" \
    "ds_read_b64_tr_b16 v[64:65], v90 offset:0\n\t" \
    "v_mfma_f32_16x16x32_bf16 a[72:75], v[72:75], v[40:43], a[72:75]\n\t" \
    "ds_read_b64_tr_b16 v[66:67], v90 offset:1024\n\t" \
    "v_mfma_f32_16x16x32_bf16 a[76:79], v[72:75], v[44:47], a[76:79]\n\t" \
    "ds_read_b128 v[0:3], v80 offset:0\n\t" \
    "v_mfma_f32_16x16x32_bf16 a[80:83], v[72:75], v[48:51], a[80:83]\n\t" \
    "ds_read_b128 v[4:7], v80 offset:512\n\t" \
    "v_mfma_f32_16x16x32_bf16 a[84:87], v[72:75], v[52:55], a[84:87]\n\t" \
    "ds_read_b128 v[8:11], v80 offset:4096\n\t" \
    "v_mfma_f32_16x16x32_bf16 a[88:91], v[72:75], v[56:59], a[88:91]\n\t" \
    "ds_read_b128 v[12:15], v80 offset:4608\n\t" \
    "v_mfma_f32_16x16x32_bf16 a[92:95], v[72:75], v[60:63], a[92:95]\n\t" \
    "ds_read_b128 v[16:19], v80 offset:8192\n\t" \
    "v_mfma_f32_16x16x32_bf16 a[96:99], v[76:79], v[32:35], a[96:99]\n\t" \
    "ds_read_b128 v[20:23], v80 offset:8704\n\t" \
    "v_mfma_f32_16x16x32_bf16 a[100:103], v[76:79], v[36:39], a[100:103]\n\t" \
    "ds_read_b128 v[24:27], v80 offset:12288\n\t" \
    "v_mfma_f32_16x16x32_bf16 a[104:107], v[76:79], v[40:43], a[104:107]\n\t" \
    "ds_read_b128 v[28:31], v80 offset:12800\n\t" \
    "v_mfma_f32_16x16x32_bf16 a[108:111], v[76:79], v[44:47], a[108:111]\n\t" \
    "ds_read_b64_tr_b16 v[68:69], v91 offset:0\n\t" \
    "v_mfma_f32_16x16x32_bf16 a[112:115], v[76:79], v[48:51], a[112:115]\n\t" \
    "ds_read_b64_tr_b16 v[70:71], v91 offset:1024\n\t" \
    "v_mfma_f32_16x16x32_bf16 a[116:119], v[76:79], v[52:55], a[116:119]\n\t" \
    "v_mfma_f32_16x16x32_bf16 a[120:123], v[76:79], v[56:59], a[120:123]\n\t" \
    "v_mfma_f32_16x16x32_bf16 a[124:127], v[76:79], v[60:63], a[124:127]\n\t" \
    "s_waitcnt lgkmcnt(9)\n\t" \
    "v_mfma_f32_16x16x32_bf16 v[128:131], v[64:67], v[0:3], v[128:131]\n\t" \
    "s_waitcnt lgkmcnt(8)\n\t" \
    "v_mfma_f32_16x16x32_bf16 v[132:135], v[64:67], v[4:7], v[132:135]\n\t" \
    "ds_read_b64_tr_b16 v[72:73], v92 offset:0\n\t" \
    "s_waitcnt lgkmcnt(8)\n\t" \
    "v_mfma_f32_16x16x32_bf16 v[136:139], v[64:67], v[8:11], v[136:139]\n\t" \
    "ds_read_b64_tr_b16 v[74:75], v92 offset:1024\n\t" \
    "s_cmp_eq_u32 s20, 0\n\t" \
    "s_cselect_b32 s23, 1, 0\n\t" \
    "v_mov_b32 v112, s23\n\t" \
    "s_waitcnt lgkmcnt(8)\n\t" \
    "v_mfma_f32_16x16x32_bf16 v[140:143], v[64:67], v[12:15], v[140:143]\n\t" \
    "ds_read_b128 v[32:35], v81 offset:0\n\t" \
    "s_lshr_b32 s23, %[bid], 4\n\t" \
    "s_lshl_b32 s23, s23, 7\n\t" \
    "s_add_u32 s23, s23, 0x80000\n\t" \
    "s_waitcnt lgkmcnt(8)\n\t" \
    "v_mfma_f32_16x16x32_bf16 v[144:147], v[64:67], v[16:19], v[144:147]\n\t" \
    "ds_read_b128 v[36:39], v81 offset:512\n\t" \
    "s_waitcnt lgkmcnt(8)\n\t" \
    "v_mfma_f32_16x16x32_bf16 v[148:151], v[64:67], v[20:23], v[148:151]\n\t" \
    "ds_read_b128 v[40:43], v81 offset:4096\n\t" \
    "s_waitcnt lgkmcnt(8)\n\t" \
    "v_mfma_f32_16x16x32_bf16 v[152:155], v[64:67], v[24:27], v[152:155]\n\t" \
    "ds_read_b128 v[44:47], v81 offset:4608\n\t" \
    "s_waitcnt lgkmcnt(8)\n\t" \
    "v_mfma_f32_16x16x32_bf16 v[156:159], v[64:67], v[28:31], v[156:159]\n\t" \
    "s_waitcnt lgkmcnt(6)\n\t" \
    "v_mfma_f32_16x16x32_bf16 v[160:163], v[68:71], v[0:3], v[160:163]\n\t" \
    "s_mov_b64 exec, 1\n\t" \
    "buffer_atomic_add v112, off, s[16:19], s23 sc1\n\t" \
    "s_mov_b64 exec, -1\n\t" \
    "v_mfma_f32_16x16x32_bf16 v[164:167], v[68:71], v[4:7], v[164:167]\n\t" \
    "ds_read_b64_tr_b16 v[76:77], v93 offset:0\n\t" \
    "v_mfma_f32_16x16x32_bf16 v[168:171], v[68:71], v[8:11], v[168:171]\n\t" \
    "ds_read_b64_tr_b16 v[78:79], v93 offset:1024\n\t" \
    "v_mfma_f32_16x16x32_bf16 v[172:175], v[68:71], v[12:15], v[172:175]\n\t" \
    "ds_read_b128 v[48:51], v81 offset:8192\n\t" \
    "v_mfma_f32_16x16x32_bf16 v[176:179], v[68:71], v[16:19], v[176:179]\n\t" \
    "ds_read_b128 v[52:55], v81 offset:8704\n\t" \
    "v_mfma_f32_16x16x32_bf16 v[180:183], v[68:71], v[20:23], v[180:183]\n\t" \
    "ds_read_b128 v[56:59], v81 offset:12288\n\t" \
    "v_mfma_f32_16x16x32_bf16 v[184:187], v[68:71], v[24:27], v[184:187]\n\t" \
    "ds_read_b128 v[60:63], v81 offset:12800\n\t" \
    "v_mfma_f32_16x16x32_bf16 v[188:191], v[68:71], v[28:31], v[188:191]\n\t" \
    "s_waitcnt lgkmcnt(10)\n\t" \
    "v_mfma_f32_16x16x32_bf16 v[192:195], v[72:75], v[0:3], v[192:195]\n\t" \
    "v_mfma_f32_16x16x32_bf16 v[196:199], v[72:75], v[4:7], v[196:199]\n\t" \
    "ds_read_b64_tr_b16 v[64:65], v94 offset:0\n\t" \
    "v_mfma_f32_16x16x32_bf16 v[200:203], v[72:75], v[8:11], v[200:203]\n\t" \
    "ds_read_b64_tr_b16 v[66:67], v94 offset:1024\n\t" \
    "v_mfma_f32_16x16x32_bf16 v[204:207], v[72:75], v[12:15], v[204:207]\n\t" \
    "s_mov_b32 s28, 2359296\n\t" \
    "s_add_u32 m0, s26, 49152\n\t" \
    "v_mfma_f32_16x16x32_bf16 v[208:211], v[72:75], v[16:19], v[208:211]\n\t" \
    "buffer_load_dwordx4 v106, s[12:15], s28 offen offset:0 lds\n\t" \
    "v_mfma_f32_16x16x32_bf16 v[212:215], v[72:75], v[20:23], v[212:215]\n\t" \
    "v_mfma_f32_16x16x32_bf16 v[216:219], v[72:75], v[24:27], v[216:219]\n\t" \
    "v_mfma_f32_16x16x32_bf16 v[220:223], v[72:75], v[28:31], v[220:223]\n\t" \
    "s_waitcnt lgkmcnt(6)\n\t" \
    "v_mfma_f32_16x16x32_bf16 v[224:227], v[76:79], v[0:3], v[224:227]\n\t" \
    "v_mfma_f32_16x16x32_bf16 v[228:231], v[76:79], v[4:7], v[228:231]\n\t" \
    "ds_read_b64_tr_b16 v[68:69], v95 offset:0\n\t" \
    "v_mfma_f32_16x16x32_bf16 v[232:235], v[76:79], v[8:11], v[232:235]\n\t" \
    "ds_read_b64_tr_b16 v[70:71], v95 offset:1024\n\t" \
    "v_mfma_f32_16x16x32_bf16 v[236:239], v[76:79], v[12:15], v[236:239]\n\t" \
    "v_mfma_f32_16x16x32_bf16 v[240:243], v[76:79], v[16:19], v[240:243]\n\t" \
    "buffer_load_dwordx4 v107, s[12:15], s28 offen offset:1024 lds\n\t" \
    "v_mfma_f32_16x16x32_bf16 v[244:247], v[76:79], v[20:23], v[244:247]\n\t" \
    "v_mfma_f32_16x16x32_bf16 v[248:251], v[76:79], v[24:27], v[248:251]\n\t" \
    "v_mfma_f32_16x16x32_bf16 v[252:255], v[76:79], v[28:31], v[252:255]\n\t" \
    "s_waitcnt lgkmcnt(2)\n\t" \
    "v_mfma_f32_16x16x32_bf16 a[0:3], v[64:67], v[0:3], a[0:3]\n\t" \
    "v_mfma_f32_16x16x32_bf16 a[4:7], v[64:67], v[4:7], a[4:7]\n\t" \
    "ds_read_b64_tr_b16 v[72:73], v96 offset:0\n\t" \
    "v_mfma_f32_16x16x32_bf16 a[8:11], v[64:67], v[8:11], a[8:11]\n\t" \
    "ds_read_b64_tr_b16 v[74:75], v96 offset:1024\n\t" \
    "v_mfma_f32_16x16x32_bf16 a[12:15], v[64:67], v[12:15], a[12:15]\n\t" \
    "v_mfma_f32_16x16x32_bf16 a[16:19], v[64:67], v[16:19], a[16:19]\n\t" \
    "buffer_load_dwordx4 v108, s[12:15], s28 offen offset:2048 lds\n\t" \
    "v_mfma_f32_16x16x32_bf16 a[20:23], v[64:67], v[20:23], a[20:23]\n\t" \
    "v_mfma_f32_16x16x32_bf16 a[24:27], v[64:67], v[24:27], a[24:27]\n\t" \
    "v_mfma_f32_16x16x32_bf16 a[28:31], v[64:67], v[28:31], a[28:31]\n\t" \
    "s_waitcnt lgkmcnt(2)\n\t" \
    "v_mfma_f32_16x16x32_bf16 a[32:35], v[68:71], v[0:3], a[32:35]\n\t" \
    "v_mfma_f32_16x16x32_bf16 a[36:39], v[68:71], v[4:7], a[36:39]\n\t" \
    "ds_read_b64_tr_b16 v[76:77], v97 offset:0\n\t" \
    "v_mfma_f32_16x16x32_bf16 a[40:43], v[68:71], v[8:11], a[40:43]\n\t" \
    "ds_read_b64_tr_b16 v[78:79], v97 offset:1024\n\t" \
    "v_mfma_f32_16x16x32_bf16 a[44:47], v[68:71], v[12:15], a[44:47]\n\t" \
    "v_mfma_f32_16x16x32_bf16 a[48:51], v[68:71], v[16:19], a[48:51]\n\t" \
    "buffer_load_dwordx4 v109, s[12:15], s28 offen offset:3072 lds\n\t" \
    "v_mfma_f32_16x16x32_bf16 a[52:55], v[68:71], v[20:23], a[52:55]\n\t" \
    "v_mfma_f32_16x16x32_bf16 a[56:59], v[68:71], v[24:27], a[56:59]\n\t" \
    "v_mfma_f32_16x16x32_bf16 a[60:63], v[68:71], v[28:31], a[60:63]\n\t" \
    "s_waitcnt vmcnt(33) lgkmcnt(0)\n\t" \
    "s_barrier\n\t" \
    "v_mfma_f32_16x16x32_bf16 a[64:67], v[72:75], v[0:3], a[64:67]\n\t" \
    "v_mfma_f32_16x16x32_bf16 a[68:71], v[72:75], v[4:7], a[68:71]\n\t" \
    "ds_read_b64_tr_b16 v[64:65], v90 offset:16384\n\t" \
    "v_mfma_f32_16x16x32_bf16 a[72:75], v[72:75], v[8:11], a[72:75]\n\t" \
    "ds_read_b64_tr_b16 v[66:67], v90 offset:17408\n\t" \
    "v_mfma_f32_16x16x32_bf16 a[76:79], v[72:75], v[12:15], a[76:79]\n\t" \
    "v_mfma_f32_16x16x32_bf16 a[80:83], v[72:75], v[16:19], a[80:83]\n\t" \
    "v_mfma_f32_16x16x32_bf16 a[84:87], v[72:75], v[20:23], a[84:87]\n\t" \
    "v_mfma_f32_16x16x32_bf16 a[88:91], v[72:75], v[24:27], a[88:91]\n\t" \
    "v_mfma_f32_16x16x32_bf16 a[92:95], v[72:75], v[28:31], a[92:95]\n\t" \
    "v_mfma_f32_16x16x32_bf16 a[96:99], v[76:79], v[0:3], a[96:99]\n\t" \
    "v_mfma_f32_16x16x32_bf16 a[100:103], v[76:79], v[4:7], a[100:103]\n\t" \
    "ds_read_b64_tr_b16 v[68:69], v91 offset:16384\n\t" \
    "v_mfma_f32_16x16x32_bf16 a[104:107], v[76:79], v[8:11], a[104:107]\n\t" \
    "ds_read_b64_tr_b16 v[70:71], v91 offset:17408\n\t" \
    "v_mfma_f32_16x16x32_bf16 a[108:111], v[76:79], v[12:15], a[108:111]\n\t" \
    "v_mfma_f32_16x16x32_bf16 a[112:115], v[76:79], v[16:19], a[112:115]\n\t" \
    "v_mfma_f32_16x16x32_bf16 a[116:119], v[76:79], v[20:23], a[116:119]\n\t" \
    "v_mfma_f32_16x16x32_bf16 a[120:123], v[76:79], v[24:27], a[120:123]\n\t" \
    "v_mfma_f32_16x16x32_bf16 a[124:127], v[76:79], v[28:31], a[124:127]\n\t" \
    "s_waitcnt lgkmcnt(2)\n\t" \
    "v_mfma_f32_16x16x32_bf16 v[128:131], v[64:67], v[32:35], v[128:131]\n\t" \
    "v_mfma_f32_16x16x32_bf16 v[132:135], v[64:67], v[36:39], v[132:135]\n\t" \
    "ds_read_b64_tr_b16 v[72:73], v92 offset:16384\n\t" \
    "v_mfma_f32_16x16x32_bf16 v[136:139], v[64:67], v[40:43], v[136:139]\n\t" \
    "ds_read_b64_tr_b16 v[74:75], v92 offset:17408\n\t" \
    "s_sub_u32 s23, 15, s22\n\t" \
    "s_cmp_lt_u32 s20, 2\n\t" \
    "v_mfma_f32_16x16x32_bf16 v[140:143], v[64:67], v[44:47], v[140:143]\n\t" \
    "s_cselect_b32 s23, s23, s22\n\t" \
    "s_lshl_b32 s29, s23, 7\n\t" \
    "s_mov_b32 s27, 512\n\t" \
    "s_add_u32 m0, s25, 0\n\t" \
    "v_mfma_f32_16x16x32_bf16 v[144:147], v[64:67], v[48:51], v[144:147]\n\t" \
    "buffer_load_dwordx4 v98, s[8:11], s27 offen offset:0 lds\n\t" \
    "s_add_u32 s29, s29, 0x80000\n\t" \
    "s_lshl_b32 s23, s23, 3\n\t" \
    "v_mfma_f32_16x16x32_bf16 v[148:151], v[64:67], v[52:55], v[148:151]\n\t" \
    "s_and_b32 s24, s20, 1\n\t" \
    "s_lshl_b32 s24, s24, 2\n\t" \
    "v_mfma_f32_16x16x32_bf16 v[152:155], v[64:67], v[56:59], v[152:155]\n\t" \
    "buffer_load_dwordx4 v99, s[8:11], s27 offen offset:1024 lds\n\t" \
    "s_add_u32 s23, s23, s24\n\t" \
    "s_lshl_b32 s23, s23, 14\n\t" \
    "v_mfma_f32_16x16x32_bf16 v[156:159], v[64:67], v[60:63], v[156:159]\n\t" \
    "s_add_u32 s32, s23, 0x100000\n\t" \
    "s_add_u32 s33, s32, 16384\n\t" \
    "s_waitcnt lgkmcnt(2)\n\t" \
    "v_mfma_f32_16x16x32_bf16 v[160:163], v[68:71], v[32:35], v[160:163]\n\t" \
    "s_add_u32 s34, s32, 32768\n\t" \
    "s_add_u32 s35, s32, 49152\n\t" \
    "v_mfma_f32_16x16x32_bf16 v[164:167], v[68:71], v[36:39], v[164:167]\n\t" \
    "ds_read_b64_tr_b16 v[76:77], v93 offset:16384\n\t" \
    "s_lshr_b32 s30, s20, 1\n\t" \
    "s_lshl_b32 s30, s30, 13\n\t" \
    "v_mfma_f32_16x16x32_bf16 v[168:171], v[68:71], v[40:43], v[168:171]\n\t" \
    "ds_read_b64_tr_b16 v[78:79], v93 offset:17408\n\t" \
    "v_mfma_f32_16x16x32_bf16 v[172:175], v[68:71], v[44:47], v[172:175]\n\t" \
    "v_mfma_f32_16x16x32_bf16 v[176:179], v[68:71], v[48:51], v[176:179]\n\t" \
    "buffer_load_dwordx4 v100, s[8:11], s27 offen offset:2048 lds\n\t" \
    "v_mfma_f32_16x16x32_bf16 v[180:183], v[68:71], v[52:55], v[180:183]\n\t" \
    "v_mfma_f32_16x16x32_bf16 v[184:187], v[68:71], v[56:59], v[184:187]\n\t" \
    "buffer_load_dwordx4 v101, s[8:11], s27 offen offset:3072 lds\n\t" \
    "v_mfma_f32_16x16x32_bf16 v[188:191], v[68:71], v[60:63], v[188:191]\n\t" \
    "s_waitcnt lgkmcnt(2)\n\t" \
    "v_mfma_f32_16x16x32_bf16 v[192:195], v[72:75], v[32:35], v[192:195]\n\t" \
    "v_mfma_f32_16x16x32_bf16 v[196:199], v[72:75], v[36:39], v[196:199]\n\t" \
    "ds_read_b64_tr_b16 v[64:65], v94 offset:16384\n\t" \
    "v_mfma_f32_16x16x32_bf16 v[200:203], v[72:75], v[40:43], v[200:203]\n\t" \
    "ds_read_b64_tr_b16 v[66:67], v94 offset:17408\n\t" \
    "s_add_u32 m0, s25, 4096\n\t" \
    "v_mfma_f32_16x16x32_bf16 v[204:207], v[72:75], v[44:47], v[204:207]\n\t" \
    "buffer_load_dwordx4 v102, s[8:11], s27 offen offset:0 lds\n\t" \
    "v_mfma_f32_16x16x32_bf16 v[208:211], v[72:75], v[48:51], v[208:211]\n\t" \
    "v_mfma_f32_16x16x32_bf16 v[212:215], v[72:75], v[52:55], v[212:215]\n\t" \
    "buffer_load_dwordx4 v103, s[8:11], s27 offen offset:1024 lds\n\t" \
    "v_mfma_f32_16x16x32_bf16 v[216:219], v[72:75], v[56:59], v[216:219]\n\t" \
    "v_mfma_f32_16x16x32_bf16 v[220:223], v[72:75], v[60:63], v[220:223]\n\t" \
    "s_waitcnt lgkmcnt(2)\n\t" \
    "v_mfma_f32_16x16x32_bf16 v[224:227], v[76:79], v[32:35], v[224:227]\n\t" \
    "v_mfma_f32_16x16x32_bf16 v[228:231], v[76:79], v[36:39], v[228:231]\n\t" \
    "ds_read_b64_tr_b16 v[68:69], v95 offset:16384\n\t" \
    "v_mfma_f32_16x16x32_bf16 v[232:235], v[76:79], v[40:43], v[232:235]\n\t" \
    "ds_read_b64_tr_b16 v[70:71], v95 offset:17408\n\t" \
    "v_mfma_f32_16x16x32_bf16 v[236:239], v[76:79], v[44:47], v[236:239]\n\t" \
    "buffer_load_dwordx4 v104, s[8:11], s27 offen offset:2048 lds\n\t" \
    "v_mfma_f32_16x16x32_bf16 v[240:243], v[76:79], v[48:51], v[240:243]\n\t" \
    "v_mfma_f32_16x16x32_bf16 v[244:247], v[76:79], v[52:55], v[244:247]\n\t" \
    "buffer_load_dwordx4 v105, s[8:11], s27 offen offset:3072 lds\n\t" \
    "v_mfma_f32_16x16x32_bf16 v[248:251], v[76:79], v[56:59], v[248:251]\n\t" \
    "v_mfma_f32_16x16x32_bf16 v[252:255], v[76:79], v[60:63], v[252:255]\n\t" \
    "s_waitcnt lgkmcnt(2)\n\t" \
    "v_mfma_f32_16x16x32_bf16 a[0:3], v[64:67], v[32:35], a[0:3]\n\t" \
    "v_mfma_f32_16x16x32_bf16 a[4:7], v[64:67], v[36:39], a[4:7]\n\t" \
    "ds_read_b64_tr_b16 v[72:73], v96 offset:16384\n\t" \
    "v_mfma_f32_16x16x32_bf16 a[8:11], v[64:67], v[40:43], a[8:11]\n\t" \
    "ds_read_b64_tr_b16 v[74:75], v96 offset:17408\n\t" \
    "s_mov_b32 s28, 2621440\n\t" \
    "s_add_u32 m0, s26, 65536\n\t" \
    "v_mfma_f32_16x16x32_bf16 a[12:15], v[64:67], v[44:47], a[12:15]\n\t" \
    "buffer_load_dwordx4 v106, s[12:15], s28 offen offset:0 lds\n\t" \
    "v_mfma_f32_16x16x32_bf16 a[16:19], v[64:67], v[48:51], a[16:19]\n\t" \
    "v_mfma_f32_16x16x32_bf16 a[20:23], v[64:67], v[52:55], a[20:23]\n\t" \
    "buffer_load_dwordx4 v107, s[12:15], s28 offen offset:1024 lds\n\t" \
    "v_mfma_f32_16x16x32_bf16 a[24:27], v[64:67], v[56:59], a[24:27]\n\t" \
    "v_mfma_f32_16x16x32_bf16 a[28:31], v[64:67], v[60:63], a[28:31]\n\t" \
    "s_waitcnt lgkmcnt(2)\n\t" \
    "v_mfma_f32_16x16x32_bf16 a[32:35], v[68:71], v[32:35], a[32:35]\n\t" \
    "v_mfma_f32_16x16x32_bf16 a[36:39], v[68:71], v[36:39], a[36:39]\n\t" \
    "ds_read_b64_tr_b16 v[76:77], v97 offset:16384\n\t" \
    "v_mfma_f32_16x16x32_bf16 a[40:43], v[68:71], v[40:43], a[40:43]\n\t" \
    "ds_read_b64_tr_b16 v[78:79], v97 offset:17408\n\t" \
    "v_mfma_f32_16x16x32_bf16 a[44:47], v[68:71], v[44:47], a[44:47]\n\t" \
    "buffer_load_dwordx4 v108, s[12:15], s28 offen offset:2048 lds\n\t" \
    "v_mfma_f32_16x16x32_bf16 a[48:51], v[68:71], v[48:51], a[48:51]\n\t" \
    "v_mfma_f32_16x16x32_bf16 a[52:55], v[68:71], v[52:55], a[52:55]\n\t" \
    "buffer_load_dwordx4 v109, s[12:15], s28 offen offset:3072 lds\n\t" \
    "v_mfma_f32_16x16x32_bf16 a[56:59], v[68:71], v[56:59], a[56:59]\n\t" \
    "v_mfma_f32_16x16x32_bf16 a[60:63], v[68:71], v[60:63], a[60:63]\n\t" \
    "s_waitcnt vmcnt(21) lgkmcnt(0)\n\t" \
    "s_barrier\n\t" \
    "v_mfma_f32_16x16x32_bf16 a[64:67], v[72:75], v[32:35], a[64:67]\n\t" \
    "v_mfma_f32_16x16x32_bf16 a[68:71], v[72:75], v[36:39], a[68:71]\n\t" \
    "ds_read_b64_tr_b16 v[64:65], v82 offset:0\n\t" \
    "v_mfma_f32_16x16x32_bf16 a[72:75], v[72:75], v[40:43], a[72:75]\n\t" \
    "ds_read_b64_tr_b16 v[66:67], v82 offset:1024\n\t" \
    "v_mfma_f32_16x16x32_bf16 a[76:79], v[72:75], v[44:47], a[76:79]\n\t" \
    "ds_read_b128 v[0:3], v80 offset:32768\n\t" \
    "v_mfma_f32_16x16x32_bf16 a[80:83], v[72:75], v[48:51], a[80:83]\n\t" \
    "ds_read_b128 v[4:7], v80 offset:33280\n\t" \
    "v_mfma_f32_16x16x32_bf16 a[84:87], v[72:75], v[52:55], a[84:87]\n\t" \
    "ds_read_b128 v[8:11], v80 offset:36864\n\t" \
    "v_mfma_f32_16x16x32_bf16 a[88:91], v[72:75], v[56:59], a[88:91]\n\t" \
    "ds_read_b128 v[12:15], v80 offset:37376\n\t" \
    "v_mfma_f32_16x16x32_bf16 a[92:95], v[72:75], v[60:63], a[92:95]\n\t" \
    "ds_read_b128 v[16:19], v80 offset:40960\n\t" \
    "v_mfma_f32_16x16x32_bf16 a[96:99], v[76:79], v[32:35], a[96:99]\n\t" \
    "ds_read_b128 v[20:23], v80 offset:41472\n\t" \
    "v_mfma_f32_16x16x32_bf16 a[100:103], v[76:79], v[36:39], a[100:103]\n\t" \
    "ds_read_b128 v[24:27], v80 offset:45056\n\t" \
    "v_mfma_f32_16x16x32_bf16 a[104:107], v[76:79], v[40:43], a[104:107]\n\t" \
    "ds_read_b128 v[28:31], v80 offset:45568\n\t" \
    "v_mfma_f32_16x16x32_bf16 a[108:111], v[76:79], v[44:47], a[108:111]\n\t" \
    "ds_read_b64_tr_b16 v[68:69], v83 offset:0\n\t" \
    "v_mfma_f32_16x16x32_bf16 a[112:115], v[76:79], v[48:51], a[112:115]\n\t" \
    "ds_read_b64_tr_b16 v[70:71], v83 offset:1024\n\t" \
    "v_mfma_f32_16x16x32_bf16 a[116:119], v[76:79], v[52:55], a[116:119]\n\t" \
    "v_mfma_f32_16x16x32_bf16 a[120:123], v[76:79], v[56:59], a[120:123]\n\t" \
    "v_mfma_f32_16x16x32_bf16 a[124:127], v[76:79], v[60:63], a[124:127]\n\t" \
    "s_waitcnt lgkmcnt(9)\n\t" \
    "v_mfma_f32_16x16x32_bf16 v[128:131], v[64:67], v[0:3], v[128:131]\n\t" \
    "s_waitcnt lgkmcnt(8)\n\t" \
    "v_mfma_f32_16x16x32_bf16 v[132:135], v[64:67], v[4:7], v[132:135]\n\t" \
    "ds_read_b64_tr_b16 v[72:73], v84 offset:0\n\t" \
    "s_waitcnt lgkmcnt(8)\n\t" \
    "v_mfma_f32_16x16x32_bf16 v[136:139], v[64:67], v[8:11], v[136:139]\n\t" \
    "ds_read_b64_tr_b16 v[74:75], v84 offset:1024\n\t" \
    "s_waitcnt lgkmcnt(8)\n\t" \
    "v_mfma_f32_16x16x32_bf16 v[140:143], v[64:67], v[12:15], v[140:143]\n\t" \
    "ds_read_b128 v[32:35], v81 offset:32768\n\t" \
    "s_waitcnt lgkmcnt(8)\n\t" \
    "v_mfma_f32_16x16x32_bf16 v[144:147], v[64:67], v[16:19], v[144:147]\n\t" \
    "ds_read_b128 v[36:39], v81 offset:33280\n\t" \
    "s_waitcnt lgkmcnt(8)\n\t" \
    "v_mfma_f32_16x16x32_bf16 v[148:151], v[64:67], v[20:23], v[148:151]\n\t" \
    "ds_read_b128 v[40:43], v81 offset:36864\n\t" \
    "s_waitcnt lgkmcnt(8)\n\t" \
    "v_mfma_f32_16x16x32_bf16 v[152:155], v[64:67], v[24:27], v[152:155]\n\t" \
    "ds_read_b128 v[44:47], v81 offset:37376\n\t" \
    "s_waitcnt lgkmcnt(8)\n\t" \
    "v_mfma_f32_16x16x32_bf16 v[156:159], v[64:67], v[28:31], v[156:159]\n\t" \
    "s_waitcnt lgkmcnt(6)\n\t" \
    "v_mfma_f32_16x16x32_bf16 v[160:163], v[68:71], v[0:3], v[160:163]\n\t" \
    "v_mfma_f32_16x16x32_bf16 v[164:167], v[68:71], v[4:7], v[164:167]\n\t" \
    "ds_read_b64_tr_b16 v[76:77], v85 offset:0\n\t" \
    "v_mfma_f32_16x16x32_bf16 v[168:171], v[68:71], v[8:11], v[168:171]\n\t" \
    "ds_read_b64_tr_b16 v[78:79], v85 offset:1024\n\t" \
    "v_mfma_f32_16x16x32_bf16 v[172:175], v[68:71], v[12:15], v[172:175]\n\t" \
    "ds_read_b128 v[48:51], v81 offset:40960\n\t" \
    "v_mfma_f32_16x16x32_bf16 v[176:179], v[68:71], v[16:19], v[176:179]\n\t" \
    "ds_read_b128 v[52:55], v81 offset:41472\n\t" \
    "v_mfma_f32_16x16x32_bf16 v[180:183], v[68:71], v[20:23], v[180:183]\n\t" \
    "ds_read_b128 v[56:59], v81 offset:45056\n\t" \
    "v_mfma_f32_16x16x32_bf16 v[184:187], v[68:71], v[24:27], v[184:187]\n\t" \
    "ds_read_b128 v[60:63], v81 offset:45568\n\t" \
    "v_mfma_f32_16x16x32_bf16 v[188:191], v[68:71], v[28:31], v[188:191]\n\t" \
    "s_waitcnt lgkmcnt(10)\n\t" \
    "v_mfma_f32_16x16x32_bf16 v[192:195], v[72:75], v[0:3], v[192:195]\n\t" \
    "v_mfma_f32_16x16x32_bf16 v[196:199], v[72:75], v[4:7], v[196:199]\n\t" \
    "ds_read_b64_tr_b16 v[64:65], v86 offset:0\n\t" \
    "v_mfma_f32_16x16x32_bf16 v[200:203], v[72:75], v[8:11], v[200:203]\n\t" \
    "ds_read_b64_tr_b16 v[66:67], v86 offset:1024\n\t" \
    "v_mfma_f32_16x16x32_bf16 v[204:207], v[72:75], v[12:15], v[204:207]\n\t" \
    "s_mov_b32 s28, 2883584\n\t" \
    "s_add_u32 m0, s26, 81920\n\t" \
    "v_mfma_f32_16x16x32_bf16 v[208:211], v[72:75], v[16:19], v[208:211]\n\t" \
    "buffer_load_dwordx4 v106, s[12:15], s28 offen offset:0 lds\n\t" \
    "v_mfma_f32_16x16x32_bf16 v[212:215], v[72:75], v[20:23], v[212:215]\n\t" \
    "v_mfma_f32_16x16x32_bf16 v[216:219], v[72:75], v[24:27], v[216:219]\n\t" \
    "v_mfma_f32_16x16x32_bf16 v[220:223], v[72:75], v[28:31], v[220:223]\n\t" \
    "s_waitcnt lgkmcnt(6)\n\t" \
    "v_mfma_f32_16x16x32_bf16 v[224:227], v[76:79], v[0:3], v[224:227]\n\t" \
    "v_mfma_f32_16x16x32_bf16 v[228:231], v[76:79], v[4:7], v[228:231]\n\t" \
    "ds_read_b64_tr_b16 v[68:69], v87 offset:0\n\t" \
    "v_mfma_f32_16x16x32_bf16 v[232:235], v[76:79], v[8:11], v[232:235]\n\t" \
    "ds_read_b64_tr_b16 v[70:71], v87 offset:1024\n\t" \
    "v_mfma_f32_16x16x32_bf16 v[236:239], v[76:79], v[12:15], v[236:239]\n\t" \
    "v_mfma_f32_16x16x32_bf16 v[240:243], v[76:79], v[16:19], v[240:243]\n\t" \
    "buffer_load_dwordx4 v107, s[12:15], s28 offen offset:1024 lds\n\t" \
    "v_mfma_f32_16x16x32_bf16 v[244:247], v[76:79], v[20:23], v[244:247]\n\t" \
    "v_mfma_f32_16x16x32_bf16 v[248:251], v[76:79], v[24:27], v[248:251]\n\t" \
    "v_mfma_f32_16x16x32_bf16 v[252:255], v[76:79], v[28:31], v[252:255]\n\t" \
    "s_waitcnt lgkmcnt(2)\n\t" \
    "v_mfma_f32_16x16x32_bf16 a[0:3], v[64:67], v[0:3], a[0:3]\n\t" \
    "v_mfma_f32_16x16x32_bf16 a[4:7], v[64:67], v[4:7], a[4:7]\n\t" \
    "ds_read_b64_tr_b16 v[72:73], v88 offset:0\n\t" \
    "v_mfma_f32_16x16x32_bf16 a[8:11], v[64:67], v[8:11], a[8:11]\n\t" \
    "ds_read_b64_tr_b16 v[74:75], v88 offset:1024\n\t" \
    "v_mfma_f32_16x16x32_bf16 a[12:15], v[64:67], v[12:15], a[12:15]\n\t" \
    "v_mfma_f32_16x16x32_bf16 a[16:19], v[64:67], v[16:19], a[16:19]\n\t" \
    "buffer_load_dwordx4 v108, s[12:15], s28 offen offset:2048 lds\n\t" \
    "v_mfma_f32_16x16x32_bf16 a[20:23], v[64:67], v[20:23], a[20:23]\n\t" \
    "v_mfma_f32_16x16x32_bf16 a[24:27], v[64:67], v[24:27], a[24:27]\n\t" \
    "v_mfma_f32_16x16x32_bf16 a[28:31], v[64:67], v[28:31], a[28:31]\n\t" \
    "s_waitcnt lgkmcnt(2)\n\t" \
    "v_mfma_f32_16x16x32_bf16 a[32:35], v[68:71], v[0:3], a[32:35]\n\t" \
    "v_mfma_f32_16x16x32_bf16 a[36:39], v[68:71], v[4:7], a[36:39]\n\t" \
    "ds_read_b64_tr_b16 v[76:77], v89 offset:0\n\t" \
    "v_mfma_f32_16x16x32_bf16 a[40:43], v[68:71], v[8:11], a[40:43]\n\t" \
    "ds_read_b64_tr_b16 v[78:79], v89 offset:1024\n\t" \
    "v_mfma_f32_16x16x32_bf16 a[44:47], v[68:71], v[12:15], a[44:47]\n\t" \
    "v_mfma_f32_16x16x32_bf16 a[48:51], v[68:71], v[16:19], a[48:51]\n\t" \
    "buffer_load_dwordx4 v109, s[12:15], s28 offen offset:3072 lds\n\t" \
    "v_mfma_f32_16x16x32_bf16 a[52:55], v[68:71], v[20:23], a[52:55]\n\t" \
    "v_mfma_f32_16x16x32_bf16 a[56:59], v[68:71], v[24:27], a[56:59]\n\t" \
    "v_mfma_f32_16x16x32_bf16 a[60:63], v[68:71], v[28:31], a[60:63]\n\t" \
    "s_waitcnt vmcnt(33) lgkmcnt(0)\n\t" \
    "s_barrier\n\t" \
    "v_mfma_f32_16x16x32_bf16 a[64:67], v[72:75], v[0:3], a[64:67]\n\t" \
    "v_mfma_f32_16x16x32_bf16 a[68:71], v[72:75], v[4:7], a[68:71]\n\t" \
    "ds_read_b64_tr_b16 v[64:65], v82 offset:16384\n\t" \
    "v_mfma_f32_16x16x32_bf16 a[72:75], v[72:75], v[8:11], a[72:75]\n\t" \
    "ds_read_b64_tr_b16 v[66:67], v82 offset:17408\n\t" \
    "v_mfma_f32_16x16x32_bf16 a[76:79], v[72:75], v[12:15], a[76:79]\n\t" \
    "v_mfma_f32_16x16x32_bf16 a[80:83], v[72:75], v[16:19], a[80:83]\n\t" \
    "v_mfma_f32_16x16x32_bf16 a[84:87], v[72:75], v[20:23], a[84:87]\n\t" \
    "v_mfma_f32_16x16x32_bf16 a[88:91], v[72:75], v[24:27], a[88:91]\n\t" \
    "v_mfma_f32_16x16x32_bf16 a[92:95], v[72:75], v[28:31], a[92:95]\n\t" \
    "v_mfma_f32_16x16x32_bf16 a[96:99], v[76:79], v[0:3], a[96:99]\n\t" \
    "v_mfma_f32_16x16x32_bf16 a[100:103], v[76:79], v[4:7], a[100:103]\n\t" \
    "ds_read_b64_tr_b16 v[68:69], v83 offset:16384\n\t" \
    "v_mfma_f32_16x16x32_bf16 a[104:107], v[76:79], v[8:11], a[104:107]\n\t" \
    "ds_read_b64_tr_b16 v[70:71], v83 offset:17408\n\t" \
    "v_mfma_f32_16x16x32_bf16 a[108:111], v[76:79], v[12:15], a[108:111]\n\t" \
    "v_mfma_f32_16x16x32_bf16 a[112:115], v[76:79], v[16:19], a[112:115]\n\t" \
    "v_mfma_f32_16x16x32_bf16 a[116:119], v[76:79], v[20:23], a[116:119]\n\t" \
    "v_mfma_f32_16x16x32_bf16 a[120:123], v[76:79], v[24:27], a[120:123]\n\t" \
    "v_mfma_f32_16x16x32_bf16 a[124:127], v[76:79], v[28:31], a[124:127]\n\t" \
    "s_waitcnt lgkmcnt(2)\n\t" \
    "v_mfma_f32_16x16x32_bf16 v[128:131], v[64:67], v[32:35], v[128:131]\n\t" \
    "v_mfma_f32_16x16x32_bf16 v[132:135], v[64:67], v[36:39], v[132:135]\n\t" \
    "ds_read_b64_tr_b16 v[72:73], v84 offset:16384\n\t" \
    "v_mfma_f32_16x16x32_bf16 v[136:139], v[64:67], v[40:43], v[136:139]\n\t" \
    "ds_read_b64_tr_b16 v[74:75], v84 offset:17408\n\t" \
    "v_mfma_f32_16x16x32_bf16 v[140:143], v[64:67], v[44:47], v[140:143]\n\t" \
    "s_mov_b32 s27, 640\n\t" \
    "s_add_u32 m0, s25, 32768\n\t" \
    "v_mfma_f32_16x16x32_bf16 v[144:147], v[64:67], v[48:51], v[144:147]\n\t" \
    "buffer_load_dwordx4 v98, s[8:11], s27 offen offset:0 lds\n\t" \
    "v_mfma_f32_16x16x32_bf16 v[148:151], v[64:67], v[52:55], v[148:151]\n\t" \
    "v_mfma_f32_16x16x32_bf16 v[152:155], v[64:67], v[56:59], v[152:155]\n\t" \
    "buffer_load_dwordx4 v99, s[8:11], s27 offen offset:1024 lds\n\t" \
    "v_mfma_f32_16x16x32_bf16 v[156:159], v[64:67], v[60:63], v[156:159]\n\t" \
    "s_waitcnt lgkmcnt(2)\n\t" \
    "v_mfma_f32_16x16x32_bf16 v[160:163], v[68:71], v[32:35], v[160:163]\n\t" \
    "v_mfma_f32_16x16x32_bf16 v[164:167], v[68:71], v[36:39], v[164:167]\n\t" \
    "ds_read_b64_tr_b16 v[76:77], v85 offset:16384\n\t" \
    "v_mfma_f32_16x16x32_bf16 v[168:171], v[68:71], v[40:43], v[168:171]\n\t" \
    "ds_read_b64_tr_b16 v[78:79], v85 offset:17408\n\t" \
    "v_mfma_f32_16x16x32_bf16 v[172:175], v[68:71], v[44:47], v[172:175]\n\t" \
    "v_mfma_f32_16x16x32_bf16 v[176:179], v[68:71], v[48:51], v[176:179]\n\t" \
    "buffer_load_dwordx4 v100, s[8:11], s27 offen offset:2048 lds\n\t" \
    "v_mfma_f32_16x16x32_bf16 v[180:183], v[68:71], v[52:55], v[180:183]\n\t" \
    "v_mfma_f32_16x16x32_bf16 v[184:187], v[68:71], v[56:59], v[184:187]\n\t" \
    "buffer_load_dwordx4 v101, s[8:11], s27 offen offset:3072 lds\n\t" \
    "v_mfma_f32_16x16x32_bf16 v[188:191], v[68:71], v[60:63], v[188:191]\n\t" \
    "s_waitcnt lgkmcnt(2)\n\t" \
    "v_mfma_f32_16x16x32_bf16 v[192:195], v[72:75], v[32:35], v[192:195]\n\t" \
    "v_mfma_f32_16x16x32_bf16 v[196:199], v[72:75], v[36:39], v[196:199]\n\t" \
    "ds_read_b64_tr_b16 v[64:65], v86 offset:16384\n\t" \
    "v_mfma_f32_16x16x32_bf16 v[200:203], v[72:75], v[40:43], v[200:203]\n\t" \
    "ds_read_b64_tr_b16 v[66:67], v86 offset:17408\n\t" \
    "s_add_u32 m0, s25, 36864\n\t" \
    "v_mfma_f32_16x16x32_bf16 v[204:207], v[72:75], v[44:47], v[204:207]\n\t" \
    "buffer_load_dwordx4 v102, s[8:11], s27 offen offset:0 lds\n\t" \
    "v_mfma_f32_16x16x32_bf16 v[208:211], v[72:75], v[48:51], v[208:211]\n\t" \
    "v_mfma_f32_16x16x32_bf16 v[212:215], v[72:75], v[52:55], v[212:215]\n\t" \
    "buffer_load_dwordx4 v103, s[8:11], s27 offen offset:1024 lds\n\t" \
    "v_mfma_f32_16x16x32_bf16 v[216:219], v[72:75], v[56:59], v[216:219]\n\t" \
    "v_mfma_f32_16x16x32_bf16 v[220:223], v[72:75], v[60:63], v[220:223]\n\t" \
    "s_waitcnt lgkmcnt(2)\n\t" \
    "v_mfma_f32_16x16x32_bf16 v[224:227], v[76:79], v[32:35], v[224:227]\n\t" \
    "v_mfma_f32_16x16x32_bf16 v[228:231], v[76:79], v[36:39], v[228:231]\n\t" \
    "ds_read_b64_tr_b16 v[68:69], v87 offset:16384\n\t" \
    "v_mfma_f32_16x16x32_bf16 v[232:235], v[76:79], v[40:43], v[232:235]\n\t" \
    "ds_read_b64_tr_b16 v[70:71], v87 offset:17408\n\t" \
    "v_mfma_f32_16x16x32_bf16 v[236:239], v[76:79], v[44:47], v[236:239]\n\t" \
    "buffer_load_dwordx4 v104, s[8:11], s27 offen offset:2048 lds\n\t" \
    "v_mfma_f32_16x16x32_bf16 v[240:243], v[76:79], v[48:51], v[240:243]\n\t" \
    "v_mfma_f32_16x16x32_bf16 v[244:247], v[76:79], v[52:55], v[244:247]\n\t" \
    "buffer_load_dwordx4 v105, s[8:11], s27 offen offset:3072 lds\n\t" \
    "v_mfma_f32_16x16x32_bf16 v[248:251], v[76:79], v[56:59], v[248:251]\n\t" \
    "v_mfma_f32_16x16x32_bf16 v[252:255], v[76:79], v[60:63], v[252:255]\n\t" \
    "s_waitcnt lgkmcnt(2)\n\t" \
    "v_mfma_f32_16x16x32_bf16 a[0:3], v[64:67], v[32:35], a[0:3]\n\t" \
    "v_mfma_f32_16x16x32_bf16 a[4:7], v[64:67], v[36:39], a[4:7]\n\t" \
    "ds_read_b64_tr_b16 v[72:73], v88 offset:16384\n\t" \
    "v_mfma_f32_16x16x32_bf16 a[8:11], v[64:67], v[40:43], a[8:11]\n\t" \
    "ds_read_b64_tr_b16 v[74:75], v88 offset:17408\n\t" \
    "s_mov_b32 s28, 3145728\n\t" \
    "s_add_u32 m0, s26, 0\n\t" \
    "v_mfma_f32_16x16x32_bf16 a[12:15], v[64:67], v[44:47], a[12:15]\n\t" \
    "buffer_load_dwordx4 v106, s[12:15], s28 offen offset:0 lds\n\t" \
    "v_mfma_f32_16x16x32_bf16 a[16:19], v[64:67], v[48:51], a[16:19]\n\t" \
    "v_mfma_f32_16x16x32_bf16 a[20:23], v[64:67], v[52:55], a[20:23]\n\t" \
    "buffer_load_dwordx4 v107, s[12:15], s28 offen offset:1024 lds\n\t" \
    "v_mfma_f32_16x16x32_bf16 a[24:27], v[64:67], v[56:59], a[24:27]\n\t" \
    "v_mfma_f32_16x16x32_bf16 a[28:31], v[64:67], v[60:63], a[28:31]\n\t" \
    "s_waitcnt lgkmcnt(2)\n\t" \
    "v_mfma_f32_16x16x32_bf16 a[32:35], v[68:71], v[32:35], a[32:35]\n\t" \
    "v_mfma_f32_16x16x32_bf16 a[36:39], v[68:71], v[36:39], a[36:39]\n\t" \
    "ds_read_b64_tr_b16 v[76:77], v89 offset:16384\n\t" \
    "v_mfma_f32_16x16x32_bf16 a[40:43], v[68:71], v[40:43], a[40:43]\n\t" \
    "ds_read_b64_tr_b16 v[78:79], v89 offset:17408\n\t" \
    "v_mfma_f32_16x16x32_bf16 a[44:47], v[68:71], v[44:47], a[44:47]\n\t" \
    "buffer_load_dwordx4 v108, s[12:15], s28 offen offset:2048 lds\n\t" \
    "v_mfma_f32_16x16x32_bf16 a[48:51], v[68:71], v[48:51], a[48:51]\n\t" \
    "v_mfma_f32_16x16x32_bf16 a[52:55], v[68:71], v[52:55], a[52:55]\n\t" \
    "buffer_load_dwordx4 v109, s[12:15], s28 offen offset:3072 lds\n\t" \
    "v_mfma_f32_16x16x32_bf16 a[56:59], v[68:71], v[56:59], a[56:59]\n\t" \
    "v_mfma_f32_16x16x32_bf16 a[60:63], v[68:71], v[60:63], a[60:63]\n\t" \
    "s_waitcnt vmcnt(20) lgkmcnt(0)\n\t" \
    "s_barrier\n\t" \
    "v_mfma_f32_16x16x32_bf16 a[64:67], v[72:75], v[32:35], a[64:67]\n\t" \
    "v_mfma_f32_16x16x32_bf16 a[68:71], v[72:75], v[36:39], a[68:71]\n\t" \
    "ds_read_b64_tr_b16 v[64:65], v82 offset:32768\n\t" \
    "v_mfma_f32_16x16x32_bf16 a[72:75], v[72:75], v[40:43], a[72:75]\n\t" \
    "ds_read_b64_tr_b16 v[66:67], v82 offset:33792\n\t" \
    "v_mfma_f32_16x16x32_bf16 a[76:79], v[72:75], v[44:47], a[76:79]\n\t" \
    "ds_read_b128 v[0:3], v80 offset:0\n\t" \
    "v_mfma_f32_16x16x32_bf16 a[80:83], v[72:75], v[48:51], a[80:83]\n\t" \
    "ds_read_b128 v[4:7], v80 offset:512\n\t" \
    "v_mfma_f32_16x16x32_bf16 a[84:87], v[72:75], v[52:55], a[84:87]\n\t" \
    "ds_read_b128 v[8:11], v80 offset:4096\n\t" \
    "v_mfma_f32_16x16x32_bf16 a[88:91], v[72:75], v[56:59], a[88:91]\n\t" \
    "ds_read_b128 v[12:15], v80 offset:4608\n\t" \
    "v_mfma_f32_16x16x32_bf16 a[92:95], v[72:75], v[60:63], a[92:95]\n\t" \
    "ds_read_b128 v[16:19], v80 offset:8192\n\t" \
    "v_mfma_f32_16x16x32_bf16 a[96:99], v[76:79], v[32:35], a[96:99]\n\t" \
    "ds_read_b128 v[20:23], v80 offset:8704\n\t" \
    "v_mfma_f32_16x16x32_bf16 a[100:103], v[76:79], v[36:39], a[100:103]\n\t" \
    "ds_read_b128 v[24:27], v80 offset:12288\n\t" \
    "v_mfma_f32_16x16x32_bf16 a[104:107], v[76:79], v[40:43], a[104:107]\n\t" \
    "ds_read_b128 v[28:31], v80 offset:12800\n\t" \
    "v_mfma_f32_16x16x32_bf16 a[108:111], v[76:79], v[44:47], a[108:111]\n\t" \
    "ds_read_b64_tr_b16 v[68:69], v83 offset:32768\n\t" \
    "v_mfma_f32_16x16x32_bf16 a[112:115], v[76:79], v[48:51], a[112:115]\n\t" \
    "ds_read_b64_tr_b16 v[70:71], v83 offset:33792\n\t" \
    "v_mfma_f32_16x16x32_bf16 a[116:119], v[76:79], v[52:55], a[116:119]\n\t" \
    "v_mfma_f32_16x16x32_bf16 a[120:123], v[76:79], v[56:59], a[120:123]\n\t" \
    "v_mfma_f32_16x16x32_bf16 a[124:127], v[76:79], v[60:63], a[124:127]\n\t" \
    "s_waitcnt lgkmcnt(9)\n\t" \
    "v_mfma_f32_16x16x32_bf16 v[128:131], v[64:67], v[0:3], v[128:131]\n\t" \
    "s_waitcnt lgkmcnt(8)\n\t" \
    "v_mfma_f32_16x16x32_bf16 v[132:135], v[64:67], v[4:7], v[132:135]\n\t" \
    "ds_read_b64_tr_b16 v[72:73], v84 offset:32768\n\t" \
    "s_waitcnt lgkmcnt(8)\n\t" \
    "v_mfma_f32_16x16x32_bf16 v[136:139], v[64:67], v[8:11], v[136:139]\n\t" \
    "ds_read_b64_tr_b16 v[74:75], v84 offset:33792\n\t" \
    "s_waitcnt lgkmcnt(8)\n\t" \
    "v_mfma_f32_16x16x32_bf16 v[140:143], v[64:67], v[12:15], v[140:143]\n\t" \
    "ds_read_b128 v[32:35], v81 offset:0\n\t" \
    "s_waitcnt lgkmcnt(8)\n\t" \
    "v_mfma_f32_16x16x32_bf16 v[144:147], v[64:67], v[16:19], v[144:147]\n\t" \
    "ds_read_b128 v[36:39], v81 offset:512\n\t" \
    "s_waitcnt lgkmcnt(8)\n\t" \
    "v_mfma_f32_16x16x32_bf16 v[148:151], v[64:67], v[20:23], v[148:151]\n\t" \
    "ds_read_b128 v[40:43], v81 offset:4096\n\t" \
    "s_waitcnt lgkmcnt(8)\n\t" \
    "v_mfma_f32_16x16x32_bf16 v[152:155], v[64:67], v[24:27], v[152:155]\n\t" \
    "ds_read_b128 v[44:47], v81 offset:4608\n\t" \
    "s_waitcnt lgkmcnt(8)\n\t" \
    "v_mfma_f32_16x16x32_bf16 v[156:159], v[64:67], v[28:31], v[156:159]\n\t" \
    "s_waitcnt lgkmcnt(6)\n\t" \
    "v_mfma_f32_16x16x32_bf16 v[160:163], v[68:71], v[0:3], v[160:163]\n\t" \
    "v_mfma_f32_16x16x32_bf16 v[164:167], v[68:71], v[4:7], v[164:167]\n\t" \
    "ds_read_b64_tr_b16 v[76:77], v85 offset:32768\n\t" \
    "v_mfma_f32_16x16x32_bf16 v[168:171], v[68:71], v[8:11], v[168:171]\n\t" \
    "ds_read_b64_tr_b16 v[78:79], v85 offset:33792\n\t" \
    "v_mfma_f32_16x16x32_bf16 v[172:175], v[68:71], v[12:15], v[172:175]\n\t" \
    "ds_read_b128 v[48:51], v81 offset:8192\n\t" \
    "v_mfma_f32_16x16x32_bf16 v[176:179], v[68:71], v[16:19], v[176:179]\n\t" \
    "ds_read_b128 v[52:55], v81 offset:8704\n\t" \
    "v_mfma_f32_16x16x32_bf16 v[180:183], v[68:71], v[20:23], v[180:183]\n\t" \
    "ds_read_b128 v[56:59], v81 offset:12288\n\t" \
    "v_mfma_f32_16x16x32_bf16 v[184:187], v[68:71], v[24:27], v[184:187]\n\t" \
    "ds_read_b128 v[60:63], v81 offset:12800\n\t" \
    "v_mfma_f32_16x16x32_bf16 v[188:191], v[68:71], v[28:31], v[188:191]\n\t" \
    "s_waitcnt lgkmcnt(10)\n\t" \
    "v_mfma_f32_16x16x32_bf16 v[192:195], v[72:75], v[0:3], v[192:195]\n\t" \
    "v_mfma_f32_16x16x32_bf16 v[196:199], v[72:75], v[4:7], v[196:199]\n\t" \
    "ds_read_b64_tr_b16 v[64:65], v86 offset:32768\n\t" \
    "v_mfma_f32_16x16x32_bf16 v[200:203], v[72:75], v[8:11], v[200:203]\n\t" \
    "ds_read_b64_tr_b16 v[66:67], v86 offset:33792\n\t" \
    "v_mfma_f32_16x16x32_bf16 v[204:207], v[72:75], v[12:15], v[204:207]\n\t" \
    "s_mov_b32 s28, 3407872\n\t" \
    "s_add_u32 m0, s26, 16384\n\t" \
    "v_mfma_f32_16x16x32_bf16 v[208:211], v[72:75], v[16:19], v[208:211]\n\t" \
    "buffer_load_dwordx4 v106, s[12:15], s28 offen offset:0 lds\n\t" \
    "v_mfma_f32_16x16x32_bf16 v[212:215], v[72:75], v[20:23], v[212:215]\n\t" \
    "v_mfma_f32_16x16x32_bf16 v[216:219], v[72:75], v[24:27], v[216:219]\n\t" \
    "v_mfma_f32_16x16x32_bf16 v[220:223], v[72:75], v[28:31], v[220:223]\n\t" \
    "s_waitcnt lgkmcnt(6)\n\t" \
    "v_mfma_f32_16x16x32_bf16 v[224:227], v[76:79], v[0:3], v[224:227]\n\t" \
    "v_mfma_f32_16x16x32_bf16 v[228:231], v[76:79], v[4:7], v[228:231]\n\t" \
    "ds_read_b64_tr_b16 v[68:69], v87 offset:32768\n\t" \
    "v_mfma_f32_16x16x32_bf16 v[232:235], v[76:79], v[8:11], v[232:235]\n\t" \
    "ds_read_b64_tr_b16 v[70:71], v87 offset:33792\n\t" \
    "v_mfma_f32_16x16x32_bf16 v[236:239], v[76:79], v[12:15], v[236:239]\n\t" \
    "v_mfma_f32_16x16x32_bf16 v[240:243], v[76:79], v[16:19], v[240:243]\n\t" \
    "buffer_load_dwordx4 v107, s[12:15], s28 offen offset:1024 lds\n\t" \
    "v_mfma_f32_16x16x32_bf16 v[244:247], v[76:79], v[20:23], v[244:247]\n\t" \
    "v_mfma_f32_16x16x32_bf16 v[248:251], v[76:79], v[24:27], v[248:251]\n\t" \
    "v_mfma_f32_16x16x32_bf16 v[252:255], v[76:79], v[28:31], v[252:255]\n\t" \
    "s_waitcnt lgkmcnt(2)\n\t" \
    "v_mfma_f32_16x16x32_bf16 a[0:3], v[64:67], v[0:3], a[0:3]\n\t" \
    "v_mfma_f32_16x16x32_bf16 a[4:7], v[64:67], v[4:7], a[4:7]\n\t" \
    "ds_read_b64_tr_b16 v[72:73], v88 offset:32768\n\t" \
    "v_mfma_f32_16x16x32_bf16 a[8:11], v[64:67], v[8:11], a[8:11]\n\t" \
    "ds_read_b64_tr_b16 v[74:75], v88 offset:33792\n\t" \
    "v_mfma_f32_16x16x32_bf16 a[12:15], v[64:67], v[12:15], a[12:15]\n\t" \
    "v_mfma_f32_16x16x32_bf16 a[16:19], v[64:67], v[16:19], a[16:19]\n\t" \
    "buffer_load_dwordx4 v108, s[12:15], s28 offen offset:2048 lds\n\t" \
    "v_mfma_f32_16x16x32_bf16 a[20:23], v[64:67], v[20:23], a[20:23]\n\t" \
    "v_mfma_f32_16x16x32_bf16 a[24:27], v[64:67], v[24:27], a[24:27]\n\t" \
    "v_mfma_f32_16x16x32_bf16 a[28:31], v[64:67], v[28:31], a[28:31]\n\t" \
    "s_waitcnt lgkmcnt(2)\n\t" \
    "v_mfma_f32_16x16x32_bf16 a[32:35], v[68:71], v[0:3], a[32:35]\n\t" \
    "v_mfma_f32_16x16x32_bf16 a[36:39], v[68:71], v[4:7], a[36:39]\n\t" \
    "ds_read_b64_tr_b16 v[76:77], v89 offset:32768\n\t" \
    "v_mfma_f32_16x16x32_bf16 a[40:43], v[68:71], v[8:11], a[40:43]\n\t" \
    "ds_read_b64_tr_b16 v[78:79], v89 offset:33792\n\t" \
    "v_mfma_f32_16x16x32_bf16 a[44:47], v[68:71], v[12:15], a[44:47]\n\t" \
    "v_mfma_f32_16x16x32_bf16 a[48:51], v[68:71], v[16:19], a[48:51]\n\t" \
    "buffer_load_dwordx4 v109, s[12:15], s28 offen offset:3072 lds\n\t" \
    "v_mfma_f32_16x16x32_bf16 a[52:55], v[68:71], v[20:23], a[52:55]\n\t" \
    "v_mfma_f32_16x16x32_bf16 a[56:59], v[68:71], v[24:27], a[56:59]\n\t" \
    "v_mfma_f32_16x16x32_bf16 a[60:63], v[68:71], v[28:31], a[60:63]\n\t" \
    "s_waitcnt vmcnt(32) lgkmcnt(0)\n\t" \
    "s_barrier\n\t" \
    "v_mfma_f32_16x16x32_bf16 a[64:67], v[72:75], v[0:3], a[64:67]\n\t" \
    "v_mfma_f32_16x16x32_bf16 a[68:71], v[72:75], v[4:7], a[68:71]\n\t" \
    "ds_read_b64_tr_b16 v[64:65], v82 offset:49152\n\t" \
    "v_mfma_f32_16x16x32_bf16 a[72:75], v[72:75], v[8:11], a[72:75]\n\t" \
    "ds_read_b64_tr_b16 v[66:67], v82 offset:50176\n\t" \
    "v_mfma_f32_16x16x32_bf16 a[76:79], v[72:75], v[12:15], a[76:79]\n\t" \
    "v_mfma_f32_16x16x32_bf16 a[80:83], v[72:75], v[16:19], a[80:83]\n\t" \
    "v_mfma_f32_16x16x32_bf16 a[84:87], v[72:75], v[20:23], a[84:87]\n\t" \
    "v_mfma_f32_16x16x32_bf16 a[88:91], v[72:75], v[24:27], a[88:91]\n\t" \
    "v_mfma_f32_16x16x32_bf16 a[92:95], v[72:75], v[28:31], a[92:95]\n\t" \
    "v_mfma_f32_16x16x32_bf16 a[96:99], v[76:79], v[0:3], a[96:99]\n\t" \
    "v_mfma_f32_16x16x32_bf16 a[100:103], v[76:79], v[4:7], a[100:103]\n\t" \
    "ds_read_b64_tr_b16 v[68:69], v83 offset:49152\n\t" \
    "v_mfma_f32_16x16x32_bf16 a[104:107], v[76:79], v[8:11], a[104:107]\n\t" \
    "ds_read_b64_tr_b16 v[70:71], v83 offset:50176\n\t" \
    "v_mfma_f32_16x16x32_bf16 a[108:111], v[76:79], v[12:15], a[108:111]\n\t" \
    "v_mfma_f32_16x16x32_bf16 a[112:115], v[76:79], v[16:19], a[112:115]\n\t" \
    "v_mfma_f32_16x16x32_bf16 a[116:119], v[76:79], v[20:23], a[116:119]\n\t" \
    "v_mfma_f32_16x16x32_bf16 a[120:123], v[76:79], v[24:27], a[120:123]\n\t" \
    "v_mfma_f32_16x16x32_bf16 a[124:127], v[76:79], v[28:31], a[124:127]\n\t" \
    "s_waitcnt lgkmcnt(2)\n\t" \
    "v_mfma_f32_16x16x32_bf16 v[128:131], v[64:67], v[32:35], v[128:131]\n\t" \
    "v_mfma_f32_16x16x32_bf16 v[132:135], v[64:67], v[36:39], v[132:135]\n\t" \
    "ds_read_b64_tr_b16 v[72:73], v84 offset:49152\n\t" \
    "v_mfma_f32_16x16x32_bf16 v[136:139], v[64:67], v[40:43], v[136:139]\n\t" \
    "ds_read_b64_tr_b16 v[74:75], v84 offset:50176\n\t" \
    "v_mfma_f32_16x16x32_bf16 v[140:143], v[64:67], v[44:47], v[140:143]\n\t" \
    "s_mov_b32 s27, 768\n\t" \
    "s_add_u32 m0, s25, 0\n\t" \
    "v_mfma_f32_16x16x32_bf16 v[144:147], v[64:67], v[48:51], v[144:147]\n\t" \
    "buffer_load_dwordx4 v98, s[8:11], s27 offen offset:0 lds\n\t" \
    "v_mfma_f32_16x16x32_bf16 v[148:151], v[64:67], v[52:55], v[148:151]\n\t" \
    "v_mfma_f32_16x16x32_bf16 v[152:155], v[64:67], v[56:59], v[152:155]\n\t" \
    "buffer_load_dwordx4 v99, s[8:11], s27 offen offset:1024 lds\n\t" \
    "v_mfma_f32_16x16x32_bf16 v[156:159], v[64:67], v[60:63], v[156:159]\n\t" \
    "s_waitcnt lgkmcnt(2)\n\t" \
    "v_mfma_f32_16x16x32_bf16 v[160:163], v[68:71], v[32:35], v[160:163]\n\t" \
    "v_mfma_f32_16x16x32_bf16 v[164:167], v[68:71], v[36:39], v[164:167]\n\t" \
    "ds_read_b64_tr_b16 v[76:77], v85 offset:49152\n\t" \
    "v_mfma_f32_16x16x32_bf16 v[168:171], v[68:71], v[40:43], v[168:171]\n\t" \
    "ds_read_b64_tr_b16 v[78:79], v85 offset:50176\n\t" \
    "v_mfma_f32_16x16x32_bf16 v[172:175], v[68:71], v[44:47], v[172:175]\n\t" \
    "v_mfma_f32_16x16x32_bf16 v[176:179], v[68:71], v[48:51], v[176:179]\n\t" \
    "buffer_load_dwordx4 v100, s[8:11], s27 offen offset:2048 lds\n\t" \
    "v_mfma_f32_16x16x32_bf16 v[180:183], v[68:71], v[52:55], v[180:183]\n\t" \
    "v_mfma_f32_16x16x32_bf16 v[184:187], v[68:71], v[56:59], v[184:187]\n\t" \
    "buffer_load_dwordx4 v101, s[8:11], s27 offen offset:3072 lds\n\t" \
    "v_mfma_f32_16x16x32_bf16 v[188:191], v[68:71], v[60:63], v[188:191]\n\t" \
    "s_waitcnt lgkmcnt(2)\n\t" \
    "v_mfma_f32_16x16x32_bf16 v[192:195], v[72:75], v[32:35], v[192:195]\n\t" \
    "v_mfma_f32_16x16x32_bf16 v[196:199], v[72:75], v[36:39], v[196:199]\n\t" \
    "ds_read_b64_tr_b16 v[64:65], v86 offset:49152\n\t" \
    "v_mfma_f32_16x16x32_bf16 v[200:203], v[72:75], v[40:43], v[200:203]\n\t" \
    "ds_read_b64_tr_b16 v[66:67], v86 offset:50176\n\t" \
    "s_add_u32 m0, s25, 4096\n\t" \
    "v_mfma_f32_16x16x32_bf16 v[204:207], v[72:75], v[44:47], v[204:207]\n\t" \
    "buffer_load_dwordx4 v102, s[8:11], s27 offen offset:0 lds\n\t" \
    "v_mfma_f32_16x16x32_bf16 v[208:211], v[72:75], v[48:51], v[208:211]\n\t" \
    "v_mfma_f32_16x16x32_bf16 v[212:215], v[72:75], v[52:55], v[212:215]\n\t" \
    "buffer_load_dwordx4 v103, s[8:11], s27 offen offset:1024 lds\n\t" \
    "v_mfma_f32_16x16x32_bf16 v[216:219], v[72:75], v[56:59], v[216:219]\n\t" \
    "v_mfma_f32_16x16x32_bf16 v[220:223], v[72:75], v[60:63], v[220:223]\n\t" \
    "s_waitcnt lgkmcnt(2)\n\t" \
    "v_mfma_f32_16x16x32_bf16 v[224:227], v[76:79], v[32:35], v[224:227]\n\t" \
    "v_mfma_f32_16x16x32_bf16 v[228:231], v[76:79], v[36:39], v[228:231]\n\t" \
    "ds_read_b64_tr_b16 v[68:69], v87 offset:49152\n\t" \
    "v_mfma_f32_16x16x32_bf16 v[232:235], v[76:79], v[40:43], v[232:235]\n\t" \
    "ds_read_b64_tr_b16 v[70:71], v87 offset:50176\n\t" \
    "v_mfma_f32_16x16x32_bf16 v[236:239], v[76:79], v[44:47], v[236:239]\n\t" \
    "buffer_load_dwordx4 v104, s[8:11], s27 offen offset:2048 lds\n\t" \
    "v_mfma_f32_16x16x32_bf16 v[240:243], v[76:79], v[48:51], v[240:243]\n\t" \
    "v_mfma_f32_16x16x32_bf16 v[244:247], v[76:79], v[52:55], v[244:247]\n\t" \
    "buffer_load_dwordx4 v105, s[8:11], s27 offen offset:3072 lds\n\t" \
    "v_mfma_f32_16x16x32_bf16 v[248:251], v[76:79], v[56:59], v[248:251]\n\t" \
    "v_mfma_f32_16x16x32_bf16 v[252:255], v[76:79], v[60:63], v[252:255]\n\t" \
    "s_waitcnt lgkmcnt(2)\n\t" \
    "v_mfma_f32_16x16x32_bf16 a[0:3], v[64:67], v[32:35], a[0:3]\n\t" \
    "v_mfma_f32_16x16x32_bf16 a[4:7], v[64:67], v[36:39], a[4:7]\n\t" \
    "ds_read_b64_tr_b16 v[72:73], v88 offset:49152\n\t" \
    "v_mfma_f32_16x16x32_bf16 a[8:11], v[64:67], v[40:43], a[8:11]\n\t" \
    "ds_read_b64_tr_b16 v[74:75], v88 offset:50176\n\t" \
    "s_mov_b32 s28, 3670016\n\t" \
    "s_add_u32 m0, s26, 32768\n\t" \
    "v_mfma_f32_16x16x32_bf16 a[12:15], v[64:67], v[44:47], a[12:15]\n\t" \
    "buffer_load_dwordx4 v106, s[12:15], s28 offen offset:0 lds\n\t" \
    "v_mfma_f32_16x16x32_bf16 a[16:19], v[64:67], v[48:51], a[16:19]\n\t" \
    "v_mfma_f32_16x16x32_bf16 a[20:23], v[64:67], v[52:55], a[20:23]\n\t" \
    "buffer_load_dwordx4 v107, s[12:15], s28 offen offset:1024 lds\n\t" \
    "v_mfma_f32_16x16x32_bf16 a[24:27], v[64:67], v[56:59], a[24:27]\n\t" \
    "v_mfma_f32_16x16x32_bf16 a[28:31], v[64:67], v[60:63], a[28:31]\n\t" \
    "s_waitcnt lgkmcnt(2)\n\t" \
    "v_mfma_f32_16x16x32_bf16 a[32:35], v[68:71], v[32:35], a[32:35]\n\t" \
    "v_mfma_f32_16x16x32_bf16 a[36:39], v[68:71], v[36:39], a[36:39]\n\t" \
    "ds_read_b64_tr_b16 v[76:77], v89 offset:49152\n\t" \
    "v_mfma_f32_16x16x32_bf16 a[40:43], v[68:71], v[40:43], a[40:43]\n\t" \
    "ds_read_b64_tr_b16 v[78:79], v89 offset:50176\n\t" \
    "v_mfma_f32_16x16x32_bf16 a[44:47], v[68:71], v[44:47], a[44:47]\n\t" \
    "buffer_load_dwordx4 v108, s[12:15], s28 offen offset:2048 lds\n\t" \
    "v_mfma_f32_16x16x32_bf16 a[48:51], v[68:71], v[48:51], a[48:51]\n\t" \
    "v_mfma_f32_16x16x32_bf16 a[52:55], v[68:71], v[52:55], a[52:55]\n\t" \
    "buffer_load_dwordx4 v109, s[12:15], s28 offen offset:3072 lds\n\t" \
    "v_mfma_f32_16x16x32_bf16 a[56:59], v[68:71], v[56:59], a[56:59]\n\t" \
    "v_mfma_f32_16x16x32_bf16 a[60:63], v[68:71], v[60:63], a[60:63]\n\t" \
    "s_waitcnt vmcnt(20) lgkmcnt(0)\n\t" \
    "s_barrier\n\t" \
    "v_mfma_f32_16x16x32_bf16 a[64:67], v[72:75], v[32:35], a[64:67]\n\t" \
    "v_mfma_f32_16x16x32_bf16 a[68:71], v[72:75], v[36:39], a[68:71]\n\t" \
    "ds_read_b64_tr_b16 v[64:65], v90 offset:0\n\t" \
    "v_mfma_f32_16x16x32_bf16 a[72:75], v[72:75], v[40:43], a[72:75]\n\t" \
    "ds_read_b64_tr_b16 v[66:67], v90 offset:1024\n\t" \
    "v_mfma_f32_16x16x32_bf16 a[76:79], v[72:75], v[44:47], a[76:79]\n\t" \
    "ds_read_b128 v[0:3], v80 offset:32768\n\t" \
    "v_mfma_f32_16x16x32_bf16 a[80:83], v[72:75], v[48:51], a[80:83]\n\t" \
    "ds_read_b128 v[4:7], v80 offset:33280\n\t" \
    "v_mfma_f32_16x16x32_bf16 a[84:87], v[72:75], v[52:55], a[84:87]\n\t" \
    "ds_read_b128 v[8:11], v80 offset:36864\n\t" \
    "v_mfma_f32_16x16x32_bf16 a[88:91], v[72:75], v[56:59], a[88:91]\n\t" \
    "ds_read_b128 v[12:15], v80 offset:37376\n\t" \
    "v_mfma_f32_16x16x32_bf16 a[92:95], v[72:75], v[60:63], a[92:95]\n\t" \
    "ds_read_b128 v[16:19], v80 offset:40960\n\t" \
    "v_mfma_f32_16x16x32_bf16 a[96:99], v[76:79], v[32:35], a[96:99]\n\t" \
    "ds_read_b128 v[20:23], v80 offset:41472\n\t" \
    "v_mfma_f32_16x16x32_bf16 a[100:103], v[76:79], v[36:39], a[100:103]\n\t" \
    "ds_read_b128 v[24:27], v80 offset:45056\n\t" \
    "v_mfma_f32_16x16x32_bf16 a[104:107], v[76:79], v[40:43], a[104:107]\n\t" \
    "ds_read_b128 v[28:31], v80 offset:45568\n\t" \
    "v_mfma_f32_16x16x32_bf16 a[108:111], v[76:79], v[44:47], a[108:111]\n\t" \
    "ds_read_b64_tr_b16 v[68:69], v91 offset:0\n\t" \
    "v_mfma_f32_16x16x32_bf16 a[112:115], v[76:79], v[48:51], a[112:115]\n\t" \
    "ds_read_b64_tr_b16 v[70:71], v91 offset:1024\n\t" \
    "v_mfma_f32_16x16x32_bf16 a[116:119], v[76:79], v[52:55], a[116:119]\n\t" \
    "v_mfma_f32_16x16x32_bf16 a[120:123], v[76:79], v[56:59], a[120:123]\n\t" \
    "v_mfma_f32_16x16x32_bf16 a[124:127], v[76:79], v[60:63], a[124:127]\n\t" \
    "s_waitcnt lgkmcnt(9)\n\t" \
    "v_mfma_f32_16x16x32_bf16 v[128:131], v[64:67], v[0:3], v[128:131]\n\t" \
    "s_waitcnt lgkmcnt(8)\n\t" \
    "v_mfma_f32_16x16x32_bf16 v[132:135], v[64:67], v[4:7], v[132:135]\n\t" \
    "ds_read_b64_tr_b16 v[72:73], v92 offset:0\n\t" \
    "s_waitcnt lgkmcnt(8)\n\t" \
    "v_mfma_f32_16x16x32_bf16 v[136:139], v[64:67], v[8:11], v[136:139]\n\t" \
    "ds_read_b64_tr_b16 v[74:75], v92 offset:1024\n\t" \
    "s_waitcnt lgkmcnt(8)\n\t" \
    "v_mfma_f32_16x16x32_bf16 v[140:143], v[64:67], v[12:15], v[140:143]\n\t" \
    "ds_read_b128 v[32:35], v81 offset:32768\n\t" \
    "s_waitcnt lgkmcnt(8)\n\t" \
    "v_mfma_f32_16x16x32_bf16 v[144:147], v[64:67], v[16:19], v[144:147]\n\t" \
    "ds_read_b128 v[36:39], v81 offset:33280\n\t" \
    "s_waitcnt lgkmcnt(8)\n\t" \
    "v_mfma_f32_16x16x32_bf16 v[148:151], v[64:67], v[20:23], v[148:151]\n\t" \
    "ds_read_b128 v[40:43], v81 offset:36864\n\t" \
    "s_waitcnt lgkmcnt(8)\n\t" \
    "v_mfma_f32_16x16x32_bf16 v[152:155], v[64:67], v[24:27], v[152:155]\n\t" \
    "ds_read_b128 v[44:47], v81 offset:37376\n\t" \
    "s_waitcnt lgkmcnt(8)\n\t" \
    "v_mfma_f32_16x16x32_bf16 v[156:159], v[64:67], v[28:31], v[156:159]\n\t" \
    "s_waitcnt lgkmcnt(6)\n\t" \
    "v_mfma_f32_16x16x32_bf16 v[160:163], v[68:71], v[0:3], v[160:163]\n\t" \
    "v_mfma_f32_16x16x32_bf16 v[164:167], v[68:71], v[4:7], v[164:167]\n\t" \
    "ds_read_b64_tr_b16 v[76:77], v93 offset:0\n\t" \
    "v_mfma_f32_16x16x32_bf16 v[168:171], v[68:71], v[8:11], v[168:171]\n\t" \
    "ds_read_b64_tr_b16 v[78:79], v93 offset:1024\n\t" \
    "v_mfma_f32_16x16x32_bf16 v[172:175], v[68:71], v[12:15], v[172:175]\n\t" \
    "ds_read_b128 v[48:51], v81 offset:40960\n\t" \
    "v_mfma_f32_16x16x32_bf16 v[176:179], v[68:71], v[16:19], v[176:179]\n\t" \
    "ds_read_b128 v[52:55], v81 offset:41472\n\t" \
    "v_mfma_f32_16x16x32_bf16 v[180:183], v[68:71], v[20:23], v[180:183]\n\t" \
    "ds_read_b128 v[56:59], v81 offset:45056\n\t" \
    "v_mfma_f32_16x16x32_bf16 v[184:187], v[68:71], v[24:27], v[184:187]\n\t" \
    "ds_read_b128 v[60:63], v81 offset:45568\n\t" \
    "v_mfma_f32_16x16x32_bf16 v[188:191], v[68:71], v[28:31], v[188:191]\n\t" \
    "s_waitcnt lgkmcnt(10)\n\t" \
    "v_mfma_f32_16x16x32_bf16 v[192:195], v[72:75], v[0:3], v[192:195]\n\t" \
    "v_mfma_f32_16x16x32_bf16 v[196:199], v[72:75], v[4:7], v[196:199]\n\t" \
    "ds_read_b64_tr_b16 v[64:65], v94 offset:0\n\t" \
    "v_mfma_f32_16x16x32_bf16 v[200:203], v[72:75], v[8:11], v[200:203]\n\t" \
    "ds_read_b64_tr_b16 v[66:67], v94 offset:1024\n\t" \
    "v_mfma_f32_16x16x32_bf16 v[204:207], v[72:75], v[12:15], v[204:207]\n\t" \
    "s_mov_b32 s28, 3932160\n\t" \
    "s_add_u32 m0, s26, 49152\n\t" \
    "v_mfma_f32_16x16x32_bf16 v[208:211], v[72:75], v[16:19], v[208:211]\n\t" \
    "buffer_load_dwordx4 v106, s[12:15], s28 offen offset:0 lds\n\t" \
    "v_mfma_f32_16x16x32_bf16 v[212:215], v[72:75], v[20:23], v[212:215]\n\t" \
    "v_mfma_f32_16x16x32_bf16 v[216:219], v[72:75], v[24:27], v[216:219]\n\t" \
    "v_mfma_f32_16x16x32_bf16 v[220:223], v[72:75], v[28:31], v[220:223]\n\t" \
    "s_waitcnt lgkmcnt(6)\n\t" \
    "v_mfma_f32_16x16x32_bf16 v[224:227], v[76:79], v[0:3], v[224:227]\n\t" \
    "v_mfma_f32_16x16x32_bf16 v[228:231], v[76:79], v[4:7], v[228:231]\n\t" \
    "ds_read_b64_tr_b16 v[68:69], v95 offset:0\n\t" \
    "v_mfma_f32_16x16x32_bf16 v[232:235], v[76:79], v[8:11], v[232:235]\n\t" \
    "ds_read_b64_tr_b16 v[70:71], v95 offset:1024\n\t" \
    "v_mfma_f32_16x16x32_bf16 v[236:239], v[76:79], v[12:15], v[236:239]\n\t" \
    "v_mfma_f32_16x16x32_bf16 v[240:243], v[76:79], v[16:19], v[240:243]\n\t" \
    "buffer_load_dwordx4 v107, s[12:15], s28 offen offset:1024 lds\n\t" \
    "v_mfma_f32_16x16x32_bf16 v[244:247], v[76:79], v[20:23], v[244:247]\n\t" \
    "v_mfma_f32_16x16x32_bf16 v[248:251], v[76:79], v[24:27], v[248:251]\n\t" \
    "v_mfma_f32_16x16x32_bf16 v[252:255], v[76:79], v[28:31], v[252:255]\n\t" \
    "s_waitcnt lgkmcnt(2)\n\t" \
    "v_mfma_f32_16x16x32_bf16 a[0:3], v[64:67], v[0:3], a[0:3]\n\t" \
    "v_mfma_f32_16x16x32_bf16 a[4:7], v[64:67], v[4:7], a[4:7]\n\t" \
    "ds_read_b64_tr_b16 v[72:73], v96 offset:0\n\t" \
    "v_mfma_f32_16x16x32_bf16 a[8:11], v[64:67], v[8:11], a[8:11]\n\t" \
    "ds_read_b64_tr_b16 v[74:75], v96 offset:1024\n\t" \
    "v_mfma_f32_16x16x32_bf16 a[12:15], v[64:67], v[12:15], a[12:15]\n\t" \
    "v_mfma_f32_16x16x32_bf16 a[16:19], v[64:67], v[16:19], a[16:19]\n\t" \
    "buffer_load_dwordx4 v108, s[12:15], s28 offen offset:2048 lds\n\t" \
    "v_mfma_f32_16x16x32_bf16 a[20:23], v[64:67], v[20:23], a[20:23]\n\t" \
    "v_mfma_f32_16x16x32_bf16 a[24:27], v[64:67], v[24:27], a[24:27]\n\t" \
    "v_mfma_f32_16x16x32_bf16 a[28:31], v[64:67], v[28:31], a[28:31]\n\t" \
    "s_waitcnt lgkmcnt(2)\n\t" \
    "v_mfma_f32_16x16x32_bf16 a[32:35], v[68:71], v[0:3], a[32:35]\n\t" \
    "v_mfma_f32_16x16x32_bf16 a[36:39], v[68:71], v[4:7], a[36:39]\n\t" \
    "ds_read_b64_tr_b16 v[76:77], v97 offset:0\n\t" \
    "v_mfma_f32_16x16x32_bf16 a[40:43], v[68:71], v[8:11], a[40:43]\n\t" \
    "ds_read_b64_tr_b16 v[78:79], v97 offset:1024\n\t" \
    "v_mfma_f32_16x16x32_bf16 a[44:47], v[68:71], v[12:15], a[44:47]\n\t" \
    "v_mfma_f32_16x16x32_bf16 a[48:51], v[68:71], v[16:19], a[48:51]\n\t" \
    "buffer_load_dwordx4 v109, s[12:15], s28 offen offset:3072 lds\n\t" \
    "v_mfma_f32_16x16x32_bf16 a[52:55], v[68:71], v[20:23], a[52:55]\n\t" \
    "v_mfma_f32_16x16x32_bf16 a[56:59], v[68:71], v[24:27], a[56:59]\n\t" \
    "v_mfma_f32_16x16x32_bf16 a[60:63], v[68:71], v[28:31], a[60:63]\n\t" \
    "s_waitcnt vmcnt(32) lgkmcnt(0)\n\t" \
    "s_barrier\n\t" \
    "v_mfma_f32_16x16x32_bf16 a[64:67], v[72:75], v[0:3], a[64:67]\n\t" \
    "v_mfma_f32_16x16x32_bf16 a[68:71], v[72:75], v[4:7], a[68:71]\n\t" \
    "ds_read_b64_tr_b16 v[64:65], v90 offset:16384\n\t" \
    "v_mfma_f32_16x16x32_bf16 a[72:75], v[72:75], v[8:11], a[72:75]\n\t" \
    "ds_read_b64_tr_b16 v[66:67], v90 offset:17408\n\t" \
    "v_mfma_f32_16x16x32_bf16 a[76:79], v[72:75], v[12:15], a[76:79]\n\t" \
    "v_mfma_f32_16x16x32_bf16 a[80:83], v[72:75], v[16:19], a[80:83]\n\t" \
    "v_mfma_f32_16x16x32_bf16 a[84:87], v[72:75], v[20:23], a[84:87]\n\t" \
    "v_mfma_f32_16x16x32_bf16 a[88:91], v[72:75], v[24:27], a[88:91]\n\t" \
    "v_mfma_f32_16x16x32_bf16 a[92:95], v[72:75], v[28:31], a[92:95]\n\t" \
    "v_mfma_f32_16x16x32_bf16 a[96:99], v[76:79], v[0:3], a[96:99]\n\t" \
    "v_mfma_f32_16x16x32_bf16 a[100:103], v[76:79], v[4:7], a[100:103]\n\t" \
    "ds_read_b64_tr_b16 v[68:69], v91 offset:16384\n\t" \
    "v_mfma_f32_16x16x32_bf16 a[104:107], v[76:79], v[8:11], a[104:107]\n\t" \
    "ds_read_b64_tr_b16 v[70:71], v91 offset:17408\n\t" \
    "v_mfma_f32_16x16x32_bf16 a[108:111], v[76:79], v[12:15], a[108:111]\n\t" \
    "v_mfma_f32_16x16x32_bf16 a[112:115], v[76:79], v[16:19], a[112:115]\n\t" \
    "v_mfma_f32_16x16x32_bf16 a[116:119], v[76:79], v[20:23], a[116:119]\n\t" \
    "v_mfma_f32_16x16x32_bf16 a[120:123], v[76:79], v[24:27], a[120:123]\n\t" \
    "v_mfma_f32_16x16x32_bf16 a[124:127], v[76:79], v[28:31], a[124:127]\n\t" \
    "s_waitcnt lgkmcnt(2)\n\t" \
    "v_mfma_f32_16x16x32_bf16 v[128:131], v[64:67], v[32:35], v[128:131]\n\t" \
    "v_mfma_f32_16x16x32_bf16 v[132:135], v[64:67], v[36:39], v[132:135]\n\t" \
    "ds_read_b64_tr_b16 v[72:73], v92 offset:16384\n\t" \
    "v_mfma_f32_16x16x32_bf16 v[136:139], v[64:67], v[40:43], v[136:139]\n\t" \
    "ds_read_b64_tr_b16 v[74:75], v92 offset:17408\n\t" \
    "s_lshr_b32 s23, s20, 1\n\t" \
    "v_mfma_f32_16x16x32_bf16 v[140:143], v[64:67], v[44:47], v[140:143]\n\t" \
    "s_lshl_b32 s23, s23, 18\n\t" \
    "s_mov_b32 s27, 896\n\t" \
    "s_add_u32 m0, s25, 32768\n\t" \
    "v_mfma_f32_16x16x32_bf16 v[144:147], v[64:67], v[48:51], v[144:147]\n\t" \
    "buffer_load_dwordx4 v98, s[8:11], s27 offen offset:0 lds\n\t" \
    "s_add_u32 s12, %[qlo], s23\n\t" \
    "v_mfma_f32_16x16x32_bf16 v[148:151], v[64:67], v[52:55], v[148:151]\n\t" \
    "s_addc_u32 s13, %[qhi], 0\n\t" \
    "v_mfma_f32_16x16x32_bf16 v[152:155], v[64:67], v[56:59], v[152:155]\n\t" \
    "buffer_load_dwordx4 v99, s[8:11], s27 offen offset:1024 lds\n\t" \
    "v_mfma_f32_16x16x32_bf16 v[156:159], v[64:67], v[60:63], v[156:159]\n\t" \
    "s_waitcnt lgkmcnt(2)\n\t" \
    "v_mfma_f32_16x16x32_bf16 v[160:163], v[68:71], v[32:35], v[160:163]\n\t" \
    "v_mfma_f32_16x16x32_bf16 v[164:167], v[68:71], v[36:39], v[164:167]\n\t" \
    "ds_read_b64_tr_b16 v[76:77], v93 offset:16384\n\t" \
    "v_mfma_f32_16x16x32_bf16 v[168:171], v[68:71], v[40:43], v[168:171]\n\t" \
    "ds_read_b64_tr_b16 v[78:79], v93 offset:17408\n\t" \
    "v_mfma_f32_16x16x32_bf16 v[172:175], v[68:71], v[44:47], v[172:175]\n\t" \
    "v_mfma_f32_16x16x32_bf16 v[176:179], v[68:71], v[48:51], v[176:179]\n\t" \
    "buffer_load_dwordx4 v100, s[8:11], s27 offen offset:2048 lds\n\t" \
    "v_mfma_f32_16x16x32_bf16 v[180:183], v[68:71], v[52:55], v[180:183]\n\t" \
    "v_mfma_f32_16x16x32_bf16 v[184:187], v[68:71], v[56:59], v[184:187]\n\t" \
    "buffer_load_dwordx4 v101, s[8:11], s27 offen offset:3072 lds\n\t" \
    "v_mfma_f32_16x16x32_bf16 v[188:191], v[68:71], v[60:63], v[188:191]\n\t" \
    "s_waitcnt lgkmcnt(2)\n\t" \
    "v_mfma_f32_16x16x32_bf16 v[192:195], v[72:75], v[32:35], v[192:195]\n\t" \
    "v_mfma_f32_16x16x32_bf16 v[196:199], v[72:75], v[36:39], v[196:199]\n\t" \
    "ds_read_b64_tr_b16 v[64:65], v94 offset:16384\n\t" \
    "v_mfma_f32_16x16x32_bf16 v[200:203], v[72:75], v[40:43], v[200:203]\n\t" \
    "ds_read_b64_tr_b16 v[66:67], v94 offset:17408\n\t" \
    "s_add_u32 m0, s25, 36864\n\t" \
    "v_mfma_f32_16x16x32_bf16 v[204:207], v[72:75], v[44:47], v[204:207]\n\t" \
    "buffer_load_dwordx4 v102, s[8:11], s27 offen offset:0 lds\n\t" \
    "v_mfma_f32_16x16x32_bf16 v[208:211], v[72:75], v[48:51], v[208:211]\n\t" \
    "v_mfma_f32_16x16x32_bf16 v[212:215], v[72:75], v[52:55], v[212:215]\n\t" \
    "buffer_load_dwordx4 v103, s[8:11], s27 offen offset:1024 lds\n\t" \
    "v_mfma_f32_16x16x32_bf16 v[216:219], v[72:75], v[56:59], v[216:219]\n\t" \
    "v_mfma_f32_16x16x32_bf16 v[220:223], v[72:75], v[60:63], v[220:223]\n\t" \
    "s_waitcnt lgkmcnt(2)\n\t" \
    "v_mfma_f32_16x16x32_bf16 v[224:227], v[76:79], v[32:35], v[224:227]\n\t" \
    "v_mfma_f32_16x16x32_bf16 v[228:231], v[76:79], v[36:39], v[228:231]\n\t" \
    "ds_read_b64_tr_b16 v[68:69], v95 offset:16384\n\t" \
    "v_mfma_f32_16x16x32_bf16 v[232:235], v[76:79], v[40:43], v[232:235]\n\t" \
    "ds_read_b64_tr_b16 v[70:71], v95 offset:17408\n\t" \
    "v_mfma_f32_16x16x32_bf16 v[236:239], v[76:79], v[44:47], v[236:239]\n\t" \
    "buffer_load_dwordx4 v104, s[8:11], s27 offen offset:2048 lds\n\t" \
    "v_mfma_f32_16x16x32_bf16 v[240:243], v[76:79], v[48:51], v[240:243]\n\t" \
    "v_mfma_f32_16x16x32_bf16 v[244:247], v[76:79], v[52:55], v[244:247]\n\t" \
    "buffer_load_dwordx4 v105, s[8:11], s27 offen offset:3072 lds\n\t" \
    "v_mfma_f32_16x16x32_bf16 v[248:251], v[76:79], v[56:59], v[248:251]\n\t" \
    "v_mfma_f32_16x16x32_bf16 v[252:255], v[76:79], v[60:63], v[252:255]\n\t" \
    "s_waitcnt lgkmcnt(2)\n\t" \
    "v_mfma_f32_16x16x32_bf16 a[0:3], v[64:67], v[32:35], a[0:3]\n\t" \
    "v_mfma_f32_16x16x32_bf16 a[4:7], v[64:67], v[36:39], a[4:7]\n\t" \
    "ds_read_b64_tr_b16 v[72:73], v96 offset:16384\n\t" \
    "v_mfma_f32_16x16x32_bf16 a[8:11], v[64:67], v[40:43], a[8:11]\n\t" \
    "ds_read_b64_tr_b16 v[74:75], v96 offset:17408\n\t" \
    "s_mov_b32 s28, 0\n\t" \
    "s_add_u32 m0, s26, 65536\n\t" \
    "v_mfma_f32_16x16x32_bf16 a[12:15], v[64:67], v[44:47], a[12:15]\n\t" \
    "buffer_load_dwordx4 v106, s[12:15], s28 offen offset:0 lds\n\t" \
    "v_mfma_f32_16x16x32_bf16 a[16:19], v[64:67], v[48:51], a[16:19]\n\t" \
    "v_mfma_f32_16x16x32_bf16 a[20:23], v[64:67], v[52:55], a[20:23]\n\t" \
    "buffer_load_dwordx4 v107, s[12:15], s28 offen offset:1024 lds\n\t" \
    "v_mfma_f32_16x16x32_bf16 a[24:27], v[64:67], v[56:59], a[24:27]\n\t" \
    "v_mfma_f32_16x16x32_bf16 a[28:31], v[64:67], v[60:63], a[28:31]\n\t" \
    "s_waitcnt lgkmcnt(2)\n\t" \
    "v_mfma_f32_16x16x32_bf16 a[32:35], v[68:71], v[32:35], a[32:35]\n\t" \
    "v_mfma_f32_16x16x32_bf16 a[36:39], v[68:71], v[36:39], a[36:39]\n\t" \
    "ds_read_b64_tr_b16 v[76:77], v97 offset:16384\n\t" \
    "v_mfma_f32_16x16x32_bf16 a[40:43], v[68:71], v[40:43], a[40:43]\n\t" \
    "ds_read_b64_tr_b16 v[78:79], v97 offset:17408\n\t" \
    "v_mfma_f32_16x16x32_bf16 a[44:47], v[68:71], v[44:47], a[44:47]\n\t" \
    "buffer_load_dwordx4 v108, s[12:15], s28 offen offset:2048 lds\n\t" \
    "v_mfma_f32_16x16x32_bf16 a[48:51], v[68:71], v[48:51], a[48:51]\n\t" \
    "v_mfma_f32_16x16x32_bf16 a[52:55], v[68:71], v[52:55], a[52:55]\n\t" \
    "buffer_load_dwordx4 v109, s[12:15], s28 offen offset:3072 lds\n\t" \
    "v_mfma_f32_16x16x32_bf16 a[56:59], v[68:71], v[56:59], a[56:59]\n\t" \
    "v_mfma_f32_16x16x32_bf16 a[60:63], v[68:71], v[60:63], a[60:63]\n\t" \
    "s_waitcnt vmcnt(20) lgkmcnt(0)\n\t" \
    "s_barrier\n\t" \
    "v_mfma_f32_16x16x32_bf16 a[64:67], v[72:75], v[32:35], a[64:67]\n\t" \
    "v_mfma_f32_16x16x32_bf16 a[68:71], v[72:75], v[36:39], a[68:71]\n\t" \
    "ds_read_b64_tr_b16 v[64:65], v82 offset:0\n\t" \
    "v_mfma_f32_16x16x32_bf16 a[72:75], v[72:75], v[40:43], a[72:75]\n\t" \
    "ds_read_b64_tr_b16 v[66:67], v82 offset:1024\n\t" \
    "v_mfma_f32_16x16x32_bf16 a[76:79], v[72:75], v[44:47], a[76:79]\n\t" \
    "ds_read_b128 v[0:3], v80 offset:0\n\t" \
    "v_mfma_f32_16x16x32_bf16 a[80:83], v[72:75], v[48:51], a[80:83]\n\t" \
    "ds_read_b128 v[4:7], v80 offset:512\n\t" \
    "v_mfma_f32_16x16x32_bf16 a[84:87], v[72:75], v[52:55], a[84:87]\n\t" \
    "ds_read_b128 v[8:11], v80 offset:4096\n\t" \
    "v_mfma_f32_16x16x32_bf16 a[88:91], v[72:75], v[56:59], a[88:91]\n\t" \
    "ds_read_b128 v[12:15], v80 offset:4608\n\t" \
    "v_mfma_f32_16x16x32_bf16 a[92:95], v[72:75], v[60:63], a[92:95]\n\t" \
    "ds_read_b128 v[16:19], v80 offset:8192\n\t" \
    "v_mfma_f32_16x16x32_bf16 a[96:99], v[76:79], v[32:35], a[96:99]\n\t" \
    "ds_read_b128 v[20:23], v80 offset:8704\n\t" \
    "v_mfma_f32_16x16x32_bf16 a[100:103], v[76:79], v[36:39], a[100:103]\n\t" \
    "ds_read_b128 v[24:27], v80 offset:12288\n\t" \
    "v_mfma_f32_16x16x32_bf16 a[104:107], v[76:79], v[40:43], a[104:107]\n\t" \
    "ds_read_b128 v[28:31], v80 offset:12800\n\t" \
    "v_mfma_f32_16x16x32_bf16 a[108:111], v[76:79], v[44:47], a[108:111]\n\t" \
    "ds_read_b64_tr_b16 v[68:69], v83 offset:0\n\t" \
    "v_mfma_f32_16x16x32_bf16 a[112:115], v[76:79], v[48:51], a[112:115]\n\t" \
    "ds_read_b64_tr_b16 v[70:71], v83 offset:1024\n\t" \
    "v_mfma_f32_16x16x32_bf16 a[116:119], v[76:79], v[52:55], a[116:119]\n\t" \
    "v_mfma_f32_16x16x32_bf16 a[120:123], v[76:79], v[56:59], a[120:123]\n\t" \
    "v_mfma_f32_16x16x32_bf16 a[124:127], v[76:79], v[60:63], a[124:127]\n\t" \
    "s_waitcnt lgkmcnt(9)\n\t" \
    "v_mfma_f32_16x16x32_bf16 v[128:131], v[64:67], v[0:3], v[128:131]\n\t" \
    "s_waitcnt lgkmcnt(8)\n\t" \
    "v_mfma_f32_16x16x32_bf16 v[132:135], v[64:67], v[4:7], v[132:135]\n\t" \
    "ds_read_b64_tr_b16 v[72:73], v84 offset:0\n\t" \
    "s_waitcnt lgkmcnt(8)\n\t" \
    "v_mfma_f32_16x16x32_bf16 v[136:139], v[64:67], v[8:11], v[136:139]\n\t" \
    "ds_read_b64_tr_b16 v[74:75], v84 offset:1024\n\t" \
    "s_waitcnt lgkmcnt(8)\n\t" \
    "v_mfma_f32_16x16x32_bf16 v[140:143], v[64:67], v[12:15], v[140:143]\n\t" \
    "ds_read_b128 v[32:35], v81 offset:0\n\t" \
    "s_waitcnt lgkmcnt(8)\n\t" \
    "v_mfma_f32_16x16x32_bf16 v[144:147], v[64:67], v[16:19], v[144:147]\n\t" \
    "ds_read_b128 v[36:39], v81 offset:512\n\t" \
    "s_waitcnt lgkmcnt(8)\n\t" \
    "v_mfma_f32_16x16x32_bf16 v[148:151], v[64:67], v[20:23], v[148:151]\n\t" \
    "ds_read_b128 v[40:43], v81 offset:4096\n\t" \
    "s_waitcnt lgkmcnt(8)\n\t" \
    "v_mfma_f32_16x16x32_bf16 v[152:155], v[64:67], v[24:27], v[152:155]\n\t" \
    "ds_read_b128 v[44:47], v81 offset:4608\n\t" \
    "s_waitcnt lgkmcnt(8)\n\t" \
    "v_mfma_f32_16x16x32_bf16 v[156:159], v[64:67], v[28:31], v[156:159]\n\t" \
    "s_waitcnt lgkmcnt(6)\n\t" \
    "v_mfma_f32_16x16x32_bf16 v[160:163], v[68:71], v[0:3], v[160:163]\n\t" \
    "v_mfma_f32_16x16x32_bf16 v[164:167], v[68:71], v[4:7], v[164:167]\n\t" \
    "ds_read_b64_tr_b16 v[76:77], v85 offset:0\n\t" \
    "v_mfma_f32_16x16x32_bf16 v[168:171], v[68:71], v[8:11], v[168:171]\n\t" \
    "ds_read_b64_tr_b16 v[78:79], v85 offset:1024\n\t" \
    "buffer_load_dword v111, off, s[16:19], s29 sc1\n\t" \
    "v_mfma_f32_16x16x32_bf16 v[172:175], v[68:71], v[12:15], v[172:175]\n\t" \
    "ds_read_b128 v[48:51], v81 offset:8192\n\t" \
    "v_mfma_f32_16x16x32_bf16 v[176:179], v[68:71], v[16:19], v[176:179]\n\t" \
    "ds_read_b128 v[52:55], v81 offset:8704\n\t" \
    "v_mfma_f32_16x16x32_bf16 v[180:183], v[68:71], v[20:23], v[180:183]\n\t" \
    "ds_read_b128 v[56:59], v81 offset:12288\n\t" \
    "v_mfma_f32_16x16x32_bf16 v[184:187], v[68:71], v[24:27], v[184:187]\n\t" \
    "ds_read_b128 v[60:63], v81 offset:12800\n\t" \
    "v_mfma_f32_16x16x32_bf16 v[188:191], v[68:71], v[28:31], v[188:191]\n\t" \
    "s_waitcnt lgkmcnt(10)\n\t" \
    "v_mfma_f32_16x16x32_bf16 v[192:195], v[72:75], v[0:3], v[192:195]\n\t" \
    "v_mfma_f32_16x16x32_bf16 v[196:199], v[72:75], v[4:7], v[196:199]\n\t" \
    "ds_read_b64_tr_b16 v[64:65], v86 offset:0\n\t" \
    "v_mfma_f32_16x16x32_bf16 v[200:203], v[72:75], v[8:11], v[200:203]\n\t" \
    "ds_read_b64_tr_b16 v[66:67], v86 offset:1024\n\t" \
    "v_mfma_f32_16x16x32_bf16 v[204:207], v[72:75], v[12:15], v[204:207]\n\t" \
    "s_mov_b32 s28, 524288\n\t" \
    "s_add_u32 m0, s26, 81920\n\t" \
    "v_mfma_f32_16x16x32_bf16 v[208:211], v[72:75], v[16:19], v[208:211]\n\t" \
    "buffer_load_dwordx4 v106, s[12:15], s28 offen offset:0 lds\n\t" \
    "v_mfma_f32_16x16x32_bf16 v[212:215], v[72:75], v[20:23], v[212:215]\n\t" \
    "v_mfma_f32_16x16x32_bf16 v[216:219], v[72:75], v[24:27], v[216:219]\n\t" \
    "v_mfma_f32_16x16x32_bf16 v[220:223], v[72:75], v[28:31], v[220:223]\n\t" \
    "s_waitcnt lgkmcnt(6)\n\t" \
    "v_mfma_f32_16x16x32_bf16 v[224:227], v[76:79], v[0:3], v[224:227]\n\t" \
    "v_mfma_f32_16x16x32_bf16 v[228:231], v[76:79], v[4:7], v[228:231]\n\t" \
    "ds_read_b64_tr_b16 v[68:69], v87 offset:0\n\t" \
    "v_mfma_f32_16x16x32_bf16 v[232:235], v[76:79], v[8:11], v[232:235]\n\t" \
    "ds_read_b64_tr_b16 v[70:71], v87 offset:1024\n\t" \
    "v_mfma_f32_16x16x32_bf16 v[236:239], v[76:79], v[12:15], v[236:239]\n\t" \
    "v_mfma_f32_16x16x32_bf16 v[240:243], v[76:79], v[16:19], v[240:243]\n\t" \
    "buffer_load_dwordx4 v107, s[12:15], s28 offen offset:1024 lds\n\t" \
    "v_mfma_f32_16x16x32_bf16 v[244:247], v[76:79], v[20:23], v[244:247]\n\t" \
    "v_mfma_f32_16x16x32_bf16 v[248:251], v[76:79], v[24:27], v[248:251]\n\t" \
    "v_mfma_f32_16x16x32_bf16 v[252:255], v[76:79], v[28:31], v[252:255]\n\t" \
    "s_waitcnt lgkmcnt(2)\n\t" \
    "v_mfma_f32_16x16x32_bf16 a[0:3], v[64:67], v[0:3], a[0:3]\n\t" \
    "v_mfma_f32_16x16x32_bf16 a[4:7], v[64:67], v[4:7], a[4:7]\n\t" \
    "ds_read_b64_tr_b16 v[72:73], v88 offset:0\n\t" \
    "v_mfma_f32_16x16x32_bf16 a[8:11], v[64:67], v[8:11], a[8:11]\n\t" \
    "ds_read_b64_tr_b16 v[74:75], v88 offset:1024\n\t" \
    "v_mfma_f32_16x16x32_bf16 a[12:15], v[64:67], v[12:15], a[12:15]\n\t" \
    "v_mfma_f32_16x16x32_bf16 a[16:19], v[64:67], v[16:19], a[16:19]\n\t" \
    "buffer_load_dwordx4 v108, s[12:15], s28 offen offset:2048 lds\n\t" \
    "v_mfma_f32_16x16x32_bf16 a[20:23], v[64:67], v[20:23], a[20:23]\n\t" \
    "v_mfma_f32_16x16x32_bf16 a[24:27], v[64:67], v[24:27], a[24:27]\n\t" \
    "v_mfma_f32_16x16x32_bf16 a[28:31], v[64:67], v[28:31], a[28:31]\n\t" \
    "s_waitcnt lgkmcnt(2)\n\t" \
    "v_mfma_f32_16x16x32_bf16 a[32:35], v[68:71], v[0:3], a[32:35]\n\t" \
    "v_mfma_f32_16x16x32_bf16 a[36:39], v[68:71], v[4:7], a[36:39]\n\t" \
    "ds_read_b64_tr_b16 v[76:77], v89 offset:0\n\t" \
    "v_mfma_f32_16x16x32_bf16 a[40:43], v[68:71], v[8:11], a[40:43]\n\t" \
    "ds_read_b64_tr_b16 v[78:79], v89 offset:1024\n\t" \
    "v_mfma_f32_16x16x32_bf16 a[44:47], v[68:71], v[12:15], a[44:47]\n\t" \
    "v_mfma_f32_16x16x32_bf16 a[48:51], v[68:71], v[16:19], a[48:51]\n\t" \
    "buffer_load_dwordx4 v109, s[12:15], s28 offen offset:3072 lds\n\t" \
    "v_mfma_f32_16x16x32_bf16 a[52:55], v[68:71], v[20:23], a[52:55]\n\t" \
    "v_mfma_f32_16x16x32_bf16 a[56:59], v[68:71], v[24:27], a[56:59]\n\t" \
    "v_mfma_f32_16x16x32_bf16 a[60:63], v[68:71], v[28:31], a[60:63]\n\t" \
    "s_waitcnt vmcnt(33) lgkmcnt(0)\n\t" \
    "s_barrier\n\t" \
    "v_mfma_f32_16x16x32_bf16 a[64:67], v[72:75], v[0:3], a[64:67]\n\t" \
    "v_mfma_f32_16x16x32_bf16 a[68:71], v[72:75], v[4:7], a[68:71]\n\t" \
    "ds_read_b64_tr_b16 v[64:65], v82 offset:16384\n\t" \
    "v_mfma_f32_16x16x32_bf16 a[72:75], v[72:75], v[8:11], a[72:75]\n\t" \
    "ds_read_b64_tr_b16 v[66:67], v82 offset:17408\n\t" \
    "v_mfma_f32_16x16x32_bf16 a[76:79], v[72:75], v[12:15], a[76:79]\n\t" \
    "v_mfma_f32_16x16x32_bf16 a[80:83], v[72:75], v[16:19], a[80:83]\n\t" \
    "v_mfma_f32_16x16x32_bf16 a[84:87], v[72:75], v[20:23], a[84:87]\n\t" \
    "v_mfma_f32_16x16x32_bf16 a[88:91], v[72:75], v[24:27], a[88:91]\n\t" \
    "v_mfma_f32_16x16x32_bf16 a[92:95], v[72:75], v[28:31], a[92:95]\n\t" \
    "v_mfma_f32_16x16x32_bf16 a[96:99], v[76:79], v[0:3], a[96:99]\n\t" \
    "v_mfma_f32_16x16x32_bf16 a[100:103], v[76:79], v[4:7], a[100:103]\n\t" \
    "ds_read_b64_tr_b16 v[68:69], v83 offset:16384\n\t" \
    "v_mfma_f32_16x16x32_bf16 a[104:107], v[76:79], v[8:11], a[104:107]\n\t" \
    "ds_read_b64_tr_b16 v[70:71], v83 offset:17408\n\t" \
    "v_mfma_f32_16x16x32_bf16 a[108:111], v[76:79], v[12:15], a[108:111]\n\t" \
    "v_mfma_f32_16x16x32_bf16 a[112:115], v[76:79], v[16:19], a[112:115]\n\t" \
    "v_mfma_f32_16x16x32_bf16 a[116:119], v[76:79], v[20:23], a[116:119]\n\t" \
    "v_mfma_f32_16x16x32_bf16 a[120:123], v[76:79], v[24:27], a[120:123]\n\t" \
    "v_mfma_f32_16x16x32_bf16 a[124:127], v[76:79], v[28:31], a[124:127]\n\t" \
    "s_waitcnt lgkmcnt(2)\n\t" \
    "v_mfma_f32_16x16x32_bf16 v[128:131], v[64:67], v[32:35], v[128:131]\n\t" \
    "v_mfma_f32_16x16x32_bf16 v[132:135], v[64:67], v[36:39], v[132:135]\n\t" \
    "ds_read_b64_tr_b16 v[72:73], v84 offset:16384\n\t" \
    "v_mfma_f32_16x16x32_bf16 v[136:139], v[64:67], v[40:43], v[136:139]\n\t" \
    "ds_read_b64_tr_b16 v[74:75], v84 offset:17408\n\t" \
    "v_mfma_f32_16x16x32_bf16 v[140:143], v[64:67], v[44:47], v[140:143]\n\t" \
    "v_mfma_f32_16x16x32_bf16 v[144:147], v[64:67], v[48:51], v[144:147]\n\t" \
    "v_mfma_f32_16x16x32_bf16 v[148:151], v[64:67], v[52:55], v[148:151]\n\t" \
    "v_mfma_f32_16x16x32_bf16 v[152:155], v[64:67], v[56:59], v[152:155]\n\t" \
    "v_mfma_f32_16x16x32_bf16 v[156:159], v[64:67], v[60:63], v[156:159]\n\t" \
    "s_waitcnt lgkmcnt(2)\n\t" \
    "v_mfma_f32_16x16x32_bf16 v[160:163], v[68:71], v[32:35], v[160:163]\n\t" \
    "v_mfma_f32_16x16x32_bf16 v[164:167], v[68:71], v[36:39], v[164:167]\n\t" \
    "ds_read_b64_tr_b16 v[76:77], v85 offset:16384\n\t" \
    "v_mfma_f32_16x16x32_bf16 v[168:171], v[68:71], v[40:43], v[168:171]\n\t" \
    "ds_read_b64_tr_b16 v[78:79], v85 offset:17408\n\t" \
    "v_mfma_f32_16x16x32_bf16 v[172:175], v[68:71], v[44:47], v[172:175]\n\t" \
    "v_mfma_f32_16x16x32_bf16 v[176:179], v[68:71], v[48:51], v[176:179]\n\t" \
    "v_mfma_f32_16x16x32_bf16 v[180:183], v[68:71], v[52:55], v[180:183]\n\t" \
    "v_mfma_f32_16x16x32_bf16 v[184:187], v[68:71], v[56:59], v[184:187]\n\t" \
    "v_mfma_f32_16x16x32_bf16 v[188:191], v[68:71], v[60:63], v[188:191]\n\t" \
    "s_waitcnt lgkmcnt(2)\n\t" \
    "v_mfma_f32_16x16x32_bf16 v[192:195], v[72:75], v[32:35], v[192:195]\n\t" \
    "v_mfma_f32_16x16x32_bf16 v[196:199], v[72:75], v[36:39], v[196:199]\n\t" \
    "ds_read_b64_tr_b16 v[64:65], v86 offset:16384\n\t" \
    "v_mfma_f32_16x16x32_bf16 v[200:203], v[72:75], v[40:43], v[200:203]\n\t" \
    "ds_read_b64_tr_b16 v[66:67], v86 offset:17408\n\t" \
    "v_mfma_f32_16x16x32_bf16 v[204:207], v[72:75], v[44:47], v[204:207]\n\t" \
    "v_mfma_f32_16x16x32_bf16 v[208:211], v[72:75], v[48:51], v[208:211]\n\t" \
    "v_mfma_f32_16x16x32_bf16 v[212:215], v[72:75], v[52:55], v[212:215]\n\t" \
    "v_mfma_f32_16x16x32_bf16 v[216:219], v[72:75], v[56:59], v[216:219]\n\t" \
    "v_mfma_f32_16x16x32_bf16 v[220:223], v[72:75], v[60:63], v[220:223]\n\t" \
    "s_waitcnt lgkmcnt(2)\n\t" \
    "v_mfma_f32_16x16x32_bf16 v[224:227], v[76:79], v[32:35], v[224:227]\n\t" \
    "v_mfma_f32_16x16x32_bf16 v[228:231], v[76:79], v[36:39], v[228:231]\n\t" \
    "ds_read_b64_tr_b16 v[68:69], v87 offset:16384\n\t" \
    "v_mfma_f32_16x16x32_bf16 v[232:235], v[76:79], v[40:43], v[232:235]\n\t" \
    "ds_read_b64_tr_b16 v[70:71], v87 offset:17408\n\t" \
    "v_mfma_f32_16x16x32_bf16 v[236:239], v[76:79], v[44:47], v[236:239]\n\t" \
    "v_mfma_f32_16x16x32_bf16 v[240:243], v[76:79], v[48:51], v[240:243]\n\t" \
    "v_mfma_f32_16x16x32_bf16 v[244:247], v[76:79], v[52:55], v[244:247]\n\t" \
    "v_mfma_f32_16x16x32_bf16 v[248:251], v[76:79], v[56:59], v[248:251]\n\t" \
    "v_mfma_f32_16x16x32_bf16 v[252:255], v[76:79], v[60:63], v[252:255]\n\t" \
    "s_waitcnt lgkmcnt(2)\n\t" \
    "v_mfma_f32_16x16x32_bf16 a[0:3], v[64:67], v[32:35], a[0:3]\n\t" \
    "v_mfma_f32_16x16x32_bf16 a[4:7], v[64:67], v[36:39], a[4:7]\n\t" \
    "ds_read_b64_tr_b16 v[72:73], v88 offset:16384\n\t" \
    "v_mfma_f32_16x16x32_bf16 a[8:11], v[64:67], v[40:43], a[8:11]\n\t" \
    "ds_read_b64_tr_b16 v[74:75], v88 offset:17408\n\t" \
    "s_mov_b32 s28, 1048576\n\t" \
    "s_add_u32 m0, s26, 0\n\t" \
    "v_mfma_f32_16x16x32_bf16 a[12:15], v[64:67], v[44:47], a[12:15]\n\t" \
    "buffer_load_dwordx4 v106, s[12:15], s28 offen offset:0 lds\n\t" \
    "v_mfma_f32_16x16x32_bf16 a[16:19], v[64:67], v[48:51], a[16:19]\n\t" \
    "v_mfma_f32_16x16x32_bf16 a[20:23], v[64:67], v[52:55], a[20:23]\n\t" \
    "buffer_load_dwordx4 v107, s[12:15], s28 offen offset:1024 lds\n\t" \
    "v_mfma_f32_16x16x32_bf16 a[24:27], v[64:67], v[56:59], a[24:27]\n\t" \
    "v_mfma_f32_16x16x32_bf16 a[28:31], v[64:67], v[60:63], a[28:31]\n\t" \
    "s_waitcnt lgkmcnt(2)\n\t" \
    "v_mfma_f32_16x16x32_bf16 a[32:35], v[68:71], v[32:35], a[32:35]\n\t" \
    "v_mfma_f32_16x16x32_bf16 a[36:39], v[68:71], v[36:39], a[36:39]\n\t" \
    "ds_read_b64_tr_b16 v[76:77], v89 offset:16384\n\t" \
    "v_mfma_f32_16x16x32_bf16 a[40:43], v[68:71], v[40:43], a[40:43]\n\t" \
    "ds_read_b64_tr_b16 v[78:79], v89 offset:17408\n\t" \
    "v_mfma_f32_16x16x32_bf16 a[44:47], v[68:71], v[44:47], a[44:47]\n\t" \
    "buffer_load_dwordx4 v108, s[12:15], s28 offen offset:2048 lds\n\t" \
    "v_mfma_f32_16x16x32_bf16 a[48:51], v[68:71], v[48:51], a[48:51]\n\t" \
    "v_mfma_f32_16x16x32_bf16 a[52:55], v[68:71], v[52:55], a[52:55]\n\t" \
    "buffer_load_dwordx4 v109, s[12:15], s28 offen offset:3072 lds\n\t" \
    "v_mfma_f32_16x16x32_bf16 a[56:59], v[68:71], v[56:59], a[56:59]\n\t" \
    "v_mfma_f32_16x16x32_bf16 a[60:63], v[68:71], v[60:63], a[60:63]\n\t" \
    "s_waitcnt vmcnt(13) lgkmcnt(0)\n\t" \
    "s_barrier\n\t" \
    "v_mfma_f32_16x16x32_bf16 a[64:67], v[72:75], v[32:35], a[64:67]\n\t" \
    "v_mfma_f32_16x16x32_bf16 a[68:71], v[72:75], v[36:39], a[68:71]\n\t" \
    "ds_read_b64_tr_b16 v[64:65], v82 offset:32768\n\t" \
    "v_mfma_f32_16x16x32_bf16 a[72:75], v[72:75], v[40:43], a[72:75]\n\t" \
    "ds_read_b64_tr_b16 v[66:67], v82 offset:33792\n\t" \
    "v_mfma_f32_16x16x32_bf16 a[76:79], v[72:75], v[44:47], a[76:79]\n\t" \
    "ds_read_b128 v[0:3], v80 offset:32768\n\t" \
    "v_mfma_f32_16x16x32_bf16 a[80:83], v[72:75], v[48:51], a[80:83]\n\t" \
    "ds_read_b128 v[4:7], v80 offset:33280\n\t" \
    "v_mfma_f32_16x16x32_bf16 a[84:87], v[72:75], v[52:55], a[84:87]\n\t" \
    "ds_read_b128 v[8:11], v80 offset:36864\n\t" \
    "v_mfma_f32_16x16x32_bf16 a[88:91], v[72:75], v[56:59], a[88:91]\n\t" \
    "ds_read_b128 v[12:15], v80 offset:37376\n\t" \
    "v_mfma_f32_16x16x32_bf16 a[92:95], v[72:75], v[60:63], a[92:95]\n\t" \
    "ds_read_b128 v[16:19], v80 offset:40960\n\t" \
    "v_mfma_f32_16x16x32_bf16 a[96:99], v[76:79], v[32:35], a[96:99]\n\t" \
    "ds_read_b128 v[20:23], v80 offset:41472\n\t" \
    "v_mfma_f32_16x16x32_bf16 a[100:103], v[76:79], v[36:39], a[100:103]\n\t" \
    "ds_read_b128 v[24:27], v80 offset:45056\n\t" \
    "v_mfma_f32_16x16x32_bf16 a[104:107], v[76:79], v[40:43], a[104:107]\n\t" \
    "ds_read_b128 v[28:31], v80 offset:45568\n\t" \
    "v_mfma_f32_16x16x32_bf16 a[108:111], v[76:79], v[44:47], a[108:111]\n\t" \
    "ds_read_b64_tr_b16 v[68:69], v83 offset:32768\n\t" \
    "v_mfma_f32_16x16x32_bf16 a[112:115], v[76:79], v[48:51], a[112:115]\n\t" \
    "ds_read_b64_tr_b16 v[70:71], v83 offset:33792\n\t" \
    "v_mfma_f32_16x16x32_bf16 a[116:119], v[76:79], v[52:55], a[116:119]\n\t" \
    "v_mfma_f32_16x16x32_bf16 a[120:123], v[76:79], v[56:59], a[120:123]\n\t" \
    "v_mfma_f32_16x16x32_bf16 a[124:127], v[76:79], v[60:63], a[124:127]\n\t" \
    "s_waitcnt lgkmcnt(9)\n\t" \
    "v_mfma_f32_16x16x32_bf16 v[128:131], v[64:67], v[0:3], v[128:131]\n\t" \
    "s_waitcnt lgkmcnt(8)\n\t" \
    "v_mfma_f32_16x16x32_bf16 v[132:135], v[64:67], v[4:7], v[132:135]\n\t" \
    "ds_read_b64_tr_b16 v[72:73], v84 offset:32768\n\t" \
    "s_waitcnt lgkmcnt(8)\n\t" \
    "v_mfma_f32_16x16x32_bf16 v[136:139], v[64:67], v[8:11], v[136:139]\n\t" \
    "ds_read_b64_tr_b16 v[74:75], v84 offset:33792\n\t" \
    "s_waitcnt lgkmcnt(8)\n\t" \
    "v_mfma_f32_16x16x32_bf16 v[140:143], v[64:67], v[12:15], v[140:143]\n\t" \
    "ds_read_b128 v[32:35], v81 offset:32768\n\t" \
    "s_waitcnt lgkmcnt(8)\n\t" \
    "v_mfma_f32_16x16x32_bf16 v[144:147], v[64:67], v[16:19], v[144:147]\n\t" \
    "ds_read_b128 v[36:39], v81 offset:33280\n\t" \
    "s_waitcnt lgkmcnt(8)\n\t" \
    "v_mfma_f32_16x16x32_bf16 v[148:151], v[64:67], v[20:23], v[148:151]\n\t" \
    "ds_read_b128 v[40:43], v81 offset:36864\n\t" \
    "s_waitcnt lgkmcnt(8)\n\t" \
    "v_mfma_f32_16x16x32_bf16 v[152:155], v[64:67], v[24:27], v[152:155]\n\t" \
    "ds_read_b128 v[44:47], v81 offset:37376\n\t" \
    "s_waitcnt lgkmcnt(8)\n\t" \
    "v_mfma_f32_16x16x32_bf16 v[156:159], v[64:67], v[28:31], v[156:159]\n\t" \
    "s_waitcnt lgkmcnt(6)\n\t" \
    "v_mfma_f32_16x16x32_bf16 v[160:163], v[68:71], v[0:3], v[160:163]\n\t" \
    "v_mfma_f32_16x16x32_bf16 v[164:167], v[68:71], v[4:7], v[164:167]\n\t" \
    "ds_read_b64_tr_b16 v[76:77], v85 offset:32768\n\t" \
    "v_mfma_f32_16x16x32_bf16 v[168:171], v[68:71], v[8:11], v[168:171]\n\t" \
    "ds_read_b64_tr_b16 v[78:79], v85 offset:33792\n\t" \
    "v_mfma_f32_16x16x32_bf16 v[172:175], v[68:71], v[12:15], v[172:175]\n\t" \
    "ds_read_b128 v[48:51], v81 offset:40960\n\t" \
    "v_mfma_f32_16x16x32_bf16 v[176:179], v[68:71], v[16:19], v[176:179]\n\t" \
    "ds_read_b128 v[52:55], v81 offset:41472\n\t" \
    "v_mfma_f32_16x16x32_bf16 v[180:183], v[68:71], v[20:23], v[180:183]\n\t" \
    "ds_read_b128 v[56:59], v81 offset:45056\n\t" \
    "v_mfma_f32_16x16x32_bf16 v[184:187], v[68:71], v[24:27], v[184:187]\n\t" \
    "ds_read_b128 v[60:63], v81 offset:45568\n\t" \
    "v_mfma_f32_16x16x32_bf16 v[188:191], v[68:71], v[28:31], v[188:191]\n\t" \
    "s_waitcnt lgkmcnt(10)\n\t" \
    "v_mfma_f32_16x16x32_bf16 v[192:195], v[72:75], v[0:3], v[192:195]\n\t" \
    "v_mfma_f32_16x16x32_bf16 v[196:199], v[72:75], v[4:7], v[196:199]\n\t" \
    "ds_read_b64_tr_b16 v[64:65], v86 offset:32768\n\t" \
    "v_mfma_f32_16x16x32_bf16 v[200:203], v[72:75], v[8:11], v[200:203]\n\t" \
    "ds_read_b64_tr_b16 v[66:67], v86 offset:33792\n\t" \
    "v_mfma_f32_16x16x32_bf16 v[204:207], v[72:75], v[12:15], v[204:207]\n\t" \
    "s_mov_b32 s28, 1572864\n\t" \
    "s_add_u32 m0, s26, 16384\n\t" \
    "v_mfma_f32_16x16x32_bf16 v[208:211], v[72:75], v[16:19], v[208:211]\n\t" \
    "buffer_load_dwordx4 v106, s[12:15], s28 offen offset:0 lds\n\t" \
    "s_waitcnt vmcnt(9)\n\t" \
    "v_readfirstlane_b32 s23, v111\n\t" \
    "s_nop 0\n\t" \
    "s_cmp_eq_u32 s23, 16\n\t" \
    "s_cbranch_scc0 HSPIN0_%=\n\t" \
    "HOK0_%=:\n\t" \
    "v_mfma_f32_16x16x32_bf16 v[212:215], v[72:75], v[20:23], v[212:215]\n\t" \
    "v_mfma_f32_16x16x32_bf16 v[216:219], v[72:75], v[24:27], v[216:219]\n\t" \
    "v_mfma_f32_16x16x32_bf16 v[220:223], v[72:75], v[28:31], v[220:223]\n\t" \
    "s_waitcnt lgkmcnt(6)\n\t" \
    "v_mfma_f32_16x16x32_bf16 v[224:227], v[76:79], v[0:3], v[224:227]\n\t" \
    "v_mfma_f32_16x16x32_bf16 v[228:231], v[76:79], v[4:7], v[228:231]\n\t" \
    "ds_read_b64_tr_b16 v[68:69], v87 offset:32768\n\t" \
    "v_mfma_f32_16x16x32_bf16 v[232:235], v[76:79], v[8:11], v[232:235]\n\t" \
    "ds_read_b64_tr_b16 v[70:71], v87 offset:33792\n\t" \
    "v_mfma_f32_16x16x32_bf16 v[236:239], v[76:79], v[12:15], v[236:239]\n\t" \
    "v_mfma_f32_16x16x32_bf16 v[240:243], v[76:79], v[16:19], v[240:243]\n\t" \
    "buffer_load_dwordx4 v107, s[12:15], s28 offen offset:1024 lds\n\t" \
    "v_mfma_f32_16x16x32_bf16 v[244:247], v[76:79], v[20:23], v[244:247]\n\t" \
    "v_mfma_f32_16x16x32_bf16 v[248:251], v[76:79], v[24:27], v[248:251]\n\t" \
    "v_mfma_f32_16x16x32_bf16 v[252:255], v[76:79], v[28:31], v[252:255]\n\t" \
    "s_waitcnt lgkmcnt(2)\n\t" \
    "v_mfma_f32_16x16x32_bf16 a[0:3], v[64:67], v[0:3], a[0:3]\n\t" \
    "v_mfma_f32_16x16x32_bf16 a[4:7], v[64:67], v[4:7], a[4:7]\n\t" \
    "ds_read_b64_tr_b16 v[72:73], v88 offset:32768\n\t" \
    "v_mfma_f32_16x16x32_bf16 a[8:11], v[64:67], v[8:11], a[8:11]\n\t" \
    "ds_read_b64_tr_b16 v[74:75], v88 offset:33792\n\t" \
    "v_mfma_f32_16x16x32_bf16 a[12:15], v[64:67], v[12:15], a[12:15]\n\t" \
    "v_mfma_f32_16x16x32_bf16 a[16:19], v[64:67], v[16:19], a[16:19]\n\t" \
    "buffer_load_dwordx4 v108, s[12:15], s28 offen offset:2048 lds\n\t" \
    "v_mfma_f32_16x16x32_bf16 a[20:23], v[64:67], v[20:23], a[20:23]\n\t" \
    "v_mfma_f32_16x16x32_bf16 a[24:27], v[64:67], v[24:27], a[24:27]\n\t" \
    "v_mfma_f32_16x16x32_bf16 a[28:31], v[64:67], v[28:31], a[28:31]\n\t" \
    "s_waitcnt lgkmcnt(2)\n\t" \
    "v_mfma_f32_16x16x32_bf16 a[32:35], v[68:71], v[0:3], a[32:35]\n\t" \
    "v_mfma_f32_16x16x32_bf16 a[36:39], v[68:71], v[4:7], a[36:39]\n\t" \
    "ds_read_b64_tr_b16 v[76:77], v89 offset:32768\n\t" \
    "v_mfma_f32_16x16x32_bf16 a[40:43], v[68:71], v[8:11], a[40:43]\n\t" \
    "ds_read_b64_tr_b16 v[78:79], v89 offset:33792\n\t" \
    "v_mfma_f32_16x16x32_bf16 a[44:47], v[68:71], v[12:15], a[44:47]\n\t" \
    "v_mfma_f32_16x16x32_bf16 a[48:51], v[68:71], v[16:19], a[48:51]\n\t" \
    "buffer_load_dwordx4 v109, s[12:15], s28 offen offset:3072 lds\n\t" \
    "v_mfma_f32_16x16x32_bf16 a[52:55], v[68:71], v[20:23], a[52:55]\n\t" \
    "v_mfma_f32_16x16x32_bf16 a[56:59], v[68:71], v[24:27], a[56:59]\n\t" \
    "v_mfma_f32_16x16x32_bf16 a[60:63], v[68:71], v[28:31], a[60:63]\n\t" \
    "s_waitcnt vmcnt(25) lgkmcnt(0)\n\t" \
    "s_barrier\n\t" \
    "v_mfma_f32_16x16x32_bf16 a[64:67], v[72:75], v[0:3], a[64:67]\n\t" \
    "v_mfma_f32_16x16x32_bf16 a[68:71], v[72:75], v[4:7], a[68:71]\n\t" \
    "ds_read_b64_tr_b16 v[64:65], v82 offset:49152\n\t" \
    "v_mfma_f32_16x16x32_bf16 a[72:75], v[72:75], v[8:11], a[72:75]\n\t" \
    "ds_read_b64_tr_b16 v[66:67], v82 offset:50176\n\t" \
    "v_mfma_f32_16x16x32_bf16 a[76:79], v[72:75], v[12:15], a[76:79]\n\t" \
    "v_mfma_f32_16x16x32_bf16 a[80:83], v[72:75], v[16:19], a[80:83]\n\t" \
    "v_mfma_f32_16x16x32_bf16 a[84:87], v[72:75], v[20:23], a[84:87]\n\t" \
    "v_mfma_f32_16x16x32_bf16 a[88:91], v[72:75], v[24:27], a[88:91]\n\t" \
    "v_mfma_f32_16x16x32_bf16 a[92:95], v[72:75], v[28:31], a[92:95]\n\t" \
    "v_mfma_f32_16x16x32_bf16 a[96:99], v[76:79], v[0:3], a[96:99]\n\t" \
    "v_mfma_f32_16x16x32_bf16 a[100:103], v[76:79], v[4:7], a[100:103]\n\t" \
    "ds_read_b64_tr_b16 v[68:69], v83 offset:49152\n\t" \
    "v_mfma_f32_16x16x32_bf16 a[104:107], v[76:79], v[8:11], a[104:107]\n\t" \
    "ds_read_b64_tr_b16 v[70:71], v83 offset:50176\n\t" \
    "v_mfma_f32_16x16x32_bf16 a[108:111], v[76:79], v[12:15], a[108:111]\n\t" \
    "v_mfma_f32_16x16x32_bf16 a[112:115], v[76:79], v[16:19], a[112:115]\n\t" \
    "v_mfma_f32_16x16x32_bf16 a[116:119], v[76:79], v[20:23], a[116:119]\n\t" \
    "v_mfma_f32_16x16x32_bf16 a[120:123], v[76:79], v[24:27], a[120:123]\n\t" \
    "v_mfma_f32_16x16x32_bf16 a[124:127], v[76:79], v[28:31], a[124:127]\n\t" \
    "s_waitcnt lgkmcnt(2)\n\t" \
    "v_mfma_f32_16x16x32_bf16 v[128:131], v[64:67], v[32:35], v[128:131]\n\t" \
    "v_mfma_f32_16x16x32_bf16 v[132:135], v[64:67], v[36:39], v[132:135]\n\t" \
    "ds_read_b64_tr_b16 v[72:73], v84 offset:49152\n\t" \
    "v_mfma_f32_16x16x32_bf16 v[136:139], v[64:67], v[40:43], v[136:139]\n\t" \
    "ds_read_b64_tr_b16 v[74:75], v84 offset:50176\n\t" \
    "v_lshlrev_b32 v80, 4, v115\n\t" \
    "v_mfma_f32_16x16x32_bf16 v[140:143], v[64:67], v[44:47], v[140:143]\n\t" \
    "v_mfma_f32_16x16x32_bf16 v[144:147], v[64:67], v[48:51], v[144:147]\n\t" \
    "v_mfma_f32_16x16x32_bf16 v[148:151], v[64:67], v[52:55], v[148:151]\n\t" \
    "v_mfma_f32_16x16x32_bf16 v[152:155], v[64:67], v[56:59], v[152:155]\n\t" \
    "buffer_load_dwordx4 v[0:3], v80, s[16:19], s32 offen offset:0 sc1\n\t" \
    "v_mfma_f32_16x16x32_bf16 v[156:159], v[64:67], v[60:63], v[156:159]\n\t" \
    "s_waitcnt lgkmcnt(2)\n\t" \
    "v_mfma_f32_16x16x32_bf16 v[160:163], v[68:71], v[32:35], v[160:163]\n\t" \
    "v_mfma_f32_16x16x32_bf16 v[164:167], v[68:71], v[36:39], v[164:167]\n\t" \
    "ds_read_b64_tr_b16 v[76:77], v85 offset:49152\n\t" \
    "v_mfma_f32_16x16x32_bf16 v[168:171], v[68:71], v[40:43], v[168:171]\n\t" \
    "ds_read_b64_tr_b16 v[78:79], v85 offset:50176\n\t" \
    "buffer_load_dwordx4 v[4:7], v80, s[16:19], s33 offen offset:0 sc1\n\t" \
    "v_mfma_f32_16x16x32_bf16 v[172:175], v[68:71], v[44:47], v[172:175]\n\t" \
    "v_mfma_f32_16x16x32_bf16 v[176:179], v[68:71], v[48:51], v[176:179]\n\t" \
    "v_mfma_f32_16x16x32_bf16 v[180:183], v[68:71], v[52:55], v[180:183]\n\t" \
    "v_mfma_f32_16x16x32_bf16 v[184:187], v[68:71], v[56:59], v[184:187]\n\t" \
    "buffer_load_dwordx4 v[8:11], v80, s[16:19], s34 offen offset:0 sc1\n\t" \
    "v_mfma_f32_16x16x32_bf16 v[188:191], v[68:71], v[60:63], v[188:191]\n\t" \
    "s_waitcnt lgkmcnt(2)\n\t" \
    "v_mfma_f32_16x16x32_bf16 v[192:195], v[72:75], v[32:35], v[192:195]\n\t" \
    "v_mfma_f32_16x16x32_bf16 v[196:199], v[72:75], v[36:39], v[196:199]\n\t" \
    "ds_read_b64_tr_b16 v[64:65], v86 offset:49152\n\t" \
    "v_mfma_f32_16x16x32_bf16 v[200:203], v[72:75], v[40:43], v[200:203]\n\t" \
    "ds_read_b64_tr_b16 v[66:67], v86 offset:50176\n\t" \
    "buffer_load_dwordx4 v[12:15], v80, s[16:19], s35 offen offset:0 sc1\n\t" \
    "v_lshrrev_b32 v111, 5, v115\n\t" \
    "v_mfma_f32_16x16x32_bf16 v[204:207], v[72:75], v[44:47], v[204:207]\n\t" \
    "v_lshlrev_b32 v111, 10, v111\n\t" \
    "v_mfma_f32_16x16x32_bf16 v[208:211], v[72:75], v[48:51], v[208:211]\n\t" \
    "v_bfe_u32 v112, v115, 4, 1\n\t" \
    "v_mfma_f32_16x16x32_bf16 v[212:215], v[72:75], v[52:55], v[212:215]\n\t" \
    "v_lshlrev_b32 v112, 9, v112\n\t" \
    "v_mfma_f32_16x16x32_bf16 v[216:219], v[72:75], v[56:59], v[216:219]\n\t" \
    "buffer_load_dwordx4 v[16:19], v80, s[16:19], s32 offen offset:1024 sc1\n\t" \
    "v_add_u32 v111, v111, v112\n\t" \
    "v_mfma_f32_16x16x32_bf16 v[220:223], v[72:75], v[60:63], v[220:223]\n\t" \
    "v_bfe_u32 v112, v115, 3, 1\n\t" \
    "s_waitcnt lgkmcnt(2)\n\t" \
    "v_mfma_f32_16x16x32_bf16 v[224:227], v[76:79], v[32:35], v[224:227]\n\t" \
    "v_lshlrev_b32 v112, 8, v112\n\t" \
    "v_mfma_f32_16x16x32_bf16 v[228:231], v[76:79], v[36:39], v[228:231]\n\t" \
    "ds_read_b64_tr_b16 v[68:69], v87 offset:49152\n\t" \
    "v_add_u32 v111, v111, v112\n\t" \
    "v_mfma_f32_16x16x32_bf16 v[232:235], v[76:79], v[40:43], v[232:235]\n\t" \
    "ds_read_b64_tr_b16 v[70:71], v87 offset:50176\n\t" \
    "buffer_load_dwordx4 v[20:23], v80, s[16:19], s33 offen offset:1024 sc1\n\t" \
    "v_and_b32 v112, 7, v115\n\t" \
    "v_mfma_f32_16x16x32_bf16 v[236:239], v[76:79], v[44:47], v[236:239]\n\t" \
    "v_lshlrev_b32 v112, 4, v112\n\t" \
    "v_mfma_f32_16x16x32_bf16 v[240:243], v[76:79], v[48:51], v[240:243]\n\t" \
    "v_add_u32 v110, v111, v112\n\t" \
    "v_mfma_f32_16x16x32_bf16 v[244:247], v[76:79], v[52:55], v[244:247]\n\t" \
    "s_lshl_b32 s23, s21, 6\n\t" \
    "v_mfma_f32_16x16x32_bf16 v[248:251], v[76:79], v[56:59], v[248:251]\n\t" \
    "buffer_load_dwordx4 v[24:27], v80, s[16:19], s34 offen offset:1024 sc1\n\t" \
    "s_lshl_b32 s24, s22, 3\n\t" \
    "v_mfma_f32_16x16x32_bf16 v[252:255], v[76:79], v[60:63], v[252:255]\n\t" \
    "s_add_u32 s23, s23, s24\n\t" \
    "s_waitcnt lgkmcnt(2)\n\t" \
    "v_mfma_f32_16x16x32_bf16 a[0:3], v[64:67], v[32:35], a[0:3]\n\t" \
    "s_and_b32 s24, s20, 1\n\t" \
    "v_mfma_f32_16x16x32_bf16 a[4:7], v[64:67], v[36:39], a[4:7]\n\t" \
    "ds_read_b64_tr_b16 v[72:73], v88 offset:49152\n\t" \
    "s_lshl_b32 s24, s24, 2\n\t" \
    "v_mfma_f32_16x16x32_bf16 a[8:11], v[64:67], v[40:43], a[8:11]\n\t" \
    "ds_read_b64_tr_b16 v[74:75], v88 offset:50176\n\t" \
    "buffer_load_dwordx4 v[28:31], v80, s[16:19], s35 offen offset:1024 sc1\n\t" \
    "s_add_u32 s23, s23, s24\n\t" \
    "s_mov_b32 s28, 2097152\n\t" \
    "s_add_u32 m0, s26, 32768\n\t" \
    "v_mfma_f32_16x16x32_bf16 a[12:15], v[64:67], v[44:47], a[12:15]\n\t" \
    "buffer_load_dwordx4 v106, s[12:15], s28 offen offset:0 lds\n\t" \
    "s_lshl_b32 s23, s23, 13\n\t" \
    "v_mfma_f32_16x16x32_bf16 a[16:19], v[64:67], v[48:51], a[16:19]\n\t" \
    "s_add_u32 s23, s23, 0x400000\n\t" \
    "v_mfma_f32_16x16x32_bf16 a[20:23], v[64:67], v[52:55], a[20:23]\n\t" \
    "buffer_load_dwordx4 v107, s[12:15], s28 offen offset:1024 lds\n\t" \
    "v_mfma_f32_16x16x32_bf16 a[24:27], v[64:67], v[56:59], a[24:27]\n\t" \
    "v_mfma_f32_16x16x32_bf16 a[28:31], v[64:67], v[60:63], a[28:31]\n\t" \
    "s_waitcnt lgkmcnt(2)\n\t" \
    "v_mfma_f32_16x16x32_bf16 a[32:35], v[68:71], v[32:35], a[32:35]\n\t" \
    "v_mfma_f32_16x16x32_bf16 a[36:39], v[68:71], v[36:39], a[36:39]\n\t" \
    "ds_read_b64_tr_b16 v[76:77], v89 offset:49152\n\t" \
    "v_mfma_f32_16x16x32_bf16 a[40:43], v[68:71], v[40:43], a[40:43]\n\t" \
    "ds_read_b64_tr_b16 v[78:79], v89 offset:50176\n\t" \
    "v_mfma_f32_16x16x32_bf16 a[44:47], v[68:71], v[44:47], a[44:47]\n\t" \
    "buffer_load_dwordx4 v108, s[12:15], s28 offen offset:2048 lds\n\t" \
    "v_subrev_u32 v82, s30, v82\n\t" \
    "v_subrev_u32 v83, s30, v83\n\t" \
    "v_subrev_u32 v84, s30, v84\n\t" \
    "v_subrev_u32 v85, s30, v85\n\t" \
    "v_mfma_f32_16x16x32_bf16 a[48:51], v[68:71], v[48:51], a[48:51]\n\t" \
    "v_subrev_u32 v86, s30, v86\n\t" \
    "v_subrev_u32 v87, s30, v87\n\t" \
    "v_subrev_u32 v88, s30, v88\n\t" \
    "v_subrev_u32 v89, s30, v89\n\t" \
    "v_mfma_f32_16x16x32_bf16 a[52:55], v[68:71], v[52:55], a[52:55]\n\t" \
    "buffer_load_dwordx4 v109, s[12:15], s28 offen offset:3072 lds\n\t" \
    "v_subrev_u32 v90, s30, v90\n\t" \
    "v_subrev_u32 v91, s30, v91\n\t" \
    "v_subrev_u32 v92, s30, v92\n\t" \
    "v_subrev_u32 v93, s30, v93\n\t" \
    "v_mfma_f32_16x16x32_bf16 a[56:59], v[68:71], v[56:59], a[56:59]\n\t" \
    "v_subrev_u32 v94, s30, v94\n\t" \
    "v_subrev_u32 v95, s30, v95\n\t" \
    "v_subrev_u32 v96, s30, v96\n\t" \
    "v_subrev_u32 v97, s30, v97\n\t" \
    "v_mfma_f32_16x16x32_bf16 a[60:63], v[68:71], v[60:63], a[60:63]\n\t" \
    "s_waitcnt vmcnt(25) lgkmcnt(0)\n\t" \
    "s_barrier\n\t" \
    "v_mfma_f32_16x16x32_bf16 a[64:67], v[72:75], v[32:35], a[64:67]\n\t" \
    "v_mfma_f32_16x16x32_bf16 a[68:71], v[72:75], v[36:39], a[68:71]\n\t" \
    "ds_read_b64_tr_b16 v[64:65], v90 offset:0\n\t" \
    "v_mfma_f32_16x16x32_bf16 a[72:75], v[72:75], v[40:43], a[72:75]\n\t" \
    "ds_read_b64_tr_b16 v[66:67], v90 offset:1024\n\t" \
    "v_mfma_f32_16x16x32_bf16 a[76:79], v[72:75], v[44:47], a[76:79]\n\t" \
    "ds_read_b64_tr_b16 v[68:69], v91 offset:0\n\t" \
    "v_mfma_f32_16x16x32_bf16 a[80:83], v[72:75], v[48:51], a[80:83]\n\t" \
    "ds_read_b64_tr_b16 v[70:71], v91 offset:1024\n\t" \
    "v_mfma_f32_16x16x32_bf16 a[84:87], v[72:75], v[52:55], a[84:87]\n\t" \
    "v_mfma_f32_16x16x32_bf16 a[88:91], v[72:75], v[56:59], a[88:91]\n\t" \
    "v_mfma_f32_16x16x32_bf16 a[92:95], v[72:75], v[60:63], a[92:95]\n\t" \
    "v_mfma_f32_16x16x32_bf16 a[96:99], v[76:79], v[32:35], a[96:99]\n\t" \
    "v_mfma_f32_16x16x32_bf16 a[100:103], v[76:79], v[36:39], a[100:103]\n\t" \
    "ds_read_b64_tr_b16 v[72:73], v92 offset:0\n\t" \
    "v_mfma_f32_16x16x32_bf16 a[104:107], v[76:79], v[40:43], a[104:107]\n\t" \
    "ds_read_b64_tr_b16 v[74:75], v92 offset:1024\n\t" \
    "v_mfma_f32_16x16x32_bf16 a[108:111], v[76:79], v[44:47], a[108:111]\n\t" \
    "v_mfma_f32_16x16x32_bf16 a[112:115], v[76:79], v[48:51], a[112:115]\n\t" \
    "v_mfma_f32_16x16x32_bf16 a[116:119], v[76:79], v[52:55], a[116:119]\n\t" \
    "v_mfma_f32_16x16x32_bf16 a[120:123], v[76:79], v[56:59], a[120:123]\n\t" \
    "v_mfma_f32_16x16x32_bf16 a[124:127], v[76:79], v[60:63], a[124:127]\n\t" \
    "s_waitcnt vmcnt(11) lgkmcnt(4)\n\t" \
    "v_mfma_f32_16x16x32_bf16 a[128:131], v[64:67], v[0:3], 0\n\t" \
    "ds_read_b64_tr_b16 v[76:77], v93 offset:0\n\t" \
    "v_cvt_pk_bf16_f32 v128, v128, v129\n\t" \
    "s_waitcnt vmcnt(10)\n\t" \
    "v_mfma_f32_16x16x32_bf16 a[132:135], v[64:67], v[4:7], 0\n\t" \
    "ds_read_b64_tr_b16 v[78:79], v93 offset:1024\n\t" \
    "v_cvt_pk_bf16_f32 v129, v130, v131\n\t" \
    "s_waitcnt vmcnt(9)\n\t" \
    "v_mfma_f32_16x16x32_bf16 a[136:139], v[64:67], v[8:11], 0\n\t" \
    "buffer_load_dwordx4 v[32:35], v80, s[16:19], s32 offen offset:2048 sc1\n\t" \
    "v_cvt_pk_bf16_f32 v130, v160, v161\n\t" \
    "s_waitcnt vmcnt(9)\n\t" \
    "v_mfma_f32_16x16x32_bf16 a[140:143], v[64:67], v[12:15], 0\n\t" \
    "v_cvt_pk_bf16_f32 v131, v162, v163\n\t" \
    "s_add_u32 s36, s23, 0\n\t" \
    "s_waitcnt lgkmcnt(4)\n\t" \
    "v_mfma_f32_16x16x32_bf16 a[144:147], v[68:71], v[0:3], 0\n\t" \
    "ds_read_b64_tr_b16 v[64:65], v94 offset:0\n\t" \
    "buffer_store_dwordx4 v[128:131], v110, s[16:19], s36 offen offset:0 sc1\n\t" \
    "v_mfma_f32_16x16x32_bf16 a[148:151], v[68:71], v[4:7], 0\n\t" \
    "ds_read_b64_tr_b16 v[66:67], v94 offset:1024\n\t" \
    "v_mfma_f32_16x16x32_bf16 a[152:155], v[68:71], v[8:11], 0\n\t" \
    "buffer_load_dwordx4 v[36:39], v80, s[16:19], s33 offen offset:2048 sc1\n\t" \
    "v_mfma_f32_16x16x32_bf16 a[156:159], v[68:71], v[12:15], 0\n\t" \
    "s_waitcnt lgkmcnt(4)\n\t" \
    "v_mfma_f32_16x16x32_bf16 a[160:163], v[72:75], v[0:3], 0\n\t" \
    "ds_read_b64_tr_b16 v[68:69], v95 offset:0\n\t" \
    "v_mfma_f32_16x16x32_bf16 a[164:167], v[72:75], v[4:7], 0\n\t" \
    "ds_read_b64_tr_b16 v[70:71], v95 offset:1024\n\t" \
    "v_mfma_f32_16x16x32_bf16 a[168:171], v[72:75], v[8:11], 0\n\t" \
    "buffer_load_dwordx4 v[40:43], v80, s[16:19], s34 offen offset:2048 sc1\n\t" \
    "v_mfma_f32_16x16x32_bf16 a[172:175], v[72:75], v[12:15], 0\n\t" \
    "s_waitcnt lgkmcnt(4)\n\t" \
    "v_mfma_f32_16x16x32_bf16 a[176:179], v[76:79], v[0:3], 0\n\t" \
    "ds_read_b64_tr_b16 v[72:73], v96 offset:0\n\t" \
    "v_mfma_f32_16x16x32_bf16 a[180:183], v[76:79], v[4:7], 0\n\t" \
    "ds_read_b64_tr_b16 v[74:75], v96 offset:1024\n\t" \
    "v_mfma_f32_16x16x32_bf16 a[184:187], v[76:79], v[8:11], 0\n\t" \
    "buffer_load_dwordx4 v[44:47], v80, s[16:19], s35 offen offset:2048 sc1\n\t" \
    "v_mfma_f32_16x16x32_bf16 a[188:191], v[76:79], v[12:15], 0\n\t" \
    "s_waitcnt lgkmcnt(4)\n\t" \
    "v_mfma_f32_16x16x32_bf16 a[192:195], v[64:67], v[0:3], 0\n\t" \
    "ds_read_b64_tr_b16 v[76:77], v97 offset:0\n\t" \
    "v_cvt_pk_bf16_f32 v132, v132, v133\n\t" \
    "v_mfma_f32_16x16x32_bf16 a[196:199], v[64:67], v[4:7], 0\n\t" \
    "ds_read_b64_tr_b16 v[78:79], v97 offset:1024\n\t" \
    "v_cvt_pk_bf16_f32 v133, v134, v135\n\t" \
    "v_mfma_f32_16x16x32_bf16 a[200:203], v[64:67], v[8:11], 0\n\t" \
    "buffer_load_dwordx4 v[48:51], v80, s[16:19], s32 offen offset:3072 sc1\n\t" \
    "v_cvt_pk_bf16_f32 v134, v164, v165\n\t" \
    "v_mfma_f32_16x16x32_bf16 a[204:207], v[64:67], v[12:15], 0\n\t" \
    "v_cvt_pk_bf16_f32 v135, v166, v167\n\t" \
    "s_add_u32 s36, s23, 0\n\t" \
    "s_waitcnt lgkmcnt(4)\n\t" \
    "v_mfma_f32_16x16x32_bf16 a[208:211], v[68:71], v[0:3], 0\n\t" \
    "ds_read_b64_tr_b16 v[64:65], v90 offset:8192\n\t" \
    "buffer_store_dwordx4 v[132:135], v110, s[16:19], s36 offen offset:128 sc1\n\t" \
    "v_mfma_f32_16x16x32_bf16 a[212:215], v[68:71], v[4:7], 0\n\t" \
    "ds_read_b64_tr_b16 v[66:67], v90 offset:9216\n\t" \
    "v_mfma_f32_16x16x32_bf16 a[216:219], v[68:71], v[8:11], 0\n\t" \
    "buffer_load_dwordx4 v[52:55], v80, s[16:19], s33 offen offset:3072 sc1\n\t" \
    "v_mfma_f32_16x16x32_bf16 a[220:223], v[68:71], v[12:15], 0\n\t" \
    "s_waitcnt lgkmcnt(4)\n\t" \
    "v_mfma_f32_16x16x32_bf16 a[224:227], v[72:75], v[0:3], 0\n\t" \
    "ds_read_b64_tr_b16 v[68:69], v91 offset:8192\n\t" \
    "v_mfma_f32_16x16x32_bf16 a[228:231], v[72:75], v[4:7], 0\n\t" \
    "ds_read_b64_tr_b16 v[70:71], v91 offset:9216\n\t" \
    "v_mfma_f32_16x16x32_bf16 a[232:235], v[72:75], v[8:11], 0\n\t" \
    "buffer_load_dwordx4 v[56:59], v80, s[16:19], s34 offen offset:3072 sc1\n\t" \
    "v_mfma_f32_16x16x32_bf16 a[236:239], v[72:75], v[12:15], 0\n\t" \
    "s_waitcnt lgkmcnt(4)\n\t" \
    "v_mfma_f32_16x16x32_bf16 a[240:243], v[76:79], v[0:3], 0\n\t" \
    "ds_read_b64_tr_b16 v[72:73], v92 offset:8192\n\t" \
    "v_mfma_f32_16x16x32_bf16 a[244:247], v[76:79], v[4:7], 0\n\t" \
    "ds_read_b64_tr_b16 v[74:75], v92 offset:9216\n\t" \
    "v_mfma_f32_16x16x32_bf16 a[248:251], v[76:79], v[8:11], 0\n\t" \
    "buffer_load_dwordx4 v[60:63], v80, s[16:19], s35 offen offset:3072 sc1\n\t" \
    "v_mfma_f32_16x16x32_bf16 a[252:255], v[76:79], v[12:15], 0\n\t" \
    "s_waitcnt vmcnt(17) lgkmcnt(4)\n\t" \
    "v_mfma_f32_16x16x32_bf16 a[128:131], v[64:67], v[16:19], a[128:131]\n\t" \
    "ds_read_b64_tr_b16 v[76:77], v93 offset:8192\n\t" \
    "v_cvt_pk_bf16_f32 v136, v136, v137\n\t" \
    "s_waitcnt vmcnt(16)\n\t" \
    "v_mfma_f32_16x16x32_bf16 a[132:135], v[64:67], v[20:23], a[132:135]\n\t" \
    "ds_read_b64_tr_b16 v[78:79], v93 offset:9216\n\t" \
    "v_cvt_pk_bf16_f32 v137, v138, v139\n\t" \
    "s_add_u32 s32, s32, 4096\n\t" \
    "s_add_u32 s33, s33, 4096\n\t" \
    "s_add_u32 s34, s34, 4096\n\t" \
    "s_add_u32 s35, s35, 4096\n\t" \
    "s_waitcnt vmcnt(15)\n\t" \
    "v_mfma_f32_16x16x32_bf16 a[136:139], v[64:67], v[24:27], a[136:139]\n\t" \
    "buffer_load_dwordx4 v[0:3], v80, s[16:19], s32 offen offset:0 sc1\n\t" \
    "s_waitcnt vmcnt(15)\n\t" \
    "v_mfma_f32_16x16x32_bf16 a[140:143], v[64:67], v[28:31], a[140:143]\n\t" \
    "v_cvt_pk_bf16_f32 v138, v168, v169\n\t" \
    "v_cvt_pk_bf16_f32 v139, v170, v171\n\t" \
    "s_waitcnt lgkmcnt(4)\n\t" \
    "v_mfma_f32_16x16x32_bf16 a[144:147], v[68:71], v[16:19], a[144:147]\n\t" \
    "ds_read_b64_tr_b16 v[64:65], v94 offset:8192\n\t" \
    "s_add_u32 s36, s23, 8192\n\t" \
    "v_mfma_f32_16x16x32_bf16 a[148:151], v[68:71], v[20:23], a[148:151]\n\t" \
    "ds_read_b64_tr_b16 v[66:67], v94 offset:9216\n\t" \
    "buffer_store_dwordx4 v[136:139], v110, s[16:19], s36 offen offset:0 sc1\n\t" \
    "v_mfma_f32_16x16x32_bf16 a[152:155], v[68:71], v[24:27], a[152:155]\n\t" \
    "v_mfma_f32_16x16x32_bf16 a[156:159], v[68:71], v[28:31], a[156:159]\n\t" \
    "s_waitcnt lgkmcnt(4)\n\t" \
    "v_mfma_f32_16x16x32_bf16 a[160:163], v[72:75], v[16:19], a[160:163]\n\t" \
    "ds_read_b64_tr_b16 v[68:69], v95 offset:8192\n\t" \
    "v_mfma_f32_16x16x32_bf16 a[164:167], v[72:75], v[20:23], a[164:167]\n\t" \
    "ds_read_b64_tr_b16 v[70:71], v95 offset:9216\n\t" \
    "v_mfma_f32_16x16x32_bf16 a[168:171], v[72:75], v[24:27], a[168:171]\n\t" \
    "buffer_load_dwordx4 v[4:7], v80, s[16:19], s33 offen offset:0 sc1\n\t" \
    "v_mfma_f32_16x16x32_bf16 a[172:175], v[72:75], v[28:31], a[172:175]\n\t" \
    "s_waitcnt lgkmcnt(4)\n\t" \
    "v_mfma_f32_16x16x32_bf16 a[176:179], v[76:79], v[16:19], a[176:179]\n\t" \
    "ds_read_b64_tr_b16 v[72:73], v96 offset:8192\n\t" \
    "v_mfma_f32_16x16x32_bf16 a[180:183], v[76:79], v[20:23], a[180:183]\n\t" \
    "ds_read_b64_tr_b16 v[74:75], v96 offset:9216\n\t" \
    "v_mfma_f32_16x16x32_bf16 a[184:187], v[76:79], v[24:27], a[184:187]\n\t" \
    "v_mfma_f32_16x16x32_bf16 a[188:191], v[76:79], v[28:31], a[188:191]\n\t" \
    "s_waitcnt lgkmcnt(4)\n\t" \
    "v_mfma_f32_16x16x32_bf16 a[192:195], v[64:67], v[16:19], a[192:195]\n\t" \
    "ds_read_b64_tr_b16 v[76:77], v97 offset:8192\n\t" \
    "v_cvt_pk_bf16_f32 v140, v140, v141\n\t" \
    "v_mfma_f32_16x16x32_bf16 a[196:199], v[64:67], v[20:23], a[196:199]\n\t" \
    "ds_read_b64_tr_b16 v[78:79], v97 offset:9216\n\t" \
    "v_cvt_pk_bf16_f32 v141, v142, v143\n\t" \
    "v_mfma_f32_16x16x32_bf16 a[200:203], v[64:67], v[24:27], a[200:203]\n\t" \
    "buffer_load_dwordx4 v[8:11], v80, s[16:19], s34 offen offset:0 sc1\n\t" \
    "v_cvt_pk_bf16_f32 v142, v172, v173\n\t" \
    "v_mfma_f32_16x16x32_bf16 a[204:207], v[64:67], v[28:31], a[204:207]\n\t" \
    "v_cvt_pk_bf16_f32 v143, v174, v175\n\t" \
    "s_add_u32 s36, s23, 8192\n\t" \
    "s_waitcnt vmcnt(34)\n\t" \
    "s_barrier\n\t" \
    "s_waitcnt lgkmcnt(4)\n\t" \
    "v_mfma_f32_16x16x32_bf16 a[208:211], v[68:71], v[16:19], a[208:211]\n\t" \
    "ds_read_b64_tr_b16 v[64:65], v90 offset:16384\n\t" \
    "v_mfma_f32_16x16x32_bf16 a[212:215], v[68:71], v[20:23], a[212:215]\n\t" \
    "ds_read_b64_tr_b16 v[66:67], v90 offset:17408\n\t" \
    "buffer_store_dwordx4 v[140:143], v110, s[16:19], s36 offen offset:128 sc1\n\t" \
    "v_mfma_f32_16x16x32_bf16 a[216:219], v[68:71], v[24:27], a[216:219]\n\t" \
    "v_mfma_f32_16x16x32_bf16 a[220:223], v[68:71], v[28:31], a[220:223]\n\t" \
    "s_waitcnt lgkmcnt(4)\n\t" \
    "v_mfma_f32_16x16x32_bf16 a[224:227], v[72:75], v[16:19], a[224:227]\n\t" \
    "ds_read_b64_tr_b16 v[68:69], v91 offset:16384\n\t" \
    "v_mfma_f32_16x16x32_bf16 a[228:231], v[72:75], v[20:23], a[228:231]\n\t" \
    "ds_read_b64_tr_b16 v[70:71], v91 offset:17408\n\t" \
    "v_mfma_f32_16x16x32_bf16 a[232:235], v[72:75], v[24:27], a[232:235]\n\t" \
    "buffer_load_dwordx4 v[12:15], v80, s[16:19], s35 offen offset:0 sc1\n\t" \
    "v_mfma_f32_16x16x32_bf16 a[236:239], v[72:75], v[28:31], a[236:239]\n\t" \
    "s_waitcnt lgkmcnt(4)\n\t" \
    "v_mfma_f32_16x16x32_bf16 a[240:243], v[76:79], v[16:19], a[240:243]\n\t" \
    "ds_read_b64_tr_b16 v[72:73], v92 offset:16384\n\t" \
    "v_mfma_f32_16x16x32_bf16 a[244:247], v[76:79], v[20:23], a[244:247]\n\t" \
    "ds_read_b64_tr_b16 v[74:75], v92 offset:17408\n\t" \
    "v_mfma_f32_16x16x32_bf16 a[248:251], v[76:79], v[24:27], a[248:251]\n\t" \
    "v_mfma_f32_16x16x32_bf16 a[252:255], v[76:79], v[28:31], a[252:255]\n\t" \
    "s_waitcnt vmcnt(15) lgkmcnt(4)\n\t" \
    "v_mfma_f32_16x16x32_bf16 a[128:131], v[64:67], v[32:35], a[128:131]\n\t" \
    "ds_read_b64_tr_b16 v[76:77], v93 offset:16384\n\t" \
    "v_cvt_pk_bf16_f32 v144, v144, v145\n\t" \
    "s_waitcnt vmcnt(13)\n\t" \
    "v_mfma_f32_16x16x32_bf16 a[132:135], v[64:67], v[36:39], a[132:135]\n\t" \
    "ds_read_b64_tr_b16 v[78:79], v93 offset:17408\n\t" \
    "v_cvt_pk_bf16_f32 v145, v146, v147\n\t" \
    "s_waitcnt vmcnt(12)\n\t" \
    "v_mfma_f32_16x16x32_bf16 a[136:139], v[64:67], v[40:43], a[136:139]\n\t" \
    "buffer_load_dwordx4 v[16:19], v80, s[16:19], s32 offen offset:1024 sc1\n\t" \
    "v_cvt_pk_bf16_f32 v146, v176, v177\n\t" \
    "s_waitcnt vmcnt(12)\n\t" \
    "v_mfma_f32_16x16x32_bf16 a[140:143], v[64:67], v[44:47], a[140:143]\n\t" \
    "v_cvt_pk_bf16_f32 v147, v178, v179\n\t" \
    "s_add_u32 s36, s23, 16384\n\t" \
    "s_waitcnt lgkmcnt(4)\n\t" \
    "v_mfma_f32_16x16x32_bf16 a[144:147], v[68:71], v[32:35], a[144:147]\n\t" \
    "ds_read_b64_tr_b16 v[64:65], v94 offset:16384\n\t" \
    "buffer_store_dwordx4 v[144:147], v110, s[16:19], s36 offen offset:0 sc1\n\t" \
    "v_mfma_f32_16x16x32_bf16 a[148:151], v[68:71], v[36:39], a[148:151]\n\t" \
    "ds_read_b64_tr_b16 v[66:67], v94 offset:17408\n\t" \
    "v_mfma_f32_16x16x32_bf16 a[152:155], v[68:71], v[40:43], a[152:155]\n\t" \
    "s_mov_b32 s28, 2621440\n\t" \
    "s_add_u32 m0, s26, 49152\n\t" \
    "v_mfma_f32_16x16x32_bf16 a[156:159], v[68:71], v[44:47], a[156:159]\n\t" \
    "buffer_load_dwordx4 v106, s[12:15], s28 offen offset:0 lds\n\t" \
    "s_waitcnt lgkmcnt(4)\n\t" \
    "v_mfma_f32_16x16x32_bf16 a[160:163], v[72:75], v[32:35], a[160:163]\n\t" \
    "ds_read_b64_tr_b16 v[68:69], v95 offset:16384\n\t" \
    "v_mfma_f32_16x16x32_bf16 a[164:167], v[72:75], v[36:39], a[164:167]\n\t" \
    "ds_read_b64_tr_b16 v[70:71], v95 offset:17408\n\t" \
    "v_mfma_f32_16x16x32_bf16 a[168:171], v[72:75], v[40:43], a[168:171]\n\t" \
    "buffer_load_dwordx4 v[20:23], v80, s[16:19], s33 offen offset:1024 sc1\n\t" \
    "v_mfma_f32_16x16x32_bf16 a[172:175], v[72:75], v[44:47], a[172:175]\n\t" \
    "s_waitcnt lgkmcnt(4)\n\t" \
    "v_mfma_f32_16x16x32_bf16 a[176:179], v[76:79], v[32:35], a[176:179]\n\t" \
    "ds_read_b64_tr_b16 v[72:73], v96 offset:16384\n\t" \
    "v_mfma_f32_16x16x32_bf16 a[180:183], v[76:79], v[36:39], a[180:183]\n\t" \
    "ds_read_b64_tr_b16 v[74:75], v96 offset:17408\n\t" \
    "v_mfma_f32_16x16x32_bf16 a[184:187], v[76:79], v[40:43], a[184:187]\n\t" \
    "v_mfma_f32_16x16x32_bf16 a[188:191], v[76:79], v[44:47], a[188:191]\n\t" \
    "buffer_load_dwordx4 v107, s[12:15], s28 offen offset:1024 lds\n\t" \
    "s_waitcnt lgkmcnt(4)\n\t" \
    "v_mfma_f32_16x16x32_bf16 a[192:195], v[64:67], v[32:35], a[192:195]\n\t" \
    "ds_read_b64_tr_b16 v[76:77], v97 offset:16384\n\t" \
    "v_cvt_pk_bf16_f32 v148, v148, v149\n\t" \
    "v_mfma_f32_16x16x32_bf16 a[196:199], v[64:67], v[36:39], a[196:199]\n\t" \
    "ds_read_b64_tr_b16 v[78:79], v97 offset:17408\n\t" \
    "v_cvt_pk_bf16_f32 v149, v150, v151\n\t" \
    "v_mfma_f32_16x16x32_bf16 a[200:203], v[64:67], v[40:43], a[200:203]\n\t" \
    "buffer_load_dwordx4 v[24:27], v80, s[16:19], s34 offen offset:1024 sc1\n\t" \
    "v_cvt_pk_bf16_f32 v150, v180, v181\n\t" \
    "v_mfma_f32_16x16x32_bf16 a[204:207], v[64:67], v[44:47], a[204:207]\n\t" \
    "v_cvt_pk_bf16_f32 v151, v182, v183\n\t" \
    "s_add_u32 s36, s23, 16384\n\t" \
    "s_waitcnt lgkmcnt(4)\n\t" \
    "v_mfma_f32_16x16x32_bf16 a[208:211], v[68:71], v[32:35], a[208:211]\n\t" \
    "ds_read_b64_tr_b16 v[64:65], v90 offset:24576\n\t" \
    "buffer_store_dwordx4 v[148:151], v110, s[16:19], s36 offen offset:128 sc1\n\t" \
    "v_mfma_f32_16x16x32_bf16 a[212:215], v[68:71], v[36:39], a[212:215]\n\t" \
    "ds_read_b64_tr_b16 v[66:67], v90 offset:25600\n\t" \
    "v_mfma_f32_16x16x32_bf16 a[216:219], v[68:71], v[40:43], a[216:219]\n\t" \
    "v_mfma_f32_16x16x32_bf16 a[220:223], v[68:71], v[44:47], a[220:223]\n\t" \
    "buffer_load_dwordx4 v108, s[12:15], s28 offen offset:2048 lds\n\t" \
    "s_waitcnt lgkmcnt(4)\n\t" \
    "v_mfma_f32_16x16x32_bf16 a[224:227], v[72:75], v[32:35], a[224:227]\n\t" \
    "ds_read_b64_tr_b16 v[68:69], v91 offset:24576\n\t" \
    "v_mfma_f32_16x16x32_bf16 a[228:231], v[72:75], v[36:39], a[228:231]\n\t" \
    "ds_read_b64_tr_b16 v[70:71], v91 offset:25600\n\t" \
    "v_mfma_f32_16x16x32_bf16 a[232:235], v[72:75], v[40:43], a[232:235]\n\t" \
    "buffer_load_dwordx4 v[28:31], v80, s[16:19], s35 offen offset:1024 sc1\n\t" \
    "v_mfma_f32_16x16x32_bf16 a[236:239], v[72:75], v[44:47], a[236:239]\n\t" \
    "s_waitcnt lgkmcnt(4)\n\t" \
    "v_mfma_f32_16x16x32_bf16 a[240:243], v[76:79], v[32:35], a[240:243]\n\t" \
    "ds_read_b64_tr_b16 v[72:73], v92 offset:24576\n\t" \
    "v_mfma_f32_16x16x32_bf16 a[244:247], v[76:79], v[36:39], a[244:247]\n\t" \
    "ds_read_b64_tr_b16 v[74:75], v92 offset:25600\n\t" \
    "v_mfma_f32_16x16x32_bf16 a[248:251], v[76:79], v[40:43], a[248:251]\n\t" \
    "v_mfma_f32_16x16x32_bf16 a[252:255], v[76:79], v[44:47], a[252:255]\n\t" \
    "buffer_load_dwordx4 v109, s[12:15], s28 offen offset:3072 lds\n\t" \
    "s_waitcnt vmcnt(20) lgkmcnt(4)\n\t" \
    "v_mfma_f32_16x16x32_bf16 a[128:131], v[64:67], v[48:51], a[128:131]\n\t" \
    "ds_read_b64_tr_b16 v[76:77], v93 offset:24576\n\t" \
    "v_cvt_pk_bf16_f32 v152, v152, v153\n\t" \
    "s_waitcnt vmcnt(18)\n\t" \
    "v_mfma_f32_16x16x32_bf16 a[132:135], v[64:67], v[52:55], a[132:135]\n\t" \
    "ds_read_b64_tr_b16 v[78:79], v93 offset:25600\n\t" \
    "v_cvt_pk_bf16_f32 v153, v154, v155\n\t" \
    "s_waitcnt vmcnt(17)\n\t" \
    "v_mfma_f32_16x16x32_bf16 a[136:139], v[64:67], v[56:59], a[136:139]\n\t" \
    "buffer_load_dwordx4 v[32:35], v80, s[16:19], s32 offen offset:2048 sc1\n\t" \
    "v_cvt_pk_bf16_f32 v154, v184, v185\n\t" \
    "s_waitcnt vmcnt(17)\n\t" \
    "v_mfma_f32_16x16x32_bf16 a[140:143], v[64:67], v[60:63], a[140:143]\n\t" \
    "v_cvt_pk_bf16_f32 v155, v186, v187\n\t" \
    "s_add_u32 s36, s23, 24576\n\t" \
    "s_waitcnt lgkmcnt(4)\n\t" \
    "v_mfma_f32_16x16x32_bf16 a[144:147], v[68:71], v[48:51], a[144:147]\n\t" \
    "ds_read_b64_tr_b16 v[64:65], v94 offset:24576\n\t" \
    "buffer_store_dwordx4 v[152:155], v110, s[16:19], s36 offen offset:0 sc1\n\t" \
    "v_mfma_f32_16x16x32_bf16 a[148:151], v[68:71], v[52:55], a[148:151]\n\t" \
    "ds_read_b64_tr_b16 v[66:67], v94 offset:25600\n\t" \
    "v_mfma_f32_16x16x32_bf16 a[152:155], v[68:71], v[56:59], a[152:155]\n\t" \
    "v_mfma_f32_16x16x32_bf16 a[156:159], v[68:71], v[60:63], a[156:159]\n\t" \
    "s_waitcnt lgkmcnt(4)\n\t" \
    "v_mfma_f32_16x16x32_bf16 a[160:163], v[72:75], v[48:51], a[160:163]\n\t" \
    "ds_read_b64_tr_b16 v[68:69], v95 offset:24576\n\t" \
    "v_mfma_f32_16x16x32_bf16 a[164:167], v[72:75], v[52:55], a[164:167]\n\t" \
    "ds_read_b64_tr_b16 v[70:71], v95 offset:25600\n\t" \
    "v_mfma_f32_16x16x32_bf16 a[168:171], v[72:75], v[56:59], a[168:171]\n\t" \
    "buffer_load_dwordx4 v[36:39], v80, s[16:19], s33 offen offset:2048 sc1\n\t" \
    "v_mfma_f32_16x16x32_bf16 a[172:175], v[72:75], v[60:63], a[172:175]\n\t" \
    "s_waitcnt lgkmcnt(4)\n\t" \
    "v_mfma_f32_16x16x32_bf16 a[176:179], v[76:79], v[48:51], a[176:179]\n\t" \
    "ds_read_b64_tr_b16 v[72:73], v96 offset:24576\n\t" \
    "v_mfma_f32_16x16x32_bf16 a[180:183], v[76:79], v[52:55], a[180:183]\n\t" \
    "ds_read_b64_tr_b16 v[74:75], v96 offset:25600\n\t" \
    "v_mfma_f32_16x16x32_bf16 a[184:187], v[76:79], v[56:59], a[184:187]\n\t" \
    "v_mfma_f32_16x16x32_bf16 a[188:191], v[76:79], v[60:63], a[188:191]\n\t" \
    "s_waitcnt lgkmcnt(4)\n\t" \
    "v_mfma_f32_16x16x32_bf16 a[192:195], v[64:67], v[48:51], a[192:195]\n\t" \
    "ds_read_b64_tr_b16 v[76:77], v97 offset:24576\n\t" \
    "v_cvt_pk_bf16_f32 v156, v156, v157\n\t" \
    "v_mfma_f32_16x16x32_bf16 a[196:199], v[64:67], v[52:55], a[196:199]\n\t" \
    "ds_read_b64_tr_b16 v[78:79], v97 offset:25600\n\t" \
    "v_cvt_pk_bf16_f32 v157, v158, v159\n\t" \
    "v_mfma_f32_16x16x32_bf16 a[200:203], v[64:67], v[56:59], a[200:203]\n\t" \
    "buffer_load_dwordx4 v[40:43], v80, s[16:19], s34 offen offset:2048 sc1\n\t" \
    "v_cvt_pk_bf16_f32 v158, v188, v189\n\t" \
    "v_mfma_f32_16x16x32_bf16 a[204:207], v[64:67], v[60:63], a[204:207]\n\t" \
    "v_cvt_pk_bf16_f32 v159, v190, v191\n\t" \
    "s_add_u32 s36, s23, 24576\n\t" \
    "s_waitcnt vmcnt(46)\n\t" \
    "s_barrier\n\t" \
    "s_waitcnt lgkmcnt(4)\n\t" \
    "v_mfma_f32_16x16x32_bf16 a[208:211], v[68:71], v[48:51], a[208:211]\n\t" \
    "ds_read_b64_tr_b16 v[64:65], v82 offset:0\n\t" \
    "v_mfma_f32_16x16x32_bf16 a[212:215], v[68:71], v[52:55], a[212:215]\n\t" \
    "ds_read_b64_tr_b16 v[66:67], v82 offset:1024\n\t" \
    "buffer_store_dwordx4 v[156:159], v110, s[16:19], s36 offen offset:128 sc1\n\t" \
    "v_mfma_f32_16x16x32_bf16 a[216:219], v[68:71], v[56:59], a[216:219]\n\t" \
    "v_mfma_f32_16x16x32_bf16 a[220:223], v[68:71], v[60:63], a[220:223]\n\t" \
    "s_waitcnt lgkmcnt(4)\n\t" \
    "v_mfma_f32_16x16x32_bf16 a[224:227], v[72:75], v[48:51], a[224:227]\n\t" \
    "ds_read_b64_tr_b16 v[68:69], v83 offset:0\n\t" \
    "v_mfma_f32_16x16x32_bf16 a[228:231], v[72:75], v[52:55], a[228:231]\n\t" \
    "ds_read_b64_tr_b16 v[70:71], v83 offset:1024\n\t" \
    "v_mfma_f32_16x16x32_bf16 a[232:235], v[72:75], v[56:59], a[232:235]\n\t" \
    "buffer_load_dwordx4 v[44:47], v80, s[16:19], s35 offen offset:2048 sc1\n\t" \
    "v_mfma_f32_16x16x32_bf16 a[236:239], v[72:75], v[60:63], a[236:239]\n\t" \
    "s_waitcnt lgkmcnt(4)\n\t" \
    "v_mfma_f32_16x16x32_bf16 a[240:243], v[76:79], v[48:51], a[240:243]\n\t" \
    "ds_read_b64_tr_b16 v[72:73], v84 offset:0\n\t" \
    "v_mfma_f32_16x16x32_bf16 a[244:247], v[76:79], v[52:55], a[244:247]\n\t" \
    "ds_read_b64_tr_b16 v[74:75], v84 offset:1024\n\t" \
    "v_mfma_f32_16x16x32_bf16 a[248:251], v[76:79], v[56:59], a[248:251]\n\t" \
    "v_mfma_f32_16x16x32_bf16 a[252:255], v[76:79], v[60:63], a[252:255]\n\t" \
    "s_waitcnt vmcnt(21) lgkmcnt(4)\n\t" \
    "v_mfma_f32_16x16x32_bf16 a[128:131], v[64:67], v[0:3], a[128:131]\n\t" \
    "ds_read_b64_tr_b16 v[76:77], v85 offset:0\n\t" \
    "v_cvt_pk_bf16_f32 v192, v192, v193\n\t" \
    "s_waitcnt vmcnt(19)\n\t" \
    "v_mfma_f32_16x16x32_bf16 a[132:135], v[64:67], v[4:7], a[132:135]\n\t" \
    "ds_read_b64_tr_b16 v[78:79], v85 offset:1024\n\t" \
    "v_cvt_pk_bf16_f32 v193, v194, v195\n\t" \
    "s_waitcnt vmcnt(18)\n\t" \
    "v_mfma_f32_16x16x32_bf16 a[136:139], v[64:67], v[8:11], a[136:139]\n\t" \
    "buffer_load_dwordx4 v[48:51], v80, s[16:19], s32 offen offset:3072 sc1\n\t" \
    "v_cvt_pk_bf16_f32 v194, v224, v225\n\t" \
    "s_waitcnt vmcnt(17)\n\t" \
    "v_mfma_f32_16x16x32_bf16 a[140:143], v[64:67], v[12:15], a[140:143]\n\t" \
    "v_cvt_pk_bf16_f32 v195, v226, v227\n\t" \
    "s_add_u32 s36, s23, 0\n\t" \
    "s_waitcnt lgkmcnt(4)\n\t" \
    "v_mfma_f32_16x16x32_bf16 a[144:147], v[68:71], v[0:3], a[144:147]\n\t" \
    "ds_read_b64_tr_b16 v[64:65], v86 offset:0\n\t" \
    "buffer_store_dwordx4 v[192:195], v110, s[16:19], s36 offen offset:2048 sc1\n\t" \
    "v_mfma_f32_16x16x32_bf16 a[148:151], v[68:71], v[4:7], a[148:151]\n\t" \
    "ds_read_b64_tr_b16 v[66:67], v86 offset:1024\n\t" \
    "v_mfma_f32_16x16x32_bf16 a[152:155], v[68:71], v[8:11], a[152:155]\n\t" \
    "s_mov_b32 s28, 3145728\n\t" \
    "s_add_u32 m0, s26, 65536\n\t" \
    "v_mfma_f32_16x16x32_bf16 a[156:159], v[68:71], v[12:15], a[156:159]\n\t" \
    "buffer_load_dwordx4 v106, s[12:15], s28 offen offset:0 lds\n\t" \
    "s_waitcnt lgkmcnt(4)\n\t" \
    "v_mfma_f32_16x16x32_bf16 a[160:163], v[72:75], v[0:3], a[160:163]\n\t" \
    "ds_read_b64_tr_b16 v[68:69], v87 offset:0\n\t" \
    "v_mfma_f32_16x16x32_bf16 a[164:167], v[72:75], v[4:7], a[164:167]\n\t" \
    "ds_read_b64_tr_b16 v[70:71], v87 offset:1024\n\t" \
    "v_mfma_f32_16x16x32_bf16 a[168:171], v[72:75], v[8:11], a[168:171]\n\t" \
    "buffer_load_dwordx4 v[52:55], v80, s[16:19], s33 offen offset:3072 sc1\n\t" \
    "v_mfma_f32_16x16x32_bf16 a[172:175], v[72:75], v[12:15], a[172:175]\n\t" \
    "s_waitcnt lgkmcnt(4)\n\t" \
    "v_mfma_f32_16x16x32_bf16 a[176:179], v[76:79], v[0:3], a[176:179]\n\t" \
    "ds_read_b64_tr_b16 v[72:73], v88 offset:0\n\t" \
    "v_mfma_f32_16x16x32_bf16 a[180:183], v[76:79], v[4:7], a[180:183]\n\t" \
    "ds_read_b64_tr_b16 v[74:75], v88 offset:1024\n\t" \
    "v_mfma_f32_16x16x32_bf16 a[184:187], v[76:79], v[8:11], a[184:187]\n\t" \
    "v_mfma_f32_16x16x32_bf16 a[188:191], v[76:79], v[12:15], a[188:191]\n\t" \
    "buffer_load_dwordx4 v107, s[12:15], s28 offen offset:1024 lds\n\t" \
    "s_waitcnt lgkmcnt(4)\n\t" \
    "v_mfma_f32_16x16x32_bf16 a[192:195], v[64:67], v[0:3], a[192:195]\n\t" \
    "ds_read_b64_tr_b16 v[76:77], v89 offset:0\n\t" \
    "v_cvt_pk_bf16_f32 v196, v196, v197\n\t" \
    "v_mfma_f32_16x16x32_bf16 a[196:199], v[64:67], v[4:7], a[196:199]\n\t" \
    "ds_read_b64_tr_b16 v[78:79], v89 offset:1024\n\t" \
    "v_cvt_pk_bf16_f32 v197, v198, v199\n\t" \
    "v_mfma_f32_16x16x32_bf16 a[200:203], v[64:67], v[8:11], a[200:203]\n\t" \
    "buffer_load_dwordx4 v[56:59], v80, s[16:19], s34 offen offset:3072 sc1\n\t" \
    "v_cvt_pk_bf16_f32 v198, v228, v229\n\t" \
    "v_mfma_f32_16x16x32_bf16 a[204:207], v[64:67], v[12:15], a[204:207]\n\t" \
    "v_cvt_pk_bf16_f32 v199, v230, v231\n\t" \
    "s_add_u32 s36, s23, 0\n\t" \
    "s_waitcnt lgkmcnt(4)\n\t" \
    "v_mfma_f32_16x16x32_bf16 a[208:211], v[68:71], v[0:3], a[208:211]\n\t" \
    "ds_read_b64_tr_b16 v[64:65], v82 offset:8192\n\t" \
    "buffer_store_dwordx4 v[196:199], v110, s[16:19], s36 offen offset:2176 sc1\n\t" \
    "v_mfma_f32_16x16x32_bf16 a[212:215], v[68:71], v[4:7], a[212:215]\n\t" \
    "ds_read_b64_tr_b16 v[66:67], v82 offset:9216\n\t" \
    "v_mfma_f32_16x16x32_bf16 a[216:219], v[68:71], v[8:11], a[216:219]\n\t" \
    "v_mfma_f32_16x16x32_bf16 a[220:223], v[68:71], v[12:15], a[220:223]\n\t" \
    "buffer_load_dwordx4 v108, s[12:15], s28 offen offset:2048 lds\n\t" \
    "s_waitcnt lgkmcnt(4)\n\t" \
    "v_mfma_f32_16x16x32_bf16 a[224:227], v[72:75], v[0:3], a[224:227]\n\t" \
    "ds_read_b64_tr_b16 v[68:69], v83 offset:8192\n\t" \
    "v_mfma_f32_16x16x32_bf16 a[228:231], v[72:75], v[4:7], a[228:231]\n\t" \
    "ds_read_b64_tr_b16 v[70:71], v83 offset:9216\n\t" \
    "v_mfma_f32_16x16x32_bf16 a[232:235], v[72:75], v[8:11], a[232:235]\n\t" \
    "buffer_load_dwordx4 v[60:63], v80, s[16:19], s35 offen offset:3072 sc1\n\t" \
    "v_mfma_f32_16x16x32_bf16 a[236:239], v[72:75], v[12:15], a[236:239]\n\t" \
    "s_waitcnt lgkmcnt(4)\n\t" \
    "v_mfma_f32_16x16x32_bf16 a[240:243], v[76:79], v[0:3], a[240:243]\n\t" \
    "ds_read_b64_tr_b16 v[72:73], v84 offset:8192\n\t" \
    "v_mfma_f32_16x16x32_bf16 a[244:247], v[76:79], v[4:7], a[244:247]\n\t" \
    "ds_read_b64_tr_b16 v[74:75], v84 offset:9216\n\t" \
    "v_mfma_f32_16x16x32_bf16 a[248:251], v[76:79], v[8:11], a[248:251]\n\t" \
    "v_mfma_f32_16x16x32_bf16 a[252:255], v[76:79], v[12:15], a[252:255]\n\t" \
    "buffer_load_dwordx4 v109, s[12:15], s28 offen offset:3072 lds\n\t" \
    "s_waitcnt vmcnt(25) lgkmcnt(4)\n\t" \
    "v_mfma_f32_16x16x32_bf16 a[128:131], v[64:67], v[16:19], a[128:131]\n\t" \
    "ds_read_b64_tr_b16 v[76:77], v85 offset:8192\n\t" \
    "v_cvt_pk_bf16_f32 v200, v200, v201\n\t" \
    "s_waitcnt vmcnt(22)\n\t" \
    "v_mfma_f32_16x16x32_bf16 a[132:135], v[64:67], v[20:23], a[132:135]\n\t" \
    "ds_read_b64_tr_b16 v[78:79], v85 offset:9216\n\t" \
    "v_cvt_pk_bf16_f32 v201, v202, v203\n\t" \
    "s_add_u32 s32, s32, 4096\n\t" \
    "s_add_u32 s33, s33, 4096\n\t" \
    "s_add_u32 s34, s34, 4096\n\t" \
    "s_add_u32 s35, s35, 4096\n\t" \
    "s_waitcnt vmcnt(20)\n\t" \
    "v_mfma_f32_16x16x32_bf16 a[136:139], v[64:67], v[24:27], a[136:139]\n\t" \
    "buffer_load_dwordx4 v[0:3], v80, s[16:19], s32 offen offset:0 sc1\n\t" \
    "s_waitcnt vmcnt(18)\n\t" \
    "v_mfma_f32_16x16x32_bf16 a[140:143], v[64:67], v[28:31], a[140:143]\n\t" \
    "v_cvt_pk_bf16_f32 v202, v232, v233\n\t" \
    "v_cvt_pk_bf16_f32 v203, v234, v235\n\t" \
    "s_waitcnt lgkmcnt(4)\n\t" \
    "v_mfma_f32_16x16x32_bf16 a[144:147], v[68:71], v[16:19], a[144:147]\n\t" \
    "ds_read_b64_tr_b16 v[64:65], v86 offset:8192\n\t" \
    "s_add_u32 s36, s23, 8192\n\t" \
    "v_mfma_f32_16x16x32_bf16 a[148:151], v[68:71], v[20:23], a[148:151]\n\t" \
    "ds_read_b64_tr_b16 v[66:67], v86 offset:9216\n\t" \
    "buffer_store_dwordx4 v[200:203], v110, s[16:19], s36 offen offset:2048 sc1\n\t" \
    "v_mfma_f32_16x16x32_bf16 a[152:155], v[68:71], v[24:27], a[152:155]\n\t" \
    "v_mfma_f32_16x16x32_bf16 a[156:159], v[68:71], v[28:31], a[156:159]\n\t" \
    "s_waitcnt lgkmcnt(4)\n\t" \
    "v_mfma_f32_16x16x32_bf16 a[160:163], v[72:75], v[16:19], a[160:163]\n\t" \
    "ds_read_b64_tr_b16 v[68:69], v87 offset:8192\n\t" \
    "v_mfma_f32_16x16x32_bf16 a[164:167], v[72:75], v[20:23], a[164:167]\n\t" \
    "ds_read_b64_tr_b16 v[70:71], v87 offset:9216\n\t" \
    "v_mfma_f32_16x16x32_bf16 a[168:171], v[72:75], v[24:27], a[168:171]\n\t" \
    "buffer_load_dwordx4 v[4:7], v80, s[16:19], s33 offen offset:0 sc1\n\t" \
    "v_mfma_f32_16x16x32_bf16 a[172:175], v[72:75], v[28:31], a[172:175]\n\t" \
    "s_waitcnt lgkmcnt(4)\n\t" \
    "v_mfma_f32_16x16x32_bf16 a[176:179], v[76:79], v[16:19], a[176:179]\n\t" \
    "ds_read_b64_tr_b16 v[72:73], v88 offset:8192\n\t" \
    "v_mfma_f32_16x16x32_bf16 a[180:183], v[76:79], v[20:23], a[180:183]\n\t" \
    "ds_read_b64_tr_b16 v[74:75], v88 offset:9216\n\t" \
    "v_mfma_f32_16x16x32_bf16 a[184:187], v[76:79], v[24:27], a[184:187]\n\t" \
    "v_mfma_f32_16x16x32_bf16 a[188:191], v[76:79], v[28:31], a[188:191]\n\t" \
    "s_waitcnt lgkmcnt(4)\n\t" \
    "v_mfma_f32_16x16x32_bf16 a[192:195], v[64:67], v[16:19], a[192:195]\n\t" \
    "ds_read_b64_tr_b16 v[76:77], v89 offset:8192\n\t" \
    "v_cvt_pk_bf16_f32 v204, v204, v205\n\t" \
    "v_mfma_f32_16x16x32_bf16 a[196:199], v[64:67], v[20:23], a[196:199]\n\t" \
    "ds_read_b64_tr_b16 v[78:79], v89 offset:9216\n\t" \
    "v_cvt_pk_bf16_f32 v205, v206, v207\n\t" \
    "v_mfma_f32_16x16x32_bf16 a[200:203], v[64:67], v[24:27], a[200:203]\n\t" \
    "buffer_load_dwordx4 v[8:11], v80, s[16:19], s34 offen offset:0 sc1\n\t" \
    "v_cvt_pk_bf16_f32 v206, v236, v237\n\t" \
    "v_mfma_f32_16x16x32_bf16 a[204:207], v[64:67], v[28:31], a[204:207]\n\t" \
    "v_cvt_pk_bf16_f32 v207, v238, v239\n\t" \
    "s_add_u32 s36, s23, 8192\n\t" \
    "s_waitcnt vmcnt(58)\n\t" \
    "s_barrier\n\t" \
    "s_waitcnt lgkmcnt(4)\n\t" \
    "v_mfma_f32_16x16x32_bf16 a[208:211], v[68:71], v[16:19], a[208:211]\n\t" \
    "ds_read_b64_tr_b16 v[64:65], v82 offset:16384\n\t" \
    "v_mfma_f32_16x16x32_bf16 a[212:215], v[68:71], v[20:23], a[212:215]\n\t" \
    "ds_read_b64_tr_b16 v[66:67], v82 offset:17408\n\t" \
    "buffer_store_dwordx4 v[204:207], v110, s[16:19], s36 offen offset:2176 sc1\n\t" \
    "v_mfma_f32_16x16x32_bf16 a[216:219], v[68:71], v[24:27], a[216:219]\n\t" \
    "v_mfma_f32_16x16x32_bf16 a[220:223], v[68:71], v[28:31], a[220:223]\n\t" \
    "s_waitcnt lgkmcnt(4)\n\t" \
    "v_mfma_f32_16x16x32_bf16 a[224:227], v[72:75], v[16:19], a[224:227]\n\t" \
    "ds_read_b64_tr_b16 v[68:69], v83 offset:16384\n\t" \
    "v_mfma_f32_16x16x32_bf16 a[228:231], v[72:75], v[20:23], a[228:231]\n\t" \
    "ds_read_b64_tr_b16 v[70:71], v83 offset:17408\n\t" \
    "v_mfma_f32_16x16x32_bf16 a[232:235], v[72:75], v[24:27], a[232:235]\n\t" \
    "buffer_load_dwordx4 v[12:15], v80, s[16:19], s35 offen offset:0 sc1\n\t" \
    "v_mfma_f32_16x16x32_bf16 a[236:239], v[72:75], v[28:31], a[236:239]\n\t" \
    "s_waitcnt lgkmcnt(4)\n\t" \
    "v_mfma_f32_16x16x32_bf16 a[240:243], v[76:79], v[16:19], a[240:243]\n\t" \
    "ds_read_b64_tr_b16 v[72:73], v84 offset:16384\n\t" \
    "v_mfma_f32_16x16x32_bf16 a[244:247], v[76:79], v[20:23], a[244:247]\n\t" \
    "ds_read_b64_tr_b16 v[74:75], v84 offset:17408\n\t" \
    "v_mfma_f32_16x16x32_bf16 a[248:251], v[76:79], v[24:27], a[248:251]\n\t" \
    "v_mfma_f32_16x16x32_bf16 a[252:255], v[76:79], v[28:31], a[252:255]\n\t" \
    "s_waitcnt vmcnt(21) lgkmcnt(4)\n\t" \
    "v_mfma_f32_16x16x32_bf16 a[128:131], v[64:67], v[32:35], a[128:131]\n\t" \
    "ds_read_b64_tr_b16 v[76:77], v85 offset:16384\n\t" \
    "v_cvt_pk_bf16_f32 v208, v208, v209\n\t" \
    "s_waitcnt vmcnt(19)\n\t" \
    "v_mfma_f32_16x16x32_bf16 a[132:135], v[64:67], v[36:39], a[132:135]\n\t" \
    "ds_read_b64_tr_b16 v[78:79], v85 offset:17408\n\t" \
    "v_cvt_pk_bf16_f32 v209, v210, v211\n\t" \
    "s_waitcnt vmcnt(18)\n\t" \
    "v_mfma_f32_16x16x32_bf16 a[136:139], v[64:67], v[40:43], a[136:139]\n\t" \
    "buffer_load_dwordx4 v[16:19], v80, s[16:19], s32 offen offset:1024 sc1\n\t" \
    "v_cvt_pk_bf16_f32 v210, v240, v241\n\t" \
    "s_waitcnt vmcnt(17)\n\t" \
    "v_mfma_f32_16x16x32_bf16 a[140:143], v[64:67], v[44:47], a[140:143]\n\t" \
    "v_cvt_pk_bf16_f32 v211, v242, v243\n\t" \
    "s_add_u32 s36, s23, 16384\n\t" \
    "s_waitcnt lgkmcnt(4)\n\t" \
    "v_mfma_f32_16x16x32_bf16 a[144:147], v[68:71], v[32:35], a[144:147]\n\t" \
    "ds_read_b64_tr_b16 v[64:65], v86 offset:16384\n\t" \
    "buffer_store_dwordx4 v[208:211], v110, s[16:19], s36 offen offset:2048 sc1\n\t" \
    "v_mfma_f32_16x16x32_bf16 a[148:151], v[68:71], v[36:39], a[148:151]\n\t" \
    "ds_read_b64_tr_b16 v[66:67], v86 offset:17408\n\t" \
    "v_mfma_f32_16x16x32_bf16 a[152:155], v[68:71], v[40:43], a[152:155]\n\t" \
    "s_mov_b32 s28, 3670016\n\t" \
    "s_add_u32 m0, s26, 81920\n\t" \
    "v_mfma_f32_16x16x32_bf16 a[156:159], v[68:71], v[44:47], a[156:159]\n\t" \
    "buffer_load_dwordx4 v106, s[12:15], s28 offen offset:0 lds\n\t" \
    "s_waitcnt lgkmcnt(4)\n\t" \
    "v_mfma_f32_16x16x32_bf16 a[160:163], v[72:75], v[32:35], a[160:163]\n\t" \
    "ds_read_b64_tr_b16 v[68:69], v87 offset:16384\n\t" \
    "v_mfma_f32_16x16x32_bf16 a[164:167], v[72:75], v[36:39], a[164:167]\n\t" \
    "ds_read_b64_tr_b16 v[70:71], v87 offset:17408\n\t" \
    "v_mfma_f32_16x16x32_bf16 a[168:171], v[72:75], v[40:43], a[168:171]\n\t" \
    "buffer_load_dwordx4 v[20:23], v80, s[16:19], s33 offen offset:1024 sc1\n\t" \
    "v_mfma_f32_16x16x32_bf16 a[172:175], v[72:75], v[44:47], a[172:175]\n\t" \
    "s_waitcnt lgkmcnt(4)\n\t" \
    "v_mfma_f32_16x16x32_bf16 a[176:179], v[76:79], v[32:35], a[176:179]\n\t" \
    "ds_read_b64_tr_b16 v[72:73], v88 offset:16384\n\t" \
    "v_mfma_f32_16x16x32_bf16 a[180:183], v[76:79], v[36:39], a[180:183]\n\t" \
    "ds_read_b64_tr_b16 v[74:75], v88 offset:17408\n\t" \
    "v_mfma_f32_16x16x32_bf16 a[184:187], v[76:79], v[40:43], a[184:187]\n\t" \
    "v_mfma_f32_16x16x32_bf16 a[188:191], v[76:79], v[44:47], a[188:191]\n\t" \
    "buffer_load_dwordx4 v107, s[12:15], s28 offen offset:1024 lds\n\t" \
    "s_waitcnt lgkmcnt(4)\n\t" \
    "v_mfma_f32_16x16x32_bf16 a[192:195], v[64:67], v[32:35], a[192:195]\n\t" \
    "ds_read_b64_tr_b16 v[76:77], v89 offset:16384\n\t" \
    "v_cvt_pk_bf16_f32 v212, v212, v213\n\t" \
    "v_mfma_f32_16x16x32_bf16 a[196:199], v[64:67], v[36:39], a[196:199]\n\t" \
    "ds_read_b64_tr_b16 v[78:79], v89 offset:17408\n\t" \
    "v_cvt_pk_bf16_f32 v213, v214, v215\n\t" \
    "v_mfma_f32_16x16x32_bf16 a[200:203], v[64:67], v[40:43], a[200:203]\n\t" \
    "buffer_load_dwordx4 v[24:27], v80, s[16:19], s34 offen offset:1024 sc1\n\t" \
    "v_cvt_pk_bf16_f32 v214, v244, v245\n\t" \
    "v_mfma_f32_16x16x32_bf16 a[204:207], v[64:67], v[44:47], a[204:207]\n\t" \
    "v_cvt_pk_bf16_f32 v215, v246, v247\n\t" \
    "s_add_u32 s36, s23, 16384\n\t" \
    "s_waitcnt lgkmcnt(4)\n\t" \
    "v_mfma_f32_16x16x32_bf16 a[208:211], v[68:71], v[32:35], a[208:211]\n\t" \
    "ds_read_b64_tr_b16 v[64:65], v82 offset:24576\n\t" \
    "buffer_store_dwordx4 v[212:215], v110, s[16:19], s36 offen offset:2176 sc1\n\t" \
    "v_mfma_f32_16x16x32_bf16 a[212:215], v[68:71], v[36:39], a[212:215]\n\t" \
    "ds_read_b64_tr_b16 v[66:67], v82 offset:25600\n\t" \
    "v_mfma_f32_16x16x32_bf16 a[216:219], v[68:71], v[40:43], a[216:219]\n\t" \
    "v_mfma_f32_16x16x32_bf16 a[220:223], v[68:71], v[44:47], a[220:223]\n\t" \
    "buffer_load_dwordx4 v108, s[12:15], s28 offen offset:2048 lds\n\t" \
    "s_waitcnt lgkmcnt(4)\n\t" \
    "v_mfma_f32_16x16x32_bf16 a[224:227], v[72:75], v[32:35], a[224:227]\n\t" \
    "ds_read_b64_tr_b16 v[68:69], v83 offset:24576\n\t" \
    "v_mfma_f32_16x16x32_bf16 a[228:231], v[72:75], v[36:39], a[228:231]\n\t" \
    "ds_read_b64_tr_b16 v[70:71], v83 offset:25600\n\t" \
    "v_mfma_f32_16x16x32_bf16 a[232:235], v[72:75], v[40:43], a[232:235]\n\t" \
    "buffer_load_dwordx4 v[28:31], v80, s[16:19], s35 offen offset:1024 sc1\n\t" \
    "v_mfma_f32_16x16x32_bf16 a[236:239], v[72:75], v[44:47], a[236:239]\n\t" \
    "s_waitcnt lgkmcnt(4)\n\t" \
    "v_mfma_f32_16x16x32_bf16 a[240:243], v[76:79], v[32:35], a[240:243]\n\t" \
    "ds_read_b64_tr_b16 v[72:73], v84 offset:24576\n\t" \
    "v_mfma_f32_16x16x32_bf16 a[244:247], v[76:79], v[36:39], a[244:247]\n\t" \
    "ds_read_b64_tr_b16 v[74:75], v84 offset:25600\n\t" \
    "v_mfma_f32_16x16x32_bf16 a[248:251], v[76:79], v[40:43], a[248:251]\n\t" \
    "v_mfma_f32_16x16x32_bf16 a[252:255], v[76:79], v[44:47], a[252:255]\n\t" \
    "buffer_load_dwordx4 v109, s[12:15], s28 offen offset:3072 lds\n\t" \
    "s_waitcnt vmcnt(25) lgkmcnt(4)\n\t" \
    "v_mfma_f32_16x16x32_bf16 a[128:131], v[64:67], v[48:51], a[128:131]\n\t" \
    "ds_read_b64_tr_b16 v[76:77], v85 offset:24576\n\t" \
    "v_cvt_pk_bf16_f32 v216, v216, v217\n\t" \
    "s_waitcnt vmcnt(22)\n\t" \
    "v_mfma_f32_16x16x32_bf16 a[132:135], v[64:67], v[52:55], a[132:135]\n\t" \
    "ds_read_b64_tr_b16 v[78:79], v85 offset:25600\n\t" \
    "v_cvt_pk_bf16_f32 v217, v218, v219\n\t" \
    "s_waitcnt vmcnt(20)\n\t" \
    "v_mfma_f32_16x16x32_bf16 a[136:139], v[64:67], v[56:59], a[136:139]\n\t" \
    "buffer_load_dwordx4 v[32:35], v80, s[16:19], s32 offen offset:2048 sc1\n\t" \
    "v_cvt_pk_bf16_f32 v218, v248, v249\n\t" \
    "s_waitcnt vmcnt(18)\n\t" \
    "v_mfma_f32_16x16x32_bf16 a[140:143], v[64:67], v[60:63], a[140:143]\n\t" \
    "v_cvt_pk_bf16_f32 v219, v250, v251\n\t" \
    "s_add_u32 s36, s23, 24576\n\t" \
    "s_waitcnt lgkmcnt(4)\n\t" \
    "v_mfma_f32_16x16x32_bf16 a[144:147], v[68:71], v[48:51], a[144:147]\n\t" \
    "ds_read_b64_tr_b16 v[64:65], v86 offset:24576\n\t" \
    "buffer_store_dwordx4 v[216:219], v110, s[16:19], s36 offen offset:2048 sc1\n\t" \
    "v_mfma_f32_16x16x32_bf16 a[148:151], v[68:71], v[52:55], a[148:151]\n\t" \
    "ds_read_b64_tr_b16 v[66:67], v86 offset:25600\n\t" \
    "v_mfma_f32_16x16x32_bf16 a[152:155], v[68:71], v[56:59], a[152:155]\n\t" \
    "v_mfma_f32_16x16x32_bf16 a[156:159], v[68:71], v[60:63], a[156:159]\n\t" \
    "s_waitcnt lgkmcnt(4)\n\t" \
    "v_mfma_f32_16x16x32_bf16 a[160:163], v[72:75], v[48:51], a[160:163]\n\t" \
    "ds_read_b64_tr_b16 v[68:69], v87 offset:24576\n\t" \
    "v_mfma_f32_16x16x32_bf16 a[164:167], v[72:75], v[52:55], a[164:167]\n\t" \
    "ds_read_b64_tr_b16 v[70:71], v87 offset:25600\n\t" \
    "v_mfma_f32_16x16x32_bf16 a[168:171], v[72:75], v[56:59], a[168:171]\n\t" \
    "buffer_load_dwordx4 v[36:39], v80, s[16:19], s33 offen offset:2048 sc1\n\t" \
    "v_mfma_f32_16x16x32_bf16 a[172:175], v[72:75], v[60:63], a[172:175]\n\t" \
    "s_waitcnt lgkmcnt(4)\n\t" \
    "v_mfma_f32_16x16x32_bf16 a[176:179], v[76:79], v[48:51], a[176:179]\n\t" \
    "ds_read_b64_tr_b16 v[72:73], v88 offset:24576\n\t" \
    "v_mfma_f32_16x16x32_bf16 a[180:183], v[76:79], v[52:55], a[180:183]\n\t" \
    "ds_read_b64_tr_b16 v[74:75], v88 offset:25600\n\t" \
    "v_mfma_f32_16x16x32_bf16 a[184:187], v[76:79], v[56:59], a[184:187]\n\t" \
    "v_mfma_f32_16x16x32_bf16 a[188:191], v[76:79], v[60:63], a[188:191]\n\t" \
    "s_waitcnt lgkmcnt(4)\n\t" \
    "v_mfma_f32_16x16x32_bf16 a[192:195], v[64:67], v[48:51], a[192:195]\n\t" \
    "ds_read_b64_tr_b16 v[76:77], v89 offset:24576\n\t" \
    "v_cvt_pk_bf16_f32 v220, v220, v221\n\t" \
    "v_mfma_f32_16x16x32_bf16 a[196:199], v[64:67], v[52:55], a[196:199]\n\t" \
    "ds_read_b64_tr_b16 v[78:79], v89 offset:25600\n\t" \
    "v_cvt_pk_bf16_f32 v221, v222, v223\n\t" \
    "v_mfma_f32_16x16x32_bf16 a[200:203], v[64:67], v[56:59], a[200:203]\n\t" \
    "buffer_load_dwordx4 v[40:43], v80, s[16:19], s34 offen offset:2048 sc1\n\t" \
    "v_cvt_pk_bf16_f32 v222, v252, v253\n\t" \
    "v_mfma_f32_16x16x32_bf16 a[204:207], v[64:67], v[60:63], a[204:207]\n\t" \
    "v_cvt_pk_bf16_f32 v223, v254, v255\n\t" \
    "s_add_u32 s36, s23, 24576\n\t" \
    "s_waitcnt vmcnt(62)\n\t" \
    "s_barrier\n\t" \
    "s_waitcnt lgkmcnt(4)\n\t" \
    "v_mfma_f32_16x16x32_bf16 a[208:211], v[68:71], v[48:51], a[208:211]\n\t" \
    "ds_read_b64_tr_b16 v[64:65], v82 offset:32768\n\t" \
    "v_mfma_f32_16x16x32_bf16 a[212:215], v[68:71], v[52:55], a[212:215]\n\t" \
    "ds_read_b64_tr_b16 v[66:67], v82 offset:33792\n\t" \
    "buffer_store_dwordx4 v[220:223], v110, s[16:19], s36 offen offset:2176 sc1\n\t" \
    "v_mfma_f32_16x16x32_bf16 a[216:219], v[68:71], v[56:59], a[216:219]\n\t" \
    "v_mfma_f32_16x16x32_bf16 a[220:223], v[68:71], v[60:63], a[220:223]\n\t" \
    "s_waitcnt lgkmcnt(4)\n\t" \
    "v_mfma_f32_16x16x32_bf16 a[224:227], v[72:75], v[48:51], a[224:227]\n\t" \
    "ds_read_b64_tr_b16 v[68:69], v83 offset:32768\n\t" \
    "v_mfma_f32_16x16x32_bf16 a[228:231], v[72:75], v[52:55], a[228:231]\n\t" \
    "ds_read_b64_tr_b16 v[70:71], v83 offset:33792\n\t" \
    "v_mfma_f32_16x16x32_bf16 a[232:235], v[72:75], v[56:59], a[232:235]\n\t" \
    "buffer_load_dwordx4 v[44:47], v80, s[16:19], s35 offen offset:2048 sc1\n\t" \
    "v_mfma_f32_16x16x32_bf16 a[236:239], v[72:75], v[60:63], a[236:239]\n\t" \
    "s_waitcnt lgkmcnt(4)\n\t" \
    "v_mfma_f32_16x16x32_bf16 a[240:243], v[76:79], v[48:51], a[240:243]\n\t" \
    "ds_read_b64_tr_b16 v[72:73], v84 offset:32768\n\t" \
    "v_mfma_f32_16x16x32_bf16 a[244:247], v[76:79], v[52:55], a[244:247]\n\t" \
    "ds_read_b64_tr_b16 v[74:75], v84 offset:33792\n\t" \
    "v_mfma_f32_16x16x32_bf16 a[248:251], v[76:79], v[56:59], a[248:251]\n\t" \
    "v_mfma_f32_16x16x32_bf16 a[252:255], v[76:79], v[60:63], a[252:255]\n\t" \
    "s_waitcnt vmcnt(21) lgkmcnt(4)\n\t" \
    "v_mfma_f32_16x16x32_bf16 a[128:131], v[64:67], v[0:3], a[128:131]\n\t" \
    "ds_read_b64_tr_b16 v[76:77], v85 offset:32768\n\t" \
    "v_accvgpr_read_b32 v116, a0\n\t" \
    "s_waitcnt vmcnt(19)\n\t" \
    "v_mfma_f32_16x16x32_bf16 a[132:135], v[64:67], v[4:7], a[132:135]\n\t" \
    "ds_read_b64_tr_b16 v[78:79], v85 offset:33792\n\t" \
    "v_accvgpr_read_b32 v117, a1\n\t" \
    "s_waitcnt vmcnt(18)\n\t" \
    "v_mfma_f32_16x16x32_bf16 a[136:139], v[64:67], v[8:11], a[136:139]\n\t" \
    "buffer_load_dwordx4 v[48:51], v80, s[16:19], s32 offen offset:3072 sc1\n\t" \
    "v_accvgpr_read_b32 v118, a2\n\t" \
    "s_waitcnt vmcnt(17)\n\t" \
    "v_mfma_f32_16x16x32_bf16 a[140:143], v[64:67], v[12:15], a[140:143]\n\t" \
    "v_accvgpr_read_b32 v119, a3\n\t" \
    "v_accvgpr_read_b32 v120, a32\n\t" \
    "s_waitcnt lgkmcnt(4)\n\t" \
    "v_mfma_f32_16x16x32_bf16 a[144:147], v[68:71], v[0:3], a[144:147]\n\t" \
    "ds_read_b64_tr_b16 v[64:65], v86 offset:32768\n\t" \
    "v_accvgpr_read_b32 v121, a33\n\t" \
    "v_mfma_f32_16x16x32_bf16 a[148:151], v[68:71], v[4:7], a[148:151]\n\t" \
    "ds_read_b64_tr_b16 v[66:67], v86 offset:33792\n\t" \
    "v_accvgpr_read_b32 v122, a34\n\t" \
    "v_mfma_f32_16x16x32_bf16 a[152:155], v[68:71], v[8:11], a[152:155]\n\t" \
    "v_accvgpr_read_b32 v123, a35\n\t" \
    "v_cvt_pk_bf16_f32 v116, v116, v117\n\t" \
    "v_mfma_f32_16x16x32_bf16 a[156:159], v[68:71], v[12:15], a[156:159]\n\t" \
    "v_cvt_pk_bf16_f32 v117, v118, v119\n\t" \
    "v_cvt_pk_bf16_f32 v118, v120, v121\n\t" \
    "s_waitcnt lgkmcnt(4)\n\t" \
    "v_mfma_f32_16x16x32_bf16 a[160:163], v[72:75], v[0:3], a[160:163]\n\t" \
    "ds_read_b64_tr_b16 v[68:69], v87 offset:32768\n\t" \
    "v_cvt_pk_bf16_f32 v119, v122, v123\n\t" \
    "v_mfma_f32_16x16x32_bf16 a[164:167], v[72:75], v[4:7], a[164:167]\n\t" \
    "ds_read_b64_tr_b16 v[70:71], v87 offset:33792\n\t" \
    "s_add_u32 s36, s23, 4096\n\t" \
    "v_mfma_f32_16x16x32_bf16 a[168:171], v[72:75], v[8:11], a[168:171]\n\t" \
    "buffer_load_dwordx4 v[52:55], v80, s[16:19], s33 offen offset:3072 sc1\n\t" \
    "buffer_store_dwordx4 v[116:119], v110, s[16:19], s36 offen offset:0 sc1\n\t" \
    "v_mfma_f32_16x16x32_bf16 a[172:175], v[72:75], v[12:15], a[172:175]\n\t" \
    "s_waitcnt lgkmcnt(4)\n\t" \
    "v_mfma_f32_16x16x32_bf16 a[176:179], v[76:79], v[0:3], a[176:179]\n\t" \
    "ds_read_b64_tr_b16 v[72:73], v88 offset:32768\n\t" \
    "v_mfma_f32_16x16x32_bf16 a[180:183], v[76:79], v[4:7], a[180:183]\n\t" \
    "ds_read_b64_tr_b16 v[74:75], v88 offset:33792\n\t" \
    "v_mfma_f32_16x16x32_bf16 a[184:187], v[76:79], v[8:11], a[184:187]\n\t" \
    "v_mfma_f32_16x16x32_bf16 a[188:191], v[76:79], v[12:15], a[188:191]\n\t" \
    "s_waitcnt lgkmcnt(4)\n\t" \
    "v_mfma_f32_16x16x32_bf16 a[192:195], v[64:67], v[0:3], a[192:195]\n\t" \
    "ds_read_b64_tr_b16 v[76:77], v89 offset:32768\n\t" \
    "v_accvgpr_read_b32 v98, a4\n\t" \
    "v_mfma_f32_16x16x32_bf16 a[196:199], v[64:67], v[4:7], a[196:199]\n\t" \
    "ds_read_b64_tr_b16 v[78:79], v89 offset:33792\n\t" \
    "v_accvgpr_read_b32 v99, a5\n\t" \
    "v_mfma_f32_16x16x32_bf16 a[200:203], v[64:67], v[8:11], a[200:203]\n\t" \
    "buffer_load_dwordx4 v[56:59], v80, s[16:19], s34 offen offset:3072 sc1\n\t" \
    "v_accvgpr_read_b32 v100, a6\n\t" \
    "v_mfma_f32_16x16x32_bf16 a[204:207], v[64:67], v[12:15], a[204:207]\n\t" \
    "v_accvgpr_read_b32 v101, a7\n\t" \
    "v_accvgpr_read_b32 v102, a36\n\t" \
    "s_waitcnt lgkmcnt(4)\n\t" \
    "v_mfma_f32_16x16x32_bf16 a[208:211], v[68:71], v[0:3], a[208:211]\n\t" \
    "ds_read_b64_tr_b16 v[64:65], v82 offset:40960\n\t" \
    "v_accvgpr_read_b32 v103, a37\n\t" \
    "v_mfma_f32_16x16x32_bf16 a[212:215], v[68:71], v[4:7], a[212:215]\n\t" \
    "ds_read_b64_tr_b16 v[66:67], v82 offset:41984\n\t" \
    "v_accvgpr_read_b32 v104, a38\n\t" \
    "v_mfma_f32_16x16x32_bf16 a[216:219], v[68:71], v[8:11], a[216:219]\n\t" \
    "v_accvgpr_read_b32 v105, a39\n\t" \
    "v_cvt_pk_bf16_f32 v98, v98, v99\n\t" \
    "v_mfma_f32_16x16x32_bf16 a[220:223], v[68:71], v[12:15], a[220:223]\n\t" \
    "v_cvt_pk_bf16_f32 v99, v100, v101\n\t" \
    "v_cvt_pk_bf16_f32 v100, v102, v103\n\t" \
    "s_waitcnt lgkmcnt(4)\n\t" \
    "v_mfma_f32_16x16x32_bf16 a[224:227], v[72:75], v[0:3], a[224:227]\n\t" \
    "ds_read_b64_tr_b16 v[68:69], v83 offset:40960\n\t" \
    "v_cvt_pk_bf16_f32 v101, v104, v105\n\t" \
    "v_mfma_f32_16x16x32_bf16 a[228:231], v[72:75], v[4:7], a[228:231]\n\t" \
    "ds_read_b64_tr_b16 v[70:71], v83 offset:41984\n\t" \
    "s_add_u32 s36, s23, 4096\n\t" \
    "v_mfma_f32_16x16x32_bf16 a[232:235], v[72:75], v[8:11], a[232:235]\n\t" \
    "buffer_load_dwordx4 v[60:63], v80, s[16:19], s35 offen offset:3072 sc1\n\t" \
    "buffer_store_dwordx4 v[98:101], v110, s[16:19], s36 offen offset:128 sc1\n\t" \
    "v_mfma_f32_16x16x32_bf16 a[236:239], v[72:75], v[12:15], a[236:239]\n\t" \
    "s_waitcnt lgkmcnt(4)\n\t" \
    "v_mfma_f32_16x16x32_bf16 a[240:243], v[76:79], v[0:3], a[240:243]\n\t" \
    "ds_read_b64_tr_b16 v[72:73], v84 offset:40960\n\t" \
    "v_mfma_f32_16x16x32_bf16 a[244:247], v[76:79], v[4:7], a[244:247]\n\t" \
    "ds_read_b64_tr_b16 v[74:75], v84 offset:41984\n\t" \
    "v_mfma_f32_16x16x32_bf16 a[248:251], v[76:79], v[8:11], a[248:251]\n\t" \
    "v_mfma_f32_16x16x32_bf16 a[252:255], v[76:79], v[12:15], a[252:255]\n\t" \
    "s_waitcnt vmcnt(21) lgkmcnt(4)\n\t" \
    "v_mfma_f32_16x16x32_bf16 a[128:131], v[64:67], v[16:19], a[128:131]\n\t" \
    "ds_read_b64_tr_b16 v[76:77], v85 offset:40960\n\t" \
    "v_accvgpr_read_b32 v116, a8\n\t" \
    "s_waitcnt vmcnt(18)\n\t" \
    "v_mfma_f32_16x16x32_bf16 a[132:135], v[64:67], v[20:23], a[132:135]\n\t" \
    "ds_read_b64_tr_b16 v[78:79], v85 offset:41984\n\t" \
    "v_accvgpr_read_b32 v117, a9\n\t" \
    "s_add_u32 s32, s32, 4096\n\t" \
    "s_add_u32 s33, s33, 4096\n\t" \
    "s_add_u32 s34, s34, 4096\n\t" \
    "s_add_u32 s35, s35, 4096\n\t" \
    "s_waitcnt vmcnt(16)\n\t" \
    "v_mfma_f32_16x16x32_bf16 a[136:139], v[64:67], v[24:27], a[136:139]\n\t" \
    "buffer_load_dwordx4 v[0:3], v80, s[16:19], s32 offen offset:0 sc1\n\t" \
    "s_waitcnt vmcnt(14)\n\t" \
    "v_mfma_f32_16x16x32_bf16 a[140:143], v[64:67], v[28:31], a[140:143]\n\t" \
    "v_accvgpr_read_b32 v118, a10\n\t" \
    "v_accvgpr_read_b32 v119, a11\n\t" \
    "s_waitcnt lgkmcnt(4)\n\t" \
    "v_mfma_f32_16x16x32_bf16 a[144:147], v[68:71], v[16:19], a[144:147]\n\t" \
    "ds_read_b64_tr_b16 v[64:65], v86 offset:40960\n\t" \
    "v_accvgpr_read_b32 v120, a40\n\t" \
    "v_mfma_f32_16x16x32_bf16 a[148:151], v[68:71], v[20:23], a[148:151]\n\t" \
    "ds_read_b64_tr_b16 v[66:67], v86 offset:41984\n\t" \
    "v_accvgpr_read_b32 v121, a41\n\t" \
    "v_mfma_f32_16x16x32_bf16 a[152:155], v[68:71], v[24:27], a[152:155]\n\t" \
    "v_accvgpr_read_b32 v122, a42\n\t" \
    "v_accvgpr_read_b32 v123, a43\n\t" \
    "v_mfma_f32_16x16x32_bf16 a[156:159], v[68:71], v[28:31], a[156:159]\n\t" \
    "v_cvt_pk_bf16_f32 v116, v116, v117\n\t" \
    "v_cvt_pk_bf16_f32 v117, v118, v119\n\t" \
    "s_waitcnt lgkmcnt(4)\n\t" \
    "v_mfma_f32_16x16x32_bf16 a[160:163], v[72:75], v[16:19], a[160:163]\n\t" \
    "ds_read_b64_tr_b16 v[68:69], v87 offset:40960\n\t" \
    "v_cvt_pk_bf16_f32 v118, v120, v121\n\t" \
    "v_mfma_f32_16x16x32_bf16 a[164:167], v[72:75], v[20:23], a[164:167]\n\t" \
    "ds_read_b64_tr_b16 v[70:71], v87 offset:41984\n\t" \
    "v_cvt_pk_bf16_f32 v119, v122, v123\n\t" \
    "v_mfma_f32_16x16x32_bf16 a[168:171], v[72:75], v[24:27], a[168:171]\n\t" \
    "buffer_load_dwordx4 v[4:7], v80, s[16:19], s33 offen offset:0 sc1\n\t" \
    "s_add_u32 s36, s23, 12288\n\t" \
    "v_mfma_f32_16x16x32_bf16 a[172:175], v[72:75], v[28:31], a[172:175]\n\t" \
    "buffer_store_dwordx4 v[116:119], v110, s[16:19], s36 offen offset:0 sc1\n\t" \
    "s_waitcnt lgkmcnt(4)\n\t" \
    "v_mfma_f32_16x16x32_bf16 a[176:179], v[76:79], v[16:19], a[176:179]\n\t" \
    "ds_read_b64_tr_b16 v[72:73], v88 offset:40960\n\t" \
    "v_mfma_f32_16x16x32_bf16 a[180:183], v[76:79], v[20:23], a[180:183]\n\t" \
    "ds_read_b64_tr_b16 v[74:75], v88 offset:41984\n\t" \
    "v_mfma_f32_16x16x32_bf16 a[184:187], v[76:79], v[24:27], a[184:187]\n\t" \
    "v_mfma_f32_16x16x32_bf16 a[188:191], v[76:79], v[28:31], a[188:191]\n\t" \
    "s_waitcnt lgkmcnt(4)\n\t" \
    "v_mfma_f32_16x16x32_bf16 a[192:195], v[64:67], v[16:19], a[192:195]\n\t" \
    "ds_read_b64_tr_b16 v[76:77], v89 offset:40960\n\t" \
    "v_accvgpr_read_b32 v98, a12\n\t" \
    "v_mfma_f32_16x16x32_bf16 a[196:199], v[64:67], v[20:23], a[196:199]\n\t" \
    "ds_read_b64_tr_b16 v[78:79], v89 offset:41984\n\t" \
    "v_accvgpr_read_b32 v99, a13\n\t" \
    "v_mfma_f32_16x16x32_bf16 a[200:203], v[64:67], v[24:27], a[200:203]\n\t" \
    "buffer_load_dwordx4 v[8:11], v80, s[16:19], s34 offen offset:0 sc1\n\t" \
    "v_accvgpr_read_b32 v100, a14\n\t" \
    "v_mfma_f32_16x16x32_bf16 a[204:207], v[64:67], v[28:31], a[204:207]\n\t" \
    "v_accvgpr_read_b32 v101, a15\n\t" \
    "v_accvgpr_read_b32 v102, a44\n\t" \
    "s_waitcnt vmcnt(48)\n\t" \
    "s_barrier\n\t" \
    "s_waitcnt lgkmcnt(4)\n\t" \
    "v_mfma_f32_16x16x32_bf16 a[208:211], v[68:71], v[16:19], a[208:211]\n\t" \
    "ds_read_b64_tr_b16 v[64:65], v82 offset:49152\n\t" \
    "v_accvgpr_read_b32 v103, a45\n\t" \
    "v_mfma_f32_16x16x32_bf16 a[212:215], v[68:71], v[20:23], a[212:215]\n\t" \
    "ds_read_b64_tr_b16 v[66:67], v82 offset:50176\n\t" \
    "v_accvgpr_read_b32 v104, a46\n\t" \
    "v_mfma_f32_16x16x32_bf16 a[216:219], v[68:71], v[24:27], a[216:219]\n\t" \
    "v_accvgpr_read_b32 v105, a47\n\t" \
    "v_cvt_pk_bf16_f32 v98, v98, v99\n\t" \
    "v_mfma_f32_16x16x32_bf16 a[220:223], v[68:71], v[28:31], a[220:223]\n\t" \
    "v_cvt_pk_bf16_f32 v99, v100, v101\n\t" \
    "v_cvt_pk_bf16_f32 v100, v102, v103\n\t" \
    "s_waitcnt lgkmcnt(4)\n\t" \
    "v_mfma_f32_16x16x32_bf16 a[224:227], v[72:75], v[16:19], a[224:227]\n\t" \
    "ds_read_b64_tr_b16 v[68:69], v83 offset:49152\n\t" \
    "v_cvt_pk_bf16_f32 v101, v104, v105\n\t" \
    "v_mfma_f32_16x16x32_bf16 a[228:231], v[72:75], v[20:23], a[228:231]\n\t" \
    "ds_read_b64_tr_b16 v[70:71], v83 offset:50176\n\t" \
    "s_add_u32 s36, s23, 12288\n\t" \
    "v_mfma_f32_16x16x32_bf16 a[232:235], v[72:75], v[24:27], a[232:235]\n\t" \
    "buffer_load_dwordx4 v[12:15], v80, s[16:19], s35 offen offset:0 sc1\n\t" \
    "buffer_store_dwordx4 v[98:101], v110, s[16:19], s36 offen offset:128 sc1\n\t" \
    "v_mfma_f32_16x16x32_bf16 a[236:239], v[72:75], v[28:31], a[236:239]\n\t" \
    "s_waitcnt lgkmcnt(4)\n\t" \
    "v_mfma_f32_16x16x32_bf16 a[240:243], v[76:79], v[16:19], a[240:243]\n\t" \
    "ds_read_b64_tr_b16 v[72:73], v84 offset:49152\n\t" \
    "v_mfma_f32_16x16x32_bf16 a[244:247], v[76:79], v[20:23], a[244:247]\n\t" \
    "ds_read_b64_tr_b16 v[74:75], v84 offset:50176\n\t" \
    "v_mfma_f32_16x16x32_bf16 a[248:251], v[76:79], v[24:27], a[248:251]\n\t" \
    "v_mfma_f32_16x16x32_bf16 a[252:255], v[76:79], v[28:31], a[252:255]\n\t" \
    "s_waitcnt vmcnt(17) lgkmcnt(4)\n\t" \
    "v_mfma_f32_16x16x32_bf16 a[128:131], v[64:67], v[32:35], a[128:131]\n\t" \
    "ds_read_b64_tr_b16 v[76:77], v85 offset:49152\n\t" \
    "v_accvgpr_read_b32 v116, a16\n\t" \
    "s_waitcnt vmcnt(15)\n\t" \
    "v_mfma_f32_16x16x32_bf16 a[132:135], v[64:67], v[36:39], a[132:135]\n\t" \
    "ds_read_b64_tr_b16 v[78:79], v85 offset:50176\n\t" \
    "v_accvgpr_read_b32 v117, a17\n\t" \
    "s_waitcnt vmcnt(14)\n\t" \
    "v_mfma_f32_16x16x32_bf16 a[136:139], v[64:67], v[40:43], a[136:139]\n\t" \
    "buffer_load_dwordx4 v[16:19], v80, s[16:19], s32 offen offset:1024 sc1\n\t" \
    "v_accvgpr_read_b32 v118, a18\n\t" \
    "s_waitcnt vmcnt(13)\n\t" \
    "v_mfma_f32_16x16x32_bf16 a[140:143], v[64:67], v[44:47], a[140:143]\n\t" \
    "v_accvgpr_read_b32 v119, a19\n\t" \
    "v_accvgpr_read_b32 v120, a48\n\t" \
    "s_waitcnt lgkmcnt(4)\n\t" \
    "v_mfma_f32_16x16x32_bf16 a[144:147], v[68:71], v[32:35], a[144:147]\n\t" \
    "ds_read_b64_tr_b16 v[64:65], v86 offset:49152\n\t" \
    "v_accvgpr_read_b32 v121, a49\n\t" \
    "v_mfma_f32_16x16x32_bf16 a[148:151], v[68:71], v[36:39], a[148:151]\n\t" \
    "ds_read_b64_tr_b16 v[66:67], v86 offset:50176\n\t" \
    "v_accvgpr_read_b32 v122, a50\n\t" \
    "v_mfma_f32_16x16x32_bf16 a[152:155], v[68:71], v[40:43], a[152:155]\n\t" \
    "v_accvgpr_read_b32 v123, a51\n\t" \
    "v_cvt_pk_bf16_f32 v116, v116, v117\n\t" \
    "v_mfma_f32_16x16x32_bf16 a[156:159], v[68:71], v[44:47], a[156:159]\n\t" \
    "v_cvt_pk_bf16_f32 v117, v118, v119\n\t" \
    "v_cvt_pk_bf16_f32 v118, v120, v121\n\t" \
    "s_waitcnt lgkmcnt(4)\n\t" \
    "v_mfma_f32_16x16x32_bf16 a[160:163], v[72:75], v[32:35], a[160:163]\n\t" \
    "ds_read_b64_tr_b16 v[68:69], v87 offset:49152\n\t" \
    "v_cvt_pk_bf16_f32 v119, v122, v123\n\t" \
    "v_mfma_f32_16x16x32_bf16 a[164:167], v[72:75], v[36:39], a[164:167]\n\t" \
    "ds_read_b64_tr_b16 v[70:71], v87 offset:50176\n\t" \
    "s_add_u32 s36, s23, 20480\n\t" \
    "v_mfma_f32_16x16x32_bf16 a[168:171], v[72:75], v[40:43], a[168:171]\n\t" \
    "buffer_load_dwordx4 v[20:23], v80, s[16:19], s33 offen offset:1024 sc1\n\t" \
    "buffer_store_dwordx4 v[116:119], v110, s[16:19], s36 offen offset:0 sc1\n\t" \
    "v_mfma_f32_16x16x32_bf16 a[172:175], v[72:75], v[44:47], a[172:175]\n\t" \
    "s_waitcnt lgkmcnt(4)\n\t" \
    "v_mfma_f32_16x16x32_bf16 a[176:179], v[76:79], v[32:35], a[176:179]\n\t" \
    "ds_read_b64_tr_b16 v[72:73], v88 offset:49152\n\t" \
    "v_mfma_f32_16x16x32_bf16 a[180:183], v[76:79], v[36:39], a[180:183]\n\t" \
    "ds_read_b64_tr_b16 v[74:75], v88 offset:50176\n\t" \
    "v_mfma_f32_16x16x32_bf16 a[184:187], v[76:79], v[40:43], a[184:187]\n\t" \
    "v_mfma_f32_16x16x32_bf16 a[188:191], v[76:79], v[44:47], a[188:191]\n\t" \
    "s_waitcnt lgkmcnt(4)\n\t" \
    "v_mfma_f32_16x16x32_bf16 a[192:195], v[64:67], v[32:35], a[192:195]\n\t" \
    "ds_read_b64_tr_b16 v[76:77], v89 offset:49152\n\t" \
    "v_accvgpr_read_b32 v98, a20\n\t" \
    "v_mfma_f32_16x16x32_bf16 a[196:199], v[64:67], v[36:39], a[196:199]\n\t" \
    "ds_read_b64_tr_b16 v[78:79], v89 offset:50176\n\t" \
    "v_accvgpr_read_b32 v99, a21\n\t" \
    "v_mfma_f32_16x16x32_bf16 a[200:203], v[64:67], v[40:43], a[200:203]\n\t" \
    "buffer_load_dwordx4 v[24:27], v80, s[16:19], s34 offen offset:1024 sc1\n\t" \
    "v_accvgpr_read_b32 v100, a22\n\t" \
    "v_mfma_f32_16x16x32_bf16 a[204:207], v[64:67], v[44:47], a[204:207]\n\t" \
    "v_accvgpr_read_b32 v101, a23\n\t" \
    "v_accvgpr_read_b32 v102, a52\n\t" \
    "s_waitcnt lgkmcnt(4)\n\t" \
    "v_mfma_f32_16x16x32_bf16 a[208:211], v[68:71], v[32:35], a[208:211]\n\t" \
    "ds_read_b64_tr_b16 v[64:65], v82 offset:57344\n\t" \
    "v_accvgpr_read_b32 v103, a53\n\t" \
    "v_mfma_f32_16x16x32_bf16 a[212:215], v[68:71], v[36:39], a[212:215]\n\t" \
    "ds_read_b64_tr_b16 v[66:67], v82 offset:58368\n\t" \
    "v_accvgpr_read_b32 v104, a54\n\t" \
    "v_mfma_f32_16x16x32_bf16 a[216:219], v[68:71], v[40:43], a[216:219]\n\t" \
    "v_accvgpr_read_b32 v105, a55\n\t" \
    "v_cvt_pk_bf16_f32 v98, v98, v99\n\t" \
    "v_mfma_f32_16x16x32_bf16 a[220:223], v[68:71], v[44:47], a[220:223]\n\t" \
    "v_cvt_pk_bf16_f32 v99, v100, v101\n\t" \
    "v_cvt_pk_bf16_f32 v100, v102, v103\n\t" \
    "s_waitcnt lgkmcnt(4)\n\t" \
    "v_mfma_f32_16x16x32_bf16 a[224:227], v[72:75], v[32:35], a[224:227]\n\t" \
    "ds_read_b64_tr_b16 v[68:69], v83 offset:57344\n\t" \
    "v_cvt_pk_bf16_f32 v101, v104, v105\n\t" \
    "v_mfma_f32_16x16x32_bf16 a[228:231], v[72:75], v[36:39], a[228:231]\n\t" \
    "ds_read_b64_tr_b16 v[70:71], v83 offset:58368\n\t" \
    "s_add_u32 s36, s23, 20480\n\t" \
    "v_mfma_f32_16x16x32_bf16 a[232:235], v[72:75], v[40:43], a[232:235]\n\t" \
    "buffer_load_dwordx4 v[28:31], v80, s[16:19], s35 offen offset:1024 sc1\n\t" \
    "buffer_store_dwordx4 v[98:101], v110, s[16:19], s36 offen offset:128 sc1\n\t" \
    "v_mfma_f32_16x16x32_bf16 a[236:239], v[72:75], v[44:47], a[236:239]\n\t" \
    "s_waitcnt lgkmcnt(4)\n\t" \
    "v_mfma_f32_16x16x32_bf16 a[240:243], v[76:79], v[32:35], a[240:243]\n\t" \
    "ds_read_b64_tr_b16 v[72:73], v84 offset:57344\n\t" \
    "v_mfma_f32_16x16x32_bf16 a[244:247], v[76:79], v[36:39], a[244:247]\n\t" \
    "ds_read_b64_tr_b16 v[74:75], v84 offset:58368\n\t" \
    "v_mfma_f32_16x16x32_bf16 a[248:251], v[76:79], v[40:43], a[248:251]\n\t" \
    "v_mfma_f32_16x16x32_bf16 a[252:255], v[76:79], v[44:47], a[252:255]\n\t" \
    "s_waitcnt vmcnt(17) lgkmcnt(4)\n\t" \
    "v_mfma_f32_16x16x32_bf16 a[128:131], v[64:67], v[48:51], a[128:131]\n\t" \
    "ds_read_b64_tr_b16 v[76:77], v85 offset:57344\n\t" \
    "v_accvgpr_read_b32 v116, a24\n\t" \
    "s_waitcnt vmcnt(16)\n\t" \
    "v_mfma_f32_16x16x32_bf16 a[132:135], v[64:67], v[52:55], a[132:135]\n\t" \
    "ds_read_b64_tr_b16 v[78:79], v85 offset:58368\n\t" \
    "v_accvgpr_read_b32 v117, a25\n\t" \
    "s_waitcnt vmcnt(14)\n\t" \
    "v_mfma_f32_16x16x32_bf16 a[136:139], v[64:67], v[56:59], a[136:139]\n\t" \
    "buffer_load_dwordx4 v[32:35], v80, s[16:19], s32 offen offset:2048 sc1\n\t" \
    "v_accvgpr_read_b32 v118, a26\n\t" \
    "s_waitcnt vmcnt(14)\n\t" \
    "v_mfma_f32_16x16x32_bf16 a[140:143], v[64:67], v[60:63], a[140:143]\n\t" \
    "v_accvgpr_read_b32 v119, a27\n\t" \
    "v_accvgpr_read_b32 v120, a56\n\t" \
    "s_waitcnt lgkmcnt(4)\n\t" \
    "v_mfma_f32_16x16x32_bf16 a[144:147], v[68:71], v[48:51], a[144:147]\n\t" \
    "ds_read_b64_tr_b16 v[64:65], v86 offset:57344\n\t" \
    "v_accvgpr_read_b32 v121, a57\n\t" \
    "v_mfma_f32_16x16x32_bf16 a[148:151], v[68:71], v[52:55], a[148:151]\n\t" \
    "ds_read_b64_tr_b16 v[66:67], v86 offset:58368\n\t" \
    "v_accvgpr_read_b32 v122, a58\n\t" \
    "v_mfma_f32_16x16x32_bf16 a[152:155], v[68:71], v[56:59], a[152:155]\n\t" \
    "v_accvgpr_read_b32 v123, a59\n\t" \
    "v_cvt_pk_bf16_f32 v116, v116, v117\n\t" \
    "v_mfma_f32_16x16x32_bf16 a[156:159], v[68:71], v[60:63], a[156:159]\n\t" \
    "v_cvt_pk_bf16_f32 v117, v118, v119\n\t" \
    "v_cvt_pk_bf16_f32 v118, v120, v121\n\t" \
    "s_waitcnt lgkmcnt(4)\n\t" \
    "v_mfma_f32_16x16x32_bf16 a[160:163], v[72:75], v[48:51], a[160:163]\n\t" \
    "ds_read_b64_tr_b16 v[68:69], v87 offset:57344\n\t" \
    "v_cvt_pk_bf16_f32 v119, v122, v123\n\t" \
    "v_mfma_f32_16x16x32_bf16 a[164:167], v[72:75], v[52:55], a[164:167]\n\t" \
    "ds_read_b64_tr_b16 v[70:71], v87 offset:58368\n\t" \
    "s_add_u32 s36, s23, 28672\n\t" \
    "v_mfma_f32_16x16x32_bf16 a[168:171], v[72:75], v[56:59], a[168:171]\n\t" \
    "buffer_load_dwordx4 v[36:39], v80, s[16:19], s33 offen offset:2048 sc1\n\t" \
    "buffer_store_dwordx4 v[116:119], v110, s[16:19], s36 offen offset:0 sc1\n\t" \
    "v_mfma_f32_16x16x32_bf16 a[172:175], v[72:75], v[60:63], a[172:175]\n\t" \
    "s_waitcnt lgkmcnt(4)\n\t" \
    "v_mfma_f32_16x16x32_bf16 a[176:179], v[76:79], v[48:51], a[176:179]\n\t" \
    "ds_read_b64_tr_b16 v[72:73], v88 offset:57344\n\t" \
    "v_mfma_f32_16x16x32_bf16 a[180:183], v[76:79], v[52:55], a[180:183]\n\t" \
    "ds_read_b64_tr_b16 v[74:75], v88 offset:58368\n\t" \
    "v_mfma_f32_16x16x32_bf16 a[184:187], v[76:79], v[56:59], a[184:187]\n\t" \
    "v_mfma_f32_16x16x32_bf16 a[188:191], v[76:79], v[60:63], a[188:191]\n\t" \
    "s_waitcnt lgkmcnt(4)\n\t" \
    "v_mfma_f32_16x16x32_bf16 a[192:195], v[64:67], v[48:51], a[192:195]\n\t" \
    "ds_read_b64_tr_b16 v[76:77], v89 offset:57344\n\t" \
    "v_accvgpr_read_b32 v98, a28\n\t" \
    "v_mfma_f32_16x16x32_bf16 a[196:199], v[64:67], v[52:55], a[196:199]\n\t" \
    "ds_read_b64_tr_b16 v[78:79], v89 offset:58368\n\t" \
    "v_accvgpr_read_b32 v99, a29\n\t" \
    "v_mfma_f32_16x16x32_bf16 a[200:203], v[64:67], v[56:59], a[200:203]\n\t" \
    "buffer_load_dwordx4 v[40:43], v80, s[16:19], s34 offen offset:2048 sc1\n\t" \
    "v_accvgpr_read_b32 v100, a30\n\t" \
    "v_mfma_f32_16x16x32_bf16 a[204:207], v[64:67], v[60:63], a[204:207]\n\t" \
    "v_accvgpr_read_b32 v101, a31\n\t" \
    "v_accvgpr_read_b32 v102, a60\n\t" \
    "s_waitcnt vmcnt(44)\n\t" \
    "s_barrier\n\t" \
    "s_waitcnt lgkmcnt(4)\n\t" \
    "v_mfma_f32_16x16x32_bf16 a[208:211], v[68:71], v[48:51], a[208:211]\n\t" \
    "ds_read_b64_tr_b16 v[64:65], v90 offset:0\n\t" \
    "v_accvgpr_read_b32 v103, a61\n\t" \
    "v_mfma_f32_16x16x32_bf16 a[212:215], v[68:71], v[52:55], a[212:215]\n\t" \
    "ds_read_b64_tr_b16 v[66:67], v90 offset:1024\n\t" \
    "v_accvgpr_read_b32 v104, a62\n\t" \
    "v_mfma_f32_16x16x32_bf16 a[216:219], v[68:71], v[56:59], a[216:219]\n\t" \
    "v_accvgpr_read_b32 v105, a63\n\t" \
    "v_cvt_pk_bf16_f32 v98, v98, v99\n\t" \
    "v_mfma_f32_16x16x32_bf16 a[220:223], v[68:71], v[60:63], a[220:223]\n\t" \
    "v_cvt_pk_bf16_f32 v99, v100, v101\n\t" \
    "v_cvt_pk_bf16_f32 v100, v102, v103\n\t" \
    "s_waitcnt lgkmcnt(4)\n\t" \
    "v_mfma_f32_16x16x32_bf16 a[224:227], v[72:75], v[48:51], a[224:227]\n\t" \
    "ds_read_b64_tr_b16 v[68:69], v91 offset:0\n\t" \
    "v_cvt_pk_bf16_f32 v101, v104, v105\n\t" \
    "v_mfma_f32_16x16x32_bf16 a[228:231], v[72:75], v[52:55], a[228:231]\n\t" \
    "ds_read_b64_tr_b16 v[70:71], v91 offset:1024\n\t" \
    "s_add_u32 s36, s23, 28672\n\t" \
    "v_mfma_f32_16x16x32_bf16 a[232:235], v[72:75], v[56:59], a[232:235]\n\t" \
    "buffer_load_dwordx4 v[44:47], v80, s[16:19], s35 offen offset:2048 sc1\n\t" \
    "buffer_store_dwordx4 v[98:101], v110, s[16:19], s36 offen offset:128 sc1\n\t" \
    "v_mfma_f32_16x16x32_bf16 a[236:239], v[72:75], v[60:63], a[236:239]\n\t" \
    "s_waitcnt lgkmcnt(4)\n\t" \
    "v_mfma_f32_16x16x32_bf16 a[240:243], v[76:79], v[48:51], a[240:243]\n\t" \
    "ds_read_b64_tr_b16 v[72:73], v92 offset:0\n\t" \
    "v_mfma_f32_16x16x32_bf16 a[244:247], v[76:79], v[52:55], a[244:247]\n\t" \
    "ds_read_b64_tr_b16 v[74:75], v92 offset:1024\n\t" \
    "v_mfma_f32_16x16x32_bf16 a[248:251], v[76:79], v[56:59], a[248:251]\n\t" \
    "v_mfma_f32_16x16x32_bf16 a[252:255], v[76:79], v[60:63], a[252:255]\n\t" \
    "s_waitcnt vmcnt(17) lgkmcnt(4)\n\t" \
    "v_mfma_f32_16x16x32_bf16 a[128:131], v[64:67], v[0:3], a[128:131]\n\t" \
    "ds_read_b64_tr_b16 v[76:77], v93 offset:0\n\t" \
    "v_accvgpr_read_b32 v116, a64\n\t" \
    "s_waitcnt vmcnt(16)\n\t" \
    "v_mfma_f32_16x16x32_bf16 a[132:135], v[64:67], v[4:7], a[132:135]\n\t" \
    "ds_read_b64_tr_b16 v[78:79], v93 offset:1024\n\t" \
    "v_accvgpr_read_b32 v117, a65\n\t" \
    "s_waitcnt vmcnt(14)\n\t" \
    "v_mfma_f32_16x16x32_bf16 a[136:139], v[64:67], v[8:11], a[136:139]\n\t" \
    "buffer_load_dwordx4 v[48:51], v80, s[16:19], s32 offen offset:3072 sc1\n\t" \
    "v_accvgpr_read_b32 v118, a66\n\t" \
    "s_waitcnt vmcnt(14)\n\t" \
    "v_mfma_f32_16x16x32_bf16 a[140:143], v[64:67], v[12:15], a[140:143]\n\t" \
    "v_accvgpr_read_b32 v119, a67\n\t" \
    "v_accvgpr_read_b32 v120, a96\n\t" \
    "s_waitcnt lgkmcnt(4)\n\t" \
    "v_mfma_f32_16x16x32_bf16 a[144:147], v[68:71], v[0:3], a[144:147]\n\t" \
    "ds_read_b64_tr_b16 v[64:65], v94 offset:0\n\t" \
    "v_accvgpr_read_b32 v121, a97\n\t" \
    "v_mfma_f32_16x16x32_bf16 a[148:151], v[68:71], v[4:7], a[148:151]\n\t" \
    "ds_read_b64_tr_b16 v[66:67], v94 offset:1024\n\t" \
    "v_accvgpr_read_b32 v122, a98\n\t" \
    "v_mfma_f32_16x16x32_bf16 a[152:155], v[68:71], v[8:11], a[152:155]\n\t" \
    "v_accvgpr_read_b32 v123, a99\n\t" \
    "v_cvt_pk_bf16_f32 v116, v116, v117\n\t" \
    "v_mfma_f32_16x16x32_bf16 a[156:159], v[68:71], v[12:15], a[156:159]\n\t" \
    "v_cvt_pk_bf16_f32 v117, v118, v119\n\t" \
    "v_cvt_pk_bf16_f32 v118, v120, v121\n\t" \
    "s_waitcnt lgkmcnt(4)\n\t" \
    "v_mfma_f32_16x16x32_bf16 a[160:163], v[72:75], v[0:3], a[160:163]\n\t" \
    "ds_read_b64_tr_b16 v[68:69], v95 offset:0\n\t" \
    "v_cvt_pk_bf16_f32 v119, v122, v123\n\t" \
    "v_mfma_f32_16x16x32_bf16 a[164:167], v[72:75], v[4:7], a[164:167]\n\t" \
    "ds_read_b64_tr_b16 v[70:71], v95 offset:1024\n\t" \
    "s_add_u32 s36, s23, 4096\n\t" \
    "v_mfma_f32_16x16x32_bf16 a[168:171], v[72:75], v[8:11], a[168:171]\n\t" \
    "buffer_load_dwordx4 v[52:55], v80, s[16:19], s33 offen offset:3072 sc1\n\t" \
    "buffer_store_dwordx4 v[116:119], v110, s[16:19], s36 offen offset:2048 sc1\n\t" \
    "v_mfma_f32_16x16x32_bf16 a[172:175], v[72:75], v[12:15], a[172:175]\n\t" \
    "s_waitcnt lgkmcnt(4)\n\t" \
    "v_mfma_f32_16x16x32_bf16 a[176:179], v[76:79], v[0:3], a[176:179]\n\t" \
    "ds_read_b64_tr_b16 v[72:73], v96 offset:0\n\t" \
    "v_mfma_f32_16x16x32_bf16 a[180:183], v[76:79], v[4:7], a[180:183]\n\t" \
    "ds_read_b64_tr_b16 v[74:75], v96 offset:1024\n\t" \
    "v_mfma_f32_16x16x32_bf16 a[184:187], v[76:79], v[8:11], a[184:187]\n\t" \
    "v_mfma_f32_16x16x32_bf16 a[188:191], v[76:79], v[12:15], a[188:191]\n\t" \
    "s_waitcnt lgkmcnt(4)\n\t" \
    "v_mfma_f32_16x16x32_bf16 a[192:195], v[64:67], v[0:3], a[192:195]\n\t" \
    "ds_read_b64_tr_b16 v[76:77], v97 offset:0\n\t" \
    "v_accvgpr_read_b32 v98, a68\n\t" \
    "v_mfma_f32_16x16x32_bf16 a[196:199], v[64:67], v[4:7], a[196:199]\n\t" \
    "ds_read_b64_tr_b16 v[78:79], v97 offset:1024\n\t" \
    "v_accvgpr_read_b32 v99, a69\n\t" \
    "v_mfma_f32_16x16x32_bf16 a[200:203], v[64:67], v[8:11], a[200:203]\n\t" \
    "buffer_load_dwordx4 v[56:59], v80, s[16:19], s34 offen offset:3072 sc1\n\t" \
    "v_accvgpr_read_b32 v100, a70\n\t" \
    "v_mfma_f32_16x16x32_bf16 a[204:207], v[64:67], v[12:15], a[204:207]\n\t" \
    "v_accvgpr_read_b32 v101, a71\n\t" \
    "v_accvgpr_read_b32 v102, a100\n\t" \
    "s_waitcnt lgkmcnt(4)\n\t" \
    "v_mfma_f32_16x16x32_bf16 a[208:211], v[68:71], v[0:3], a[208:211]\n\t" \
    "ds_read_b64_tr_b16 v[64:65], v90 offset:8192\n\t" \
    "v_accvgpr_read_b32 v103, a101\n\t" \
    "v_mfma_f32_16x16x32_bf16 a[212:215], v[68:71], v[4:7], a[212:215]\n\t" \
    "ds_read_b64_tr_b16 v[66:67], v90 offset:9216\n\t" \
    "v_accvgpr_read_b32 v104, a102\n\t" \
    "v_mfma_f32_16x16x32_bf16 a[216:219], v[68:71], v[8:11], a[216:219]\n\t" \
    "v_accvgpr_read_b32 v105, a103\n\t" \
    "v_cvt_pk_bf16_f32 v98, v98, v99\n\t" \
    "v_mfma_f32_16x16x32_bf16 a[220:223], v[68:71], v[12:15], a[220:223]\n\t" \
    "v_cvt_pk_bf16_f32 v99, v100, v101\n\t" \
    "v_cvt_pk_bf16_f32 v100, v102, v103\n\t" \
    "s_waitcnt lgkmcnt(4)\n\t" \
    "v_mfma_f32_16x16x32_bf16 a[224:227], v[72:75], v[0:3], a[224:227]\n\t" \
    "ds_read_b64_tr_b16 v[68:69], v91 offset:8192\n\t" \
    "v_cvt_pk_bf16_f32 v101, v104, v105\n\t" \
    "v_mfma_f32_16x16x32_bf16 a[228:231], v[72:75], v[4:7], a[228:231]\n\t" \
    "ds_read_b64_tr_b16 v[70:71], v91 offset:9216\n\t" \
    "s_add_u32 s36, s23, 4096\n\t" \
    "v_mfma_f32_16x16x32_bf16 a[232:235], v[72:75], v[8:11], a[232:235]\n\t" \
    "buffer_load_dwordx4 v[60:63], v80, s[16:19], s35 offen offset:3072 sc1\n\t" \
    "buffer_store_dwordx4 v[98:101], v110, s[16:19], s36 offen offset:2176 sc1\n\t" \
    "v_mfma_f32_16x16x32_bf16 a[236:239], v[72:75], v[12:15], a[236:239]\n\t" \
    "s_waitcnt lgkmcnt(4)\n\t" \
    "v_mfma_f32_16x16x32_bf16 a[240:243], v[76:79], v[0:3], a[240:243]\n\t" \
    "ds_read_b64_tr_b16 v[72:73], v92 offset:8192\n\t" \
    "v_mfma_f32_16x16x32_bf16 a[244:247], v[76:79], v[4:7], a[244:247]\n\t" \
    "ds_read_b64_tr_b16 v[74:75], v92 offset:9216\n\t" \
    "v_mfma_f32_16x16x32_bf16 a[248:251], v[76:79], v[8:11], a[248:251]\n\t" \
    "v_mfma_f32_16x16x32_bf16 a[252:255], v[76:79], v[12:15], a[252:255]\n\t" \
    "s_waitcnt vmcnt(17) lgkmcnt(4)\n\t" \
    "v_mfma_f32_16x16x32_bf16 a[128:131], v[64:67], v[16:19], a[128:131]\n\t" \
    "ds_read_b64_tr_b16 v[76:77], v93 offset:8192\n\t" \
    "v_accvgpr_read_b32 v116, a72\n\t" \
    "s_waitcnt vmcnt(16)\n\t" \
    "v_mfma_f32_16x16x32_bf16 a[132:135], v[64:67], v[20:23], a[132:135]\n\t" \
    "ds_read_b64_tr_b16 v[78:79], v93 offset:9216\n\t" \
    "v_accvgpr_read_b32 v117, a73\n\t" \
    "s_waitcnt vmcnt(14)\n\t" \
    "v_mfma_f32_16x16x32_bf16 a[136:139], v[64:67], v[24:27], a[136:139]\n\t" \
    "v_accvgpr_read_b32 v118, a74\n\t" \
    "v_accvgpr_read_b32 v119, a75\n\t" \
    "s_waitcnt vmcnt(13)\n\t" \
    "v_mfma_f32_16x16x32_bf16 a[140:143], v[64:67], v[28:31], a[140:143]\n\t" \
    "v_accvgpr_read_b32 v120, a104\n\t" \
    "v_accvgpr_read_b32 v121, a105\n\t" \
    "s_waitcnt lgkmcnt(4)\n\t" \
    "v_mfma_f32_16x16x32_bf16 a[144:147], v[68:71], v[16:19], a[144:147]\n\t" \
    "ds_read_b64_tr_b16 v[64:65], v94 offset:8192\n\t" \
    "v_accvgpr_read_b32 v122, a106\n\t" \
    "v_mfma_f32_16x16x32_bf16 a[148:151], v[68:71], v[20:23], a[148:151]\n\t" \
    "ds_read_b64_tr_b16 v[66:67], v94 offset:9216\n\t" \
    "v_accvgpr_read_b32 v123, a107\n\t" \
    "v_mfma_f32_16x16x32_bf16 a[152:155], v[68:71], v[24:27], a[152:155]\n\t" \
    "v_cvt_pk_bf16_f32 v116, v116, v117\n\t" \
    "v_cvt_pk_bf16_f32 v117, v118, v119\n\t" \
    "v_mfma_f32_16x16x32_bf16 a[156:159], v[68:71], v[28:31], a[156:159]\n\t" \
    "v_cvt_pk_bf16_f32 v118, v120, v121\n\t" \
    "v_cvt_pk_bf16_f32 v119, v122, v123\n\t" \
    "s_waitcnt lgkmcnt(4)\n\t" \
    "v_mfma_f32_16x16x32_bf16 a[160:163], v[72:75], v[16:19], a[160:163]\n\t" \
    "ds_read_b64_tr_b16 v[68:69], v95 offset:8192\n\t" \
    "s_add_u32 s36, s23, 12288\n\t" \
    "v_mfma_f32_16x16x32_bf16 a[164:167], v[72:75], v[20:23], a[164:167]\n\t" \
    "ds_read_b64_tr_b16 v[70:71], v95 offset:9216\n\t" \
    "buffer_store_dwordx4 v[116:119], v110, s[16:19], s36 offen offset:2048 sc1\n\t" \
    "v_mfma_f32_16x16x32_bf16 a[168:171], v[72:75], v[24:27], a[168:171]\n\t" \
    "v_mfma_f32_16x16x32_bf16 a[172:175], v[72:75], v[28:31], a[172:175]\n\t" \
    "s_waitcnt lgkmcnt(4)\n\t" \
    "v_mfma_f32_16x16x32_bf16 a[176:179], v[76:79], v[16:19], a[176:179]\n\t" \
    "ds_read_b64_tr_b16 v[72:73], v96 offset:8192\n\t" \
    "v_mfma_f32_16x16x32_bf16 a[180:183], v[76:79], v[20:23], a[180:183]\n\t" \
    "ds_read_b64_tr_b16 v[74:75], v96 offset:9216\n\t" \
    "v_mfma_f32_16x16x32_bf16 a[184:187], v[76:79], v[24:27], a[184:187]\n\t" \
    "v_mfma_f32_16x16x32_bf16 a[188:191], v[76:79], v[28:31], a[188:191]\n\t" \
    "s_waitcnt lgkmcnt(4)\n\t" \
    "v_mfma_f32_16x16x32_bf16 a[192:195], v[64:67], v[16:19], a[192:195]\n\t" \
    "ds_read_b64_tr_b16 v[76:77], v97 offset:8192\n\t" \
    "v_accvgpr_read_b32 v98, a76\n\t" \
    "v_mfma_f32_16x16x32_bf16 a[196:199], v[64:67], v[20:23], a[196:199]\n\t" \
    "ds_read_b64_tr_b16 v[78:79], v97 offset:9216\n\t" \
    "v_accvgpr_read_b32 v99, a77\n\t" \
    "v_mfma_f32_16x16x32_bf16 a[200:203], v[64:67], v[24:27], a[200:203]\n\t" \
    "v_accvgpr_read_b32 v100, a78\n\t" \
    "v_accvgpr_read_b32 v101, a79\n\t" \
    "v_mfma_f32_16x16x32_bf16 a[204:207], v[64:67], v[28:31], a[204:207]\n\t" \
    "v_accvgpr_read_b32 v102, a108\n\t" \
    "v_accvgpr_read_b32 v103, a109\n\t" \
    "s_waitcnt vmcnt(37)\n\t" \
    "s_barrier\n\t" \
    "s_waitcnt lgkmcnt(4)\n\t" \
    "v_mfma_f32_16x16x32_bf16 a[208:211], v[68:71], v[16:19], a[208:211]\n\t" \
    "ds_read_b64_tr_b16 v[64:65], v90 offset:16384\n\t" \
    "v_accvgpr_read_b32 v104, a110\n\t" \
    "v_mfma_f32_16x16x32_bf16 a[212:215], v[68:71], v[20:23], a[212:215]\n\t" \
    "ds_read_b64_tr_b16 v[66:67], v90 offset:17408\n\t" \
    "v_accvgpr_read_b32 v105, a111\n\t" \
    "v_mfma_f32_16x16x32_bf16 a[216:219], v[68:71], v[24:27], a[216:219]\n\t" \
    "v_cvt_pk_bf16_f32 v98, v98, v99\n\t" \
    "v_cvt_pk_bf16_f32 v99, v100, v101\n\t" \
    "v_mfma_f32_16x16x32_bf16 a[220:223], v[68:71], v[28:31], a[220:223]\n\t" \
    "v_cvt_pk_bf16_f32 v100, v102, v103\n\t" \
    "v_cvt_pk_bf16_f32 v101, v104, v105\n\t" \
    "s_waitcnt lgkmcnt(4)\n\t" \
    "v_mfma_f32_16x16x32_bf16 a[224:227], v[72:75], v[16:19], a[224:227]\n\t" \
    "ds_read_b64_tr_b16 v[68:69], v91 offset:16384\n\t" \
    "s_add_u32 s36, s23, 12288\n\t" \
    "v_mfma_f32_16x16x32_bf16 a[228:231], v[72:75], v[20:23], a[228:231]\n\t" \
    "ds_read_b64_tr_b16 v[70:71], v91 offset:17408\n\t" \
    "buffer_store_dwordx4 v[98:101], v110, s[16:19], s36 offen offset:2176 sc1\n\t" \
    "v_mfma_f32_16x16x32_bf16 a[232:235], v[72:75], v[24:27], a[232:235]\n\t" \
    "v_mfma_f32_16x16x32_bf16 a[236:239], v[72:75], v[28:31], a[236:239]\n\t" \
    "s_waitcnt lgkmcnt(4)\n\t" \
    "v_mfma_f32_16x16x32_bf16 a[240:243], v[76:79], v[16:19], a[240:243]\n\t" \
    "ds_read_b64_tr_b16 v[72:73], v92 offset:16384\n\t" \
    "v_mfma_f32_16x16x32_bf16 a[244:247], v[76:79], v[20:23], a[244:247]\n\t" \
    "ds_read_b64_tr_b16 v[74:75], v92 offset:17408\n\t" \
    "v_mfma_f32_16x16x32_bf16 a[248:251], v[76:79], v[24:27], a[248:251]\n\t" \
    "v_mfma_f32_16x16x32_bf16 a[252:255], v[76:79], v[28:31], a[252:255]\n\t" \
    "s_waitcnt vmcnt(13) lgkmcnt(4)\n\t" \
    "v_mfma_f32_16x16x32_bf16 a[128:131], v[64:67], v[32:35], a[128:131]\n\t" \
    "ds_read_b64_tr_b16 v[76:77], v93 offset:16384\n\t" \
    "v_accvgpr_read_b32 v116, a80\n\t" \
    "s_waitcnt vmcnt(12)\n\t" \
    "v_mfma_f32_16x16x32_bf16 a[132:135], v[64:67], v[36:39], a[132:135]\n\t" \
    "ds_read_b64_tr_b16 v[78:79], v93 offset:17408\n\t" \
    "v_accvgpr_read_b32 v117, a81\n\t" \
    "s_waitcnt vmcnt(10)\n\t" \
    "v_mfma_f32_16x16x32_bf16 a[136:139], v[64:67], v[40:43], a[136:139]\n\t" \
    "v_accvgpr_read_b32 v118, a82\n\t" \
    "v_accvgpr_read_b32 v119, a83\n\t" \
    "s_waitcnt vmcnt(9)\n\t" \
    "v_mfma_f32_16x16x32_bf16 a[140:143], v[64:67], v[44:47], a[140:143]\n\t" \
    "v_accvgpr_read_b32 v120, a112\n\t" \
    "v_accvgpr_read_b32 v121, a113\n\t" \
    "s_waitcnt lgkmcnt(4)\n\t" \
    "v_mfma_f32_16x16x32_bf16 a[144:147], v[68:71], v[32:35], a[144:147]\n\t" \
    "ds_read_b64_tr_b16 v[64:65], v94 offset:16384\n\t" \
    "v_accvgpr_read_b32 v122, a114\n\t" \
    "v_mfma_f32_16x16x32_bf16 a[148:151], v[68:71], v[36:39], a[148:151]\n\t" \
    "ds_read_b64_tr_b16 v[66:67], v94 offset:17408\n\t" \
    "v_accvgpr_read_b32 v123, a115\n\t" \
    "v_mfma_f32_16x16x32_bf16 a[152:155], v[68:71], v[40:43], a[152:155]\n\t" \
    "v_cvt_pk_bf16_f32 v116, v116, v117\n\t" \
    "v_cvt_pk_bf16_f32 v117, v118, v119\n\t" \
    "v_mfma_f32_16x16x32_bf16 a[156:159], v[68:71], v[44:47], a[156:159]\n\t" \
    "v_cvt_pk_bf16_f32 v118, v120, v121\n\t" \
    "v_cvt_pk_bf16_f32 v119, v122, v123\n\t" \
    "s_waitcnt lgkmcnt(4)\n\t" \
    "v_mfma_f32_16x16x32_bf16 a[160:163], v[72:75], v[32:35], a[160:163]\n\t" \
    "ds_read_b64_tr_b16 v[68:69], v95 offset:16384\n\t" \
    "s_add_u32 s36, s23, 20480\n\t" \
    "v_mfma_f32_16x16x32_bf16 a[164:167], v[72:75], v[36:39], a[164:167]\n\t" \
    "ds_read_b64_tr_b16 v[70:71], v95 offset:17408\n\t" \
    "buffer_store_dwordx4 v[116:119], v110, s[16:19], s36 offen offset:2048 sc1\n\t" \
    "v_mfma_f32_16x16x32_bf16 a[168:171], v[72:75], v[40:43], a[168:171]\n\t" \
    "v_mfma_f32_16x16x32_bf16 a[172:175], v[72:75], v[44:47], a[172:175]\n\t" \
    "s_waitcnt lgkmcnt(4)\n\t" \
    "v_mfma_f32_16x16x32_bf16 a[176:179], v[76:79], v[32:35], a[176:179]\n\t" \
    "ds_read_b64_tr_b16 v[72:73], v96 offset:16384\n\t" \
    "v_mfma_f32_16x16x32_bf16 a[180:183], v[76:79], v[36:39], a[180:183]\n\t" \
    "ds_read_b64_tr_b16 v[74:75], v96 offset:17408\n\t" \
    "v_mfma_f32_16x16x32_bf16 a[184:187], v[76:79], v[40:43], a[184:187]\n\t" \
    "v_mfma_f32_16x16x32_bf16 a[188:191], v[76:79], v[44:47], a[188:191]\n\t" \
    "s_waitcnt lgkmcnt(4)\n\t" \
    "v_mfma_f32_16x16x32_bf16 a[192:195], v[64:67], v[32:35], a[192:195]\n\t" \
    "ds_read_b64_tr_b16 v[76:77], v97 offset:16384\n\t" \
    "v_accvgpr_read_b32 v98, a84\n\t" \
    "v_mfma_f32_16x16x32_bf16 a[196:199], v[64:67], v[36:39], a[196:199]\n\t" \
    "ds_read_b64_tr_b16 v[78:79], v97 offset:17408\n\t" \
    "v_accvgpr_read_b32 v99, a85\n\t" \
    "v_mfma_f32_16x16x32_bf16 a[200:203], v[64:67], v[40:43], a[200:203]\n\t" \
    "v_accvgpr_read_b32 v100, a86\n\t" \
    "v_accvgpr_read_b32 v101, a87\n\t" \
    "v_mfma_f32_16x16x32_bf16 a[204:207], v[64:67], v[44:47], a[204:207]\n\t" \
    "v_accvgpr_read_b32 v102, a116\n\t" \
    "v_accvgpr_read_b32 v103, a117\n\t" \
    "s_waitcnt lgkmcnt(4)\n\t" \
    "v_mfma_f32_16x16x32_bf16 a[208:211], v[68:71], v[32:35], a[208:211]\n\t" \
    "ds_read_b64_tr_b16 v[64:65], v90 offset:24576\n\t" \
    "v_accvgpr_read_b32 v104, a118\n\t" \
    "v_mfma_f32_16x16x32_bf16 a[212:215], v[68:71], v[36:39], a[212:215]\n\t" \
    "ds_read_b64_tr_b16 v[66:67], v90 offset:25600\n\t" \
    "v_accvgpr_read_b32 v105, a119\n\t" \
    "v_mfma_f32_16x16x32_bf16 a[216:219], v[68:71], v[40:43], a[216:219]\n\t" \
    "v_cvt_pk_bf16_f32 v98, v98, v99\n\t" \
    "v_cvt_pk_bf16_f32 v99, v100, v101\n\t" \
    "v_mfma_f32_16x16x32_bf16 a[220:223], v[68:71], v[44:47], a[220:223]\n\t" \
    "v_cvt_pk_bf16_f32 v100, v102, v103\n\t" \
    "v_cvt_pk_bf16_f32 v101, v104, v105\n\t" \
    "s_waitcnt lgkmcnt(4)\n\t" \
    "v_mfma_f32_16x16x32_bf16 a[224:227], v[72:75], v[32:35], a[224:227]\n\t" \
    "ds_read_b64_tr_b16 v[68:69], v91 offset:24576\n\t" \
    "s_add_u32 s36, s23, 20480\n\t" \
    "v_mfma_f32_16x16x32_bf16 a[228:231], v[72:75], v[36:39], a[228:231]\n\t" \
    "ds_read_b64_tr_b16 v[70:71], v91 offset:25600\n\t" \
    "buffer_store_dwordx4 v[98:101], v110, s[16:19], s36 offen offset:2176 sc1\n\t" \
    "v_mfma_f32_16x16x32_bf16 a[232:235], v[72:75], v[40:43], a[232:235]\n\t" \
    "v_mfma_f32_16x16x32_bf16 a[236:239], v[72:75], v[44:47], a[236:239]\n\t" \
    "s_waitcnt lgkmcnt(4)\n\t" \
    "v_mfma_f32_16x16x32_bf16 a[240:243], v[76:79], v[32:35], a[240:243]\n\t" \
    "ds_read_b64_tr_b16 v[72:73], v92 offset:24576\n\t" \
    "v_mfma_f32_16x16x32_bf16 a[244:247], v[76:79], v[36:39], a[244:247]\n\t" \
    "ds_read_b64_tr_b16 v[74:75], v92 offset:25600\n\t" \
    "v_mfma_f32_16x16x32_bf16 a[248:251], v[76:79], v[40:43], a[248:251]\n\t" \
    "v_mfma_f32_16x16x32_bf16 a[252:255], v[76:79], v[44:47], a[252:255]\n\t" \
    "s_waitcnt vmcnt(9) lgkmcnt(4)\n\t" \
    "v_mfma_f32_16x16x32_bf16 a[128:131], v[64:67], v[48:51], a[128:131]\n\t" \
    "ds_read_b64_tr_b16 v[76:77], v93 offset:24576\n\t" \
    "v_accvgpr_read_b32 v116, a88\n\t" \
    "s_waitcnt vmcnt(8)\n\t" \
    "v_mfma_f32_16x16x32_bf16 a[132:135], v[64:67], v[52:55], a[132:135]\n\t" \
    "ds_read_b64_tr_b16 v[78:79], v93 offset:25600\n\t" \
    "v_accvgpr_read_b32 v117, a89\n\t" \
    "s_waitcnt vmcnt(6)\n\t" \
    "v_mfma_f32_16x16x32_bf16 a[136:139], v[64:67], v[56:59], a[136:139]\n\t" \
    "v_accvgpr_read_b32 v118, a90\n\t" \
    "v_accvgpr_read_b32 v119, a91\n\t" \
    "s_waitcnt vmcnt(5)\n\t" \
    "v_mfma_f32_16x16x32_bf16 a[140:143], v[64:67], v[60:63], a[140:143]\n\t" \
    "v_accvgpr_read_b32 v120, a120\n\t" \
    "v_accvgpr_read_b32 v121, a121\n\t" \
    "s_waitcnt lgkmcnt(4)\n\t" \
    "v_mfma_f32_16x16x32_bf16 a[144:147], v[68:71], v[48:51], a[144:147]\n\t" \
    "ds_read_b64_tr_b16 v[64:65], v94 offset:24576\n\t" \
    "v_accvgpr_read_b32 v122, a122\n\t" \
    "v_mfma_f32_16x16x32_bf16 a[148:151], v[68:71], v[52:55], a[148:151]\n\t" \
    "ds_read_b64_tr_b16 v[66:67], v94 offset:25600\n\t" \
    "v_accvgpr_read_b32 v123, a123\n\t" \
    "v_mfma_f32_16x16x32_bf16 a[152:155], v[68:71], v[56:59], a[152:155]\n\t" \
    "v_cvt_pk_bf16_f32 v116, v116, v117\n\t" \
    "v_cvt_pk_bf16_f32 v117, v118, v119\n\t" \
    "v_mfma_f32_16x16x32_bf16 a[156:159], v[68:71], v[60:63], a[156:159]\n\t" \
    "v_cvt_pk_bf16_f32 v118, v120, v121\n\t" \
    "v_cvt_pk_bf16_f32 v119, v122, v123\n\t" \
    "s_waitcnt lgkmcnt(4)\n\t" \
    "v_mfma_f32_16x16x32_bf16 a[160:163], v[72:75], v[48:51], a[160:163]\n\t" \
    "ds_read_b64_tr_b16 v[68:69], v95 offset:24576\n\t" \
    "s_add_u32 s36, s23, 28672\n\t" \
    "v_mfma_f32_16x16x32_bf16 a[164:167], v[72:75], v[52:55], a[164:167]\n\t" \
    "ds_read_b64_tr_b16 v[70:71], v95 offset:25600\n\t" \
    "buffer_store_dwordx4 v[116:119], v110, s[16:19], s36 offen offset:2048 sc1\n\t" \
    "v_mfma_f32_16x16x32_bf16 a[168:171], v[72:75], v[56:59], a[168:171]\n\t" \
    "v_mfma_f32_16x16x32_bf16 a[172:175], v[72:75], v[60:63], a[172:175]\n\t" \
    "s_waitcnt lgkmcnt(4)\n\t" \
    "v_mfma_f32_16x16x32_bf16 a[176:179], v[76:79], v[48:51], a[176:179]\n\t" \
    "ds_read_b64_tr_b16 v[72:73], v96 offset:24576\n\t" \
    "v_mfma_f32_16x16x32_bf16 a[180:183], v[76:79], v[52:55], a[180:183]\n\t" \
    "ds_read_b64_tr_b16 v[74:75], v96 offset:25600\n\t" \
    "v_mfma_f32_16x16x32_bf16 a[184:187], v[76:79], v[56:59], a[184:187]\n\t" \
    "v_mfma_f32_16x16x32_bf16 a[188:191], v[76:79], v[60:63], a[188:191]\n\t" \
    "s_waitcnt lgkmcnt(4)\n\t" \
    "v_mfma_f32_16x16x32_bf16 a[192:195], v[64:67], v[48:51], a[192:195]\n\t" \
    "ds_read_b64_tr_b16 v[76:77], v97 offset:24576\n\t" \
    "v_accvgpr_read_b32 v98, a92\n\t" \
    "v_mfma_f32_16x16x32_bf16 a[196:199], v[64:67], v[52:55], a[196:199]\n\t" \
    "ds_read_b64_tr_b16 v[78:79], v97 offset:25600\n\t" \
    "v_accvgpr_read_b32 v99, a93\n\t" \
    "v_mfma_f32_16x16x32_bf16 a[200:203], v[64:67], v[56:59], a[200:203]\n\t" \
    "v_accvgpr_read_b32 v100, a94\n\t" \
    "v_accvgpr_read_b32 v101, a95\n\t" \
    "v_mfma_f32_16x16x32_bf16 a[204:207], v[64:67], v[60:63], a[204:207]\n\t" \
    "v_accvgpr_read_b32 v102, a124\n\t" \
    "v_accvgpr_read_b32 v103, a125\n\t" \
    "s_waitcnt lgkmcnt(4)\n\t" \
    "v_mfma_f32_16x16x32_bf16 a[208:211], v[68:71], v[48:51], a[208:211]\n\t" \
    "v_accvgpr_read_b32 v104, a126\n\t" \
    "v_accvgpr_read_b32 v105, a127\n\t" \
    "v_mfma_f32_16x16x32_bf16 a[212:215], v[68:71], v[52:55], a[212:215]\n\t" \
    "v_cvt_pk_bf16_f32 v98, v98, v99\n\t" \
    "v_cvt_pk_bf16_f32 v99, v100, v101\n\t" \
    "v_mfma_f32_16x16x32_bf16 a[216:219], v[68:71], v[56:59], a[216:219]\n\t" \
    "v_cvt_pk_bf16_f32 v100, v102, v103\n\t" \
    "v_cvt_pk_bf16_f32 v101, v104, v105\n\t" \
    "v_mfma_f32_16x16x32_bf16 a[220:223], v[68:71], v[60:63], a[220:223]\n\t" \
    "s_add_u32 s36, s23, 28672\n\t" \
    "s_waitcnt lgkmcnt(2)\n\t" \
    "v_mfma_f32_16x16x32_bf16 a[224:227], v[72:75], v[48:51], a[224:227]\n\t" \
    "buffer_store_dwordx4 v[98:101], v110, s[16:19], s36 offen offset:2176 sc1\n\t" \
    "v_mfma_f32_16x16x32_bf16 a[228:231], v[72:75], v[52:55], a[228:231]\n\t" \
    "v_mfma_f32_16x16x32_bf16 a[232:235], v[72:75], v[56:59], a[232:235]\n\t" \
    "v_mfma_f32_16x16x32_bf16 a[236:239], v[72:75], v[60:63], a[236:239]\n\t" \
    "s_waitcnt lgkmcnt(0)\n\t" \
    "v_mfma_f32_16x16x32_bf16 a[240:243], v[76:79], v[48:51], a[240:243]\n\t" \
    "v_mfma_f32_16x16x32_bf16 a[244:247], v[76:79], v[52:55], a[244:247]\n\t" \
    "v_mfma_f32_16x16x32_bf16 a[248:251], v[76:79], v[56:59], a[248:251]\n\t" \
    "v_mfma_f32_16x16x32_bf16 a[252:255], v[76:79], v[60:63], a[252:255]\n\t" \
    "s_branch QEND_%=\n\t" \
    "VP_%=:\n\t" \
    "s_waitcnt lgkmcnt(9)\n\t" \
    "v_mfma_f32_16x16x32_bf16 v[128:131], v[0:3], v[64:67], 0\n\t" \
    "s_waitcnt lgkmcnt(8)\n\t" \
    "v_mfma_f32_16x16x32_bf16 v[132:135], v[4:7], v[64:67], 0\n\t" \
    "ds_read_b64_tr_b16 v[72:73], v84 offset:0\n\t" \
    "s_waitcnt lgkmcnt(8)\n\t" \
    "v_mfma_f32_16x16x32_bf16 v[136:139], v[8:11], v[64:67], 0\n\t" \
    "ds_read_b64_tr_b16 v[74:75], v84 offset:1024\n\t" \
    "s_waitcnt lgkmcnt(8)\n\t" \
    "v_mfma_f32_16x16x32_bf16 v[140:143], v[12:15], v[64:67], 0\n\t" \
    "ds_read_b128 v[32:35], v81 offset:0\n\t" \
    "s_waitcnt lgkmcnt(8)\n\t" \
    "v_mfma_f32_16x16x32_bf16 v[144:147], v[16:19], v[64:67], 0\n\t" \
    "ds_read_b128 v[36:39], v81 offset:512\n\t" \
    "s_waitcnt lgkmcnt(8)\n\t" \
    "v_mfma_f32_16x16x32_bf16 v[148:151], v[20:23], v[64:67], 0\n\t" \
    "ds_read_b128 v[40:43], v81 offset:4096\n\t" \
    "s_waitcnt lgkmcnt(8)\n\t" \
    "v_mfma_f32_16x16x32_bf16 v[152:155], v[24:27], v[64:67], 0\n\t" \
    "ds_read_b128 v[44:47], v81 offset:4608\n\t" \
    "s_waitcnt lgkmcnt(8)\n\t" \
    "v_mfma_f32_16x16x32_bf16 v[156:159], v[28:31], v[64:67], 0\n\t" \
    "s_waitcnt lgkmcnt(6)\n\t" \
    "v_mfma_f32_16x16x32_bf16 v[160:163], v[0:3], v[68:71], 0\n\t" \
    "v_mfma_f32_16x16x32_bf16 v[164:167], v[4:7], v[68:71], 0\n\t" \
    "ds_read_b64_tr_b16 v[76:77], v85 offset:0\n\t" \
    "v_mfma_f32_16x16x32_bf16 v[168:171], v[8:11], v[68:71], 0\n\t" \
    "ds_read_b64_tr_b16 v[78:79], v85 offset:1024\n\t" \
    "v_mfma_f32_16x16x32_bf16 v[172:175], v[12:15], v[68:71], 0\n\t" \
    "ds_read_b128 v[48:51], v81 offset:8192\n\t" \
    "v_mfma_f32_16x16x32_bf16 v[176:179], v[16:19], v[68:71], 0\n\t" \
    "ds_read_b128 v[52:55], v81 offset:8704\n\t" \
    "v_mfma_f32_16x16x32_bf16 v[180:183], v[20:23], v[68:71], 0\n\t" \
    "ds_read_b128 v[56:59], v81 offset:12288\n\t" \
    "v_mfma_f32_16x16x32_bf16 v[184:187], v[24:27], v[68:71], 0\n\t" \
    "ds_read_b128 v[60:63], v81 offset:12800\n\t" \
    "v_mfma_f32_16x16x32_bf16 v[188:191], v[28:31], v[68:71], 0\n\t" \
    "s_waitcnt lgkmcnt(10)\n\t" \
    "v_mfma_f32_16x16x32_bf16 v[192:195], v[0:3], v[72:75], 0\n\t" \
    "v_mfma_f32_16x16x32_bf16 v[196:199], v[4:7], v[72:75], 0\n\t" \
    "ds_read_b64_tr_b16 v[64:65], v86 offset:0\n\t" \
    "v_mfma_f32_16x16x32_bf16 v[200:203], v[8:11], v[72:75], 0\n\t" \
    "ds_read_b64_tr_b16 v[66:67], v86 offset:1024\n\t" \
    "v_mfma_f32_16x16x32_bf16 v[204:207], v[12:15], v[72:75], 0\n\t" \
    "s_mov_b32 s28, 1310720\n\t" \
    "s_add_u32 m0, s26, 81920\n\t" \
    "v_mfma_f32_16x16x32_bf16 v[208:211], v[16:19], v[72:75], 0\n\t" \
    "buffer_load_dwordx4 v106, s[12:15], s28 offen offset:0 lds\n\t" \
    "v_mfma_f32_16x16x32_bf16 v[212:215], v[20:23], v[72:75], 0\n\t" \
    "v_mfma_f32_16x16x32_bf16 v[216:219], v[24:27], v[72:75], 0\n\t" \
    "v_mfma_f32_16x16x32_bf16 v[220:223], v[28:31], v[72:75], 0\n\t" \
    "s_waitcnt lgkmcnt(6)\n\t" \
    "v_mfma_f32_16x16x32_bf16 v[224:227], v[0:3], v[76:79], 0\n\t" \
    "v_mfma_f32_16x16x32_bf16 v[228:231], v[4:7], v[76:79], 0\n\t" \
    "ds_read_b64_tr_b16 v[68:69], v87 offset:0\n\t" \
    "v_mfma_f32_16x16x32_bf16 v[232:235], v[8:11], v[76:79], 0\n\t" \
    "ds_read_b64_tr_b16 v[70:71], v87 offset:1024\n\t" \
    "v_mfma_f32_16x16x32_bf16 v[236:239], v[12:15], v[76:79], 0\n\t" \
    "v_mfma_f32_16x16x32_bf16 v[240:243], v[16:19], v[76:79], 0\n\t" \
    "buffer_load_dwordx4 v107, s[12:15], s28 offen offset:1024 lds\n\t" \
    "v_mfma_f32_16x16x32_bf16 v[244:247], v[20:23], v[76:79], 0\n\t" \
    "v_mfma_f32_16x16x32_bf16 v[248:251], v[24:27], v[76:79], 0\n\t" \
    "v_mfma_f32_16x16x32_bf16 v[252:255], v[28:31], v[76:79], 0\n\t" \
    "s_waitcnt lgkmcnt(2)\n\t" \
    "v_mfma_f32_16x16x32_bf16 a[0:3], v[0:3], v[64:67], 0\n\t" \
    "v_mfma_f32_16x16x32_bf16 a[4:7], v[4:7], v[64:67], 0\n\t" \
    "ds_read_b64_tr_b16 v[72:73], v88 offset:0\n\t" \
    "v_mfma_f32_16x16x32_bf16 a[8:11], v[8:11], v[64:67], 0\n\t" \
    "ds_read_b64_tr_b16 v[74:75], v88 offset:1024\n\t" \
    "v_mfma_f32_16x16x32_bf16 a[12:15], v[12:15], v[64:67], 0\n\t" \
    "v_mfma_f32_16x16x32_bf16 a[16:19], v[16:19], v[64:67], 0\n\t" \
    "buffer_load_dwordx4 v108, s[12:15], s28 offen offset:2048 lds\n\t" \
    "v_mfma_f32_16x16x32_bf16 a[20:23], v[20:23], v[64:67], 0\n\t" \
    "v_mfma_f32_16x16x32_bf16 a[24:27], v[24:27], v[64:67], 0\n\t" \
    "v_mfma_f32_16x16x32_bf16 a[28:31], v[28:31], v[64:67], 0\n\t" \
    "s_waitcnt lgkmcnt(2)\n\t" \
    "v_mfma_f32_16x16x32_bf16 a[32:35], v[0:3], v[68:71], 0\n\t" \
    "v_mfma_f32_16x16x32_bf16 a[36:39], v[4:7], v[68:71], 0\n\t" \
    "ds_read_b64_tr_b16 v[76:77], v89 offset:0\n\t" \
    "v_mfma_f32_16x16x32_bf16 a[40:43], v[8:11], v[68:71], 0\n\t" \
    "ds_read_b64_tr_b16 v[78:79], v89 offset:1024\n\t" \
    "v_mfma_f32_16x16x32_bf16 a[44:47], v[12:15], v[68:71], 0\n\t" \
    "v_mfma_f32_16x16x32_bf16 a[48:51], v[16:19], v[68:71], 0\n\t" \
    "buffer_load_dwordx4 v109, s[12:15], s28 offen offset:3072 lds\n\t" \
    "v_mfma_f32_16x16x32_bf16 a[52:55], v[20:23], v[68:71], 0\n\t" \
    "v_mfma_f32_16x16x32_bf16 a[56:59], v[24:27], v[68:71], 0\n\t" \
    "v_mfma_f32_16x16x32_bf16 a[60:63], v[28:31], v[68:71], 0\n\t" \
    "s_waitcnt vmcnt(26) lgkmcnt(0)\n\t" \
    "s_barrier\n\t" \
    "v_mfma_f32_16x16x32_bf16 a[64:67], v[0:3], v[72:75], 0\n\t" \
    "v_mfma_f32_16x16x32_bf16 a[68:71], v[4:7], v[72:75], 0\n\t" \
    "ds_read_b64_tr_b16 v[64:65], v82 offset:16384\n\t" \
    "v_mfma_f32_16x16x32_bf16 a[72:75], v[8:11], v[72:75], 0\n\t" \
    "ds_read_b64_tr_b16 v[66:67], v82 offset:17408\n\t" \
    "v_mfma_f32_16x16x32_bf16 a[76:79], v[12:15], v[72:75], 0\n\t" \
    "v_mfma_f32_16x16x32_bf16 a[80:83], v[16:19], v[72:75], 0\n\t" \
    "v_mfma_f32_16x16x32_bf16 a[84:87], v[20:23], v[72:75], 0\n\t" \
    "v_mfma_f32_16x16x32_bf16 a[88:91], v[24:27], v[72:75], 0\n\t" \
    "v_mfma_f32_16x16x32_bf16 a[92:95], v[28:31], v[72:75], 0\n\t" \
    "v_mfma_f32_16x16x32_bf16 a[96:99], v[0:3], v[76:79], 0\n\t" \
    "v_mfma_f32_16x16x32_bf16 a[100:103], v[4:7], v[76:79], 0\n\t" \
    "ds_read_b64_tr_b16 v[68:69], v83 offset:16384\n\t" \
    "v_mfma_f32_16x16x32_bf16 a[104:107], v[8:11], v[76:79], 0\n\t" \
    "ds_read_b64_tr_b16 v[70:71], v83 offset:17408\n\t" \
    "v_mfma_f32_16x16x32_bf16 a[108:111], v[12:15], v[76:79], 0\n\t" \
    "v_mfma_f32_16x16x32_bf16 a[112:115], v[16:19], v[76:79], 0\n\t" \
    "v_mfma_f32_16x16x32_bf16 a[116:119], v[20:23], v[76:79], 0\n\t" \
    "v_mfma_f32_16x16x32_bf16 a[120:123], v[24:27], v[76:79], 0\n\t" \
    "v_mfma_f32_16x16x32_bf16 a[124:127], v[28:31], v[76:79], 0\n\t" \
    "s_waitcnt lgkmcnt(2)\n\t" \
    "v_mfma_f32_16x16x32_bf16 v[128:131], v[32:35], v[64:67], v[128:131]\n\t" \
    "v_mfma_f32_16x16x32_bf16 v[132:135], v[36:39], v[64:67], v[132:135]\n\t" \
    "ds_read_b64_tr_b16 v[72:73], v84 offset:16384\n\t" \
    "v_mfma_f32_16x16x32_bf16 v[136:139], v[40:43], v[64:67], v[136:139]\n\t" \
    "ds_read_b64_tr_b16 v[74:75], v84 offset:17408\n\t" \
    "v_mfma_f32_16x16x32_bf16 v[140:143], v[44:47], v[64:67], v[140:143]\n\t" \
    "s_mov_b32 s27, 256\n\t" \
    "s_add_u32 m0, s25, 0\n\t" \
    "v_mfma_f32_16x16x32_bf16 v[144:147], v[48:51], v[64:67], v[144:147]\n\t" \
    "buffer_load_dwordx4 v98, s[8:11], s27 offen offset:0 lds\n\t" \
    "v_mfma_f32_16x16x32_bf16 v[148:151], v[52:55], v[64:67], v[148:151]\n\t" \
    "v_mfma_f32_16x16x32_bf16 v[152:155], v[56:59], v[64:67], v[152:155]\n\t" \
    "buffer_load_dwordx4 v99, s[8:11], s27 offen offset:1024 lds\n\t" \
    "v_mfma_f32_16x16x32_bf16 v[156:159], v[60:63], v[64:67], v[156:159]\n\t" \
    "s_waitcnt lgkmcnt(2)\n\t" \
    "v_mfma_f32_16x16x32_bf16 v[160:163], v[32:35], v[68:71], v[160:163]\n\t" \
    "v_mfma_f32_16x16x32_bf16 v[164:167], v[36:39], v[68:71], v[164:167]\n\t" \
    "ds_read_b64_tr_b16 v[76:77], v85 offset:16384\n\t" \
    "v_mfma_f32_16x16x32_bf16 v[168:171], v[40:43], v[68:71], v[168:171]\n\t" \
    "ds_read_b64_tr_b16 v[78:79], v85 offset:17408\n\t" \
    "v_mfma_f32_16x16x32_bf16 v[172:175], v[44:47], v[68:71], v[172:175]\n\t" \
    "v_mfma_f32_16x16x32_bf16 v[176:179], v[48:51], v[68:71], v[176:179]\n\t" \
    "buffer_load_dwordx4 v100, s[8:11], s27 offen offset:2048 lds\n\t" \
    "v_mfma_f32_16x16x32_bf16 v[180:183], v[52:55], v[68:71], v[180:183]\n\t" \
    "v_mfma_f32_16x16x32_bf16 v[184:187], v[56:59], v[68:71], v[184:187]\n\t" \
    "buffer_load_dwordx4 v101, s[8:11], s27 offen offset:3072 lds\n\t" \
    "v_mfma_f32_16x16x32_bf16 v[188:191], v[60:63], v[68:71], v[188:191]\n\t" \
    "s_waitcnt lgkmcnt(2)\n\t" \
    "v_mfma_f32_16x16x32_bf16 v[192:195], v[32:35], v[72:75], v[192:195]\n\t" \
    "v_mfma_f32_16x16x32_bf16 v[196:199], v[36:39], v[72:75], v[196:199]\n\t" \
    "ds_read_b64_tr_b16 v[64:65], v86 offset:16384\n\t" \
    "v_mfma_f32_16x16x32_bf16 v[200:203], v[40:43], v[72:75], v[200:203]\n\t" \
    "ds_read_b64_tr_b16 v[66:67], v86 offset:17408\n\t" \
    "s_add_u32 m0, s25, 4096\n\t" \
    "v_mfma_f32_16x16x32_bf16 v[204:207], v[44:47], v[72:75], v[204:207]\n\t" \
    "buffer_load_dwordx4 v102, s[8:11], s27 offen offset:0 lds\n\t" \
    "v_mfma_f32_16x16x32_bf16 v[208:211], v[48:51], v[72:75], v[208:211]\n\t" \
    "v_mfma_f32_16x16x32_bf16 v[212:215], v[52:55], v[72:75], v[212:215]\n\t" \
    "buffer_load_dwordx4 v103, s[8:11], s27 offen offset:1024 lds\n\t" \
    "v_mfma_f32_16x16x32_bf16 v[216:219], v[56:59], v[72:75], v[216:219]\n\t" \
    "v_mfma_f32_16x16x32_bf16 v[220:223], v[60:63], v[72:75], v[220:223]\n\t" \
    "s_waitcnt lgkmcnt(2)\n\t" \
    "v_mfma_f32_16x16x32_bf16 v[224:227], v[32:35], v[76:79], v[224:227]\n\t" \
    "v_mfma_f32_16x16x32_bf16 v[228:231], v[36:39], v[76:79], v[228:231]\n\t" \
    "ds_read_b64_tr_b16 v[68:69], v87 offset:16384\n\t" \
    "v_mfma_f32_16x16x32_bf16 v[232:235], v[40:43], v[76:79], v[232:235]\n\t" \
    "ds_read_b64_tr_b16 v[70:71], v87 offset:17408\n\t" \
    "v_mfma_f32_16x16x32_bf16 v[236:239], v[44:47], v[76:79], v[236:239]\n\t" \
    "buffer_load_dwordx4 v104, s[8:11], s27 offen offset:2048 lds\n\t" \
    "v_mfma_f32_16x16x32_bf16 v[240:243], v[48:51], v[76:79], v[240:243]\n\t" \
    "v_mfma_f32_16x16x32_bf16 v[244:247], v[52:55], v[76:79], v[244:247]\n\t" \
    "buffer_load_dwordx4 v105, s[8:11], s27 offen offset:3072 lds\n\t" \
    "v_mfma_f32_16x16x32_bf16 v[248:251], v[56:59], v[76:79], v[248:251]\n\t" \
    "v_mfma_f32_16x16x32_bf16 v[252:255], v[60:63], v[76:79], v[252:255]\n\t" \
    "s_waitcnt lgkmcnt(2)\n\t" \
    "v_mfma_f32_16x16x32_bf16 a[0:3], v[32:35], v[64:67], a[0:3]\n\t" \
    "v_mfma_f32_16x16x32_bf16 a[4:7], v[36:39], v[64:67], a[4:7]\n\t" \
    "ds_read_b64_tr_b16 v[72:73], v88 offset:16384\n\t" \
    "v_mfma_f32_16x16x32_bf16 a[8:11], v[40:43], v[64:67], a[8:11]\n\t" \
    "ds_read_b64_tr_b16 v[74:75], v88 offset:17408\n\t" \
    "s_mov_b32 s28, 1572864\n\t" \
    "s_add_u32 m0, s26, 0\n\t" \
    "v_mfma_f32_16x16x32_bf16 a[12:15], v[44:47], v[64:67], a[12:15]\n\t" \
    "buffer_load_dwordx4 v106, s[12:15], s28 offen offset:0 lds\n\t" \
    "v_mfma_f32_16x16x32_bf16 a[16:19], v[48:51], v[64:67], a[16:19]\n\t" \
    "v_mfma_f32_16x16x32_bf16 a[20:23], v[52:55], v[64:67], a[20:23]\n\t" \
    "buffer_load_dwordx4 v107, s[12:15], s28 offen offset:1024 lds\n\t" \
    "v_mfma_f32_16x16x32_bf16 a[24:27], v[56:59], v[64:67], a[24:27]\n\t" \
    "v_mfma_f32_16x16x32_bf16 a[28:31], v[60:63], v[64:67], a[28:31]\n\t" \
    "s_waitcnt lgkmcnt(2)\n\t" \
    "v_mfma_f32_16x16x32_bf16 a[32:35], v[32:35], v[68:71], a[32:35]\n\t" \
    "v_mfma_f32_16x16x32_bf16 a[36:39], v[36:39], v[68:71], a[36:39]\n\t" \
    "ds_read_b64_tr_b16 v[76:77], v89 offset:16384\n\t" \
    "v_mfma_f32_16x16x32_bf16 a[40:43], v[40:43], v[68:71], a[40:43]\n\t" \
    "ds_read_b64_tr_b16 v[78:79], v89 offset:17408\n\t" \
    "v_mfma_f32_16x16x32_bf16 a[44:47], v[44:47], v[68:71], a[44:47]\n\t" \
    "buffer_load_dwordx4 v108, s[12:15], s28 offen offset:2048 lds\n\t" \
    "v_mfma_f32_16x16x32_bf16 a[48:51], v[48:51], v[68:71], a[48:51]\n\t" \
    "v_mfma_f32_16x16x32_bf16 a[52:55], v[52:55], v[68:71], a[52:55]\n\t" \
    "buffer_load_dwordx4 v109, s[12:15], s28 offen offset:3072 lds\n\t" \
    "v_mfma_f32_16x16x32_bf16 a[56:59], v[56:59], v[68:71], a[56:59]\n\t" \
    "v_mfma_f32_16x16x32_bf16 a[60:63], v[60:63], v[68:71], a[60:63]\n\t" \
    "s_waitcnt vmcnt(26) lgkmcnt(0)\n\t" \
    "s_barrier\n\t" \
    "v_mfma_f32_16x16x32_bf16 a[64:67], v[32:35], v[72:75], a[64:67]\n\t" \
    "v_mfma_f32_16x16x32_bf16 a[68:71], v[36:39], v[72:75], a[68:71]\n\t" \
    "ds_read_b64_tr_b16 v[64:65], v82 offset:32768\n\t" \
    "v_mfma_f32_16x16x32_bf16 a[72:75], v[40:43], v[72:75], a[72:75]\n\t" \
    "ds_read_b64_tr_b16 v[66:67], v82 offset:33792\n\t" \
    "v_mfma_f32_16x16x32_bf16 a[76:79], v[44:47], v[72:75], a[76:79]\n\t" \
    "ds_read_b128 v[0:3], v80 offset:32768\n\t" \
    "v_mfma_f32_16x16x32_bf16 a[80:83], v[48:51], v[72:75], a[80:83]\n\t" \
    "ds_read_b128 v[4:7], v80 offset:33280\n\t" \
    "v_mfma_f32_16x16x32_bf16 a[84:87], v[52:55], v[72:75], a[84:87]\n\t" \
    "ds_read_b128 v[8:11], v80 offset:36864\n\t" \
    "v_mfma_f32_16x16x32_bf16 a[88:91], v[56:59], v[72:75], a[88:91]\n\t" \
    "ds_read_b128 v[12:15], v80 offset:37376\n\t" \
    "v_mfma_f32_16x16x32_bf16 a[92:95], v[60:63], v[72:75], a[92:95]\n\t" \
    "ds_read_b128 v[16:19], v80 offset:40960\n\t" \
    "v_mfma_f32_16x16x32_bf16 a[96:99], v[32:35], v[76:79], a[96:99]\n\t" \
    "ds_read_b128 v[20:23], v80 offset:41472\n\t" \
    "v_mfma_f32_16x16x32_bf16 a[100:103], v[36:39], v[76:79], a[100:103]\n\t" \
    "ds_read_b128 v[24:27], v80 offset:45056\n\t" \
    "v_mfma_f32_16x16x32_bf16 a[104:107], v[40:43], v[76:79], a[104:107]\n\t" \
    "ds_read_b128 v[28:31], v80 offset:45568\n\t" \
    "v_mfma_f32_16x16x32_bf16 a[108:111], v[44:47], v[76:79], a[108:111]\n\t" \
    "ds_read_b64_tr_b16 v[68:69], v83 offset:32768\n\t" \
    "v_mfma_f32_16x16x32_bf16 a[112:115], v[48:51], v[76:79], a[112:115]\n\t" \
    "ds_read_b64_tr_b16 v[70:71], v83 offset:33792\n\t" \
    "v_mfma_f32_16x16x32_bf16 a[116:119], v[52:55], v[76:79], a[116:119]\n\t" \
    "v_mfma_f32_16x16x32_bf16 a[120:123], v[56:59], v[76:79], a[120:123]\n\t" \
    "v_mfma_f32_16x16x32_bf16 a[124:127], v[60:63], v[76:79], a[124:127]\n\t" \
    "s_waitcnt lgkmcnt(9)\n\t" \
    "v_mfma_f32_16x16x32_bf16 v[128:131], v[0:3], v[64:67], v[128:131]\n\t" \
    "s_waitcnt lgkmcnt(8)\n\t" \
    "v_mfma_f32_16x16x32_bf16 v[132:135], v[4:7], v[64:67], v[132:135]\n\t" \
    "ds_read_b64_tr_b16 v[72:73], v84 offset:32768\n\t" \
    "s_waitcnt lgkmcnt(8)\n\t" \
    "v_mfma_f32_16x16x32_bf16 v[136:139], v[8:11], v[64:67], v[136:139]\n\t" \
    "ds_read_b64_tr_b16 v[74:75], v84 offset:33792\n\t" \
    "s_waitcnt lgkmcnt(8)\n\t" \
    "v_mfma_f32_16x16x32_bf16 v[140:143], v[12:15], v[64:67], v[140:143]\n\t" \
    "ds_read_b128 v[32:35], v81 offset:32768\n\t" \
    "s_waitcnt lgkmcnt(8)\n\t" \
    "v_mfma_f32_16x16x32_bf16 v[144:147], v[16:19], v[64:67], v[144:147]\n\t" \
    "ds_read_b128 v[36:39], v81 offset:33280\n\t" \
    "s_waitcnt lgkmcnt(8)\n\t" \
    "v_mfma_f32_16x16x32_bf16 v[148:151], v[20:23], v[64:67], v[148:151]\n\t" \
    "ds_read_b128 v[40:43], v81 offset:36864\n\t" \
    "s_waitcnt lgkmcnt(8)\n\t" \
    "v_mfma_f32_16x16x32_bf16 v[152:155], v[24:27], v[64:67], v[152:155]\n\t" \
    "ds_read_b128 v[44:47], v81 offset:37376\n\t" \
    "s_waitcnt lgkmcnt(8)\n\t" \
    "v_mfma_f32_16x16x32_bf16 v[156:159], v[28:31], v[64:67], v[156:159]\n\t" \
    "s_waitcnt lgkmcnt(6)\n\t" \
    "v_mfma_f32_16x16x32_bf16 v[160:163], v[0:3], v[68:71], v[160:163]\n\t" \
    "v_mfma_f32_16x16x32_bf16 v[164:167], v[4:7], v[68:71], v[164:167]\n\t" \
    "ds_read_b64_tr_b16 v[76:77], v85 offset:32768\n\t" \
    "v_mfma_f32_16x16x32_bf16 v[168:171], v[8:11], v[68:71], v[168:171]\n\t" \
    "ds_read_b64_tr_b16 v[78:79], v85 offset:33792\n\t" \
    "v_mfma_f32_16x16x32_bf16 v[172:175], v[12:15], v[68:71], v[172:175]\n\t" \
    "ds_read_b128 v[48:51], v81 offset:40960\n\t" \
    "v_mfma_f32_16x16x32_bf16 v[176:179], v[16:19], v[68:71], v[176:179]\n\t" \
    "ds_read_b128 v[52:55], v81 offset:41472\n\t" \
    "v_mfma_f32_16x16x32_bf16 v[180:183], v[20:23], v[68:71], v[180:183]\n\t" \
    "ds_read_b128 v[56:59], v81 offset:45056\n\t" \
    "v_mfma_f32_16x16x32_bf16 v[184:187], v[24:27], v[68:71], v[184:187]\n\t" \
    "ds_read_b128 v[60:63], v81 offset:45568\n\t" \
    "v_mfma_f32_16x16x32_bf16 v[188:191], v[28:31], v[68:71], v[188:191]\n\t" \
    "s_waitcnt lgkmcnt(10)\n\t" \
    "v_mfma_f32_16x16x32_bf16 v[192:195], v[0:3], v[72:75], v[192:195]\n\t" \
    "v_mfma_f32_16x16x32_bf16 v[196:199], v[4:7], v[72:75], v[196:199]\n\t" \
    "ds_read_b64_tr_b16 v[64:65], v86 offset:32768\n\t" \
    "v_mfma_f32_16x16x32_bf16 v[200:203], v[8:11], v[72:75], v[200:203]\n\t" \
    "ds_read_b64_tr_b16 v[66:67], v86 offset:33792\n\t" \
    "v_mfma_f32_16x16x32_bf16 v[204:207], v[12:15], v[72:75], v[204:207]\n\t" \
    "s_mov_b32 s28, 1835008\n\t" \
    "s_add_u32 m0, s26, 16384\n\t" \
    "v_mfma_f32_16x16x32_bf16 v[208:211], v[16:19], v[72:75], v[208:211]\n\t" \
    "buffer_load_dwordx4 v106, s[12:15], s28 offen offset:0 lds\n\t" \
    "v_mfma_f32_16x16x32_bf16 v[212:215], v[20:23], v[72:75], v[212:215]\n\t" \
    "v_mfma_f32_16x16x32_bf16 v[216:219], v[24:27], v[72:75], v[216:219]\n\t" \
    "v_mfma_f32_16x16x32_bf16 v[220:223], v[28:31], v[72:75], v[220:223]\n\t" \
    "s_waitcnt lgkmcnt(6)\n\t" \
    "v_mfma_f32_16x16x32_bf16 v[224:227], v[0:3], v[76:79], v[224:227]\n\t" \
    "v_mfma_f32_16x16x32_bf16 v[228:231], v[4:7], v[76:79], v[228:231]\n\t" \
    "ds_read_b64_tr_b16 v[68:69], v87 offset:32768\n\t" \
    "v_mfma_f32_16x16x32_bf16 v[232:235], v[8:11], v[76:79], v[232:235]\n\t" \
    "ds_read_b64_tr_b16 v[70:71], v87 offset:33792\n\t" \
    "v_mfma_f32_16x16x32_bf16 v[236:239], v[12:15], v[76:79], v[236:239]\n\t" \
    "v_mfma_f32_16x16x32_bf16 v[240:243], v[16:19], v[76:79], v[240:243]\n\t" \
    "buffer_load_dwordx4 v107, s[12:15], s28 offen offset:1024 lds\n\t" \
    "v_mfma_f32_16x16x32_bf16 v[244:247], v[20:23], v[76:79], v[244:247]\n\t" \
    "v_mfma_f32_16x16x32_bf16 v[248:251], v[24:27], v[76:79], v[248:251]\n\t" \
    "v_mfma_f32_16x16x32_bf16 v[252:255], v[28:31], v[76:79], v[252:255]\n\t" \
    "s_waitcnt lgkmcnt(2)\n\t" \
    "v_mfma_f32_16x16x32_bf16 a[0:3], v[0:3], v[64:67], a[0:3]\n\t" \
    "v_mfma_f32_16x16x32_bf16 a[4:7], v[4:7], v[64:67], a[4:7]\n\t" \
    "ds_read_b64_tr_b16 v[72:73], v88 offset:32768\n\t" \
    "v_mfma_f32_16x16x32_bf16 a[8:11], v[8:11], v[64:67], a[8:11]\n\t" \
    "ds_read_b64_tr_b16 v[74:75], v88 offset:33792\n\t" \
    "v_mfma_f32_16x16x32_bf16 a[12:15], v[12:15], v[64:67], a[12:15]\n\t" \
    "v_mfma_f32_16x16x32_bf16 a[16:19], v[16:19], v[64:67], a[16:19]\n\t" \
    "buffer_load_dwordx4 v108, s[12:15], s28 offen offset:2048 lds\n\t" \
    "v_mfma_f32_16x16x32_bf16 a[20:23], v[20:23], v[64:67], a[20:23]\n\t" \
    "v_mfma_f32_16x16x32_bf16 a[24:27], v[24:27], v[64:67], a[24:27]\n\t" \
    "v_mfma_f32_16x16x32_bf16 a[28:31], v[28:31], v[64:67], a[28:31]\n\t" \
    "s_waitcnt lgkmcnt(2)\n\t" \
    "v_mfma_f32_16x16x32_bf16 a[32:35], v[0:3], v[68:71], a[32:35]\n\t" \
    "v_mfma_f32_16x16x32_bf16 a[36:39], v[4:7], v[68:71], a[36:39]\n\t" \
    "ds_read_b64_tr_b16 v[76:77], v89 offset:32768\n\t" \
    "v_mfma_f32_16x16x32_bf16 a[40:43], v[8:11], v[68:71], a[40:43]\n\t" \
    "ds_read_b64_tr_b16 v[78:79], v89 offset:33792\n\t" \
    "v_mfma_f32_16x16x32_bf16 a[44:47], v[12:15], v[68:71], a[44:47]\n\t" \
    "v_mfma_f32_16x16x32_bf16 a[48:51], v[16:19], v[68:71], a[48:51]\n\t" \
    "buffer_load_dwordx4 v109, s[12:15], s28 offen offset:3072 lds\n\t" \
    "v_mfma_f32_16x16x32_bf16 a[52:55], v[20:23], v[68:71], a[52:55]\n\t" \
    "v_mfma_f32_16x16x32_bf16 a[56:59], v[24:27], v[68:71], a[56:59]\n\t" \
    "v_mfma_f32_16x16x32_bf16 a[60:63], v[28:31], v[68:71], a[60:63]\n\t" \
    "s_waitcnt vmcnt(26) lgkmcnt(0)\n\t" \
    "s_barrier\n\t" \
    "v_mfma_f32_16x16x32_bf16 a[64:67], v[0:3], v[72:75], a[64:67]\n\t" \
    "v_mfma_f32_16x16x32_bf16 a[68:71], v[4:7], v[72:75], a[68:71]\n\t" \
    "ds_read_b64_tr_b16 v[64:65], v82 offset:49152\n\t" \
    "v_mfma_f32_16x16x32_bf16 a[72:75], v[8:11], v[72:75], a[72:75]\n\t" \
    "ds_read_b64_tr_b16 v[66:67], v82 offset:50176\n\t" \
    "v_mfma_f32_16x16x32_bf16 a[76:79], v[12:15], v[72:75], a[76:79]\n\t" \
    "v_mfma_f32_16x16x32_bf16 a[80:83], v[16:19], v[72:75], a[80:83]\n\t" \
    "v_mfma_f32_16x16x32_bf16 a[84:87], v[20:23], v[72:75], a[84:87]\n\t" \
    "v_mfma_f32_16x16x32_bf16 a[88:91], v[24:27], v[72:75], a[88:91]\n\t" \
    "v_mfma_f32_16x16x32_bf16 a[92:95], v[28:31], v[72:75], a[92:95]\n\t" \
    "v_mfma_f32_16x16x32_bf16 a[96:99], v[0:3], v[76:79], a[96:99]\n\t" \
    "v_mfma_f32_16x16x32_bf16 a[100:103], v[4:7], v[76:79], a[100:103]\n\t" \
    "ds_read_b64_tr_b16 v[68:69], v83 offset:49152\n\t" \
    "v_mfma_f32_16x16x32_bf16 a[104:107], v[8:11], v[76:79], a[104:107]\n\t" \
    "ds_read_b64_tr_b16 v[70:71], v83 offset:50176\n\t" \
    "v_mfma_f32_16x16x32_bf16 a[108:111], v[12:15], v[76:79], a[108:111]\n\t" \
    "v_mfma_f32_16x16x32_bf16 a[112:115], v[16:19], v[76:79], a[112:115]\n\t" \
    "v_mfma_f32_16x16x32_bf16 a[116:119], v[20:23], v[76:79], a[116:119]\n\t" \
    "v_mfma_f32_16x16x32_bf16 a[120:123], v[24:27], v[76:79], a[120:123]\n\t" \
    "v_mfma_f32_16x16x32_bf16 a[124:127], v[28:31], v[76:79], a[124:127]\n\t" \
    "s_waitcnt lgkmcnt(2)\n\t" \
    "v_mfma_f32_16x16x32_bf16 v[128:131], v[32:35], v[64:67], v[128:131]\n\t" \
    "v_mfma_f32_16x16x32_bf16 v[132:135], v[36:39], v[64:67], v[132:135]\n\t" \
    "ds_read_b64_tr_b16 v[72:73], v84 offset:49152\n\t" \
    "v_mfma_f32_16x16x32_bf16 v[136:139], v[40:43], v[64:67], v[136:139]\n\t" \
    "ds_read_b64_tr_b16 v[74:75], v84 offset:50176\n\t" \
    "v_mfma_f32_16x16x32_bf16 v[140:143], v[44:47], v[64:67], v[140:143]\n\t" \
    "s_mov_b32 s27, 384\n\t" \
    "s_add_u32 m0, s25, 32768\n\t" \
    "v_mfma_f32_16x16x32_bf16 v[144:147], v[48:51], v[64:67], v[144:147]\n\t" \
    "buffer_load_dwordx4 v98, s[8:11], s27 offen offset:0 lds\n\t" \
    "v_mfma_f32_16x16x32_bf16 v[148:151], v[52:55], v[64:67], v[148:151]\n\t" \
    "v_mfma_f32_16x16x32_bf16 v[152:155], v[56:59], v[64:67], v[152:155]\n\t" \
    "buffer_load_dwordx4 v99, s[8:11], s27 offen offset:1024 lds\n\t" \
    "v_mfma_f32_16x16x32_bf16 v[156:159], v[60:63], v[64:67], v[156:159]\n\t" \
    "s_waitcnt lgkmcnt(2)\n\t" \
    "v_mfma_f32_16x16x32_bf16 v[160:163], v[32:35], v[68:71], v[160:163]\n\t" \
    "v_mfma_f32_16x16x32_bf16 v[164:167], v[36:39], v[68:71], v[164:167]\n\t" \
    "ds_read_b64_tr_b16 v[76:77], v85 offset:49152\n\t" \
    "v_mfma_f32_16x16x32_bf16 v[168:171], v[40:43], v[68:71], v[168:171]\n\t" \
    "ds_read_b64_tr_b16 v[78:79], v85 offset:50176\n\t" \
    "v_mfma_f32_16x16x32_bf16 v[172:175], v[44:47], v[68:71], v[172:175]\n\t" \
    "v_mfma_f32_16x16x32_bf16 v[176:179], v[48:51], v[68:71], v[176:179]\n\t" \
    "buffer_load_dwordx4 v100, s[8:11], s27 offen offset:2048 lds\n\t" \
    "v_mfma_f32_16x16x32_bf16 v[180:183], v[52:55], v[68:71], v[180:183]\n\t" \
    "v_mfma_f32_16x16x32_bf16 v[184:187], v[56:59], v[68:71], v[184:187]\n\t" \
    "buffer_load_dwordx4 v101, s[8:11], s27 offen offset:3072 lds\n\t" \
    "v_mfma_f32_16x16x32_bf16 v[188:191], v[60:63], v[68:71], v[188:191]\n\t" \
    "s_waitcnt lgkmcnt(2)\n\t" \
    "v_mfma_f32_16x16x32_bf16 v[192:195], v[32:35], v[72:75], v[192:195]\n\t" \
    "v_mfma_f32_16x16x32_bf16 v[196:199], v[36:39], v[72:75], v[196:199]\n\t" \
    "ds_read_b64_tr_b16 v[64:65], v86 offset:49152\n\t" \
    "v_mfma_f32_16x16x32_bf16 v[200:203], v[40:43], v[72:75], v[200:203]\n\t" \
    "ds_read_b64_tr_b16 v[66:67], v86 offset:50176\n\t" \
    "s_add_u32 m0, s25, 36864\n\t" \
    "v_mfma_f32_16x16x32_bf16 v[204:207], v[44:47], v[72:75], v[204:207]\n\t" \
    "buffer_load_dwordx4 v102, s[8:11], s27 offen offset:0 lds\n\t" \
    "v_mfma_f32_16x16x32_bf16 v[208:211], v[48:51], v[72:75], v[208:211]\n\t" \
    "v_mfma_f32_16x16x32_bf16 v[212:215], v[52:55], v[72:75], v[212:215]\n\t" \
    "buffer_load_dwordx4 v103, s[8:11], s27 offen offset:1024 lds\n\t" \
    "v_mfma_f32_16x16x32_bf16 v[216:219], v[56:59], v[72:75], v[216:219]\n\t" \
    "v_mfma_f32_16x16x32_bf16 v[220:223], v[60:63], v[72:75], v[220:223]\n\t" \
    "s_waitcnt lgkmcnt(2)\n\t" \
    "v_mfma_f32_16x16x32_bf16 v[224:227], v[32:35], v[76:79], v[224:227]\n\t" \
    "v_mfma_f32_16x16x32_bf16 v[228:231], v[36:39], v[76:79], v[228:231]\n\t" \
    "ds_read_b64_tr_b16 v[68:69], v87 offset:49152\n\t" \
    "v_mfma_f32_16x16x32_bf16 v[232:235], v[40:43], v[76:79], v[232:235]\n\t" \
    "ds_read_b64_tr_b16 v[70:71], v87 offset:50176\n\t" \
    "v_mfma_f32_16x16x32_bf16 v[236:239], v[44:47], v[76:79], v[236:239]\n\t" \
    "buffer_load_dwordx4 v104, s[8:11], s27 offen offset:2048 lds\n\t" \
    "v_mfma_f32_16x16x32_bf16 v[240:243], v[48:51], v[76:79], v[240:243]\n\t" \
    "v_mfma_f32_16x16x32_bf16 v[244:247], v[52:55], v[76:79], v[244:247]\n\t" \
    "buffer_load_dwordx4 v105, s[8:11], s27 offen offset:3072 lds\n\t" \
    "v_mfma_f32_16x16x32_bf16 v[248:251], v[56:59], v[76:79], v[248:251]\n\t" \
    "v_mfma_f32_16x16x32_bf16 v[252:255], v[60:63], v[76:79], v[252:255]\n\t" \
    "s_waitcnt lgkmcnt(2)\n\t" \
    "v_mfma_f32_16x16x32_bf16 a[0:3], v[32:35], v[64:67], a[0:3]\n\t" \
    "v_mfma_f32_16x16x32_bf16 a[4:7], v[36:39], v[64:67], a[4:7]\n\t" \
    "ds_read_b64_tr_b16 v[72:73], v88 offset:49152\n\t" \
    "v_mfma_f32_16x16x32_bf16 a[8:11], v[40:43], v[64:67], a[8:11]\n\t" \
    "ds_read_b64_tr_b16 v[74:75], v88 offset:50176\n\t" \
    "s_mov_b32 s28, 2097152\n\t" \
    "s_add_u32 m0, s26, 32768\n\t" \
    "v_mfma_f32_16x16x32_bf16 a[12:15], v[44:47], v[64:67], a[12:15]\n\t" \
    "buffer_load_dwordx4 v106, s[12:15], s28 offen offset:0 lds\n\t" \
    "v_mfma_f32_16x16x32_bf16 a[16:19], v[48:51], v[64:67], a[16:19]\n\t" \
    "v_mfma_f32_16x16x32_bf16 a[20:23], v[52:55], v[64:67], a[20:23]\n\t" \
    "buffer_load_dwordx4 v107, s[12:15], s28 offen offset:1024 lds\n\t" \
    "v_mfma_f32_16x16x32_bf16 a[24:27], v[56:59], v[64:67], a[24:27]\n\t" \
    "v_mfma_f32_16x16x32_bf16 a[28:31], v[60:63], v[64:67], a[28:31]\n\t" \
    "s_waitcnt lgkmcnt(2)\n\t" \
    "v_mfma_f32_16x16x32_bf16 a[32:35], v[32:35], v[68:71], a[32:35]\n\t" \
    "v_mfma_f32_16x16x32_bf16 a[36:39], v[36:39], v[68:71], a[36:39]\n\t" \
    "ds_read_b64_tr_b16 v[76:77], v89 offset:49152\n\t" \
    "v_mfma_f32_16x16x32_bf16 a[40:43], v[40:43], v[68:71], a[40:43]\n\t" \
    "ds_read_b64_tr_b16 v[78:79], v89 offset:50176\n\t" \
    "v_mfma_f32_16x16x32_bf16 a[44:47], v[44:47], v[68:71], a[44:47]\n\t" \
    "buffer_load_dwordx4 v108, s[12:15], s28 offen offset:2048 lds\n\t" \
    "v_mfma_f32_16x16x32_bf16 a[48:51], v[48:51], v[68:71], a[48:51]\n\t" \
    "v_mfma_f32_16x16x32_bf16 a[52:55], v[52:55], v[68:71], a[52:55]\n\t" \
    "buffer_load_dwordx4 v109, s[12:15], s28 offen offset:3072 lds\n\t" \
    "v_mfma_f32_16x16x32_bf16 a[56:59], v[56:59], v[68:71], a[56:59]\n\t" \
    "v_mfma_f32_16x16x32_bf16 a[60:63], v[60:63], v[68:71], a[60:63]\n\t" \
    "s_waitcnt vmcnt(20) lgkmcnt(0)\n\t" \
    "s_barrier\n\t" \
    "v_mfma_f32_16x16x32_bf16 a[64:67], v[32:35], v[72:75], a[64:67]\n\t" \
    "v_mfma_f32_16x16x32_bf16 a[68:71], v[36:39], v[72:75], a[68:71]\n\t" \
    "ds_read_b64_tr_b16 v[64:65], v90 offset:0\n\t" \
    "v_mfma_f32_16x16x32_bf16 a[72:75], v[40:43], v[72:75], a[72:75]\n\t" \
    "ds_read_b64_tr_b16 v[66:67], v90 offset:1024\n\t" \
    "v_mfma_f32_16x16x32_bf16 a[76:79], v[44:47], v[72:75], a[76:79]\n\t" \
    "ds_read_b128 v[0:3], v80 offset:0\n\t" \
    "v_mfma_f32_16x16x32_bf16 a[80:83], v[48:51], v[72:75], a[80:83]\n\t" \
    "ds_read_b128 v[4:7], v80 offset:512\n\t" \
    "v_mfma_f32_16x16x32_bf16 a[84:87], v[52:55], v[72:75], a[84:87]\n\t" \
    "ds_read_b128 v[8:11], v80 offset:4096\n\t" \
    "v_mfma_f32_16x16x32_bf16 a[88:91], v[56:59], v[72:75], a[88:91]\n\t" \
    "ds_read_b128 v[12:15], v80 offset:4608\n\t" \
    "v_mfma_f32_16x16x32_bf16 a[92:95], v[60:63], v[72:75], a[92:95]\n\t" \
    "ds_read_b128 v[16:19], v80 offset:8192\n\t" \
    "v_mfma_f32_16x16x32_bf16 a[96:99], v[32:35], v[76:79], a[96:99]\n\t" \
    "ds_read_b128 v[20:23], v80 offset:8704\n\t" \
    "v_mfma_f32_16x16x32_bf16 a[100:103], v[36:39], v[76:79], a[100:103]\n\t" \
    "ds_read_b128 v[24:27], v80 offset:12288\n\t" \
    "v_mfma_f32_16x16x32_bf16 a[104:107], v[40:43], v[76:79], a[104:107]\n\t" \
    "ds_read_b128 v[28:31], v80 offset:12800\n\t" \
    "v_mfma_f32_16x16x32_bf16 a[108:111], v[44:47], v[76:79], a[108:111]\n\t" \
    "ds_read_b64_tr_b16 v[68:69], v91 offset:0\n\t" \
    "v_mfma_f32_16x16x32_bf16 a[112:115], v[48:51], v[76:79], a[112:115]\n\t" \
    "ds_read_b64_tr_b16 v[70:71], v91 offset:1024\n\t" \
    "v_mfma_f32_16x16x32_bf16 a[116:119], v[52:55], v[76:79], a[116:119]\n\t" \
    "v_mfma_f32_16x16x32_bf16 a[120:123], v[56:59], v[76:79], a[120:123]\n\t" \
    "v_mfma_f32_16x16x32_bf16 a[124:127], v[60:63], v[76:79], a[124:127]\n\t" \
    "s_waitcnt lgkmcnt(9)\n\t" \
    "v_mfma_f32_16x16x32_bf16 v[128:131], v[0:3], v[64:67], v[128:131]\n\t" \
    "s_waitcnt lgkmcnt(8)\n\t" \
    "v_mfma_f32_16x16x32_bf16 v[132:135], v[4:7], v[64:67], v[132:135]\n\t" \
    "ds_read_b64_tr_b16 v[72:73], v92 offset:0\n\t" \
    "s_waitcnt lgkmcnt(8)\n\t" \
    "v_mfma_f32_16x16x32_bf16 v[136:139], v[8:11], v[64:67], v[136:139]\n\t" \
    "ds_read_b64_tr_b16 v[74:75], v92 offset:1024\n\t" \
    "s_cmp_eq_u32 s20, 0\n\t" \
    "s_cselect_b32 s23, 1, 0\n\t" \
    "v_mov_b32 v112, s23\n\t" \
    "s_waitcnt lgkmcnt(8)\n\t" \
    "v_mfma_f32_16x16x32_bf16 v[140:143], v[12:15], v[64:67], v[140:143]\n\t" \
    "ds_read_b128 v[32:35], v81 offset:0\n\t" \
    "s_lshr_b32 s23, %[bid], 4\n\t" \
    "s_lshl_b32 s23, s23, 7\n\t" \
    "s_add_u32 s23, s23, 0x80000\n\t" \
    "s_waitcnt lgkmcnt(8)\n\t" \
    "v_mfma_f32_16x16x32_bf16 v[144:147], v[16:19], v[64:67], v[144:147]\n\t" \
    "ds_read_b128 v[36:39], v81 offset:512\n\t" \
    "s_waitcnt lgkmcnt(8)\n\t" \
    "v_mfma_f32_16x16x32_bf16 v[148:151], v[20:23], v[64:67], v[148:151]\n\t" \
    "ds_read_b128 v[40:43], v81 offset:4096\n\t" \
    "s_waitcnt lgkmcnt(8)\n\t" \
    "v_mfma_f32_16x16x32_bf16 v[152:155], v[24:27], v[64:67], v[152:155]\n\t" \
    "ds_read_b128 v[44:47], v81 offset:4608\n\t" \
    "s_waitcnt lgkmcnt(8)\n\t" \
    "v_mfma_f32_16x16x32_bf16 v[156:159], v[28:31], v[64:67], v[156:159]\n\t" \
    "s_waitcnt lgkmcnt(6)\n\t" \
    "v_mfma_f32_16x16x32_bf16 v[160:163], v[0:3], v[68:71], v[160:163]\n\t" \
    "s_mov_b64 exec, 1\n\t" \
    "buffer_atomic_add v112, off, s[16:19], s23 sc1\n\t" \
    "s_mov_b64 exec, -1\n\t" \
    "v_mfma_f32_16x16x32_bf16 v[164:167], v[4:7], v[68:71], v[164:167]\n\t" \
    "ds_read_b64_tr_b16 v[76:77], v93 offset:0\n\t" \
    "v_mfma_f32_16x16x32_bf16 v[168:171], v[8:11], v[68:71], v[168:171]\n\t" \
    "ds_read_b64_tr_b16 v[78:79], v93 offset:1024\n\t" \
    "v_mfma_f32_16x16x32_bf16 v[172:175], v[12:15], v[68:71], v[172:175]\n\t" \
    "ds_read_b128 v[48:51], v81 offset:8192\n\t" \
    "v_mfma_f32_16x16x32_bf16 v[176:179], v[16:19], v[68:71], v[176:179]\n\t" \
    "ds_read_b128 v[52:55], v81 offset:8704\n\t" \
    "v_mfma_f32_16x16x32_bf16 v[180:183], v[20:23], v[68:71], v[180:183]\n\t" \
    "ds_read_b128 v[56:59], v81 offset:12288\n\t" \
    "v_mfma_f32_16x16x32_bf16 v[184:187], v[24:27], v[68:71], v[184:187]\n\t" \
    "ds_read_b128 v[60:63], v81 offset:12800\n\t" \
    "v_mfma_f32_16x16x32_bf16 v[188:191], v[28:31], v[68:71], v[188:191]\n\t" \
    "s_waitcnt lgkmcnt(10)\n\t" \
    "v_mfma_f32_16x16x32_bf16 v[192:195], v[0:3], v[72:75], v[192:195]\n\t" \
    "v_mfma_f32_16x16x32_bf16 v[196:199], v[4:7], v[72:75], v[196:199]\n\t" \
    "ds_read_b64_tr_b16 v[64:65], v94 offset:0\n\t" \
    "v_mfma_f32_16x16x32_bf16 v[200:203], v[8:11], v[72:75], v[200:203]\n\t" \
    "ds_read_b64_tr_b16 v[66:67], v94 offset:1024\n\t" \
    "v_mfma_f32_16x16x32_bf16 v[204:207], v[12:15], v[72:75], v[204:207]\n\t" \
    "s_mov_b32 s28, 2359296\n\t" \
    "s_add_u32 m0, s26, 49152\n\t" \
    "v_mfma_f32_16x16x32_bf16 v[208:211], v[16:19], v[72:75], v[208:211]\n\t" \
    "buffer_load_dwordx4 v106, s[12:15], s28 offen offset:0 lds\n\t" \
    "v_mfma_f32_16x16x32_bf16 v[212:215], v[20:23], v[72:75], v[212:215]\n\t" \
    "v_mfma_f32_16x16x32_bf16 v[216:219], v[24:27], v[72:75], v[216:219]\n\t" \
    "v_mfma_f32_16x16x32_bf16 v[220:223], v[28:31], v[72:75], v[220:223]\n\t" \
    "s_waitcnt lgkmcnt(6)\n\t" \
    "v_mfma_f32_16x16x32_bf16 v[224:227], v[0:3], v[76:79], v[224:227]\n\t" \
    "v_mfma_f32_16x16x32_bf16 v[228:231], v[4:7], v[76:79], v[228:231]\n\t" \
    "ds_read_b64_tr_b16 v[68:69], v95 offset:0\n\t" \
    "v_mfma_f32_16x16x32_bf16 v[232:235], v[8:11], v[76:79], v[232:235]\n\t" \
    "ds_read_b64_tr_b16 v[70:71], v95 offset:1024\n\t" \
    "v_mfma_f32_16x16x32_bf16 v[236:239], v[12:15], v[76:79], v[236:239]\n\t" \
    "v_mfma_f32_16x16x32_bf16 v[240:243], v[16:19], v[76:79], v[240:243]\n\t" \
    "buffer_load_dwordx4 v107, s[12:15], s28 offen offset:1024 lds\n\t" \
    "v_mfma_f32_16x16x32_bf16 v[244:247], v[20:23], v[76:79], v[244:247]\n\t" \
    "v_mfma_f32_16x16x32_bf16 v[248:251], v[24:27], v[76:79], v[248:251]\n\t" \
    "v_mfma_f32_16x16x32_bf16 v[252:255], v[28:31], v[76:79], v[252:255]\n\t" \
    "s_waitcnt lgkmcnt(2)\n\t" \
    "v_mfma_f32_16x16x32_bf16 a[0:3], v[0:3], v[64:67], a[0:3]\n\t" \
    "v_mfma_f32_16x16x32_bf16 a[4:7], v[4:7], v[64:67], a[4:7]\n\t" \
    "ds_read_b64_tr_b16 v[72:73], v96 offset:0\n\t" \
    "v_mfma_f32_16x16x32_bf16 a[8:11], v[8:11], v[64:67], a[8:11]\n\t" \
    "ds_read_b64_tr_b16 v[74:75], v96 offset:1024\n\t" \
    "v_mfma_f32_16x16x32_bf16 a[12:15], v[12:15], v[64:67], a[12:15]\n\t" \
    "v_mfma_f32_16x16x32_bf16 a[16:19], v[16:19], v[64:67], a[16:19]\n\t" \
    "buffer_load_dwordx4 v108, s[12:15], s28 offen offset:2048 lds\n\t" \
    "v_mfma_f32_16x16x32_bf16 a[20:23], v[20:23], v[64:67], a[20:23]\n\t" \
    "v_mfma_f32_16x16x32_bf16 a[24:27], v[24:27], v[64:67], a[24:27]\n\t" \
    "v_mfma_f32_16x16x32_bf16 a[28:31], v[28:31], v[64:67], a[28:31]\n\t" \
    "s_waitcnt lgkmcnt(2)\n\t" \
    "v_mfma_f32_16x16x32_bf16 a[32:35], v[0:3], v[68:71], a[32:35]\n\t" \
    "v_mfma_f32_16x16x32_bf16 a[36:39], v[4:7], v[68:71], a[36:39]\n\t" \
    "ds_read_b64_tr_b16 v[76:77], v97 offset:0\n\t" \
    "v_mfma_f32_16x16x32_bf16 a[40:43], v[8:11], v[68:71], a[40:43]\n\t" \
    "ds_read_b64_tr_b16 v[78:79], v97 offset:1024\n\t" \
    "v_mfma_f32_16x16x32_bf16 a[44:47], v[12:15], v[68:71], a[44:47]\n\t" \
    "v_mfma_f32_16x16x32_bf16 a[48:51], v[16:19], v[68:71], a[48:51]\n\t" \
    "buffer_load_dwordx4 v109, s[12:15], s28 offen offset:3072 lds\n\t" \
    "v_mfma_f32_16x16x32_bf16 a[52:55], v[20:23], v[68:71], a[52:55]\n\t" \
    "v_mfma_f32_16x16x32_bf16 a[56:59], v[24:27], v[68:71], a[56:59]\n\t" \
    "v_mfma_f32_16x16x32_bf16 a[60:63], v[28:31], v[68:71], a[60:63]\n\t" \
    "s_waitcnt vmcnt(33) lgkmcnt(0)\n\t" \
    "s_barrier\n\t" \
    "v_mfma_f32_16x16x32_bf16 a[64:67], v[0:3], v[72:75], a[64:67]\n\t" \
    "v_mfma_f32_16x16x32_bf16 a[68:71], v[4:7], v[72:75], a[68:71]\n\t" \
    "ds_read_b64_tr_b16 v[64:65], v90 offset:16384\n\t" \
    "v_mfma_f32_16x16x32_bf16 a[72:75], v[8:11], v[72:75], a[72:75]\n\t" \
    "ds_read_b64_tr_b16 v[66:67], v90 offset:17408\n\t" \
    "v_mfma_f32_16x16x32_bf16 a[76:79], v[12:15], v[72:75], a[76:79]\n\t" \
    "v_mfma_f32_16x16x32_bf16 a[80:83], v[16:19], v[72:75], a[80:83]\n\t" \
    "v_mfma_f32_16x16x32_bf16 a[84:87], v[20:23], v[72:75], a[84:87]\n\t" \
    "v_mfma_f32_16x16x32_bf16 a[88:91], v[24:27], v[72:75], a[88:91]\n\t" \
    "v_mfma_f32_16x16x32_bf16 a[92:95], v[28:31], v[72:75], a[92:95]\n\t" \
    "v_mfma_f32_16x16x32_bf16 a[96:99], v[0:3], v[76:79], a[96:99]\n\t" \
    "v_mfma_f32_16x16x32_bf16 a[100:103], v[4:7], v[76:79], a[100:103]\n\t" \
    "ds_read_b64_tr_b16 v[68:69], v91 offset:16384\n\t" \
    "v_mfma_f32_16x16x32_bf16 a[104:107], v[8:11], v[76:79], a[104:107]\n\t" \
    "ds_read_b64_tr_b16 v[70:71], v91 offset:17408\n\t" \
    "v_mfma_f32_16x16x32_bf16 a[108:111], v[12:15], v[76:79], a[108:111]\n\t" \
    "v_mfma_f32_16x16x32_bf16 a[112:115], v[16:19], v[76:79], a[112:115]\n\t" \
    "v_mfma_f32_16x16x32_bf16 a[116:119], v[20:23], v[76:79], a[116:119]\n\t" \
    "v_mfma_f32_16x16x32_bf16 a[120:123], v[24:27], v[76:79], a[120:123]\n\t" \
    "v_mfma_f32_16x16x32_bf16 a[124:127], v[28:31], v[76:79], a[124:127]\n\t" \
    "s_waitcnt lgkmcnt(2)\n\t" \
    "v_mfma_f32_16x16x32_bf16 v[128:131], v[32:35], v[64:67], v[128:131]\n\t" \
    "v_mfma_f32_16x16x32_bf16 v[132:135], v[36:39], v[64:67], v[132:135]\n\t" \
    "ds_read_b64_tr_b16 v[72:73], v92 offset:16384\n\t" \
    "v_mfma_f32_16x16x32_bf16 v[136:139], v[40:43], v[64:67], v[136:139]\n\t" \
    "ds_read_b64_tr_b16 v[74:75], v92 offset:17408\n\t" \
    "s_sub_u32 s23, 15, s22\n\t" \
    "s_cmp_lt_u32 s20, 2\n\t" \
    "v_mfma_f32_16x16x32_bf16 v[140:143], v[44:47], v[64:67], v[140:143]\n\t" \
    "s_cselect_b32 s23, s23, s22\n\t" \
    "s_lshl_b32 s29, s23, 7\n\t" \
    "s_mov_b32 s27, 512\n\t" \
    "s_add_u32 m0, s25, 0\n\t" \
    "v_mfma_f32_16x16x32_bf16 v[144:147], v[48:51], v[64:67], v[144:147]\n\t" \
    "buffer_load_dwordx4 v98, s[8:11], s27 offen offset:0 lds\n\t" \
    "s_add_u32 s29, s29, 0x80000\n\t" \
    "s_lshl_b32 s23, s23, 3\n\t" \
    "v_mfma_f32_16x16x32_bf16 v[148:151], v[52:55], v[64:67], v[148:151]\n\t" \
    "s_and_b32 s24, s20, 1\n\t" \
    "s_lshl_b32 s24, s24, 2\n\t" \
    "v_mfma_f32_16x16x32_bf16 v[152:155], v[56:59], v[64:67], v[152:155]\n\t" \
    "buffer_load_dwordx4 v99, s[8:11], s27 offen offset:1024 lds\n\t" \
    "s_add_u32 s23, s23, s24\n\t" \
    "s_lshl_b32 s23, s23, 14\n\t" \
    "v_mfma_f32_16x16x32_bf16 v[156:159], v[60:63], v[64:67], v[156:159]\n\t" \
    "s_add_u32 s32, s23, 0x100000\n\t" \
    "s_add_u32 s33, s32, 16384\n\t" \
    "s_waitcnt lgkmcnt(2)\n\t" \
    "v_mfma_f32_16x16x32_bf16 v[160:163], v[32:35], v[68:71], v[160:163]\n\t" \
    "s_add_u32 s34, s32, 32768\n\t" \
    "s_add_u32 s35, s32, 49152\n\t" \
    "v_mfma_f32_16x16x32_bf16 v[164:167], v[36:39], v[68:71], v[164:167]\n\t" \
    "ds_read_b64_tr_b16 v[76:77], v93 offset:16384\n\t" \
    "s_lshr_b32 s30, s20, 1\n\t" \
    "s_lshl_b32 s30, s30, 13\n\t" \
    "v_mfma_f32_16x16x32_bf16 v[168:171], v[40:43], v[68:71], v[168:171]\n\t" \
    "ds_read_b64_tr_b16 v[78:79], v93 offset:17408\n\t" \
    "v_mfma_f32_16x16x32_bf16 v[172:175], v[44:47], v[68:71], v[172:175]\n\t" \
    "v_mfma_f32_16x16x32_bf16 v[176:179], v[48:51], v[68:71], v[176:179]\n\t" \
    "buffer_load_dwordx4 v100, s[8:11], s27 offen offset:2048 lds\n\t" \
    "v_mfma_f32_16x16x32_bf16 v[180:183], v[52:55], v[68:71], v[180:183]\n\t" \
    "v_mfma_f32_16x16x32_bf16 v[184:187], v[56:59], v[68:71], v[184:187]\n\t" \
    "buffer_load_dwordx4 v101, s[8:11], s27 offen offset:3072 lds\n\t" \
    "v_mfma_f32_16x16x32_bf16 v[188:191], v[60:63], v[68:71], v[188:191]\n\t" \
    "s_waitcnt lgkmcnt(2)\n\t" \
    "v_mfma_f32_16x16x32_bf16 v[192:195], v[32:35], v[72:75], v[192:195]\n\t" \
    "v_mfma_f32_16x16x32_bf16 v[196:199], v[36:39], v[72:75], v[196:199]\n\t" \
    "ds_read_b64_tr_b16 v[64:65], v94 offset:16384\n\t" \
    "v_mfma_f32_16x16x32_bf16 v[200:203], v[40:43], v[72:75], v[200:203]\n\t" \
    "ds_read_b64_tr_b16 v[66:67], v94 offset:17408\n\t" \
    "s_add_u32 m0, s25, 4096\n\t" \
    "v_mfma_f32_16x16x32_bf16 v[204:207], v[44:47], v[72:75], v[204:207]\n\t" \
    "buffer_load_dwordx4 v102, s[8:11], s27 offen offset:0 lds\n\t" \
    "v_mfma_f32_16x16x32_bf16 v[208:211], v[48:51], v[72:75], v[208:211]\n\t" \
    "v_mfma_f32_16x16x32_bf16 v[212:215], v[52:55], v[72:75], v[212:215]\n\t" \
    "buffer_load_dwordx4 v103, s[8:11], s27 offen offset:1024 lds\n\t" \
    "v_mfma_f32_16x16x32_bf16 v[216:219], v[56:59], v[72:75], v[216:219]\n\t" \
    "v_mfma_f32_16x16x32_bf16 v[220:223], v[60:63], v[72:75], v[220:223]\n\t" \
    "s_waitcnt lgkmcnt(2)\n\t" \
    "v_mfma_f32_16x16x32_bf16 v[224:227], v[32:35], v[76:79], v[224:227]\n\t" \
    "v_mfma_f32_16x16x32_bf16 v[228:231], v[36:39], v[76:79], v[228:231]\n\t" \
    "ds_read_b64_tr_b16 v[68:69], v95 offset:16384\n\t" \
    "v_mfma_f32_16x16x32_bf16 v[232:235], v[40:43], v[76:79], v[232:235]\n\t" \
    "ds_read_b64_tr_b16 v[70:71], v95 offset:17408\n\t" \
    "v_mfma_f32_16x16x32_bf16 v[236:239], v[44:47], v[76:79], v[236:239]\n\t" \
    "buffer_load_dwordx4 v104, s[8:11], s27 offen offset:2048 lds\n\t" \
    "v_mfma_f32_16x16x32_bf16 v[240:243], v[48:51], v[76:79], v[240:243]\n\t" \
    "v_mfma_f32_16x16x32_bf16 v[244:247], v[52:55], v[76:79], v[244:247]\n\t" \
    "buffer_load_dwordx4 v105, s[8:11], s27 offen offset:3072 lds\n\t" \
    "v_mfma_f32_16x16x32_bf16 v[248:251], v[56:59], v[76:79], v[248:251]\n\t" \
    "v_mfma_f32_16x16x32_bf16 v[252:255], v[60:63], v[76:79], v[252:255]\n\t" \
    "s_waitcnt lgkmcnt(2)\n\t" \
    "v_mfma_f32_16x16x32_bf16 a[0:3], v[32:35], v[64:67], a[0:3]\n\t" \
    "v_mfma_f32_16x16x32_bf16 a[4:7], v[36:39], v[64:67], a[4:7]\n\t" \
    "ds_read_b64_tr_b16 v[72:73], v96 offset:16384\n\t" \
    "v_mfma_f32_16x16x32_bf16 a[8:11], v[40:43], v[64:67], a[8:11]\n\t" \
    "ds_read_b64_tr_b16 v[74:75], v96 offset:17408\n\t" \
    "s_mov_b32 s28, 2621440\n\t" \
    "s_add_u32 m0, s26, 65536\n\t" \
    "v_mfma_f32_16x16x32_bf16 a[12:15], v[44:47], v[64:67], a[12:15]\n\t" \
    "buffer_load_dwordx4 v106, s[12:15], s28 offen offset:0 lds\n\t" \
    "v_mfma_f32_16x16x32_bf16 a[16:19], v[48:51], v[64:67], a[16:19]\n\t" \
    "v_mfma_f32_16x16x32_bf16 a[20:23], v[52:55], v[64:67], a[20:23]\n\t" \
    "buffer_load_dwordx4 v107, s[12:15], s28 offen offset:1024 lds\n\t" \
    "v_mfma_f32_16x16x32_bf16 a[24:27], v[56:59], v[64:67], a[24:27]\n\t" \
    "v_mfma_f32_16x16x32_bf16 a[28:31], v[60:63], v[64:67], a[28:31]\n\t" \
    "s_waitcnt lgkmcnt(2)\n\t" \
    "v_mfma_f32_16x16x32_bf16 a[32:35], v[32:35], v[68:71], a[32:35]\n\t" \
    "v_mfma_f32_16x16x32_bf16 a[36:39], v[36:39], v[68:71], a[36:39]\n\t" \
    "ds_read_b64_tr_b16 v[76:77], v97 offset:16384\n\t" \
    "v_mfma_f32_16x16x32_bf16 a[40:43], v[40:43], v[68:71], a[40:43]\n\t" \
    "ds_read_b64_tr_b16 v[78:79], v97 offset:17408\n\t" \
    "v_mfma_f32_16x16x32_bf16 a[44:47], v[44:47], v[68:71], a[44:47]\n\t" \
    "buffer_load_dwordx4 v108, s[12:15], s28 offen offset:2048 lds\n\t" \
    "v_mfma_f32_16x16x32_bf16 a[48:51], v[48:51], v[68:71], a[48:51]\n\t" \
    "v_mfma_f32_16x16x32_bf16 a[52:55], v[52:55], v[68:71], a[52:55]\n\t" \
    "buffer_load_dwordx4 v109, s[12:15], s28 offen offset:3072 lds\n\t" \
    "v_mfma_f32_16x16x32_bf16 a[56:59], v[56:59], v[68:71], a[56:59]\n\t" \
    "v_mfma_f32_16x16x32_bf16 a[60:63], v[60:63], v[68:71], a[60:63]\n\t" \
    "s_waitcnt vmcnt(21) lgkmcnt(0)\n\t" \
    "s_barrier\n\t" \
    "v_mfma_f32_16x16x32_bf16 a[64:67], v[32:35], v[72:75], a[64:67]\n\t" \
    "v_mfma_f32_16x16x32_bf16 a[68:71], v[36:39], v[72:75], a[68:71]\n\t" \
    "ds_read_b64_tr_b16 v[64:65], v82 offset:0\n\t" \
    "v_mfma_f32_16x16x32_bf16 a[72:75], v[40:43], v[72:75], a[72:75]\n\t" \
    "ds_read_b64_tr_b16 v[66:67], v82 offset:1024\n\t" \
    "v_mfma_f32_16x16x32_bf16 a[76:79], v[44:47], v[72:75], a[76:79]\n\t" \
    "ds_read_b128 v[0:3], v80 offset:32768\n\t" \
    "v_mfma_f32_16x16x32_bf16 a[80:83], v[48:51], v[72:75], a[80:83]\n\t" \
    "ds_read_b128 v[4:7], v80 offset:33280\n\t" \
    "v_mfma_f32_16x16x32_bf16 a[84:87], v[52:55], v[72:75], a[84:87]\n\t" \
    "ds_read_b128 v[8:11], v80 offset:36864\n\t" \
    "v_mfma_f32_16x16x32_bf16 a[88:91], v[56:59], v[72:75], a[88:91]\n\t" \
    "ds_read_b128 v[12:15], v80 offset:37376\n\t" \
    "v_mfma_f32_16x16x32_bf16 a[92:95], v[60:63], v[72:75], a[92:95]\n\t" \
    "ds_read_b128 v[16:19], v80 offset:40960\n\t" \
    "v_mfma_f32_16x16x32_bf16 a[96:99], v[32:35], v[76:79], a[96:99]\n\t" \
    "ds_read_b128 v[20:23], v80 offset:41472\n\t" \
    "v_mfma_f32_16x16x32_bf16 a[100:103], v[36:39], v[76:79], a[100:103]\n\t" \
    "ds_read_b128 v[24:27], v80 offset:45056\n\t" \
    "v_mfma_f32_16x16x32_bf16 a[104:107], v[40:43], v[76:79], a[104:107]\n\t" \
    "ds_read_b128 v[28:31], v80 offset:45568\n\t" \
    "v_mfma_f32_16x16x32_bf16 a[108:111], v[44:47], v[76:79], a[108:111]\n\t" \
    "ds_read_b64_tr_b16 v[68:69], v83 offset:0\n\t" \
    "v_mfma_f32_16x16x32_bf16 a[112:115], v[48:51], v[76:79], a[112:115]\n\t" \
    "ds_read_b64_tr_b16 v[70:71], v83 offset:1024\n\t" \
    "v_mfma_f32_16x16x32_bf16 a[116:119], v[52:55], v[76:79], a[116:119]\n\t" \
    "v_mfma_f32_16x16x32_bf16 a[120:123], v[56:59], v[76:79], a[120:123]\n\t" \
    "v_mfma_f32_16x16x32_bf16 a[124:127], v[60:63], v[76:79], a[124:127]\n\t" \
    "s_waitcnt lgkmcnt(9)\n\t" \
    "v_mfma_f32_16x16x32_bf16 v[128:131], v[0:3], v[64:67], v[128:131]\n\t" \
    "s_waitcnt lgkmcnt(8)\n\t" \
    "v_mfma_f32_16x16x32_bf16 v[132:135], v[4:7], v[64:67], v[132:135]\n\t" \
    "ds_read_b64_tr_b16 v[72:73], v84 offset:0\n\t" \
    "s_waitcnt lgkmcnt(8)\n\t" \
    "v_mfma_f32_16x16x32_bf16 v[136:139], v[8:11], v[64:67], v[136:139]\n\t" \
    "ds_read_b64_tr_b16 v[74:75], v84 offset:1024\n\t" \
    "s_waitcnt lgkmcnt(8)\n\t" \
    "v_mfma_f32_16x16x32_bf16 v[140:143], v[12:15], v[64:67], v[140:143]\n\t" \
    "ds_read_b128 v[32:35], v81 offset:32768\n\t" \
    "s_waitcnt lgkmcnt(8)\n\t" \
    "v_mfma_f32_16x16x32_bf16 v[144:147], v[16:19], v[64:67], v[144:147]\n\t" \
    "ds_read_b128 v[36:39], v81 offset:33280\n\t" \
    "s_waitcnt lgkmcnt(8)\n\t" \
    "v_mfma_f32_16x16x32_bf16 v[148:151], v[20:23], v[64:67], v[148:151]\n\t" \
    "ds_read_b128 v[40:43], v81 offset:36864\n\t" \
    "s_waitcnt lgkmcnt(8)\n\t" \
    "v_mfma_f32_16x16x32_bf16 v[152:155], v[24:27], v[64:67], v[152:155]\n\t" \
    "ds_read_b128 v[44:47], v81 offset:37376\n\t" \
    "s_waitcnt lgkmcnt(8)\n\t" \
    "v_mfma_f32_16x16x32_bf16 v[156:159], v[28:31], v[64:67], v[156:159]\n\t" \
    "s_waitcnt lgkmcnt(6)\n\t" \
    "v_mfma_f32_16x16x32_bf16 v[160:163], v[0:3], v[68:71], v[160:163]\n\t" \
    "v_mfma_f32_16x16x32_bf16 v[164:167], v[4:7], v[68:71], v[164:167]\n\t" \
    "ds_read_b64_tr_b16 v[76:77], v85 offset:0\n\t" \
    "v_mfma_f32_16x16x32_bf16 v[168:171], v[8:11], v[68:71], v[168:171]\n\t" \
    "ds_read_b64_tr_b16 v[78:79], v85 offset:1024\n\t" \
    "v_mfma_f32_16x16x32_bf16 v[172:175], v[12:15], v[68:71], v[172:175]\n\t" \
    "ds_read_b128 v[48:51], v81 offset:40960\n\t" \
    "v_mfma_f32_16x16x32_bf16 v[176:179], v[16:19], v[68:71], v[176:179]\n\t" \
    "ds_read_b128 v[52:55], v81 offset:41472\n\t" \
    "v_mfma_f32_16x16x32_bf16 v[180:183], v[20:23], v[68:71], v[180:183]\n\t" \
    "ds_read_b128 v[56:59], v81 offset:45056\n\t" \
    "v_mfma_f32_16x16x32_bf16 v[184:187], v[24:27], v[68:71], v[184:187]\n\t" \
    "ds_read_b128 v[60:63], v81 offset:45568\n\t" \
    "v_mfma_f32_16x16x32_bf16 v[188:191], v[28:31], v[68:71], v[188:191]\n\t" \
    "s_waitcnt lgkmcnt(10)\n\t" \
    "v_mfma_f32_16x16x32_bf16 v[192:195], v[0:3], v[72:75], v[192:195]\n\t" \
    "v_mfma_f32_16x16x32_bf16 v[196:199], v[4:7], v[72:75], v[196:199]\n\t" \
    "ds_read_b64_tr_b16 v[64:65], v86 offset:0\n\t" \
    "v_mfma_f32_16x16x32_bf16 v[200:203], v[8:11], v[72:75], v[200:203]\n\t" \
    "ds_read_b64_tr_b16 v[66:67], v86 offset:1024\n\t" \
    "v_mfma_f32_16x16x32_bf16 v[204:207], v[12:15], v[72:75], v[204:207]\n\t" \
    "s_mov_b32 s28, 2883584\n\t" \
    "s_add_u32 m0, s26, 81920\n\t" \
    "v_mfma_f32_16x16x32_bf16 v[208:211], v[16:19], v[72:75], v[208:211]\n\t" \
    "buffer_load_dwordx4 v106, s[12:15], s28 offen offset:0 lds\n\t" \
    "v_mfma_f32_16x16x32_bf16 v[212:215], v[20:23], v[72:75], v[212:215]\n\t" \
    "v_mfma_f32_16x16x32_bf16 v[216:219], v[24:27], v[72:75], v[216:219]\n\t" \
    "v_mfma_f32_16x16x32_bf16 v[220:223], v[28:31], v[72:75], v[220:223]\n\t" \
    "s_waitcnt lgkmcnt(6)\n\t" \
    "v_mfma_f32_16x16x32_bf16 v[224:227], v[0:3], v[76:79], v[224:227]\n\t" \
    "v_mfma_f32_16x16x32_bf16 v[228:231], v[4:7], v[76:79], v[228:231]\n\t" \
    "ds_read_b64_tr_b16 v[68:69], v87 offset:0\n\t" \
    "v_mfma_f32_16x16x32_bf16 v[232:235], v[8:11], v[76:79], v[232:235]\n\t" \
    "ds_read_b64_tr_b16 v[70:71], v87 offset:1024\n\t" \
    "v_mfma_f32_16x16x32_bf16 v[236:239], v[12:15], v[76:79], v[236:239]\n\t" \
    "v_mfma_f32_16x16x32_bf16 v[240:243], v[16:19], v[76:79], v[240:243]\n\t" \
    "buffer_load_dwordx4 v107, s[12:15], s28 offen offset:1024 lds\n\t" \
    "v_mfma_f32_16x16x32_bf16 v[244:247], v[20:23], v[76:79], v[244:247]\n\t" \
    "v_mfma_f32_16x16x32_bf16 v[248:251], v[24:27], v[76:79], v[248:251]\n\t" \
    "v_mfma_f32_16x16x32_bf16 v[252:255], v[28:31], v[76:79], v[252:255]\n\t" \
    "s_waitcnt lgkmcnt(2)\n\t" \
    "v_mfma_f32_16x16x32_bf16 a[0:3], v[0:3], v[64:67], a[0:3]\n\t" \
    "v_mfma_f32_16x16x32_bf16 a[4:7], v[4:7], v[64:67], a[4:7]\n\t" \
    "ds_read_b64_tr_b16 v[72:73], v88 offset:0\n\t" \
    "v_mfma_f32_16x16x32_bf16 a[8:11], v[8:11], v[64:67], a[8:11]\n\t" \
    "ds_read_b64_tr_b16 v[74:75], v88 offset:1024\n\t" \
    "v_mfma_f32_16x16x32_bf16 a[12:15], v[12:15], v[64:67], a[12:15]\n\t" \
    "v_mfma_f32_16x16x32_bf16 a[16:19], v[16:19], v[64:67], a[16:19]\n\t" \
    "buffer_load_dwordx4 v108, s[12:15], s28 offen offset:2048 lds\n\t" \
    "v_mfma_f32_16x16x32_bf16 a[20:23], v[20:23], v[64:67], a[20:23]\n\t" \
    "v_mfma_f32_16x16x32_bf16 a[24:27], v[24:27], v[64:67], a[24:27]\n\t" \
    "v_mfma_f32_16x16x32_bf16 a[28:31], v[28:31], v[64:67], a[28:31]\n\t" \
    "s_waitcnt lgkmcnt(2)\n\t" \
    "v_mfma_f32_16x16x32_bf16 a[32:35], v[0:3], v[68:71], a[32:35]\n\t" \
    "v_mfma_f32_16x16x32_bf16 a[36:39], v[4:7], v[68:71], a[36:39]\n\t" \
    "ds_read_b64_tr_b16 v[76:77], v89 offset:0\n\t" \
    "v_mfma_f32_16x16x32_bf16 a[40:43], v[8:11], v[68:71], a[40:43]\n\t" \
    "ds_read_b64_tr_b16 v[78:79], v89 offset:1024\n\t" \
    "v_mfma_f32_16x16x32_bf16 a[44:47], v[12:15], v[68:71], a[44:47]\n\t" \
    "v_mfma_f32_16x16x32_bf16 a[48:51], v[16:19], v[68:71], a[48:51]\n\t" \
    "buffer_load_dwordx4 v109, s[12:15], s28 offen offset:3072 lds\n\t" \
    "v_mfma_f32_16x16x32_bf16 a[52:55], v[20:23], v[68:71], a[52:55]\n\t" \
    "v_mfma_f32_16x16x32_bf16 a[56:59], v[24:27], v[68:71], a[56:59]\n\t" \
    "v_mfma_f32_16x16x32_bf16 a[60:63], v[28:31], v[68:71], a[60:63]\n\t" \
    "s_waitcnt vmcnt(33) lgkmcnt(0)\n\t" \
    "s_barrier\n\t" \
    "v_mfma_f32_16x16x32_bf16 a[64:67], v[0:3], v[72:75], a[64:67]\n\t" \
    "v_mfma_f32_16x16x32_bf16 a[68:71], v[4:7], v[72:75], a[68:71]\n\t" \
    "ds_read_b64_tr_b16 v[64:65], v82 offset:16384\n\t" \
    "v_mfma_f32_16x16x32_bf16 a[72:75], v[8:11], v[72:75], a[72:75]\n\t" \
    "ds_read_b64_tr_b16 v[66:67], v82 offset:17408\n\t" \
    "v_mfma_f32_16x16x32_bf16 a[76:79], v[12:15], v[72:75], a[76:79]\n\t" \
    "v_mfma_f32_16x16x32_bf16 a[80:83], v[16:19], v[72:75], a[80:83]\n\t" \
    "v_mfma_f32_16x16x32_bf16 a[84:87], v[20:23], v[72:75], a[84:87]\n\t" \
    "v_mfma_f32_16x16x32_bf16 a[88:91], v[24:27], v[72:75], a[88:91]\n\t" \
    "v_mfma_f32_16x16x32_bf16 a[92:95], v[28:31], v[72:75], a[92:95]\n\t" \
    "v_mfma_f32_16x16x32_bf16 a[96:99], v[0:3], v[76:79], a[96:99]\n\t" \
    "v_mfma_f32_16x16x32_bf16 a[100:103], v[4:7], v[76:79], a[100:103]\n\t" \
    "ds_read_b64_tr_b16 v[68:69], v83 offset:16384\n\t" \
    "v_mfma_f32_16x16x32_bf16 a[104:107], v[8:11], v[76:79], a[104:107]\n\t" \
    "ds_read_b64_tr_b16 v[70:71], v83 offset:17408\n\t" \
    "v_mfma_f32_16x16x32_bf16 a[108:111], v[12:15], v[76:79], a[108:111]\n\t" \
    "v_mfma_f32_16x16x32_bf16 a[112:115], v[16:19], v[76:79], a[112:115]\n\t" \
    "v_mfma_f32_16x16x32_bf16 a[116:119], v[20:23], v[76:79], a[116:119]\n\t" \
    "v_mfma_f32_16x16x32_bf16 a[120:123], v[24:27], v[76:79], a[120:123]\n\t" \
    "v_mfma_f32_16x16x32_bf16 a[124:127], v[28:31], v[76:79], a[124:127]\n\t" \
    "s_waitcnt lgkmcnt(2)\n\t" \
    "v_mfma_f32_16x16x32_bf16 v[128:131], v[32:35], v[64:67], v[128:131]\n\t" \
    "v_mfma_f32_16x16x32_bf16 v[132:135], v[36:39], v[64:67], v[132:135]\n\t" \
    "ds_read_b64_tr_b16 v[72:73], v84 offset:16384\n\t" \
    "v_mfma_f32_16x16x32_bf16 v[136:139], v[40:43], v[64:67], v[136:139]\n\t" \
    "ds_read_b64_tr_b16 v[74:75], v84 offset:17408\n\t" \
    "v_mfma_f32_16x16x32_bf16 v[140:143], v[44:47], v[64:67], v[140:143]\n\t" \
    "s_mov_b32 s27, 640\n\t" \
    "s_add_u32 m0, s25, 32768\n\t" \
    "v_mfma_f32_16x16x32_bf16 v[144:147], v[48:51], v[64:67], v[144:147]\n\t" \
    "buffer_load_dwordx4 v98, s[8:11], s27 offen offset:0 lds\n\t" \
    "v_mfma_f32_16x16x32_bf16 v[148:151], v[52:55], v[64:67], v[148:151]\n\t" \
    "v_mfma_f32_16x16x32_bf16 v[152:155], v[56:59], v[64:67], v[152:155]\n\t" \
    "buffer_load_dwordx4 v99, s[8:11], s27 offen offset:1024 lds\n\t" \
    "v_mfma_f32_16x16x32_bf16 v[156:159], v[60:63], v[64:67], v[156:159]\n\t" \
    "s_waitcnt lgkmcnt(2)\n\t" \
    "v_mfma_f32_16x16x32_bf16 v[160:163], v[32:35], v[68:71], v[160:163]\n\t" \
    "v_mfma_f32_16x16x32_bf16 v[164:167], v[36:39], v[68:71], v[164:167]\n\t" \
    "ds_read_b64_tr_b16 v[76:77], v85 offset:16384\n\t" \
    "v_mfma_f32_16x16x32_bf16 v[168:171], v[40:43], v[68:71], v[168:171]\n\t" \
    "ds_read_b64_tr_b16 v[78:79], v85 offset:17408\n\t" \
    "v_mfma_f32_16x16x32_bf16 v[172:175], v[44:47], v[68:71], v[172:175]\n\t" \
    "v_mfma_f32_16x16x32_bf16 v[176:179], v[48:51], v[68:71], v[176:179]\n\t" \
    "buffer_load_dwordx4 v100, s[8:11], s27 offen offset:2048 lds\n\t" \
    "v_mfma_f32_16x16x32_bf16 v[180:183], v[52:55], v[68:71], v[180:183]\n\t" \
    "v_mfma_f32_16x16x32_bf16 v[184:187], v[56:59], v[68:71], v[184:187]\n\t" \
    "buffer_load_dwordx4 v101, s[8:11], s27 offen offset:3072 lds\n\t" \
    "v_mfma_f32_16x16x32_bf16 v[188:191], v[60:63], v[68:71], v[188:191]\n\t" \
    "s_waitcnt lgkmcnt(2)\n\t" \
    "v_mfma_f32_16x16x32_bf16 v[192:195], v[32:35], v[72:75], v[192:195]\n\t" \
    "v_mfma_f32_16x16x32_bf16 v[196:199], v[36:39], v[72:75], v[196:199]\n\t" \
    "ds_read_b64_tr_b16 v[64:65], v86 offset:16384\n\t" \
    "v_mfma_f32_16x16x32_bf16 v[200:203], v[40:43], v[72:75], v[200:203]\n\t" \
    "ds_read_b64_tr_b16 v[66:67], v86 offset:17408\n\t" \
    "s_add_u32 m0, s25, 36864\n\t" \
    "v_mfma_f32_16x16x32_bf16 v[204:207], v[44:47], v[72:75], v[204:207]\n\t" \
    "buffer_load_dwordx4 v102, s[8:11], s27 offen offset:0 lds\n\t" \
    "v_mfma_f32_16x16x32_bf16 v[208:211], v[48:51], v[72:75], v[208:211]\n\t" \
    "v_mfma_f32_16x16x32_bf16 v[212:215], v[52:55], v[72:75], v[212:215]\n\t" \
    "buffer_load_dwordx4 v103, s[8:11], s27 offen offset:1024 lds\n\t" \
    "v_mfma_f32_16x16x32_bf16 v[216:219], v[56:59], v[72:75], v[216:219]\n\t" \
    "v_mfma_f32_16x16x32_bf16 v[220:223], v[60:63], v[72:75], v[220:223]\n\t" \
    "s_waitcnt lgkmcnt(2)\n\t" \
    "v_mfma_f32_16x16x32_bf16 v[224:227], v[32:35], v[76:79], v[224:227]\n\t" \
    "v_mfma_f32_16x16x32_bf16 v[228:231], v[36:39], v[76:79], v[228:231]\n\t" \
    "ds_read_b64_tr_b16 v[68:69], v87 offset:16384\n\t" \
    "v_mfma_f32_16x16x32_bf16 v[232:235], v[40:43], v[76:79], v[232:235]\n\t" \
    "ds_read_b64_tr_b16 v[70:71], v87 offset:17408\n\t" \
    "v_mfma_f32_16x16x32_bf16 v[236:239], v[44:47], v[76:79], v[236:239]\n\t" \
    "buffer_load_dwordx4 v104, s[8:11], s27 offen offset:2048 lds\n\t" \
    "v_mfma_f32_16x16x32_bf16 v[240:243], v[48:51], v[76:79], v[240:243]\n\t" \
    "v_mfma_f32_16x16x32_bf16 v[244:247], v[52:55], v[76:79], v[244:247]\n\t" \
    "buffer_load_dwordx4 v105, s[8:11], s27 offen offset:3072 lds\n\t" \
    "v_mfma_f32_16x16x32_bf16 v[248:251], v[56:59], v[76:79], v[248:251]\n\t" \
    "v_mfma_f32_16x16x32_bf16 v[252:255], v[60:63], v[76:79], v[252:255]\n\t" \
    "s_waitcnt lgkmcnt(2)\n\t" \
    "v_mfma_f32_16x16x32_bf16 a[0:3], v[32:35], v[64:67], a[0:3]\n\t" \
    "v_mfma_f32_16x16x32_bf16 a[4:7], v[36:39], v[64:67], a[4:7]\n\t" \
    "ds_read_b64_tr_b16 v[72:73], v88 offset:16384\n\t" \
    "v_mfma_f32_16x16x32_bf16 a[8:11], v[40:43], v[64:67], a[8:11]\n\t" \
    "ds_read_b64_tr_b16 v[74:75], v88 offset:17408\n\t" \
    "s_mov_b32 s28, 3145728\n\t" \
    "s_add_u32 m0, s26, 0\n\t" \
    "v_mfma_f32_16x16x32_bf16 a[12:15], v[44:47], v[64:67], a[12:15]\n\t" \
    "buffer_load_dwordx4 v106, s[12:15], s28 offen offset:0 lds\n\t" \
    "v_mfma_f32_16x16x32_bf16 a[16:19], v[48:51], v[64:67], a[16:19]\n\t" \
    "v_mfma_f32_16x16x32_bf16 a[20:23], v[52:55], v[64:67], a[20:23]\n\t" \
    "buffer_load_dwordx4 v107, s[12:15], s28 offen offset:1024 lds\n\t" \
    "v_mfma_f32_16x16x32_bf16 a[24:27], v[56:59], v[64:67], a[24:27]\n\t" \
    "v_mfma_f32_16x16x32_bf16 a[28:31], v[60:63], v[64:67], a[28:31]\n\t" \
    "s_waitcnt lgkmcnt(2)\n\t" \
    "v_mfma_f32_16x16x32_bf16 a[32:35], v[32:35], v[68:71], a[32:35]\n\t" \
    "v_mfma_f32_16x16x32_bf16 a[36:39], v[36:39], v[68:71], a[36:39]\n\t" \
    "ds_read_b64_tr_b16 v[76:77], v89 offset:16384\n\t" \
    "v_mfma_f32_16x16x32_bf16 a[40:43], v[40:43], v[68:71], a[40:43]\n\t" \
    "ds_read_b64_tr_b16 v[78:79], v89 offset:17408\n\t" \
    "v_mfma_f32_16x16x32_bf16 a[44:47], v[44:47], v[68:71], a[44:47]\n\t" \
    "buffer_load_dwordx4 v108, s[12:15], s28 offen offset:2048 lds\n\t" \
    "v_mfma_f32_16x16x32_bf16 a[48:51], v[48:51], v[68:71], a[48:51]\n\t" \
    "v_mfma_f32_16x16x32_bf16 a[52:55], v[52:55], v[68:71], a[52:55]\n\t" \
    "buffer_load_dwordx4 v109, s[12:15], s28 offen offset:3072 lds\n\t" \
    "v_mfma_f32_16x16x32_bf16 a[56:59], v[56:59], v[68:71], a[56:59]\n\t" \
    "v_mfma_f32_16x16x32_bf16 a[60:63], v[60:63], v[68:71], a[60:63]\n\t" \
    "s_waitcnt vmcnt(20) lgkmcnt(0)\n\t" \
    "s_barrier\n\t" \
    "v_mfma_f32_16x16x32_bf16 a[64:67], v[32:35], v[72:75], a[64:67]\n\t" \
    "v_mfma_f32_16x16x32_bf16 a[68:71], v[36:39], v[72:75], a[68:71]\n\t" \
    "ds_read_b64_tr_b16 v[64:65], v82 offset:32768\n\t" \
    "v_mfma_f32_16x16x32_bf16 a[72:75], v[40:43], v[72:75], a[72:75]\n\t" \
    "ds_read_b64_tr_b16 v[66:67], v82 offset:33792\n\t" \
    "v_mfma_f32_16x16x32_bf16 a[76:79], v[44:47], v[72:75], a[76:79]\n\t" \
    "ds_read_b128 v[0:3], v80 offset:0\n\t" \
    "v_mfma_f32_16x16x32_bf16 a[80:83], v[48:51], v[72:75], a[80:83]\n\t" \
    "ds_read_b128 v[4:7], v80 offset:512\n\t" \
    "v_mfma_f32_16x16x32_bf16 a[84:87], v[52:55], v[72:75], a[84:87]\n\t" \
    "ds_read_b128 v[8:11], v80 offset:4096\n\t" \
    "v_mfma_f32_16x16x32_bf16 a[88:91], v[56:59], v[72:75], a[88:91]\n\t" \
    "ds_read_b128 v[12:15], v80 offset:4608\n\t" \
    "v_mfma_f32_16x16x32_bf16 a[92:95], v[60:63], v[72:75], a[92:95]\n\t" \
    "ds_read_b128 v[16:19], v80 offset:8192\n\t" \
    "v_mfma_f32_16x16x32_bf16 a[96:99], v[32:35], v[76:79], a[96:99]\n\t" \
    "ds_read_b128 v[20:23], v80 offset:8704\n\t" \
    "v_mfma_f32_16x16x32_bf16 a[100:103], v[36:39], v[76:79], a[100:103]\n\t" \
    "ds_read_b128 v[24:27], v80 offset:12288\n\t" \
    "v_mfma_f32_16x16x32_bf16 a[104:107], v[40:43], v[76:79], a[104:107]\n\t" \
    "ds_read_b128 v[28:31], v80 offset:12800\n\t" \
    "v_mfma_f32_16x16x32_bf16 a[108:111], v[44:47], v[76:79], a[108:111]\n\t" \
    "ds_read_b64_tr_b16 v[68:69], v83 offset:32768\n\t" \
    "v_mfma_f32_16x16x32_bf16 a[112:115], v[48:51], v[76:79], a[112:115]\n\t" \
    "ds_read_b64_tr_b16 v[70:71], v83 offset:33792\n\t" \
    "v_mfma_f32_16x16x32_bf16 a[116:119], v[52:55], v[76:79], a[116:119]\n\t" \
    "v_mfma_f32_16x16x32_bf16 a[120:123], v[56:59], v[76:79], a[120:123]\n\t" \
    "v_mfma_f32_16x16x32_bf16 a[124:127], v[60:63], v[76:79], a[124:127]\n\t" \
    "s_waitcnt lgkmcnt(9)\n\t" \
    "v_mfma_f32_16x16x32_bf16 v[128:131], v[0:3], v[64:67], v[128:131]\n\t" \
    "s_waitcnt lgkmcnt(8)\n\t" \
    "v_mfma_f32_16x16x32_bf16 v[132:135], v[4:7], v[64:67], v[132:135]\n\t" \
    "ds_read_b64_tr_b16 v[72:73], v84 offset:32768\n\t" \
    "s_waitcnt lgkmcnt(8)\n\t" \
    "v_mfma_f32_16x16x32_bf16 v[136:139], v[8:11], v[64:67], v[136:139]\n\t" \
    "ds_read_b64_tr_b16 v[74:75], v84 offset:33792\n\t" \
    "s_waitcnt lgkmcnt(8)\n\t" \
    "v_mfma_f32_16x16x32_bf16 v[140:143], v[12:15], v[64:67], v[140:143]\n\t" \
    "ds_read_b128 v[32:35], v81 offset:0\n\t" \
    "s_waitcnt lgkmcnt(8)\n\t" \
    "v_mfma_f32_16x16x32_bf16 v[144:147], v[16:19], v[64:67], v[144:147]\n\t" \
    "ds_read_b128 v[36:39], v81 offset:512\n\t" \
    "s_waitcnt lgkmcnt(8)\n\t" \
    "v_mfma_f32_16x16x32_bf16 v[148:151], v[20:23], v[64:67], v[148:151]\n\t" \
    "ds_read_b128 v[40:43], v81 offset:4096\n\t" \
    "s_waitcnt lgkmcnt(8)\n\t" \
    "v_mfma_f32_16x16x32_bf16 v[152:155], v[24:27], v[64:67], v[152:155]\n\t" \
    "ds_read_b128 v[44:47], v81 offset:4608\n\t" \
    "s_waitcnt lgkmcnt(8)\n\t" \
    "v_mfma_f32_16x16x32_bf16 v[156:159], v[28:31], v[64:67], v[156:159]\n\t" \
    "s_waitcnt lgkmcnt(6)\n\t" \
    "v_mfma_f32_16x16x32_bf16 v[160:163], v[0:3], v[68:71], v[160:163]\n\t" \
    "v_mfma_f32_16x16x32_bf16 v[164:167], v[4:7], v[68:71], v[164:167]\n\t" \
    "ds_read_b64_tr_b16 v[76:77], v85 offset:32768\n\t" \
    "v_mfma_f32_16x16x32_bf16 v[168:171], v[8:11], v[68:71], v[168:171]\n\t" \
    "ds_read_b64_tr_b16 v[78:79], v85 offset:33792\n\t" \
    "v_mfma_f32_16x16x32_bf16 v[172:175], v[12:15], v[68:71], v[172:175]\n\t" \
    "ds_read_b128 v[48:51], v81 offset:8192\n\t" \
    "v_mfma_f32_16x16x32_bf16 v[176:179], v[16:19], v[68:71], v[176:179]\n\t" \
    "ds_read_b128 v[52:55], v81 offset:8704\n\t" \
    "v_mfma_f32_16x16x32_bf16 v[180:183], v[20:23], v[68:71], v[180:183]\n\t" \
    "ds_read_b128 v[56:59], v81 offset:12288\n\t" \
    "v_mfma_f32_16x16x32_bf16 v[184:187], v[24:27], v[68:71], v[184:187]\n\t" \
    "ds_read_b128 v[60:63], v81 offset:12800\n\t" \
    "v_mfma_f32_16x16x32_bf16 v[188:191], v[28:31], v[68:71], v[188:191]\n\t" \
    "s_waitcnt lgkmcnt(10)\n\t" \
    "v_mfma_f32_16x16x32_bf16 v[192:195], v[0:3], v[72:75], v[192:195]\n\t" \
    "v_mfma_f32_16x16x32_bf16 v[196:199], v[4:7], v[72:75], v[196:199]\n\t" \
    "ds_read_b64_tr_b16 v[64:65], v86 offset:32768\n\t" \
    "v_mfma_f32_16x16x32_bf16 v[200:203], v[8:11], v[72:75], v[200:203]\n\t" \
    "ds_read_b64_tr_b16 v[66:67], v86 offset:33792\n\t" \
    "v_mfma_f32_16x16x32_bf16 v[204:207], v[12:15], v[72:75], v[204:207]\n\t" \
    "s_mov_b32 s28, 3407872\n\t" \
    "s_add_u32 m0, s26, 16384\n\t" \
    "v_mfma_f32_16x16x32_bf16 v[208:211], v[16:19], v[72:75], v[208:211]\n\t" \
    "buffer_load_dwordx4 v106, s[12:15], s28 offen offset:0 lds\n\t" \
    "v_mfma_f32_16x16x32_bf16 v[212:215], v[20:23], v[72:75], v[212:215]\n\t" \
    "v_mfma_f32_16x16x32_bf16 v[216:219], v[24:27], v[72:75], v[216:219]\n\t" \
    "v_mfma_f32_16x16x32_bf16 v[220:223], v[28:31], v[72:75], v[220:223]\n\t" \
    "s_waitcnt lgkmcnt(6)\n\t" \
    "v_mfma_f32_16x16x32_bf16 v[224:227], v[0:3], v[76:79], v[224:227]\n\t" \
    "v_mfma_f32_16x16x32_bf16 v[228:231], v[4:7], v[76:79], v[228:231]\n\t" \
    "ds_read_b64_tr_b16 v[68:69], v87 offset:32768\n\t" \
    "v_mfma_f32_16x16x32_bf16 v[232:235], v[8:11], v[76:79], v[232:235]\n\t" \
    "ds_read_b64_tr_b16 v[70:71], v87 offset:33792\n\t" \
    "v_mfma_f32_16x16x32_bf16 v[236:239], v[12:15], v[76:79], v[236:239]\n\t" \
    "v_mfma_f32_16x16x32_bf16 v[240:243], v[16:19], v[76:79], v[240:243]\n\t" \
    "buffer_load_dwordx4 v107, s[12:15], s28 offen offset:1024 lds\n\t" \
    "v_mfma_f32_16x16x32_bf16 v[244:247], v[20:23], v[76:79], v[244:247]\n\t" \
    "v_mfma_f32_16x16x32_bf16 v[248:251], v[24:27], v[76:79], v[248:251]\n\t" \
    "v_mfma_f32_16x16x32_bf16 v[252:255], v[28:31], v[76:79], v[252:255]\n\t" \
    "s_waitcnt lgkmcnt(2)\n\t" \
    "v_mfma_f32_16x16x32_bf16 a[0:3], v[0:3], v[64:67], a[0:3]\n\t" \
    "v_mfma_f32_16x16x32_bf16 a[4:7], v[4:7], v[64:67], a[4:7]\n\t" \
    "ds_read_b64_tr_b16 v[72:73], v88 offset:32768\n\t" \
    "v_mfma_f32_16x16x32_bf16 a[8:11], v[8:11], v[64:67], a[8:11]\n\t" \
    "ds_read_b64_tr_b16 v[74:75], v88 offset:33792\n\t" \
    "v_mfma_f32_16x16x32_bf16 a[12:15], v[12:15], v[64:67], a[12:15]\n\t" \
    "v_mfma_f32_16x16x32_bf16 a[16:19], v[16:19], v[64:67], a[16:19]\n\t" \
    "buffer_load_dwordx4 v108, s[12:15], s28 offen offset:2048 lds\n\t" \
    "v_mfma_f32_16x16x32_bf16 a[20:23], v[20:23], v[64:67], a[20:23]\n\t" \
    "v_mfma_f32_16x16x32_bf16 a[24:27], v[24:27], v[64:67], a[24:27]\n\t" \
    "v_mfma_f32_16x16x32_bf16 a[28:31], v[28:31], v[64:67], a[28:31]\n\t" \
    "s_waitcnt lgkmcnt(2)\n\t" \
    "v_mfma_f32_16x16x32_bf16 a[32:35], v[0:3], v[68:71], a[32:35]\n\t" \
    "v_mfma_f32_16x16x32_bf16 a[36:39], v[4:7], v[68:71], a[36:39]\n\t" \
    "ds_read_b64_tr_b16 v[76:77], v89 offset:32768\n\t" \
    "v_mfma_f32_16x16x32_bf16 a[40:43], v[8:11], v[68:71], a[40:43]\n\t" \
    "ds_read_b64_tr_b16 v[78:79], v89 offset:33792\n\t" \
    "v_mfma_f32_16x16x32_bf16 a[44:47], v[12:15], v[68:71], a[44:47]\n\t" \
    "v_mfma_f32_16x16x32_bf16 a[48:51], v[16:19], v[68:71], a[48:51]\n\t" \
    "buffer_load_dwordx4 v109, s[12:15], s28 offen offset:3072 lds\n\t" \
    "v_mfma_f32_16x16x32_bf16 a[52:55], v[20:23], v[68:71], a[52:55]\n\t" \
    "v_mfma_f32_16x16x32_bf16 a[56:59], v[24:27], v[68:71], a[56:59]\n\t" \
    "v_mfma_f32_16x16x32_bf16 a[60:63], v[28:31], v[68:71], a[60:63]\n\t" \
    "s_waitcnt vmcnt(32) lgkmcnt(0)\n\t" \
    "s_barrier\n\t" \
    "v_mfma_f32_16x16x32_bf16 a[64:67], v[0:3], v[72:75], a[64:67]\n\t" \
    "v_mfma_f32_16x16x32_bf16 a[68:71], v[4:7], v[72:75], a[68:71]\n\t" \
    "ds_read_b64_tr_b16 v[64:65], v82 offset:49152\n\t" \
    "v_mfma_f32_16x16x32_bf16 a[72:75], v[8:11], v[72:75], a[72:75]\n\t" \
    "ds_read_b64_tr_b16 v[66:67], v82 offset:50176\n\t" \
    "v_mfma_f32_16x16x32_bf16 a[76:79], v[12:15], v[72:75], a[76:79]\n\t" \
    "v_mfma_f32_16x16x32_bf16 a[80:83], v[16:19], v[72:75], a[80:83]\n\t" \
    "v_mfma_f32_16x16x32_bf16 a[84:87], v[20:23], v[72:75], a[84:87]\n\t" \
    "v_mfma_f32_16x16x32_bf16 a[88:91], v[24:27], v[72:75], a[88:91]\n\t" \
    "v_mfma_f32_16x16x32_bf16 a[92:95], v[28:31], v[72:75], a[92:95]\n\t" \
    "v_mfma_f32_16x16x32_bf16 a[96:99], v[0:3], v[76:79], a[96:99]\n\t" \
    "v_mfma_f32_16x16x32_bf16 a[100:103], v[4:7], v[76:79], a[100:103]\n\t" \
    "ds_read_b64_tr_b16 v[68:69], v83 offset:49152\n\t" \
    "v_mfma_f32_16x16x32_bf16 a[104:107], v[8:11], v[76:79], a[104:107]\n\t" \
    "ds_read_b64_tr_b16 v[70:71], v83 offset:50176\n\t" \
    "v_mfma_f32_16x16x32_bf16 a[108:111], v[12:15], v[76:79], a[108:111]\n\t" \
    "v_mfma_f32_16x16x32_bf16 a[112:115], v[16:19], v[76:79], a[112:115]\n\t" \
    "v_mfma_f32_16x16x32_bf16 a[116:119], v[20:23], v[76:79], a[116:119]\n\t" \
    "v_mfma_f32_16x16x32_bf16 a[120:123], v[24:27], v[76:79], a[120:123]\n\t" \
    "v_mfma_f32_16x16x32_bf16 a[124:127], v[28:31], v[76:79], a[124:127]\n\t" \
    "s_waitcnt lgkmcnt(2)\n\t" \
    "v_mfma_f32_16x16x32_bf16 v[128:131], v[32:35], v[64:67], v[128:131]\n\t" \
    "v_mfma_f32_16x16x32_bf16 v[132:135], v[36:39], v[64:67], v[132:135]\n\t" \
    "ds_read_b64_tr_b16 v[72:73], v84 offset:49152\n\t" \
    "v_mfma_f32_16x16x32_bf16 v[136:139], v[40:43], v[64:67], v[136:139]\n\t" \
    "ds_read_b64_tr_b16 v[74:75], v84 offset:50176\n\t" \
    "v_mfma_f32_16x16x32_bf16 v[140:143], v[44:47], v[64:67], v[140:143]\n\t" \
    "s_mov_b32 s27, 768\n\t" \
    "s_add_u32 m0, s25, 0\n\t" \
    "v_mfma_f32_16x16x32_bf16 v[144:147], v[48:51], v[64:67], v[144:147]\n\t" \
    "buffer_load_dwordx4 v98, s[8:11], s27 offen offset:0 lds\n\t" \
    "v_mfma_f32_16x16x32_bf16 v[148:151], v[52:55], v[64:67], v[148:151]\n\t" \
    "v_mfma_f32_16x16x32_bf16 v[152:155], v[56:59], v[64:67], v[152:155]\n\t" \
    "buffer_load_dwordx4 v99, s[8:11], s27 offen offset:1024 lds\n\t" \
    "v_mfma_f32_16x16x32_bf16 v[156:159], v[60:63], v[64:67], v[156:159]\n\t" \
    "s_waitcnt lgkmcnt(2)\n\t" \
    "v_mfma_f32_16x16x32_bf16 v[160:163], v[32:35], v[68:71], v[160:163]\n\t" \
    "v_mfma_f32_16x16x32_bf16 v[164:167], v[36:39], v[68:71], v[164:167]\n\t" \
    "ds_read_b64_tr_b16 v[76:77], v85 offset:49152\n\t" \
    "v_mfma_f32_16x16x32_bf16 v[168:171], v[40:43], v[68:71], v[168:171]\n\t" \
    "ds_read_b64_tr_b16 v[78:79], v85 offset:50176\n\t" \
    "v_mfma_f32_16x16x32_bf16 v[172:175], v[44:47], v[68:71], v[172:175]\n\t" \
    "v_mfma_f32_16x16x32_bf16 v[176:179], v[48:51], v[68:71], v[176:179]\n\t" \
    "buffer_load_dwordx4 v100, s[8:11], s27 offen offset:2048 lds\n\t" \
    "v_mfma_f32_16x16x32_bf16 v[180:183], v[52:55], v[68:71], v[180:183]\n\t" \
    "v_mfma_f32_16x16x32_bf16 v[184:187], v[56:59], v[68:71], v[184:187]\n\t" \
    "buffer_load_dwordx4 v101, s[8:11], s27 offen offset:3072 lds\n\t" \
    "v_mfma_f32_16x16x32_bf16 v[188:191], v[60:63], v[68:71], v[188:191]\n\t" \
    "s_waitcnt lgkmcnt(2)\n\t" \
    "v_mfma_f32_16x16x32_bf16 v[192:195], v[32:35], v[72:75], v[192:195]\n\t" \
    "v_mfma_f32_16x16x32_bf16 v[196:199], v[36:39], v[72:75], v[196:199]\n\t" \
    "ds_read_b64_tr_b16 v[64:65], v86 offset:49152\n\t" \
    "v_mfma_f32_16x16x32_bf16 v[200:203], v[40:43], v[72:75], v[200:203]\n\t" \
    "ds_read_b64_tr_b16 v[66:67], v86 offset:50176\n\t" \
    "s_add_u32 m0, s25, 4096\n\t" \
    "v_mfma_f32_16x16x32_bf16 v[204:207], v[44:47], v[72:75], v[204:207]\n\t" \
    "buffer_load_dwordx4 v102, s[8:11], s27 offen offset:0 lds\n\t" \
    "v_mfma_f32_16x16x32_bf16 v[208:211], v[48:51], v[72:75], v[208:211]\n\t" \
    "v_mfma_f32_16x16x32_bf16 v[212:215], v[52:55], v[72:75], v[212:215]\n\t" \
    "buffer_load_dwordx4 v103, s[8:11], s27 offen offset:1024 lds\n\t" \
    "v_mfma_f32_16x16x32_bf16 v[216:219], v[56:59], v[72:75], v[216:219]\n\t" \
    "v_mfma_f32_16x16x32_bf16 v[220:223], v[60:63], v[72:75], v[220:223]\n\t" \
    "s_waitcnt lgkmcnt(2)\n\t" \
    "v_mfma_f32_16x16x32_bf16 v[224:227], v[32:35], v[76:79], v[224:227]\n\t" \
    "v_mfma_f32_16x16x32_bf16 v[228:231], v[36:39], v[76:79], v[228:231]\n\t" \
    "ds_read_b64_tr_b16 v[68:69], v87 offset:49152\n\t" \
    "v_mfma_f32_16x16x32_bf16 v[232:235], v[40:43], v[76:79], v[232:235]\n\t" \
    "ds_read_b64_tr_b16 v[70:71], v87 offset:50176\n\t" \
    "v_mfma_f32_16x16x32_bf16 v[236:239], v[44:47], v[76:79], v[236:239]\n\t" \
    "buffer_load_dwordx4 v104, s[8:11], s27 offen offset:2048 lds\n\t" \
    "v_mfma_f32_16x16x32_bf16 v[240:243], v[48:51], v[76:79], v[240:243]\n\t" \
    "v_mfma_f32_16x16x32_bf16 v[244:247], v[52:55], v[76:79], v[244:247]\n\t" \
    "buffer_load_dwordx4 v105, s[8:11], s27 offen offset:3072 lds\n\t" \
    "v_mfma_f32_16x16x32_bf16 v[248:251], v[56:59], v[76:79], v[248:251]\n\t" \
    "v_mfma_f32_16x16x32_bf16 v[252:255], v[60:63], v[76:79], v[252:255]\n\t" \
    "s_waitcnt lgkmcnt(2)\n\t" \
    "v_mfma_f32_16x16x32_bf16 a[0:3], v[32:35], v[64:67], a[0:3]\n\t" \
    "v_mfma_f32_16x16x32_bf16 a[4:7], v[36:39], v[64:67], a[4:7]\n\t" \
    "ds_read_b64_tr_b16 v[72:73], v88 offset:49152\n\t" \
    "v_mfma_f32_16x16x32_bf16 a[8:11], v[40:43], v[64:67], a[8:11]\n\t" \
    "ds_read_b64_tr_b16 v[74:75], v88 offset:50176\n\t" \
    "s_mov_b32 s28, 3670016\n\t" \
    "s_add_u32 m0, s26, 32768\n\t" \
    "v_mfma_f32_16x16x32_bf16 a[12:15], v[44:47], v[64:67], a[12:15]\n\t" \
    "buffer_load_dwordx4 v106, s[12:15], s28 offen offset:0 lds\n\t" \
    "v_mfma_f32_16x16x32_bf16 a[16:19], v[48:51], v[64:67], a[16:19]\n\t" \
    "v_mfma_f32_16x16x32_bf16 a[20:23], v[52:55], v[64:67], a[20:23]\n\t" \
    "buffer_load_dwordx4 v107, s[12:15], s28 offen offset:1024 lds\n\t" \
    "v_mfma_f32_16x16x32_bf16 a[24:27], v[56:59], v[64:67], a[24:27]\n\t" \
    "v_mfma_f32_16x16x32_bf16 a[28:31], v[60:63], v[64:67], a[28:31]\n\t" \
    "s_waitcnt lgkmcnt(2)\n\t" \
    "v_mfma_f32_16x16x32_bf16 a[32:35], v[32:35], v[68:71], a[32:35]\n\t" \
    "v_mfma_f32_16x16x32_bf16 a[36:39], v[36:39], v[68:71], a[36:39]\n\t" \
    "ds_read_b64_tr_b16 v[76:77], v89 offset:49152\n\t" \
    "v_mfma_f32_16x16x32_bf16 a[40:43], v[40:43], v[68:71], a[40:43]\n\t" \
    "ds_read_b64_tr_b16 v[78:79], v89 offset:50176\n\t" \
    "v_mfma_f32_16x16x32_bf16 a[44:47], v[44:47], v[68:71], a[44:47]\n\t" \
    "buffer_load_dwordx4 v108, s[12:15], s28 offen offset:2048 lds\n\t" \
    "v_mfma_f32_16x16x32_bf16 a[48:51], v[48:51], v[68:71], a[48:51]\n\t" \
    "v_mfma_f32_16x16x32_bf16 a[52:55], v[52:55], v[68:71], a[52:55]\n\t" \
    "buffer_load_dwordx4 v109, s[12:15], s28 offen offset:3072 lds\n\t" \
    "v_mfma_f32_16x16x32_bf16 a[56:59], v[56:59], v[68:71], a[56:59]\n\t" \
    "v_mfma_f32_16x16x32_bf16 a[60:63], v[60:63], v[68:71], a[60:63]\n\t" \
    "s_waitcnt vmcnt(20) lgkmcnt(0)\n\t" \
    "s_barrier\n\t" \
    "v_mfma_f32_16x16x32_bf16 a[64:67], v[32:35], v[72:75], a[64:67]\n\t" \
    "v_mfma_f32_16x16x32_bf16 a[68:71], v[36:39], v[72:75], a[68:71]\n\t" \
    "ds_read_b64_tr_b16 v[64:65], v90 offset:0\n\t" \
    "v_mfma_f32_16x16x32_bf16 a[72:75], v[40:43], v[72:75], a[72:75]\n\t" \
    "ds_read_b64_tr_b16 v[66:67], v90 offset:1024\n\t" \
    "v_mfma_f32_16x16x32_bf16 a[76:79], v[44:47], v[72:75], a[76:79]\n\t" \
    "ds_read_b128 v[0:3], v80 offset:32768\n\t" \
    "v_mfma_f32_16x16x32_bf16 a[80:83], v[48:51], v[72:75], a[80:83]\n\t" \
    "ds_read_b128 v[4:7], v80 offset:33280\n\t" \
    "v_mfma_f32_16x16x32_bf16 a[84:87], v[52:55], v[72:75], a[84:87]\n\t" \
    "ds_read_b128 v[8:11], v80 offset:36864\n\t" \
    "v_mfma_f32_16x16x32_bf16 a[88:91], v[56:59], v[72:75], a[88:91]\n\t" \
    "ds_read_b128 v[12:15], v80 offset:37376\n\t" \
    "v_mfma_f32_16x16x32_bf16 a[92:95], v[60:63], v[72:75], a[92:95]\n\t" \
    "ds_read_b128 v[16:19], v80 offset:40960\n\t" \
    "v_mfma_f32_16x16x32_bf16 a[96:99], v[32:35], v[76:79], a[96:99]\n\t" \
    "ds_read_b128 v[20:23], v80 offset:41472\n\t" \
    "v_mfma_f32_16x16x32_bf16 a[100:103], v[36:39], v[76:79], a[100:103]\n\t" \
    "ds_read_b128 v[24:27], v80 offset:45056\n\t" \
    "v_mfma_f32_16x16x32_bf16 a[104:107], v[40:43], v[76:79], a[104:107]\n\t" \
    "ds_read_b128 v[28:31], v80 offset:45568\n\t" \
    "v_mfma_f32_16x16x32_bf16 a[108:111], v[44:47], v[76:79], a[108:111]\n\t" \
    "ds_read_b64_tr_b16 v[68:69], v91 offset:0\n\t" \
    "v_mfma_f32_16x16x32_bf16 a[112:115], v[48:51], v[76:79], a[112:115]\n\t" \
    "ds_read_b64_tr_b16 v[70:71], v91 offset:1024\n\t" \
    "v_mfma_f32_16x16x32_bf16 a[116:119], v[52:55], v[76:79], a[116:119]\n\t" \
    "v_mfma_f32_16x16x32_bf16 a[120:123], v[56:59], v[76:79], a[120:123]\n\t" \
    "v_mfma_f32_16x16x32_bf16 a[124:127], v[60:63], v[76:79], a[124:127]\n\t" \
    "s_waitcnt lgkmcnt(9)\n\t" \
    "v_mfma_f32_16x16x32_bf16 v[128:131], v[0:3], v[64:67], v[128:131]\n\t" \
    "s_waitcnt lgkmcnt(8)\n\t" \
    "v_mfma_f32_16x16x32_bf16 v[132:135], v[4:7], v[64:67], v[132:135]\n\t" \
    "ds_read_b64_tr_b16 v[72:73], v92 offset:0\n\t" \
    "s_waitcnt lgkmcnt(8)\n\t" \
    "v_mfma_f32_16x16x32_bf16 v[136:139], v[8:11], v[64:67], v[136:139]\n\t" \
    "ds_read_b64_tr_b16 v[74:75], v92 offset:1024\n\t" \
    "s_waitcnt lgkmcnt(8)\n\t" \
    "v_mfma_f32_16x16x32_bf16 v[140:143], v[12:15], v[64:67], v[140:143]\n\t" \
    "ds_read_b128 v[32:35], v81 offset:32768\n\t" \
    "s_waitcnt lgkmcnt(8)\n\t" \
    "v_mfma_f32_16x16x32_bf16 v[144:147], v[16:19], v[64:67], v[144:147]\n\t" \
    "ds_read_b128 v[36:39], v81 offset:33280\n\t" \
    "s_waitcnt lgkmcnt(8)\n\t" \
    "v_mfma_f32_16x16x32_bf16 v[148:151], v[20:23], v[64:67], v[148:151]\n\t" \
    "ds_read_b128 v[40:43], v81 offset:36864\n\t" \
    "s_waitcnt lgkmcnt(8)\n\t" \
    "v_mfma_f32_16x16x32_bf16 v[152:155], v[24:27], v[64:67], v[152:155]\n\t" \
    "ds_read_b128 v[44:47], v81 offset:37376\n\t" \
    "s_waitcnt lgkmcnt(8)\n\t" \
    "v_mfma_f32_16x16x32_bf16 v[156:159], v[28:31], v[64:67], v[156:159]\n\t" \
    "s_waitcnt lgkmcnt(6)\n\t" \
    "v_mfma_f32_16x16x32_bf16 v[160:163], v[0:3], v[68:71], v[160:163]\n\t" \
    "v_mfma_f32_16x16x32_bf16 v[164:167], v[4:7], v[68:71], v[164:167]\n\t" \
    "ds_read_b64_tr_b16 v[76:77], v93 offset:0\n\t" \
    "v_mfma_f32_16x16x32_bf16 v[168:171], v[8:11], v[68:71], v[168:171]\n\t" \
    "ds_read_b64_tr_b16 v[78:79], v93 offset:1024\n\t" \
    "v_mfma_f32_16x16x32_bf16 v[172:175], v[12:15], v[68:71], v[172:175]\n\t" \
    "ds_read_b128 v[48:51], v81 offset:40960\n\t" \
    "v_mfma_f32_16x16x32_bf16 v[176:179], v[16:19], v[68:71], v[176:179]\n\t" \
    "ds_read_b128 v[52:55], v81 offset:41472\n\t" \
    "v_mfma_f32_16x16x32_bf16 v[180:183], v[20:23], v[68:71], v[180:183]\n\t" \
    "ds_read_b128 v[56:59], v81 offset:45056\n\t" \
    "v_mfma_f32_16x16x32_bf16 v[184:187], v[24:27], v[68:71], v[184:187]\n\t" \
    "ds_read_b128 v[60:63], v81 offset:45568\n\t" \
    "v_mfma_f32_16x16x32_bf16 v[188:191], v[28:31], v[68:71], v[188:191]\n\t" \
    "s_waitcnt lgkmcnt(10)\n\t" \
    "v_mfma_f32_16x16x32_bf16 v[192:195], v[0:3], v[72:75], v[192:195]\n\t" \
    "v_mfma_f32_16x16x32_bf16 v[196:199], v[4:7], v[72:75], v[196:199]\n\t" \
    "ds_read_b64_tr_b16 v[64:65], v94 offset:0\n\t" \
    "v_mfma_f32_16x16x32_bf16 v[200:203], v[8:11], v[72:75], v[200:203]\n\t" \
    "ds_read_b64_tr_b16 v[66:67], v94 offset:1024\n\t" \
    "v_mfma_f32_16x16x32_bf16 v[204:207], v[12:15], v[72:75], v[204:207]\n\t" \
    "s_mov_b32 s28, 3932160\n\t" \
    "s_add_u32 m0, s26, 49152\n\t" \
    "v_mfma_f32_16x16x32_bf16 v[208:211], v[16:19], v[72:75], v[208:211]\n\t" \
    "buffer_load_dwordx4 v106, s[12:15], s28 offen offset:0 lds\n\t" \
    "v_mfma_f32_16x16x32_bf16 v[212:215], v[20:23], v[72:75], v[212:215]\n\t" \
    "v_mfma_f32_16x16x32_bf16 v[216:219], v[24:27], v[72:75], v[216:219]\n\t" \
    "v_mfma_f32_16x16x32_bf16 v[220:223], v[28:31], v[72:75], v[220:223]\n\t" \
    "s_waitcnt lgkmcnt(6)\n\t" \
    "v_mfma_f32_16x16x32_bf16 v[224:227], v[0:3], v[76:79], v[224:227]\n\t" \
    "v_mfma_f32_16x16x32_bf16 v[228:231], v[4:7], v[76:79], v[228:231]\n\t" \
    "ds_read_b64_tr_b16 v[68:69], v95 offset:0\n\t" \
    "v_mfma_f32_16x16x32_bf16 v[232:235], v[8:11], v[76:79], v[232:235]\n\t" \
    "ds_read_b64_tr_b16 v[70:71], v95 offset:1024\n\t" \
    "v_mfma_f32_16x16x32_bf16 v[236:239], v[12:15], v[76:79], v[236:239]\n\t" \
    "v_mfma_f32_16x16x32_bf16 v[240:243], v[16:19], v[76:79], v[240:243]\n\t" \
    "buffer_load_dwordx4 v107, s[12:15], s28 offen offset:1024 lds\n\t" \
    "v_mfma_f32_16x16x32_bf16 v[244:247], v[20:23], v[76:79], v[244:247]\n\t" \
    "v_mfma_f32_16x16x32_bf16 v[248:251], v[24:27], v[76:79], v[248:251]\n\t" \
    "v_mfma_f32_16x16x32_bf16 v[252:255], v[28:31], v[76:79], v[252:255]\n\t" \
    "s_waitcnt lgkmcnt(2)\n\t" \
    "v_mfma_f32_16x16x32_bf16 a[0:3], v[0:3], v[64:67], a[0:3]\n\t" \
    "v_mfma_f32_16x16x32_bf16 a[4:7], v[4:7], v[64:67], a[4:7]\n\t" \
    "ds_read_b64_tr_b16 v[72:73], v96 offset:0\n\t" \
    "v_mfma_f32_16x16x32_bf16 a[8:11], v[8:11], v[64:67], a[8:11]\n\t" \
    "ds_read_b64_tr_b16 v[74:75], v96 offset:1024\n\t" \
    "v_mfma_f32_16x16x32_bf16 a[12:15], v[12:15], v[64:67], a[12:15]\n\t" \
    "v_mfma_f32_16x16x32_bf16 a[16:19], v[16:19], v[64:67], a[16:19]\n\t" \
    "buffer_load_dwordx4 v108, s[12:15], s28 offen offset:2048 lds\n\t" \
    "v_mfma_f32_16x16x32_bf16 a[20:23], v[20:23], v[64:67], a[20:23]\n\t" \
    "v_mfma_f32_16x16x32_bf16 a[24:27], v[24:27], v[64:67], a[24:27]\n\t" \
    "v_mfma_f32_16x16x32_bf16 a[28:31], v[28:31], v[64:67], a[28:31]\n\t" \
    "s_waitcnt lgkmcnt(2)\n\t" \
    "v_mfma_f32_16x16x32_bf16 a[32:35], v[0:3], v[68:71], a[32:35]\n\t" \
    "v_mfma_f32_16x16x32_bf16 a[36:39], v[4:7], v[68:71], a[36:39]\n\t" \
    "ds_read_b64_tr_b16 v[76:77], v97 offset:0\n\t" \
    "v_mfma_f32_16x16x32_bf16 a[40:43], v[8:11], v[68:71], a[40:43]\n\t" \
    "ds_read_b64_tr_b16 v[78:79], v97 offset:1024\n\t" \
    "v_mfma_f32_16x16x32_bf16 a[44:47], v[12:15], v[68:71], a[44:47]\n\t" \
    "v_mfma_f32_16x16x32_bf16 a[48:51], v[16:19], v[68:71], a[48:51]\n\t" \
    "buffer_load_dwordx4 v109, s[12:15], s28 offen offset:3072 lds\n\t" \
    "v_mfma_f32_16x16x32_bf16 a[52:55], v[20:23], v[68:71], a[52:55]\n\t" \
    "v_mfma_f32_16x16x32_bf16 a[56:59], v[24:27], v[68:71], a[56:59]\n\t" \
    "v_mfma_f32_16x16x32_bf16 a[60:63], v[28:31], v[68:71], a[60:63]\n\t" \
    "s_waitcnt vmcnt(32) lgkmcnt(0)\n\t" \
    "s_barrier\n\t" \
    "v_mfma_f32_16x16x32_bf16 a[64:67], v[0:3], v[72:75], a[64:67]\n\t" \
    "v_mfma_f32_16x16x32_bf16 a[68:71], v[4:7], v[72:75], a[68:71]\n\t" \
    "ds_read_b64_tr_b16 v[64:65], v90 offset:16384\n\t" \
    "v_mfma_f32_16x16x32_bf16 a[72:75], v[8:11], v[72:75], a[72:75]\n\t" \
    "ds_read_b64_tr_b16 v[66:67], v90 offset:17408\n\t" \
    "v_mfma_f32_16x16x32_bf16 a[76:79], v[12:15], v[72:75], a[76:79]\n\t" \
    "v_mfma_f32_16x16x32_bf16 a[80:83], v[16:19], v[72:75], a[80:83]\n\t" \
    "v_mfma_f32_16x16x32_bf16 a[84:87], v[20:23], v[72:75], a[84:87]\n\t" \
    "v_mfma_f32_16x16x32_bf16 a[88:91], v[24:27], v[72:75], a[88:91]\n\t" \
    "v_mfma_f32_16x16x32_bf16 a[92:95], v[28:31], v[72:75], a[92:95]\n\t" \
    "v_mfma_f32_16x16x32_bf16 a[96:99], v[0:3], v[76:79], a[96:99]\n\t" \
    "v_mfma_f32_16x16x32_bf16 a[100:103], v[4:7], v[76:79], a[100:103]\n\t" \
    "ds_read_b64_tr_b16 v[68:69], v91 offset:16384\n\t" \
    "v_mfma_f32_16x16x32_bf16 a[104:107], v[8:11], v[76:79], a[104:107]\n\t" \
    "ds_read_b64_tr_b16 v[70:71], v91 offset:17408\n\t" \
    "v_mfma_f32_16x16x32_bf16 a[108:111], v[12:15], v[76:79], a[108:111]\n\t" \
    "v_mfma_f32_16x16x32_bf16 a[112:115], v[16:19], v[76:79], a[112:115]\n\t" \
    "v_mfma_f32_16x16x32_bf16 a[116:119], v[20:23], v[76:79], a[116:119]\n\t" \
    "v_mfma_f32_16x16x32_bf16 a[120:123], v[24:27], v[76:79], a[120:123]\n\t" \
    "v_mfma_f32_16x16x32_bf16 a[124:127], v[28:31], v[76:79], a[124:127]\n\t" \
    "s_waitcnt lgkmcnt(2)\n\t" \
    "v_mfma_f32_16x16x32_bf16 v[128:131], v[32:35], v[64:67], v[128:131]\n\t" \
    "v_mfma_f32_16x16x32_bf16 v[132:135], v[36:39], v[64:67], v[132:135]\n\t" \
    "ds_read_b64_tr_b16 v[72:73], v92 offset:16384\n\t" \
    "v_mfma_f32_16x16x32_bf16 v[136:139], v[40:43], v[64:67], v[136:139]\n\t" \
    "ds_read_b64_tr_b16 v[74:75], v92 offset:17408\n\t" \
    "s_lshr_b32 s23, s20, 1\n\t" \
    "v_mfma_f32_16x16x32_bf16 v[140:143], v[44:47], v[64:67], v[140:143]\n\t" \
    "s_lshl_b32 s23, s23, 18\n\t" \
    "s_mov_b32 s27, 896\n\t" \
    "s_add_u32 m0, s25, 32768\n\t" \
    "v_mfma_f32_16x16x32_bf16 v[144:147], v[48:51], v[64:67], v[144:147]\n\t" \
    "buffer_load_dwordx4 v98, s[8:11], s27 offen offset:0 lds\n\t" \
    "s_add_u32 s12, %[qlo], s23\n\t" \
    "v_mfma_f32_16x16x32_bf16 v[148:151], v[52:55], v[64:67], v[148:151]\n\t" \
    "s_addc_u32 s13, %[qhi], 0\n\t" \
    "v_mfma_f32_16x16x32_bf16 v[152:155], v[56:59], v[64:67], v[152:155]\n\t" \
    "buffer_load_dwordx4 v99, s[8:11], s27 offen offset:1024 lds\n\t" \
    "v_mfma_f32_16x16x32_bf16 v[156:159], v[60:63], v[64:67], v[156:159]\n\t" \
    "s_waitcnt lgkmcnt(2)\n\t" \
    "v_mfma_f32_16x16x32_bf16 v[160:163], v[32:35], v[68:71], v[160:163]\n\t" \
    "v_mfma_f32_16x16x32_bf16 v[164:167], v[36:39], v[68:71], v[164:167]\n\t" \
    "ds_read_b64_tr_b16 v[76:77], v93 offset:16384\n\t" \
    "v_mfma_f32_16x16x32_bf16 v[168:171], v[40:43], v[68:71], v[168:171]\n\t" \
    "ds_read_b64_tr_b16 v[78:79], v93 offset:17408\n\t" \
    "v_mfma_f32_16x16x32_bf16 v[172:175], v[44:47], v[68:71], v[172:175]\n\t" \
    "v_mfma_f32_16x16x32_bf16 v[176:179], v[48:51], v[68:71], v[176:179]\n\t" \
    "buffer_load_dwordx4 v100, s[8:11], s27 offen offset:2048 lds\n\t" \
    "v_mfma_f32_16x16x32_bf16 v[180:183], v[52:55], v[68:71], v[180:183]\n\t" \
    "v_mfma_f32_16x16x32_bf16 v[184:187], v[56:59], v[68:71], v[184:187]\n\t" \
    "buffer_load_dwordx4 v101, s[8:11], s27 offen offset:3072 lds\n\t" \
    "v_mfma_f32_16x16x32_bf16 v[188:191], v[60:63], v[68:71], v[188:191]\n\t" \
    "s_waitcnt lgkmcnt(2)\n\t" \
    "v_mfma_f32_16x16x32_bf16 v[192:195], v[32:35], v[72:75], v[192:195]\n\t" \
    "v_mfma_f32_16x16x32_bf16 v[196:199], v[36:39], v[72:75], v[196:199]\n\t" \
    "ds_read_b64_tr_b16 v[64:65], v94 offset:16384\n\t" \
    "v_mfma_f32_16x16x32_bf16 v[200:203], v[40:43], v[72:75], v[200:203]\n\t" \
    "ds_read_b64_tr_b16 v[66:67], v94 offset:17408\n\t" \
    "s_add_u32 m0, s25, 36864\n\t" \
    "v_mfma_f32_16x16x32_bf16 v[204:207], v[44:47], v[72:75], v[204:207]\n\t" \
    "buffer_load_dwordx4 v102, s[8:11], s27 offen offset:0 lds\n\t" \
    "v_mfma_f32_16x16x32_bf16 v[208:211], v[48:51], v[72:75], v[208:211]\n\t" \
    "v_mfma_f32_16x16x32_bf16 v[212:215], v[52:55], v[72:75], v[212:215]\n\t" \
    "buffer_load_dwordx4 v103, s[8:11], s27 offen offset:1024 lds\n\t" \
    "v_mfma_f32_16x16x32_bf16 v[216:219], v[56:59], v[72:75], v[216:219]\n\t" \
    "v_mfma_f32_16x16x32_bf16 v[220:223], v[60:63], v[72:75], v[220:223]\n\t" \
    "s_waitcnt lgkmcnt(2)\n\t" \
    "v_mfma_f32_16x16x32_bf16 v[224:227], v[32:35], v[76:79], v[224:227]\n\t" \
    "v_mfma_f32_16x16x32_bf16 v[228:231], v[36:39], v[76:79], v[228:231]\n\t" \
    "ds_read_b64_tr_b16 v[68:69], v95 offset:16384\n\t" \
    "v_mfma_f32_16x16x32_bf16 v[232:235], v[40:43], v[76:79], v[232:235]\n\t" \
    "ds_read_b64_tr_b16 v[70:71], v95 offset:17408\n\t" \
    "v_mfma_f32_16x16x32_bf16 v[236:239], v[44:47], v[76:79], v[236:239]\n\t" \
    "buffer_load_dwordx4 v104, s[8:11], s27 offen offset:2048 lds\n\t" \
    "v_mfma_f32_16x16x32_bf16 v[240:243], v[48:51], v[76:79], v[240:243]\n\t" \
    "v_mfma_f32_16x16x32_bf16 v[244:247], v[52:55], v[76:79], v[244:247]\n\t" \
    "buffer_load_dwordx4 v105, s[8:11], s27 offen offset:3072 lds\n\t" \
    "v_mfma_f32_16x16x32_bf16 v[248:251], v[56:59], v[76:79], v[248:251]\n\t" \
    "v_mfma_f32_16x16x32_bf16 v[252:255], v[60:63], v[76:79], v[252:255]\n\t" \
    "s_waitcnt lgkmcnt(2)\n\t" \
    "v_mfma_f32_16x16x32_bf16 a[0:3], v[32:35], v[64:67], a[0:3]\n\t" \
    "v_mfma_f32_16x16x32_bf16 a[4:7], v[36:39], v[64:67], a[4:7]\n\t" \
    "ds_read_b64_tr_b16 v[72:73], v96 offset:16384\n\t" \
    "v_mfma_f32_16x16x32_bf16 a[8:11], v[40:43], v[64:67], a[8:11]\n\t" \
    "ds_read_b64_tr_b16 v[74:75], v96 offset:17408\n\t" \
    "s_mov_b32 s28, 0\n\t" \
    "s_add_u32 m0, s26, 65536\n\t" \
    "v_mfma_f32_16x16x32_bf16 a[12:15], v[44:47], v[64:67], a[12:15]\n\t" \
    "buffer_load_dwordx4 v106, s[12:15], s28 offen offset:0 lds\n\t" \
    "v_mfma_f32_16x16x32_bf16 a[16:19], v[48:51], v[64:67], a[16:19]\n\t" \
    "v_mfma_f32_16x16x32_bf16 a[20:23], v[52:55], v[64:67], a[20:23]\n\t" \
    "buffer_load_dwordx4 v107, s[12:15], s28 offen offset:1024 lds\n\t" \
    "v_mfma_f32_16x16x32_bf16 a[24:27], v[56:59], v[64:67], a[24:27]\n\t" \
    "v_mfma_f32_16x16x32_bf16 a[28:31], v[60:63], v[64:67], a[28:31]\n\t" \
    "s_waitcnt lgkmcnt(2)\n\t" \
    "v_mfma_f32_16x16x32_bf16 a[32:35], v[32:35], v[68:71], a[32:35]\n\t" \
    "v_mfma_f32_16x16x32_bf16 a[36:39], v[36:39], v[68:71], a[36:39]\n\t" \
    "ds_read_b64_tr_b16 v[76:77], v97 offset:16384\n\t" \
    "v_mfma_f32_16x16x32_bf16 a[40:43], v[40:43], v[68:71], a[40:43]\n\t" \
    "ds_read_b64_tr_b16 v[78:79], v97 offset:17408\n\t" \
    "v_mfma_f32_16x16x32_bf16 a[44:47], v[44:47], v[68:71], a[44:47]\n\t" \
    "buffer_load_dwordx4 v108, s[12:15], s28 offen offset:2048 lds\n\t" \
    "v_mfma_f32_16x16x32_bf16 a[48:51], v[48:51], v[68:71], a[48:51]\n\t" \
    "v_mfma_f32_16x16x32_bf16 a[52:55], v[52:55], v[68:71], a[52:55]\n\t" \
    "buffer_load_dwordx4 v109, s[12:15], s28 offen offset:3072 lds\n\t" \
    "v_mfma_f32_16x16x32_bf16 a[56:59], v[56:59], v[68:71], a[56:59]\n\t" \
    "v_mfma_f32_16x16x32_bf16 a[60:63], v[60:63], v[68:71], a[60:63]\n\t" \
    "s_waitcnt vmcnt(20) lgkmcnt(0)\n\t" \
    "s_barrier\n\t" \
    "v_mfma_f32_16x16x32_bf16 a[64:67], v[32:35], v[72:75], a[64:67]\n\t" \
    "v_mfma_f32_16x16x32_bf16 a[68:71], v[36:39], v[72:75], a[68:71]\n\t" \
    "ds_read_b64_tr_b16 v[64:65], v82 offset:0\n\t" \
    "v_mfma_f32_16x16x32_bf16 a[72:75], v[40:43], v[72:75], a[72:75]\n\t" \
    "ds_read_b64_tr_b16 v[66:67], v82 offset:1024\n\t" \
    "v_mfma_f32_16x16x32_bf16 a[76:79], v[44:47], v[72:75], a[76:79]\n\t" \
    "ds_read_b128 v[0:3], v80 offset:0\n\t" \
    "v_mfma_f32_16x16x32_bf16 a[80:83], v[48:51], v[72:75], a[80:83]\n\t" \
    "ds_read_b128 v[4:7], v80 offset:512\n\t" \
    "v_mfma_f32_16x16x32_bf16 a[84:87], v[52:55], v[72:75], a[84:87]\n\t" \
    "ds_read_b128 v[8:11], v80 offset:4096\n\t" \
    "v_mfma_f32_16x16x32_bf16 a[88:91], v[56:59], v[72:75], a[88:91]\n\t" \
    "ds_read_b128 v[12:15], v80 offset:4608\n\t" \
    "v_mfma_f32_16x16x32_bf16 a[92:95], v[60:63], v[72:75], a[92:95]\n\t" \
    "ds_read_b128 v[16:19], v80 offset:8192\n\t" \
    "v_mfma_f32_16x16x32_bf16 a[96:99], v[32:35], v[76:79], a[96:99]\n\t" \
    "ds_read_b128 v[20:23], v80 offset:8704\n\t" \
    "v_mfma_f32_16x16x32_bf16 a[100:103], v[36:39], v[76:79], a[100:103]\n\t" \
    "ds_read_b128 v[24:27], v80 offset:12288\n\t" \
    "v_mfma_f32_16x16x32_bf16 a[104:107], v[40:43], v[76:79], a[104:107]\n\t" \
    "ds_read_b128 v[28:31], v80 offset:12800\n\t" \
    "v_mfma_f32_16x16x32_bf16 a[108:111], v[44:47], v[76:79], a[108:111]\n\t" \
    "ds_read_b64_tr_b16 v[68:69], v83 offset:0\n\t" \
    "v_mfma_f32_16x16x32_bf16 a[112:115], v[48:51], v[76:79], a[112:115]\n\t" \
    "ds_read_b64_tr_b16 v[70:71], v83 offset:1024\n\t" \
    "v_mfma_f32_16x16x32_bf16 a[116:119], v[52:55], v[76:79], a[116:119]\n\t" \
    "v_mfma_f32_16x16x32_bf16 a[120:123], v[56:59], v[76:79], a[120:123]\n\t" \
    "v_mfma_f32_16x16x32_bf16 a[124:127], v[60:63], v[76:79], a[124:127]\n\t" \
    "s_waitcnt lgkmcnt(9)\n\t" \
    "v_mfma_f32_16x16x32_bf16 v[128:131], v[0:3], v[64:67], v[128:131]\n\t" \
    "s_waitcnt lgkmcnt(8)\n\t" \
    "v_mfma_f32_16x16x32_bf16 v[132:135], v[4:7], v[64:67], v[132:135]\n\t" \
    "ds_read_b64_tr_b16 v[72:73], v84 offset:0\n\t" \
    "s_waitcnt lgkmcnt(8)\n\t" \
    "v_mfma_f32_16x16x32_bf16 v[136:139], v[8:11], v[64:67], v[136:139]\n\t" \
    "ds_read_b64_tr_b16 v[74:75], v84 offset:1024\n\t" \
    "s_waitcnt lgkmcnt(8)\n\t" \
    "v_mfma_f32_16x16x32_bf16 v[140:143], v[12:15], v[64:67], v[140:143]\n\t" \
    "ds_read_b128 v[32:35], v81 offset:0\n\t" \
    "s_waitcnt lgkmcnt(8)\n\t" \
    "v_mfma_f32_16x16x32_bf16 v[144:147], v[16:19], v[64:67], v[144:147]\n\t" \
    "ds_read_b128 v[36:39], v81 offset:512\n\t" \
    "s_waitcnt lgkmcnt(8)\n\t" \
    "v_mfma_f32_16x16x32_bf16 v[148:151], v[20:23], v[64:67], v[148:151]\n\t" \
    "ds_read_b128 v[40:43], v81 offset:4096\n\t" \
    "s_waitcnt lgkmcnt(8)\n\t" \
    "v_mfma_f32_16x16x32_bf16 v[152:155], v[24:27], v[64:67], v[152:155]\n\t" \
    "ds_read_b128 v[44:47], v81 offset:4608\n\t" \
    "s_waitcnt lgkmcnt(8)\n\t" \
    "v_mfma_f32_16x16x32_bf16 v[156:159], v[28:31], v[64:67], v[156:159]\n\t" \
    "s_waitcnt lgkmcnt(6)\n\t" \
    "v_mfma_f32_16x16x32_bf16 v[160:163], v[0:3], v[68:71], v[160:163]\n\t" \
    "v_mfma_f32_16x16x32_bf16 v[164:167], v[4:7], v[68:71], v[164:167]\n\t" \
    "ds_read_b64_tr_b16 v[76:77], v85 offset:0\n\t" \
    "v_mfma_f32_16x16x32_bf16 v[168:171], v[8:11], v[68:71], v[168:171]\n\t" \
    "ds_read_b64_tr_b16 v[78:79], v85 offset:1024\n\t" \
    "buffer_load_dword v111, off, s[16:19], s29 sc1\n\t" \
    "v_mfma_f32_16x16x32_bf16 v[172:175], v[12:15], v[68:71], v[172:175]\n\t" \
    "ds_read_b128 v[48:51], v81 offset:8192\n\t" \
    "v_mfma_f32_16x16x32_bf16 v[176:179], v[16:19], v[68:71], v[176:179]\n\t" \
    "ds_read_b128 v[52:55], v81 offset:8704\n\t" \
    "v_mfma_f32_16x16x32_bf16 v[180:183], v[20:23], v[68:71], v[180:183]\n\t" \
    "ds_read_b128 v[56:59], v81 offset:12288\n\t" \
    "v_mfma_f32_16x16x32_bf16 v[184:187], v[24:27], v[68:71], v[184:187]\n\t" \
    "ds_read_b128 v[60:63], v81 offset:12800\n\t" \
    "v_mfma_f32_16x16x32_bf16 v[188:191], v[28:31], v[68:71], v[188:191]\n\t" \
    "s_waitcnt lgkmcnt(10)\n\t" \
    "v_mfma_f32_16x16x32_bf16 v[192:195], v[0:3], v[72:75], v[192:195]\n\t" \
    "v_mfma_f32_16x16x32_bf16 v[196:199], v[4:7], v[72:75], v[196:199]\n\t" \
    "ds_read_b64_tr_b16 v[64:65], v86 offset:0\n\t" \
    "v_mfma_f32_16x16x32_bf16 v[200:203], v[8:11], v[72:75], v[200:203]\n\t" \
    "ds_read_b64_tr_b16 v[66:67], v86 offset:1024\n\t" \
    "v_mfma_f32_16x16x32_bf16 v[204:207], v[12:15], v[72:75], v[204:207]\n\t" \
    "s_mov_b32 s28, 524288\n\t" \
    "s_add_u32 m0, s26, 81920\n\t" \
    "v_mfma_f32_16x16x32_bf16 v[208:211], v[16:19], v[72:75], v[208:211]\n\t" \
    "buffer_load_dwordx4 v106, s[12:15], s28 offen offset:0 lds\n\t" \
    "v_mfma_f32_16x16x32_bf16 v[212:215], v[20:23], v[72:75], v[212:215]\n\t" \
    "v_mfma_f32_16x16x32_bf16 v[216:219], v[24:27], v[72:75], v[216:219]\n\t" \
    "v_mfma_f32_16x16x32_bf16 v[220:223], v[28:31], v[72:75], v[220:223]\n\t" \
    "s_waitcnt lgkmcnt(6)\n\t" \
    "v_mfma_f32_16x16x32_bf16 v[224:227], v[0:3], v[76:79], v[224:227]\n\t" \
    "v_mfma_f32_16x16x32_bf16 v[228:231], v[4:7], v[76:79], v[228:231]\n\t" \
    "ds_read_b64_tr_b16 v[68:69], v87 offset:0\n\t" \
    "v_mfma_f32_16x16x32_bf16 v[232:235], v[8:11], v[76:79], v[232:235]\n\t" \
    "ds_read_b64_tr_b16 v[70:71], v87 offset:1024\n\t" \
    "v_mfma_f32_16x16x32_bf16 v[236:239], v[12:15], v[76:79], v[236:239]\n\t" \
    "v_mfma_f32_16x16x32_bf16 v[240:243], v[16:19], v[76:79], v[240:243]\n\t" \
    "buffer_load_dwordx4 v107, s[12:15], s28 offen offset:1024 lds\n\t" \
    "v_mfma_f32_16x16x32_bf16 v[244:247], v[20:23], v[76:79], v[244:247]\n\t" \
    "v_mfma_f32_16x16x32_bf16 v[248:251], v[24:27], v[76:79], v[248:251]\n\t" \
    "v_mfma_f32_16x16x32_bf16 v[252:255], v[28:31], v[76:79], v[252:255]\n\t" \
    "s_waitcnt lgkmcnt(2)\n\t" \
    "v_mfma_f32_16x16x32_bf16 a[0:3], v[0:3], v[64:67], a[0:3]\n\t" \
    "v_mfma_f32_16x16x32_bf16 a[4:7], v[4:7], v[64:67], a[4:7]\n\t" \
    "ds_read_b64_tr_b16 v[72:73], v88 offset:0\n\t" \
    "v_mfma_f32_16x16x32_bf16 a[8:11], v[8:11], v[64:67], a[8:11]\n\t" \
    "ds_read_b64_tr_b16 v[74:75], v88 offset:1024\n\t" \
    "v_mfma_f32_16x16x32_bf16 a[12:15], v[12:15], v[64:67], a[12:15]\n\t" \
    "v_mfma_f32_16x16x32_bf16 a[16:19], v[16:19], v[64:67], a[16:19]\n\t" \
    "buffer_load_dwordx4 v108, s[12:15], s28 offen offset:2048 lds\n\t" \
    "v_mfma_f32_16x16x32_bf16 a[20:23], v[20:23], v[64:67], a[20:23]\n\t" \
    "v_mfma_f32_16x16x32_bf16 a[24:27], v[24:27], v[64:67], a[24:27]\n\t" \
    "v_mfma_f32_16x16x32_bf16 a[28:31], v[28:31], v[64:67], a[28:31]\n\t" \
    "s_waitcnt lgkmcnt(2)\n\t" \
    "v_mfma_f32_16x16x32_bf16 a[32:35], v[0:3], v[68:71], a[32:35]\n\t" \
    "v_mfma_f32_16x16x32_bf16 a[36:39], v[4:7], v[68:71], a[36:39]\n\t" \
    "ds_read_b64_tr_b16 v[76:77], v89 offset:0\n\t" \
    "v_mfma_f32_16x16x32_bf16 a[40:43], v[8:11], v[68:71], a[40:43]\n\t" \
    "ds_read_b64_tr_b16 v[78:79], v89 offset:1024\n\t" \
    "v_mfma_f32_16x16x32_bf16 a[44:47], v[12:15], v[68:71], a[44:47]\n\t" \
    "v_mfma_f32_16x16x32_bf16 a[48:51], v[16:19], v[68:71], a[48:51]\n\t" \
    "buffer_load_dwordx4 v109, s[12:15], s28 offen offset:3072 lds\n\t" \
    "v_mfma_f32_16x16x32_bf16 a[52:55], v[20:23], v[68:71], a[52:55]\n\t" \
    "v_mfma_f32_16x16x32_bf16 a[56:59], v[24:27], v[68:71], a[56:59]\n\t" \
    "v_mfma_f32_16x16x32_bf16 a[60:63], v[28:31], v[68:71], a[60:63]\n\t" \
    "s_waitcnt vmcnt(33) lgkmcnt(0)\n\t" \
    "s_barrier\n\t" \
    "v_mfma_f32_16x16x32_bf16 a[64:67], v[0:3], v[72:75], a[64:67]\n\t" \
    "v_mfma_f32_16x16x32_bf16 a[68:71], v[4:7], v[72:75], a[68:71]\n\t" \
    "ds_read_b64_tr_b16 v[64:65], v82 offset:16384\n\t" \
    "v_mfma_f32_16x16x32_bf16 a[72:75], v[8:11], v[72:75], a[72:75]\n\t" \
    "ds_read_b64_tr_b16 v[66:67], v82 offset:17408\n\t" \
    "v_mfma_f32_16x16x32_bf16 a[76:79], v[12:15], v[72:75], a[76:79]\n\t" \
    "v_mfma_f32_16x16x32_bf16 a[80:83], v[16:19], v[72:75], a[80:83]\n\t" \
    "v_mfma_f32_16x16x32_bf16 a[84:87], v[20:23], v[72:75], a[84:87]\n\t" \
    "v_mfma_f32_16x16x32_bf16 a[88:91], v[24:27], v[72:75], a[88:91]\n\t" \
    "v_mfma_f32_16x16x32_bf16 a[92:95], v[28:31], v[72:75], a[92:95]\n\t" \
    "v_mfma_f32_16x16x32_bf16 a[96:99], v[0:3], v[76:79], a[96:99]\n\t" \
    "v_mfma_f32_16x16x32_bf16 a[100:103], v[4:7], v[76:79], a[100:103]\n\t" \
    "ds_read_b64_tr_b16 v[68:69], v83 offset:16384\n\t" \
    "v_mfma_f32_16x16x32_bf16 a[104:107], v[8:11], v[76:79], a[104:107]\n\t" \
    "ds_read_b64_tr_b16 v[70:71], v83 offset:17408\n\t" \
    "v_mfma_f32_16x16x32_bf16 a[108:111], v[12:15], v[76:79], a[108:111]\n\t" \
    "v_mfma_f32_16x16x32_bf16 a[112:115], v[16:19], v[76:79], a[112:115]\n\t" \
    "v_mfma_f32_16x16x32_bf16 a[116:119], v[20:23], v[76:79], a[116:119]\n\t" \
    "v_mfma_f32_16x16x32_bf16 a[120:123], v[24:27], v[76:79], a[120:123]\n\t" \
    "v_mfma_f32_16x16x32_bf16 a[124:127], v[28:31], v[76:79], a[124:127]\n\t" \
    "s_waitcnt lgkmcnt(2)\n\t" \
    "v_mfma_f32_16x16x32_bf16 v[128:131], v[32:35], v[64:67], v[128:131]\n\t" \
    "v_mfma_f32_16x16x32_bf16 v[132:135], v[36:39], v[64:67], v[132:135]\n\t" \
    "ds_read_b64_tr_b16 v[72:73], v84 offset:16384\n\t" \
    "v_mfma_f32_16x16x32_bf16 v[136:139], v[40:43], v[64:67], v[136:139]\n\t" \
    "ds_read_b64_tr_b16 v[74:75], v84 offset:17408\n\t" \
    "v_mfma_f32_16x16x32_bf16 v[140:143], v[44:47], v[64:67], v[140:143]\n\t" \
    "v_mfma_f32_16x16x32_bf16 v[144:147], v[48:51], v[64:67], v[144:147]\n\t" \
    "v_mfma_f32_16x16x32_bf16 v[148:151], v[52:55], v[64:67], v[148:151]\n\t" \
    "v_mfma_f32_16x16x32_bf16 v[152:155], v[56:59], v[64:67], v[152:155]\n\t" \
    "v_mfma_f32_16x16x32_bf16 v[156:159], v[60:63], v[64:67], v[156:159]\n\t" \
    "s_waitcnt lgkmcnt(2)\n\t" \
    "v_mfma_f32_16x16x32_bf16 v[160:163], v[32:35], v[68:71], v[160:163]\n\t" \
    "v_mfma_f32_16x16x32_bf16 v[164:167], v[36:39], v[68:71], v[164:167]\n\t" \
    "ds_read_b64_tr_b16 v[76:77], v85 offset:16384\n\t" \
    "v_mfma_f32_16x16x32_bf16 v[168:171], v[40:43], v[68:71], v[168:171]\n\t" \
    "ds_read_b64_tr_b16 v[78:79], v85 offset:17408\n\t" \
    "v_mfma_f32_16x16x32_bf16 v[172:175], v[44:47], v[68:71], v[172:175]\n\t" \
    "v_mfma_f32_16x16x32_bf16 v[176:179], v[48:51], v[68:71], v[176:179]\n\t" \
    "v_mfma_f32_16x16x32_bf16 v[180:183], v[52:55], v[68:71], v[180:183]\n\t" \
    "v_mfma_f32_16x16x32_bf16 v[184:187], v[56:59], v[68:71], v[184:187]\n\t" \
    "v_mfma_f32_16x16x32_bf16 v[188:191], v[60:63], v[68:71], v[188:191]\n\t" \
    "s_waitcnt lgkmcnt(2)\n\t" \
    "v_mfma_f32_16x16x32_bf16 v[192:195], v[32:35], v[72:75], v[192:195]\n\t" \
    "v_mfma_f32_16x16x32_bf16 v[196:199], v[36:39], v[72:75], v[196:199]\n\t" \
    "ds_read_b64_tr_b16 v[64:65], v86 offset:16384\n\t" \
    "v_mfma_f32_16x16x32_bf16 v[200:203], v[40:43], v[72:75], v[200:203]\n\t" \
    "ds_read_b64_tr_b16 v[66:67], v86 offset:17408\n\t" \
    "v_mfma_f32_16x16x32_bf16 v[204:207], v[44:47], v[72:75], v[204:207]\n\t" \
    "v_mfma_f32_16x16x32_bf16 v[208:211], v[48:51], v[72:75], v[208:211]\n\t" \
    "v_mfma_f32_16x16x32_bf16 v[212:215], v[52:55], v[72:75], v[212:215]\n\t" \
    "v_mfma_f32_16x16x32_bf16 v[216:219], v[56:59], v[72:75], v[216:219]\n\t" \
    "v_mfma_f32_16x16x32_bf16 v[220:223], v[60:63], v[72:75], v[220:223]\n\t" \
    "s_waitcnt lgkmcnt(2)\n\t" \
    "v_mfma_f32_16x16x32_bf16 v[224:227], v[32:35], v[76:79], v[224:227]\n\t" \
    "v_mfma_f32_16x16x32_bf16 v[228:231], v[36:39], v[76:79], v[228:231]\n\t" \
    "ds_read_b64_tr_b16 v[68:69], v87 offset:16384\n\t" \
    "v_mfma_f32_16x16x32_bf16 v[232:235], v[40:43], v[76:79], v[232:235]\n\t" \
    "ds_read_b64_tr_b16 v[70:71], v87 offset:17408\n\t" \
    "v_mfma_f32_16x16x32_bf16 v[236:239], v[44:47], v[76:79], v[236:239]\n\t" \
    "v_mfma_f32_16x16x32_bf16 v[240:243], v[48:51], v[76:79], v[240:243]\n\t" \
    "v_mfma_f32_16x16x32_bf16 v[244:247], v[52:55], v[76:79], v[244:247]\n\t" \
    "v_mfma_f32_16x16x32_bf16 v[248:251], v[56:59], v[76:79], v[248:251]\n\t" \
    "v_mfma_f32_16x16x32_bf16 v[252:255], v[60:63], v[76:79], v[252:255]\n\t" \
    "s_waitcnt lgkmcnt(2)\n\t" \
    "v_mfma_f32_16x16x32_bf16 a[0:3], v[32:35], v[64:67], a[0:3]\n\t" \
    "v_mfma_f32_16x16x32_bf16 a[4:7], v[36:39], v[64:67], a[4:7]\n\t" \
    "ds_read_b64_tr_b16 v[72:73], v88 offset:16384\n\t" \
    "v_mfma_f32_16x16x32_bf16 a[8:11], v[40:43], v[64:67], a[8:11]\n\t" \
    "ds_read_b64_tr_b16 v[74:75], v88 offset:17408\n\t" \
    "s_mov_b32 s28, 1048576\n\t" \
    "s_add_u32 m0, s26, 0\n\t" \
    "v_mfma_f32_16x16x32_bf16 a[12:15], v[44:47], v[64:67], a[12:15]\n\t" \
    "buffer_load_dwordx4 v106, s[12:15], s28 offen offset:0 lds\n\t" \
    "v_mfma_f32_16x16x32_bf16 a[16:19], v[48:51], v[64:67], a[16:19]\n\t" \
    "v_mfma_f32_16x16x32_bf16 a[20:23], v[52:55], v[64:67], a[20:23]\n\t" \
    "buffer_load_dwordx4 v107, s[12:15], s28 offen offset:1024 lds\n\t" \
    "v_mfma_f32_16x16x32_bf16 a[24:27], v[56:59], v[64:67], a[24:27]\n\t" \
    "v_mfma_f32_16x16x32_bf16 a[28:31], v[60:63], v[64:67], a[28:31]\n\t" \
    "s_waitcnt lgkmcnt(2)\n\t" \
    "v_mfma_f32_16x16x32_bf16 a[32:35], v[32:35], v[68:71], a[32:35]\n\t" \
    "v_mfma_f32_16x16x32_bf16 a[36:39], v[36:39], v[68:71], a[36:39]\n\t" \
    "ds_read_b64_tr_b16 v[76:77], v89 offset:16384\n\t" \
    "v_mfma_f32_16x16x32_bf16 a[40:43], v[40:43], v[68:71], a[40:43]\n\t" \
    "ds_read_b64_tr_b16 v[78:79], v89 offset:17408\n\t" \
    "v_mfma_f32_16x16x32_bf16 a[44:47], v[44:47], v[68:71], a[44:47]\n\t" \
    "buffer_load_dwordx4 v108, s[12:15], s28 offen offset:2048 lds\n\t" \
    "v_mfma_f32_16x16x32_bf16 a[48:51], v[48:51], v[68:71], a[48:51]\n\t" \
    "v_mfma_f32_16x16x32_bf16 a[52:55], v[52:55], v[68:71], a[52:55]\n\t" \
    "buffer_load_dwordx4 v109, s[12:15], s28 offen offset:3072 lds\n\t" \
    "v_mfma_f32_16x16x32_bf16 a[56:59], v[56:59], v[68:71], a[56:59]\n\t" \
    "v_mfma_f32_16x16x32_bf16 a[60:63], v[60:63], v[68:71], a[60:63]\n\t" \
    "s_waitcnt vmcnt(13) lgkmcnt(0)\n\t" \
    "s_barrier\n\t" \
    "v_mfma_f32_16x16x32_bf16 a[64:67], v[32:35], v[72:75], a[64:67]\n\t" \
    "v_mfma_f32_16x16x32_bf16 a[68:71], v[36:39], v[72:75], a[68:71]\n\t" \
    "ds_read_b64_tr_b16 v[64:65], v82 offset:32768\n\t" \
    "v_mfma_f32_16x16x32_bf16 a[72:75], v[40:43], v[72:75], a[72:75]\n\t" \
    "ds_read_b64_tr_b16 v[66:67], v82 offset:33792\n\t" \
    "v_mfma_f32_16x16x32_bf16 a[76:79], v[44:47], v[72:75], a[76:79]\n\t" \
    "ds_read_b128 v[0:3], v80 offset:32768\n\t" \
    "v_mfma_f32_16x16x32_bf16 a[80:83], v[48:51], v[72:75], a[80:83]\n\t" \
    "ds_read_b128 v[4:7], v80 offset:33280\n\t" \
    "v_mfma_f32_16x16x32_bf16 a[84:87], v[52:55], v[72:75], a[84:87]\n\t" \
    "ds_read_b128 v[8:11], v80 offset:36864\n\t" \
    "v_mfma_f32_16x16x32_bf16 a[88:91], v[56:59], v[72:75], a[88:91]\n\t" \
    "ds_read_b128 v[12:15], v80 offset:37376\n\t" \
    "v_mfma_f32_16x16x32_bf16 a[92:95], v[60:63], v[72:75], a[92:95]\n\t" \
    "ds_read_b128 v[16:19], v80 offset:40960\n\t" \
    "v_mfma_f32_16x16x32_bf16 a[96:99], v[32:35], v[76:79], a[96:99]\n\t" \
    "ds_read_b128 v[20:23], v80 offset:41472\n\t" \
    "v_mfma_f32_16x16x32_bf16 a[100:103], v[36:39], v[76:79], a[100:103]\n\t" \
    "ds_read_b128 v[24:27], v80 offset:45056\n\t" \
    "v_mfma_f32_16x16x32_bf16 a[104:107], v[40:43], v[76:79], a[104:107]\n\t" \
    "ds_read_b128 v[28:31], v80 offset:45568\n\t" \
    "v_mfma_f32_16x16x32_bf16 a[108:111], v[44:47], v[76:79], a[108:111]\n\t" \
    "ds_read_b64_tr_b16 v[68:69], v83 offset:32768\n\t" \
    "v_mfma_f32_16x16x32_bf16 a[112:115], v[48:51], v[76:79], a[112:115]\n\t" \
    "ds_read_b64_tr_b16 v[70:71], v83 offset:33792\n\t" \
    "v_mfma_f32_16x16x32_bf16 a[116:119], v[52:55], v[76:79], a[116:119]\n\t" \
    "v_mfma_f32_16x16x32_bf16 a[120:123], v[56:59], v[76:79], a[120:123]\n\t" \
    "v_mfma_f32_16x16x32_bf16 a[124:127], v[60:63], v[76:79], a[124:127]\n\t" \
    "s_waitcnt lgkmcnt(9)\n\t" \
    "v_mfma_f32_16x16x32_bf16 v[128:131], v[0:3], v[64:67], v[128:131]\n\t" \
    "s_waitcnt lgkmcnt(8)\n\t" \
    "v_mfma_f32_16x16x32_bf16 v[132:135], v[4:7], v[64:67], v[132:135]\n\t" \
    "ds_read_b64_tr_b16 v[72:73], v84 offset:32768\n\t" \
    "s_waitcnt lgkmcnt(8)\n\t" \
    "v_mfma_f32_16x16x32_bf16 v[136:139], v[8:11], v[64:67], v[136:139]\n\t" \
    "ds_read_b64_tr_b16 v[74:75], v84 offset:33792\n\t" \
    "s_waitcnt lgkmcnt(8)\n\t" \
    "v_mfma_f32_16x16x32_bf16 v[140:143], v[12:15], v[64:67], v[140:143]\n\t" \
    "ds_read_b128 v[32:35], v81 offset:32768\n\t" \
    "s_waitcnt lgkmcnt(8)\n\t" \
    "v_mfma_f32_16x16x32_bf16 v[144:147], v[16:19], v[64:67], v[144:147]\n\t" \
    "ds_read_b128 v[36:39], v81 offset:33280\n\t" \
    "s_waitcnt lgkmcnt(8)\n\t" \
    "v_mfma_f32_16x16x32_bf16 v[148:151], v[20:23], v[64:67], v[148:151]\n\t" \
    "ds_read_b128 v[40:43], v81 offset:36864\n\t" \
    "s_waitcnt lgkmcnt(8)\n\t" \
    "v_mfma_f32_16x16x32_bf16 v[152:155], v[24:27], v[64:67], v[152:155]\n\t" \
    "ds_read_b128 v[44:47], v81 offset:37376\n\t" \
    "s_waitcnt lgkmcnt(8)\n\t" \
    "v_mfma_f32_16x16x32_bf16 v[156:159], v[28:31], v[64:67], v[156:159]\n\t" \
    "s_waitcnt lgkmcnt(6)\n\t" \
    "v_mfma_f32_16x16x32_bf16 v[160:163], v[0:3], v[68:71], v[160:163]\n\t" \
    "v_mfma_f32_16x16x32_bf16 v[164:167], v[4:7], v[68:71], v[164:167]\n\t" \
    "ds_read_b64_tr_b16 v[76:77], v85 offset:32768\n\t" \
    "v_mfma_f32_16x16x32_bf16 v[168:171], v[8:11], v[68:71], v[168:171]\n\t" \
    "ds_read_b64_tr_b16 v[78:79], v85 offset:33792\n\t" \
    "v_mfma_f32_16x16x32_bf16 v[172:175], v[12:15], v[68:71], v[172:175]\n\t" \
    "ds_read_b128 v[48:51], v81 offset:40960\n\t" \
    "v_mfma_f32_16x16x32_bf16 v[176:179], v[16:19], v[68:71], v[176:179]\n\t" \
    "ds_read_b128 v[52:55], v81 offset:41472\n\t" \
    "v_mfma_f32_16x16x32_bf16 v[180:183], v[20:23], v[68:71], v[180:183]\n\t" \
    "ds_read_b128 v[56:59], v81 offset:45056\n\t" \
    "v_mfma_f32_16x16x32_bf16 v[184:187], v[24:27], v[68:71], v[184:187]\n\t" \
    "ds_read_b128 v[60:63], v81 offset:45568\n\t" \
    "v_mfma_f32_16x16x32_bf16 v[188:191], v[28:31], v[68:71], v[188:191]\n\t" \
    "s_waitcnt lgkmcnt(10)\n\t" \
    "v_mfma_f32_16x16x32_bf16 v[192:195], v[0:3], v[72:75], v[192:195]\n\t" \
    "v_mfma_f32_16x16x32_bf16 v[196:199], v[4:7], v[72:75], v[196:199]\n\t" \
    "ds_read_b64_tr_b16 v[64:65], v86 offset:32768\n\t" \
    "v_mfma_f32_16x16x32_bf16 v[200:203], v[8:11], v[72:75], v[200:203]\n\t" \
    "ds_read_b64_tr_b16 v[66:67], v86 offset:33792\n\t" \
    "v_mfma_f32_16x16x32_bf16 v[204:207], v[12:15], v[72:75], v[204:207]\n\t" \
    "s_mov_b32 s28, 1572864\n\t" \
    "s_add_u32 m0, s26, 16384\n\t" \
    "v_mfma_f32_16x16x32_bf16 v[208:211], v[16:19], v[72:75], v[208:211]\n\t" \
    "buffer_load_dwordx4 v106, s[12:15], s28 offen offset:0 lds\n\t" \
    "s_waitcnt vmcnt(9)\n\t" \
    "v_readfirstlane_b32 s23, v111\n\t" \
    "s_nop 0\n\t" \
    "s_cmp_eq_u32 s23, 16\n\t" \
    "s_cbranch_scc0 HSPIN1_%=\n\t" \
    "HOK1_%=:\n\t" \
    "v_mfma_f32_16x16x32_bf16 v[212:215], v[20:23], v[72:75], v[212:215]\n\t" \
    "v_mfma_f32_16x16x32_bf16 v[216:219], v[24:27], v[72:75], v[216:219]\n\t" \
    "v_mfma_f32_16x16x32_bf16 v[220:223], v[28:31], v[72:75], v[220:223]\n\t" \
    "s_waitcnt lgkmcnt(6)\n\t" \
    "v_mfma_f32_16x16x32_bf16 v[224:227], v[0:3], v[76:79], v[224:227]\n\t" \
    "v_mfma_f32_16x16x32_bf16 v[228:231], v[4:7], v[76:79], v[228:231]\n\t" \
    "ds_read_b64_tr_b16 v[68:69], v87 offset:32768\n\t" \
    "v_mfma_f32_16x16x32_bf16 v[232:235], v[8:11], v[76:79], v[232:235]\n\t" \
    "ds_read_b64_tr_b16 v[70:71], v87 offset:33792\n\t" \
    "v_mfma_f32_16x16x32_bf16 v[236:239], v[12:15], v[76:79], v[236:239]\n\t" \
    "v_mfma_f32_16x16x32_bf16 v[240:243], v[16:19], v[76:79], v[240:243]\n\t" \
    "buffer_load_dwordx4 v107, s[12:15], s28 offen offset:1024 lds\n\t" \
    "v_mfma_f32_16x16x32_bf16 v[244:247], v[20:23], v[76:79], v[244:247]\n\t" \
    "v_mfma_f32_16x16x32_bf16 v[248:251], v[24:27], v[76:79], v[248:251]\n\t" \
    "v_mfma_f32_16x16x32_bf16 v[252:255], v[28:31], v[76:79], v[252:255]\n\t" \
    "s_waitcnt lgkmcnt(2)\n\t" \
    "v_mfma_f32_16x16x32_bf16 a[0:3], v[0:3], v[64:67], a[0:3]\n\t" \
    "v_mfma_f32_16x16x32_bf16 a[4:7], v[4:7], v[64:67], a[4:7]\n\t" \
    "ds_read_b64_tr_b16 v[72:73], v88 offset:32768\n\t" \
    "v_mfma_f32_16x16x32_bf16 a[8:11], v[8:11], v[64:67], a[8:11]\n\t" \
    "ds_read_b64_tr_b16 v[74:75], v88 offset:33792\n\t" \
    "v_mfma_f32_16x16x32_bf16 a[12:15], v[12:15], v[64:67], a[12:15]\n\t" \
    "v_mfma_f32_16x16x32_bf16 a[16:19], v[16:19], v[64:67], a[16:19]\n\t" \
    "buffer_load_dwordx4 v108, s[12:15], s28 offen offset:2048 lds\n\t" \
    "v_mfma_f32_16x16x32_bf16 a[20:23], v[20:23], v[64:67], a[20:23]\n\t" \
    "v_mfma_f32_16x16x32_bf16 a[24:27], v[24:27], v[64:67], a[24:27]\n\t" \
    "v_mfma_f32_16x16x32_bf16 a[28:31], v[28:31], v[64:67], a[28:31]\n\t" \
    "s_waitcnt lgkmcnt(2)\n\t" \
    "v_mfma_f32_16x16x32_bf16 a[32:35], v[0:3], v[68:71], a[32:35]\n\t" \
    "v_mfma_f32_16x16x32_bf16 a[36:39], v[4:7], v[68:71], a[36:39]\n\t" \
    "ds_read_b64_tr_b16 v[76:77], v89 offset:32768\n\t" \
    "v_mfma_f32_16x16x32_bf16 a[40:43], v[8:11], v[68:71], a[40:43]\n\t" \
    "ds_read_b64_tr_b16 v[78:79], v89 offset:33792\n\t" \
    "v_mfma_f32_16x16x32_bf16 a[44:47], v[12:15], v[68:71], a[44:47]\n\t" \
    "v_mfma_f32_16x16x32_bf16 a[48:51], v[16:19], v[68:71], a[48:51]\n\t" \
    "buffer_load_dwordx4 v109, s[12:15], s28 offen offset:3072 lds\n\t" \
    "v_mfma_f32_16x16x32_bf16 a[52:55], v[20:23], v[68:71], a[52:55]\n\t" \
    "v_mfma_f32_16x16x32_bf16 a[56:59], v[24:27], v[68:71], a[56:59]\n\t" \
    "v_mfma_f32_16x16x32_bf16 a[60:63], v[28:31], v[68:71], a[60:63]\n\t" \
    "s_waitcnt vmcnt(25) lgkmcnt(0)\n\t" \
    "s_barrier\n\t" \
    "v_mfma_f32_16x16x32_bf16 a[64:67], v[0:3], v[72:75], a[64:67]\n\t" \
    "v_mfma_f32_16x16x32_bf16 a[68:71], v[4:7], v[72:75], a[68:71]\n\t" \
    "ds_read_b64_tr_b16 v[64:65], v82 offset:49152\n\t" \
    "v_mfma_f32_16x16x32_bf16 a[72:75], v[8:11], v[72:75], a[72:75]\n\t" \
    "ds_read_b64_tr_b16 v[66:67], v82 offset:50176\n\t" \
    "v_mfma_f32_16x16x32_bf16 a[76:79], v[12:15], v[72:75], a[76:79]\n\t" \
    "v_mfma_f32_16x16x32_bf16 a[80:83], v[16:19], v[72:75], a[80:83]\n\t" \
    "v_mfma_f32_16x16x32_bf16 a[84:87], v[20:23], v[72:75], a[84:87]\n\t" \
    "v_mfma_f32_16x16x32_bf16 a[88:91], v[24:27], v[72:75], a[88:91]\n\t" \
    "v_mfma_f32_16x16x32_bf16 a[92:95], v[28:31], v[72:75], a[92:95]\n\t" \
    "v_mfma_f32_16x16x32_bf16 a[96:99], v[0:3], v[76:79], a[96:99]\n\t" \
    "v_mfma_f32_16x16x32_bf16 a[100:103], v[4:7], v[76:79], a[100:103]\n\t" \
    "ds_read_b64_tr_b16 v[68:69], v83 offset:49152\n\t" \
    "v_mfma_f32_16x16x32_bf16 a[104:107], v[8:11], v[76:79], a[104:107]\n\t" \
    "ds_read_b64_tr_b16 v[70:71], v83 offset:50176\n\t" \
    "v_mfma_f32_16x16x32_bf16 a[108:111], v[12:15], v[76:79], a[108:111]\n\t" \
    "v_mfma_f32_16x16x32_bf16 a[112:115], v[16:19], v[76:79], a[112:115]\n\t" \
    "v_mfma_f32_16x16x32_bf16 a[116:119], v[20:23], v[76:79], a[116:119]\n\t" \
    "v_mfma_f32_16x16x32_bf16 a[120:123], v[24:27], v[76:79], a[120:123]\n\t" \
    "v_mfma_f32_16x16x32_bf16 a[124:127], v[28:31], v[76:79], a[124:127]\n\t" \
    "s_waitcnt lgkmcnt(2)\n\t" \
    "v_mfma_f32_16x16x32_bf16 v[128:131], v[32:35], v[64:67], v[128:131]\n\t" \
    "v_mfma_f32_16x16x32_bf16 v[132:135], v[36:39], v[64:67], v[132:135]\n\t" \
    "ds_read_b64_tr_b16 v[72:73], v84 offset:49152\n\t" \
    "v_mfma_f32_16x16x32_bf16 v[136:139], v[40:43], v[64:67], v[136:139]\n\t" \
    "ds_read_b64_tr_b16 v[74:75], v84 offset:50176\n\t" \
    "v_lshlrev_b32 v80, 4, v115\n\t" \
    "v_mfma_f32_16x16x32_bf16 v[140:143], v[44:47], v[64:67], v[140:143]\n\t" \
    "v_mfma_f32_16x16x32_bf16 v[144:147], v[48:51], v[64:67], v[144:147]\n\t" \
    "v_mfma_f32_16x16x32_bf16 v[148:151], v[52:55], v[64:67], v[148:151]\n\t" \
    "v_mfma_f32_16x16x32_bf16 v[152:155], v[56:59], v[64:67], v[152:155]\n\t" \
    "buffer_load_dwordx4 v[0:3], v80, s[16:19], s32 offen offset:0 sc1\n\t" \
    "v_mfma_f32_16x16x32_bf16 v[156:159], v[60:63], v[64:67], v[156:159]\n\t" \
    "s_waitcnt lgkmcnt(2)\n\t" \
    "v_mfma_f32_16x16x32_bf16 v[160:163], v[32:35], v[68:71], v[160:163]\n\t" \
    "v_mfma_f32_16x16x32_bf16 v[164:167], v[36:39], v[68:71], v[164:167]\n\t" \
    "ds_read_b64_tr_b16 v[76:77], v85 offset:49152\n\t" \
    "v_mfma_f32_16x16x32_bf16 v[168:171], v[40:43], v[68:71], v[168:171]\n\t" \
    "ds_read_b64_tr_b16 v[78:79], v85 offset:50176\n\t" \
    "buffer_load_dwordx4 v[4:7], v80, s[16:19], s33 offen offset:0 sc1\n\t" \
    "v_mfma_f32_16x16x32_bf16 v[172:175], v[44:47], v[68:71], v[172:175]\n\t" \
    "v_mfma_f32_16x16x32_bf16 v[176:179], v[48:51], v[68:71], v[176:179]\n\t" \
    "v_mfma_f32_16x16x32_bf16 v[180:183], v[52:55], v[68:71], v[180:183]\n\t" \
    "v_mfma_f32_16x16x32_bf16 v[184:187], v[56:59], v[68:71], v[184:187]\n\t" \
    "buffer_load_dwordx4 v[8:11], v80, s[16:19], s34 offen offset:0 sc1\n\t" \
    "v_mfma_f32_16x16x32_bf16 v[188:191], v[60:63], v[68:71], v[188:191]\n\t" \
    "s_waitcnt lgkmcnt(2)\n\t" \
    "v_mfma_f32_16x16x32_bf16 v[192:195], v[32:35], v[72:75], v[192:195]\n\t" \
    "v_mfma_f32_16x16x32_bf16 v[196:199], v[36:39], v[72:75], v[196:199]\n\t" \
    "ds_read_b64_tr_b16 v[64:65], v86 offset:49152\n\t" \
    "v_mfma_f32_16x16x32_bf16 v[200:203], v[40:43], v[72:75], v[200:203]\n\t" \
    "ds_read_b64_tr_b16 v[66:67], v86 offset:50176\n\t" \
    "buffer_load_dwordx4 v[12:15], v80, s[16:19], s35 offen offset:0 sc1\n\t" \
    "v_lshrrev_b32 v111, 5, v115\n\t" \
    "v_mfma_f32_16x16x32_bf16 v[204:207], v[44:47], v[72:75], v[204:207]\n\t" \
    "v_lshlrev_b32 v111, 12, v111\n\t" \
    "v_mfma_f32_16x16x32_bf16 v[208:211], v[48:51], v[72:75], v[208:211]\n\t" \
    "v_bfe_u32 v112, v115, 4, 1\n\t" \
    "v_mfma_f32_16x16x32_bf16 v[212:215], v[52:55], v[72:75], v[212:215]\n\t" \
    "v_lshlrev_b32 v112, 9, v112\n\t" \
    "v_mfma_f32_16x16x32_bf16 v[216:219], v[56:59], v[72:75], v[216:219]\n\t" \
    "buffer_load_dwordx4 v[16:19], v80, s[16:19], s32 offen offset:1024 sc1\n\t" \
    "v_add_u32 v111, v111, v112\n\t" \
    "v_mfma_f32_16x16x32_bf16 v[220:223], v[60:63], v[72:75], v[220:223]\n\t" \
    "v_bfe_u32 v112, v115, 2, 1\n\t" \
    "s_waitcnt lgkmcnt(2)\n\t" \
    "v_mfma_f32_16x16x32_bf16 v[224:227], v[32:35], v[76:79], v[224:227]\n\t" \
    "v_lshlrev_b32 v112, 7, v112\n\t" \
    "v_mfma_f32_16x16x32_bf16 v[228:231], v[36:39], v[76:79], v[228:231]\n\t" \
    "ds_read_b64_tr_b16 v[68:69], v87 offset:49152\n\t" \
    "v_add_u32 v111, v111, v112\n\t" \
    "v_mfma_f32_16x16x32_bf16 v[232:235], v[40:43], v[76:79], v[232:235]\n\t" \
    "ds_read_b64_tr_b16 v[70:71], v87 offset:50176\n\t" \
    "buffer_load_dwordx4 v[20:23], v80, s[16:19], s33 offen offset:1024 sc1\n\t" \
    "v_bfe_u32 v112, v115, 3, 1\n\t" \
    "v_mfma_f32_16x16x32_bf16 v[236:239], v[44:47], v[76:79], v[236:239]\n\t" \
    "v_lshlrev_b32 v112, 6, v112\n\t" \
    "v_mfma_f32_16x16x32_bf16 v[240:243], v[48:51], v[76:79], v[240:243]\n\t" \
    "v_add_u32 v111, v111, v112\n\t" \
    "v_mfma_f32_16x16x32_bf16 v[244:247], v[52:55], v[76:79], v[244:247]\n\t" \
    "v_and_b32 v112, 3, v115\n\t" \
    "v_mfma_f32_16x16x32_bf16 v[248:251], v[56:59], v[76:79], v[248:251]\n\t" \
    "buffer_load_dwordx4 v[24:27], v80, s[16:19], s34 offen offset:1024 sc1\n\t" \
    "v_lshlrev_b32 v112, 4, v112\n\t" \
    "v_mfma_f32_16x16x32_bf16 v[252:255], v[60:63], v[76:79], v[252:255]\n\t" \
    "v_add_u32 v110, v111, v112\n\t" \
    "s_waitcnt lgkmcnt(2)\n\t" \
    "v_mfma_f32_16x16x32_bf16 a[0:3], v[32:35], v[64:67], a[0:3]\n\t" \
    "s_lshl_b32 s23, s21, 7\n\t" \
    "v_mfma_f32_16x16x32_bf16 a[4:7], v[36:39], v[64:67], a[4:7]\n\t" \
    "ds_read_b64_tr_b16 v[72:73], v88 offset:49152\n\t" \
    "s_lshl_b32 s24, s22, 4\n\t" \
    "v_mfma_f32_16x16x32_bf16 a[8:11], v[40:43], v[64:67], a[8:11]\n\t" \
    "ds_read_b64_tr_b16 v[74:75], v88 offset:50176\n\t" \
    "buffer_load_dwordx4 v[28:31], v80, s[16:19], s35 offen offset:1024 sc1\n\t" \
    "s_add_u32 s23, s23, s24\n\t" \
    "s_mov_b32 s28, 2097152\n\t" \
    "s_add_u32 m0, s26, 32768\n\t" \
    "v_mfma_f32_16x16x32_bf16 a[12:15], v[44:47], v[64:67], a[12:15]\n\t" \
    "buffer_load_dwordx4 v106, s[12:15], s28 offen offset:0 lds\n\t" \
    "s_and_b32 s24, s20, 1\n\t" \
    "v_mfma_f32_16x16x32_bf16 a[16:19], v[48:51], v[64:67], a[16:19]\n\t" \
    "s_lshl_b32 s24, s24, 3\n\t" \
    "v_mfma_f32_16x16x32_bf16 a[20:23], v[52:55], v[64:67], a[20:23]\n\t" \
    "buffer_load_dwordx4 v107, s[12:15], s28 offen offset:1024 lds\n\t" \
    "s_add_u32 s23, s23, s24\n\t" \
    "v_mfma_f32_16x16x32_bf16 a[24:27], v[56:59], v[64:67], a[24:27]\n\t" \
    "s_lshl_b32 s23, s23, 12\n\t" \
    "v_mfma_f32_16x16x32_bf16 a[28:31], v[60:63], v[64:67], a[28:31]\n\t" \
    "s_add_u32 s23, s23, 0x1400000\n\t" \
    "s_waitcnt lgkmcnt(2)\n\t" \
    "v_mfma_f32_16x16x32_bf16 a[32:35], v[32:35], v[68:71], a[32:35]\n\t" \
    "v_mfma_f32_16x16x32_bf16 a[36:39], v[36:39], v[68:71], a[36:39]\n\t" \
    "ds_read_b64_tr_b16 v[76:77], v89 offset:49152\n\t" \
    "v_mfma_f32_16x16x32_bf16 a[40:43], v[40:43], v[68:71], a[40:43]\n\t" \
    "ds_read_b64_tr_b16 v[78:79], v89 offset:50176\n\t" \
    "v_mfma_f32_16x16x32_bf16 a[44:47], v[44:47], v[68:71], a[44:47]\n\t" \
    "buffer_load_dwordx4 v108, s[12:15], s28 offen offset:2048 lds\n\t" \
    "v_subrev_u32 v82, s30, v82\n\t" \
    "v_subrev_u32 v83, s30, v83\n\t" \
    "v_subrev_u32 v84, s30, v84\n\t" \
    "v_subrev_u32 v85, s30, v85\n\t" \
    "v_mfma_f32_16x16x32_bf16 a[48:51], v[48:51], v[68:71], a[48:51]\n\t" \
    "v_subrev_u32 v86, s30, v86\n\t" \
    "v_subrev_u32 v87, s30, v87\n\t" \
    "v_subrev_u32 v88, s30, v88\n\t" \
    "v_subrev_u32 v89, s30, v89\n\t" \
    "v_mfma_f32_16x16x32_bf16 a[52:55], v[52:55], v[68:71], a[52:55]\n\t" \
    "buffer_load_dwordx4 v109, s[12:15], s28 offen offset:3072 lds\n\t" \
    "v_subrev_u32 v90, s30, v90\n\t" \
    "v_subrev_u32 v91, s30, v91\n\t" \
    "v_subrev_u32 v92, s30, v92\n\t" \
    "v_subrev_u32 v93, s30, v93\n\t" \
    "v_mfma_f32_16x16x32_bf16 a[56:59], v[56:59], v[68:71], a[56:59]\n\t" \
    "v_subrev_u32 v94, s30, v94\n\t" \
    "v_subrev_u32 v95, s30, v95\n\t" \
    "v_subrev_u32 v96, s30, v96\n\t" \
    "v_subrev_u32 v97, s30, v97\n\t" \
    "v_mfma_f32_16x16x32_bf16 a[60:63], v[60:63], v[68:71], a[60:63]\n\t" \
    "s_waitcnt vmcnt(25) lgkmcnt(0)\n\t" \
    "s_barrier\n\t" \
    "v_mfma_f32_16x16x32_bf16 a[64:67], v[32:35], v[72:75], a[64:67]\n\t" \
    "v_mfma_f32_16x16x32_bf16 a[68:71], v[36:39], v[72:75], a[68:71]\n\t" \
    "ds_read_b64_tr_b16 v[64:65], v90 offset:0\n\t" \
    "v_mfma_f32_16x16x32_bf16 a[72:75], v[40:43], v[72:75], a[72:75]\n\t" \
    "ds_read_b64_tr_b16 v[66:67], v90 offset:1024\n\t" \
    "v_mfma_f32_16x16x32_bf16 a[76:79], v[44:47], v[72:75], a[76:79]\n\t" \
    "ds_read_b64_tr_b16 v[68:69], v91 offset:0\n\t" \
    "v_mfma_f32_16x16x32_bf16 a[80:83], v[48:51], v[72:75], a[80:83]\n\t" \
    "ds_read_b64_tr_b16 v[70:71], v91 offset:1024\n\t" \
    "v_mfma_f32_16x16x32_bf16 a[84:87], v[52:55], v[72:75], a[84:87]\n\t" \
    "v_mfma_f32_16x16x32_bf16 a[88:91], v[56:59], v[72:75], a[88:91]\n\t" \
    "v_mfma_f32_16x16x32_bf16 a[92:95], v[60:63], v[72:75], a[92:95]\n\t" \
    "v_mfma_f32_16x16x32_bf16 a[96:99], v[32:35], v[76:79], a[96:99]\n\t" \
    "v_mfma_f32_16x16x32_bf16 a[100:103], v[36:39], v[76:79], a[100:103]\n\t" \
    "ds_read_b64_tr_b16 v[72:73], v92 offset:0\n\t" \
    "v_mfma_f32_16x16x32_bf16 a[104:107], v[40:43], v[76:79], a[104:107]\n\t" \
    "ds_read_b64_tr_b16 v[74:75], v92 offset:1024\n\t" \
    "v_mfma_f32_16x16x32_bf16 a[108:111], v[44:47], v[76:79], a[108:111]\n\t" \
    "v_mfma_f32_16x16x32_bf16 a[112:115], v[48:51], v[76:79], a[112:115]\n\t" \
    "v_mfma_f32_16x16x32_bf16 a[116:119], v[52:55], v[76:79], a[116:119]\n\t" \
    "v_mfma_f32_16x16x32_bf16 a[120:123], v[56:59], v[76:79], a[120:123]\n\t" \
    "v_mfma_f32_16x16x32_bf16 a[124:127], v[60:63], v[76:79], a[124:127]\n\t" \
    "s_waitcnt vmcnt(11) lgkmcnt(4)\n\t" \
    "v_mfma_f32_16x16x32_bf16 a[128:131], v[64:67], v[0:3], 0\n\t" \
    "ds_read_b64_tr_b16 v[76:77], v93 offset:0\n\t" \
    "v_cvt_pk_bf16_f32 v128, v128, v129\n\t" \
    "s_waitcnt vmcnt(10)\n\t" \
    "v_mfma_f32_16x16x32_bf16 a[132:135], v[64:67], v[4:7], 0\n\t" \
    "ds_read_b64_tr_b16 v[78:79], v93 offset:1024\n\t" \
    "v_cvt_pk_bf16_f32 v129, v130, v131\n\t" \
    "s_waitcnt vmcnt(9)\n\t" \
    "v_mfma_f32_16x16x32_bf16 a[136:139], v[64:67], v[8:11], 0\n\t" \
    "buffer_load_dwordx4 v[32:35], v80, s[16:19], s32 offen offset:2048 sc1\n\t" \
    "v_cvt_pk_bf16_f32 v130, v132, v133\n\t" \
    "s_waitcnt vmcnt(9)\n\t" \
    "v_mfma_f32_16x16x32_bf16 a[140:143], v[64:67], v[12:15], 0\n\t" \
    "v_cvt_pk_bf16_f32 v131, v134, v135\n\t" \
    "s_add_u32 s36, s23, 0\n\t" \
    "s_waitcnt lgkmcnt(4)\n\t" \
    "v_mfma_f32_16x16x32_bf16 a[144:147], v[68:71], v[0:3], 0\n\t" \
    "ds_read_b64_tr_b16 v[64:65], v94 offset:0\n\t" \
    "buffer_store_dwordx4 v[128:131], v110, s[16:19], s36 offen offset:0 sc1\n\t" \
    "v_mfma_f32_16x16x32_bf16 a[148:151], v[68:71], v[4:7], 0\n\t" \
    "ds_read_b64_tr_b16 v[66:67], v94 offset:1024\n\t" \
    "v_mfma_f32_16x16x32_bf16 a[152:155], v[68:71], v[8:11], 0\n\t" \
    "buffer_load_dwordx4 v[36:39], v80, s[16:19], s33 offen offset:2048 sc1\n\t" \
    "v_mfma_f32_16x16x32_bf16 a[156:159], v[68:71], v[12:15], 0\n\t" \
    "s_waitcnt lgkmcnt(4)\n\t" \
    "v_mfma_f32_16x16x32_bf16 a[160:163], v[72:75], v[0:3], 0\n\t" \
    "ds_read_b64_tr_b16 v[68:69], v95 offset:0\n\t" \
    "v_mfma_f32_16x16x32_bf16 a[164:167], v[72:75], v[4:7], 0\n\t" \
    "ds_read_b64_tr_b16 v[70:71], v95 offset:1024\n\t" \
    "v_mfma_f32_16x16x32_bf16 a[168:171], v[72:75], v[8:11], 0\n\t" \
    "buffer_load_dwordx4 v[40:43], v80, s[16:19], s34 offen offset:2048 sc1\n\t" \
    "v_mfma_f32_16x16x32_bf16 a[172:175], v[72:75], v[12:15], 0\n\t" \
    "s_waitcnt lgkmcnt(4)\n\t" \
    "v_mfma_f32_16x16x32_bf16 a[176:179], v[76:79], v[0:3], 0\n\t" \
    "ds_read_b64_tr_b16 v[72:73], v96 offset:0\n\t" \
    "v_mfma_f32_16x16x32_bf16 a[180:183], v[76:79], v[4:7], 0\n\t" \
    "ds_read_b64_tr_b16 v[74:75], v96 offset:1024\n\t" \
    "v_mfma_f32_16x16x32_bf16 a[184:187], v[76:79], v[8:11], 0\n\t" \
    "buffer_load_dwordx4 v[44:47], v80, s[16:19], s35 offen offset:2048 sc1\n\t" \
    "v_mfma_f32_16x16x32_bf16 a[188:191], v[76:79], v[12:15], 0\n\t" \
    "s_waitcnt lgkmcnt(4)\n\t" \
    "v_mfma_f32_16x16x32_bf16 a[192:195], v[64:67], v[0:3], 0\n\t" \
    "ds_read_b64_tr_b16 v[76:77], v97 offset:0\n\t" \
    "v_cvt_pk_bf16_f32 v136, v136, v137\n\t" \
    "v_mfma_f32_16x16x32_bf16 a[196:199], v[64:67], v[4:7], 0\n\t" \
    "ds_read_b64_tr_b16 v[78:79], v97 offset:1024\n\t" \
    "v_cvt_pk_bf16_f32 v137, v138, v139\n\t" \
    "v_mfma_f32_16x16x32_bf16 a[200:203], v[64:67], v[8:11], 0\n\t" \
    "buffer_load_dwordx4 v[48:51], v80, s[16:19], s32 offen offset:3072 sc1\n\t" \
    "v_cvt_pk_bf16_f32 v138, v140, v141\n\t" \
    "v_mfma_f32_16x16x32_bf16 a[204:207], v[64:67], v[12:15], 0\n\t" \
    "v_cvt_pk_bf16_f32 v139, v142, v143\n\t" \
    "s_add_u32 s36, s23, 8192\n\t" \
    "s_waitcnt lgkmcnt(4)\n\t" \
    "v_mfma_f32_16x16x32_bf16 a[208:211], v[68:71], v[0:3], 0\n\t" \
    "ds_read_b64_tr_b16 v[64:65], v90 offset:8192\n\t" \
    "buffer_store_dwordx4 v[136:139], v110, s[16:19], s36 offen offset:0 sc1\n\t" \
    "v_mfma_f32_16x16x32_bf16 a[212:215], v[68:71], v[4:7], 0\n\t" \
    "ds_read_b64_tr_b16 v[66:67], v90 offset:9216\n\t" \
    "v_mfma_f32_16x16x32_bf16 a[216:219], v[68:71], v[8:11], 0\n\t" \
    "buffer_load_dwordx4 v[52:55], v80, s[16:19], s33 offen offset:3072 sc1\n\t" \
    "v_mfma_f32_16x16x32_bf16 a[220:223], v[68:71], v[12:15], 0\n\t" \
    "s_waitcnt lgkmcnt(4)\n\t" \
    "v_mfma_f32_16x16x32_bf16 a[224:227], v[72:75], v[0:3], 0\n\t" \
    "ds_read_b64_tr_b16 v[68:69], v91 offset:8192\n\t" \
    "v_mfma_f32_16x16x32_bf16 a[228:231], v[72:75], v[4:7], 0\n\t" \
    "ds_read_b64_tr_b16 v[70:71], v91 offset:9216\n\t" \
    "v_mfma_f32_16x16x32_bf16 a[232:235], v[72:75], v[8:11], 0\n\t" \
    "buffer_load_dwordx4 v[56:59], v80, s[16:19], s34 offen offset:3072 sc1\n\t" \
    "v_mfma_f32_16x16x32_bf16 a[236:239], v[72:75], v[12:15], 0\n\t" \
    "s_waitcnt lgkmcnt(4)\n\t" \
    "v_mfma_f32_16x16x32_bf16 a[240:243], v[76:79], v[0:3], 0\n\t" \
    "ds_read_b64_tr_b16 v[72:73], v92 offset:8192\n\t" \
    "v_mfma_f32_16x16x32_bf16 a[244:247], v[76:79], v[4:7], 0\n\t" \
    "ds_read_b64_tr_b16 v[74:75], v92 offset:9216\n\t" \
    "v_mfma_f32_16x16x32_bf16 a[248:251], v[76:79], v[8:11], 0\n\t" \
    "buffer_load_dwordx4 v[60:63], v80, s[16:19], s35 offen offset:3072 sc1\n\t" \
    "v_mfma_f32_16x16x32_bf16 a[252:255], v[76:79], v[12:15], 0\n\t" \
    "s_waitcnt vmcnt(17) lgkmcnt(4)\n\t" \
    "v_mfma_f32_16x16x32_bf16 a[128:131], v[64:67], v[16:19], a[128:131]\n\t" \
    "ds_read_b64_tr_b16 v[76:77], v93 offset:8192\n\t" \
    "v_cvt_pk_bf16_f32 v144, v144, v145\n\t" \
    "s_waitcnt vmcnt(16)\n\t" \
    "v_mfma_f32_16x16x32_bf16 a[132:135], v[64:67], v[20:23], a[132:135]\n\t" \
    "ds_read_b64_tr_b16 v[78:79], v93 offset:9216\n\t" \
    "v_cvt_pk_bf16_f32 v145, v146, v147\n\t" \
    "s_add_u32 s32, s32, 4096\n\t" \
    "s_add_u32 s33, s33, 4096\n\t" \
    "s_add_u32 s34, s34, 4096\n\t" \
    "s_add_u32 s35, s35, 4096\n\t" \
    "s_waitcnt vmcnt(15)\n\t" \
    "v_mfma_f32_16x16x32_bf16 a[136:139], v[64:67], v[24:27], a[136:139]\n\t" \
    "buffer_load_dwordx4 v[0:3], v80, s[16:19], s32 offen offset:0 sc1\n\t" \
    "s_waitcnt vmcnt(15)\n\t" \
    "v_mfma_f32_16x16x32_bf16 a[140:143], v[64:67], v[28:31], a[140:143]\n\t" \
    "v_cvt_pk_bf16_f32 v146, v148, v149\n\t" \
    "v_cvt_pk_bf16_f32 v147, v150, v151\n\t" \
    "s_waitcnt lgkmcnt(4)\n\t" \
    "v_mfma_f32_16x16x32_bf16 a[144:147], v[68:71], v[16:19], a[144:147]\n\t" \
    "ds_read_b64_tr_b16 v[64:65], v94 offset:8192\n\t" \
    "s_add_u32 s36, s23, 16384\n\t" \
    "v_mfma_f32_16x16x32_bf16 a[148:151], v[68:71], v[20:23], a[148:151]\n\t" \
    "ds_read_b64_tr_b16 v[66:67], v94 offset:9216\n\t" \
    "buffer_store_dwordx4 v[144:147], v110, s[16:19], s36 offen offset:0 sc1\n\t" \
    "v_mfma_f32_16x16x32_bf16 a[152:155], v[68:71], v[24:27], a[152:155]\n\t" \
    "v_mfma_f32_16x16x32_bf16 a[156:159], v[68:71], v[28:31], a[156:159]\n\t" \
    "s_waitcnt lgkmcnt(4)\n\t" \
    "v_mfma_f32_16x16x32_bf16 a[160:163], v[72:75], v[16:19], a[160:163]\n\t" \
    "ds_read_b64_tr_b16 v[68:69], v95 offset:8192\n\t" \
    "v_mfma_f32_16x16x32_bf16 a[164:167], v[72:75], v[20:23], a[164:167]\n\t" \
    "ds_read_b64_tr_b16 v[70:71], v95 offset:9216\n\t" \
    "v_mfma_f32_16x16x32_bf16 a[168:171], v[72:75], v[24:27], a[168:171]\n\t" \
    "buffer_load_dwordx4 v[4:7], v80, s[16:19], s33 offen offset:0 sc1\n\t" \
    "v_mfma_f32_16x16x32_bf16 a[172:175], v[72:75], v[28:31], a[172:175]\n\t" \
    "s_waitcnt lgkmcnt(4)\n\t" \
    "v_mfma_f32_16x16x32_bf16 a[176:179], v[76:79], v[16:19], a[176:179]\n\t" \
    "ds_read_b64_tr_b16 v[72:73], v96 offset:8192\n\t" \
    "v_mfma_f32_16x16x32_bf16 a[180:183], v[76:79], v[20:23], a[180:183]\n\t" \
    "ds_read_b64_tr_b16 v[74:75], v96 offset:9216\n\t" \
    "v_mfma_f32_16x16x32_bf16 a[184:187], v[76:79], v[24:27], a[184:187]\n\t" \
    "v_mfma_f32_16x16x32_bf16 a[188:191], v[76:79], v[28:31], a[188:191]\n\t" \
    "s_waitcnt lgkmcnt(4)\n\t" \
    "v_mfma_f32_16x16x32_bf16 a[192:195], v[64:67], v[16:19], a[192:195]\n\t" \
    "ds_read_b64_tr_b16 v[76:77], v97 offset:8192\n\t" \
    "v_cvt_pk_bf16_f32 v152, v152, v153\n\t" \
    "v_mfma_f32_16x16x32_bf16 a[196:199], v[64:67], v[20:23], a[196:199]\n\t" \
    "ds_read_b64_tr_b16 v[78:79], v97 offset:9216\n\t" \
    "v_cvt_pk_bf16_f32 v153, v154, v155\n\t" \
    "v_mfma_f32_16x16x32_bf16 a[200:203], v[64:67], v[24:27], a[200:203]\n\t" \
    "buffer_load_dwordx4 v[8:11], v80, s[16:19], s34 offen offset:0 sc1\n\t" \
    "v_cvt_pk_bf16_f32 v154, v156, v157\n\t" \
    "v_mfma_f32_16x16x32_bf16 a[204:207], v[64:67], v[28:31], a[204:207]\n\t" \
    "v_cvt_pk_bf16_f32 v155, v158, v159\n\t" \
    "s_add_u32 s36, s23, 24576\n\t" \
    "s_waitcnt vmcnt(34)\n\t" \
    "s_barrier\n\t" \
    "s_waitcnt lgkmcnt(4)\n\t" \
    "v_mfma_f32_16x16x32_bf16 a[208:211], v[68:71], v[16:19], a[208:211]\n\t" \
    "ds_read_b64_tr_b16 v[64:65], v90 offset:16384\n\t" \
    "v_mfma_f32_16x16x32_bf16 a[212:215], v[68:71], v[20:23], a[212:215]\n\t" \
    "ds_read_b64_tr_b16 v[66:67], v90 offset:17408\n\t" \
    "buffer_store_dwordx4 v[152:155], v110, s[16:19], s36 offen offset:0 sc1\n\t" \
    "v_mfma_f32_16x16x32_bf16 a[216:219], v[68:71], v[24:27], a[216:219]\n\t" \
    "v_mfma_f32_16x16x32_bf16 a[220:223], v[68:71], v[28:31], a[220:223]\n\t" \
    "s_waitcnt lgkmcnt(4)\n\t" \
    "v_mfma_f32_16x16x32_bf16 a[224:227], v[72:75], v[16:19], a[224:227]\n\t" \
    "ds_read_b64_tr_b16 v[68:69], v91 offset:16384\n\t" \
    "v_mfma_f32_16x16x32_bf16 a[228:231], v[72:75], v[20:23], a[228:231]\n\t" \
    "ds_read_b64_tr_b16 v[70:71], v91 offset:17408\n\t" \
    "v_mfma_f32_16x16x32_bf16 a[232:235], v[72:75], v[24:27], a[232:235]\n\t" \
    "buffer_load_dwordx4 v[12:15], v80, s[16:19], s35 offen offset:0 sc1\n\t" \
    "v_mfma_f32_16x16x32_bf16 a[236:239], v[72:75], v[28:31], a[236:239]\n\t" \
    "s_waitcnt lgkmcnt(4)\n\t" \
    "v_mfma_f32_16x16x32_bf16 a[240:243], v[76:79], v[16:19], a[240:243]\n\t" \
    "ds_read_b64_tr_b16 v[72:73], v92 offset:16384\n\t" \
    "v_mfma_f32_16x16x32_bf16 a[244:247], v[76:79], v[20:23], a[244:247]\n\t" \
    "ds_read_b64_tr_b16 v[74:75], v92 offset:17408\n\t" \
    "v_mfma_f32_16x16x32_bf16 a[248:251], v[76:79], v[24:27], a[248:251]\n\t" \
    "v_mfma_f32_16x16x32_bf16 a[252:255], v[76:79], v[28:31], a[252:255]\n\t" \
    "s_waitcnt vmcnt(15) lgkmcnt(4)\n\t" \
    "v_mfma_f32_16x16x32_bf16 a[128:131], v[64:67], v[32:35], a[128:131]\n\t" \
    "ds_read_b64_tr_b16 v[76:77], v93 offset:16384\n\t" \
    "v_cvt_pk_bf16_f32 v160, v160, v161\n\t" \
    "s_waitcnt vmcnt(13)\n\t" \
    "v_mfma_f32_16x16x32_bf16 a[132:135], v[64:67], v[36:39], a[132:135]\n\t" \
    "ds_read_b64_tr_b16 v[78:79], v93 offset:17408\n\t" \
    "v_cvt_pk_bf16_f32 v161, v162, v163\n\t" \
    "s_waitcnt vmcnt(12)\n\t" \
    "v_mfma_f32_16x16x32_bf16 a[136:139], v[64:67], v[40:43], a[136:139]\n\t" \
    "buffer_load_dwordx4 v[16:19], v80, s[16:19], s32 offen offset:1024 sc1\n\t" \
    "v_cvt_pk_bf16_f32 v162, v164, v165\n\t" \
    "s_waitcnt vmcnt(12)\n\t" \
    "v_mfma_f32_16x16x32_bf16 a[140:143], v[64:67], v[44:47], a[140:143]\n\t" \
    "v_cvt_pk_bf16_f32 v163, v166, v167\n\t" \
    "s_add_u32 s36, s23, 0\n\t" \
    "s_waitcnt lgkmcnt(4)\n\t" \
    "v_mfma_f32_16x16x32_bf16 a[144:147], v[68:71], v[32:35], a[144:147]\n\t" \
    "ds_read_b64_tr_b16 v[64:65], v94 offset:16384\n\t" \
    "buffer_store_dwordx4 v[160:163], v110, s[16:19], s36 offen offset:256 sc1\n\t" \
    "v_mfma_f32_16x16x32_bf16 a[148:151], v[68:71], v[36:39], a[148:151]\n\t" \
    "ds_read_b64_tr_b16 v[66:67], v94 offset:17408\n\t" \
    "v_mfma_f32_16x16x32_bf16 a[152:155], v[68:71], v[40:43], a[152:155]\n\t" \
    "s_mov_b32 s28, 2621440\n\t" \
    "s_add_u32 m0, s26, 49152\n\t" \
    "v_mfma_f32_16x16x32_bf16 a[156:159], v[68:71], v[44:47], a[156:159]\n\t" \
    "buffer_load_dwordx4 v106, s[12:15], s28 offen offset:0 lds\n\t" \
    "s_waitcnt lgkmcnt(4)\n\t" \
    "v_mfma_f32_16x16x32_bf16 a[160:163], v[72:75], v[32:35], a[160:163]\n\t" \
    "ds_read_b64_tr_b16 v[68:69], v95 offset:16384\n\t" \
    "v_mfma_f32_16x16x32_bf16 a[164:167], v[72:75], v[36:39], a[164:167]\n\t" \
    "ds_read_b64_tr_b16 v[70:71], v95 offset:17408\n\t" \
    "v_mfma_f32_16x16x32_bf16 a[168:171], v[72:75], v[40:43], a[168:171]\n\t" \
    "buffer_load_dwordx4 v[20:23], v80, s[16:19], s33 offen offset:1024 sc1\n\t" \
    "v_mfma_f32_16x16x32_bf16 a[172:175], v[72:75], v[44:47], a[172:175]\n\t" \
    "s_waitcnt lgkmcnt(4)\n\t" \
    "v_mfma_f32_16x16x32_bf16 a[176:179], v[76:79], v[32:35], a[176:179]\n\t" \
    "ds_read_b64_tr_b16 v[72:73], v96 offset:16384\n\t" \
    "v_mfma_f32_16x16x32_bf16 a[180:183], v[76:79], v[36:39], a[180:183]\n\t" \
    "ds_read_b64_tr_b16 v[74:75], v96 offset:17408\n\t" \
    "v_mfma_f32_16x16x32_bf16 a[184:187], v[76:79], v[40:43], a[184:187]\n\t" \
    "v_mfma_f32_16x16x32_bf16 a[188:191], v[76:79], v[44:47], a[188:191]\n\t" \
    "buffer_load_dwordx4 v107, s[12:15], s28 offen offset:1024 lds\n\t" \
    "s_waitcnt lgkmcnt(4)\n\t" \
    "v_mfma_f32_16x16x32_bf16 a[192:195], v[64:67], v[32:35], a[192:195]\n\t" \
    "ds_read_b64_tr_b16 v[76:77], v97 offset:16384\n\t" \
    "v_cvt_pk_bf16_f32 v168, v168, v169\n\t" \
    "v_mfma_f32_16x16x32_bf16 a[196:199], v[64:67], v[36:39], a[196:199]\n\t" \
    "ds_read_b64_tr_b16 v[78:79], v97 offset:17408\n\t" \
    "v_cvt_pk_bf16_f32 v169, v170, v171\n\t" \
    "v_mfma_f32_16x16x32_bf16 a[200:203], v[64:67], v[40:43], a[200:203]\n\t" \
    "buffer_load_dwordx4 v[24:27], v80, s[16:19], s34 offen offset:1024 sc1\n\t" \
    "v_cvt_pk_bf16_f32 v170, v172, v173\n\t" \
    "v_mfma_f32_16x16x32_bf16 a[204:207], v[64:67], v[44:47], a[204:207]\n\t" \
    "v_cvt_pk_bf16_f32 v171, v174, v175\n\t" \
    "s_add_u32 s36, s23, 8192\n\t" \
    "s_waitcnt lgkmcnt(4)\n\t" \
    "v_mfma_f32_16x16x32_bf16 a[208:211], v[68:71], v[32:35], a[208:211]\n\t" \
    "ds_read_b64_tr_b16 v[64:65], v90 offset:24576\n\t" \
    "buffer_store_dwordx4 v[168:171], v110, s[16:19], s36 offen offset:256 sc1\n\t" \
    "v_mfma_f32_16x16x32_bf16 a[212:215], v[68:71], v[36:39], a[212:215]\n\t" \
    "ds_read_b64_tr_b16 v[66:67], v90 offset:25600\n\t" \
    "v_mfma_f32_16x16x32_bf16 a[216:219], v[68:71], v[40:43], a[216:219]\n\t" \
    "v_mfma_f32_16x16x32_bf16 a[220:223], v[68:71], v[44:47], a[220:223]\n\t" \
    "buffer_load_dwordx4 v108, s[12:15], s28 offen offset:2048 lds\n\t" \
    "s_waitcnt lgkmcnt(4)\n\t" \
    "v_mfma_f32_16x16x32_bf16 a[224:227], v[72:75], v[32:35], a[224:227]\n\t" \
    "ds_read_b64_tr_b16 v[68:69], v91 offset:24576\n\t" \
    "v_mfma_f32_16x16x32_bf16 a[228:231], v[72:75], v[36:39], a[228:231]\n\t" \
    "ds_read_b64_tr_b16 v[70:71], v91 offset:25600\n\t" \
    "v_mfma_f32_16x16x32_bf16 a[232:235], v[72:75], v[40:43], a[232:235]\n\t" \
    "buffer_load_dwordx4 v[28:31], v80, s[16:19], s35 offen offset:1024 sc1\n\t" \
    "v_mfma_f32_16x16x32_bf16 a[236:239], v[72:75], v[44:47], a[236:239]\n\t" \
    "s_waitcnt lgkmcnt(4)\n\t" \
    "v_mfma_f32_16x16x32_bf16 a[240:243], v[76:79], v[32:35], a[240:243]\n\t" \
    "ds_read_b64_tr_b16 v[72:73], v92 offset:24576\n\t" \
    "v_mfma_f32_16x16x32_bf16 a[244:247], v[76:79], v[36:39], a[244:247]\n\t" \
    "ds_read_b64_tr_b16 v[74:75], v92 offset:25600\n\t" \
    "v_mfma_f32_16x16x32_bf16 a[248:251], v[76:79], v[40:43], a[248:251]\n\t" \
    "v_mfma_f32_16x16x32_bf16 a[252:255], v[76:79], v[44:47], a[252:255]\n\t" \
    "buffer_load_dwordx4 v109, s[12:15], s28 offen offset:3072 lds\n\t" \
    "s_waitcnt vmcnt(20) lgkmcnt(4)\n\t" \
    "v_mfma_f32_16x16x32_bf16 a[128:131], v[64:67], v[48:51], a[128:131]\n\t" \
    "ds_read_b64_tr_b16 v[76:77], v93 offset:24576\n\t" \
    "v_cvt_pk_bf16_f32 v176, v176, v177\n\t" \
    "s_waitcnt vmcnt(18)\n\t" \
    "v_mfma_f32_16x16x32_bf16 a[132:135], v[64:67], v[52:55], a[132:135]\n\t" \
    "ds_read_b64_tr_b16 v[78:79], v93 offset:25600\n\t" \
    "v_cvt_pk_bf16_f32 v177, v178, v179\n\t" \
    "s_waitcnt vmcnt(17)\n\t" \
    "v_mfma_f32_16x16x32_bf16 a[136:139], v[64:67], v[56:59], a[136:139]\n\t" \
    "buffer_load_dwordx4 v[32:35], v80, s[16:19], s32 offen offset:2048 sc1\n\t" \
    "v_cvt_pk_bf16_f32 v178, v180, v181\n\t" \
    "s_waitcnt vmcnt(17)\n\t" \
    "v_mfma_f32_16x16x32_bf16 a[140:143], v[64:67], v[60:63], a[140:143]\n\t" \
    "v_cvt_pk_bf16_f32 v179, v182, v183\n\t" \
    "s_add_u32 s36, s23, 16384\n\t" \
    "s_waitcnt lgkmcnt(4)\n\t" \
    "v_mfma_f32_16x16x32_bf16 a[144:147], v[68:71], v[48:51], a[144:147]\n\t" \
    "ds_read_b64_tr_b16 v[64:65], v94 offset:24576\n\t" \
    "buffer_store_dwordx4 v[176:179], v110, s[16:19], s36 offen offset:256 sc1\n\t" \
    "v_mfma_f32_16x16x32_bf16 a[148:151], v[68:71], v[52:55], a[148:151]\n\t" \
    "ds_read_b64_tr_b16 v[66:67], v94 offset:25600\n\t" \
    "v_mfma_f32_16x16x32_bf16 a[152:155], v[68:71], v[56:59], a[152:155]\n\t" \
    "v_mfma_f32_16x16x32_bf16 a[156:159], v[68:71], v[60:63], a[156:159]\n\t" \
    "s_waitcnt lgkmcnt(4)\n\t" \
    "v_mfma_f32_16x16x32_bf16 a[160:163], v[72:75], v[48:51], a[160:163]\n\t" \
    "ds_read_b64_tr_b16 v[68:69], v95 offset:24576\n\t" \
    "v_mfma_f32_16x16x32_bf16 a[164:167], v[72:75], v[52:55], a[164:167]\n\t" \
    "ds_read_b64_tr_b16 v[70:71], v95 offset:25600\n\t" \
    "v_mfma_f32_16x16x32_bf16 a[168:171], v[72:75], v[56:59], a[168:171]\n\t" \
    "buffer_load_dwordx4 v[36:39], v80, s[16:19], s33 offen offset:2048 sc1\n\t" \
    "v_mfma_f32_16x16x32_bf16 a[172:175], v[72:75], v[60:63], a[172:175]\n\t" \
    "s_waitcnt lgkmcnt(4)\n\t" \
    "v_mfma_f32_16x16x32_bf16 a[176:179], v[76:79], v[48:51], a[176:179]\n\t" \
    "ds_read_b64_tr_b16 v[72:73], v96 offset:24576\n\t" \
    "v_mfma_f32_16x16x32_bf16 a[180:183], v[76:79], v[52:55], a[180:183]\n\t" \
    "ds_read_b64_tr_b16 v[74:75], v96 offset:25600\n\t" \
    "v_mfma_f32_16x16x32_bf16 a[184:187], v[76:79], v[56:59], a[184:187]\n\t" \
    "v_mfma_f32_16x16x32_bf16 a[188:191], v[76:79], v[60:63], a[188:191]\n\t" \
    "s_waitcnt lgkmcnt(4)\n\t" \
    "v_mfma_f32_16x16x32_bf16 a[192:195], v[64:67], v[48:51], a[192:195]\n\t" \
    "ds_read_b64_tr_b16 v[76:77], v97 offset:24576\n\t" \
    "v_cvt_pk_bf16_f32 v184, v184, v185\n\t" \
    "v_mfma_f32_16x16x32_bf16 a[196:199], v[64:67], v[52:55], a[196:199]\n\t" \
    "ds_read_b64_tr_b16 v[78:79], v97 offset:25600\n\t" \
    "v_cvt_pk_bf16_f32 v185, v186, v187\n\t" \
    "v_mfma_f32_16x16x32_bf16 a[200:203], v[64:67], v[56:59], a[200:203]\n\t" \
    "buffer_load_dwordx4 v[40:43], v80, s[16:19], s34 offen offset:2048 sc1\n\t" \
    "v_cvt_pk_bf16_f32 v186, v188, v189\n\t" \
    "v_mfma_f32_16x16x32_bf16 a[204:207], v[64:67], v[60:63], a[204:207]\n\t" \
    "v_cvt_pk_bf16_f32 v187, v190, v191\n\t" \
    "s_add_u32 s36, s23, 24576\n\t" \
    "s_waitcnt vmcnt(46)\n\t" \
    "s_barrier\n\t" \
    "s_waitcnt lgkmcnt(4)\n\t" \
    "v_mfma_f32_16x16x32_bf16 a[208:211], v[68:71], v[48:51], a[208:211]\n\t" \
    "ds_read_b64_tr_b16 v[64:65], v82 offset:0\n\t" \
    "v_mfma_f32_16x16x32_bf16 a[212:215], v[68:71], v[52:55], a[212:215]\n\t" \
    "ds_read_b64_tr_b16 v[66:67], v82 offset:1024\n\t" \
    "buffer_store_dwordx4 v[184:187], v110, s[16:19], s36 offen offset:256 sc1\n\t" \
    "v_mfma_f32_16x16x32_bf16 a[216:219], v[68:71], v[56:59], a[216:219]\n\t" \
    "v_mfma_f32_16x16x32_bf16 a[220:223], v[68:71], v[60:63], a[220:223]\n\t" \
    "s_waitcnt lgkmcnt(4)\n\t" \
    "v_mfma_f32_16x16x32_bf16 a[224:227], v[72:75], v[48:51], a[224:227]\n\t" \
    "ds_read_b64_tr_b16 v[68:69], v83 offset:0\n\t" \
    "v_mfma_f32_16x16x32_bf16 a[228:231], v[72:75], v[52:55], a[228:231]\n\t" \
    "ds_read_b64_tr_b16 v[70:71], v83 offset:1024\n\t" \
    "v_mfma_f32_16x16x32_bf16 a[232:235], v[72:75], v[56:59], a[232:235]\n\t" \
    "buffer_load_dwordx4 v[44:47], v80, s[16:19], s35 offen offset:2048 sc1\n\t" \
    "v_mfma_f32_16x16x32_bf16 a[236:239], v[72:75], v[60:63], a[236:239]\n\t" \
    "s_waitcnt lgkmcnt(4)\n\t" \
    "v_mfma_f32_16x16x32_bf16 a[240:243], v[76:79], v[48:51], a[240:243]\n\t" \
    "ds_read_b64_tr_b16 v[72:73], v84 offset:0\n\t" \
    "v_mfma_f32_16x16x32_bf16 a[244:247], v[76:79], v[52:55], a[244:247]\n\t" \
    "ds_read_b64_tr_b16 v[74:75], v84 offset:1024\n\t" \
    "v_mfma_f32_16x16x32_bf16 a[248:251], v[76:79], v[56:59], a[248:251]\n\t" \
    "v_mfma_f32_16x16x32_bf16 a[252:255], v[76:79], v[60:63], a[252:255]\n\t" \
    "s_waitcnt vmcnt(21) lgkmcnt(4)\n\t" \
    "v_mfma_f32_16x16x32_bf16 a[128:131], v[64:67], v[0:3], a[128:131]\n\t" \
    "ds_read_b64_tr_b16 v[76:77], v85 offset:0\n\t" \
    "v_cvt_pk_bf16_f32 v192, v192, v193\n\t" \
    "s_waitcnt vmcnt(19)\n\t" \
    "v_mfma_f32_16x16x32_bf16 a[132:135], v[64:67], v[4:7], a[132:135]\n\t" \
    "ds_read_b64_tr_b16 v[78:79], v85 offset:1024\n\t" \
    "v_cvt_pk_bf16_f32 v193, v194, v195\n\t" \
    "s_waitcnt vmcnt(18)\n\t" \
    "v_mfma_f32_16x16x32_bf16 a[136:139], v[64:67], v[8:11], a[136:139]\n\t" \
    "buffer_load_dwordx4 v[48:51], v80, s[16:19], s32 offen offset:3072 sc1\n\t" \
    "v_cvt_pk_bf16_f32 v194, v196, v197\n\t" \
    "s_waitcnt vmcnt(17)\n\t" \
    "v_mfma_f32_16x16x32_bf16 a[140:143], v[64:67], v[12:15], a[140:143]\n\t" \
    "v_cvt_pk_bf16_f32 v195, v198, v199\n\t" \
    "s_add_u32 s36, s23, 0\n\t" \
    "s_waitcnt lgkmcnt(4)\n\t" \
    "v_mfma_f32_16x16x32_bf16 a[144:147], v[68:71], v[0:3], a[144:147]\n\t" \
    "ds_read_b64_tr_b16 v[64:65], v86 offset:0\n\t" \
    "buffer_store_dwordx4 v[192:195], v110, s[16:19], s36 offen offset:1024 sc1\n\t" \
    "v_mfma_f32_16x16x32_bf16 a[148:151], v[68:71], v[4:7], a[148:151]\n\t" \
    "ds_read_b64_tr_b16 v[66:67], v86 offset:1024\n\t" \
    "v_mfma_f32_16x16x32_bf16 a[152:155], v[68:71], v[8:11], a[152:155]\n\t" \
    "s_mov_b32 s28, 3145728\n\t" \
    "s_add_u32 m0, s26, 65536\n\t" \
    "v_mfma_f32_16x16x32_bf16 a[156:159], v[68:71], v[12:15], a[156:159]\n\t" \
    "buffer_load_dwordx4 v106, s[12:15], s28 offen offset:0 lds\n\t" \
    "s_waitcnt lgkmcnt(4)\n\t" \
    "v_mfma_f32_16x16x32_bf16 a[160:163], v[72:75], v[0:3], a[160:163]\n\t" \
    "ds_read_b64_tr_b16 v[68:69], v87 offset:0\n\t" \
    "v_mfma_f32_16x16x32_bf16 a[164:167], v[72:75], v[4:7], a[164:167]\n\t" \
    "ds_read_b64_tr_b16 v[70:71], v87 offset:1024\n\t" \
    "v_mfma_f32_16x16x32_bf16 a[168:171], v[72:75], v[8:11], a[168:171]\n\t" \
    "buffer_load_dwordx4 v[52:55], v80, s[16:19], s33 offen offset:3072 sc1\n\t" \
    "v_mfma_f32_16x16x32_bf16 a[172:175], v[72:75], v[12:15], a[172:175]\n\t" \
    "s_waitcnt lgkmcnt(4)\n\t" \
    "v_mfma_f32_16x16x32_bf16 a[176:179], v[76:79], v[0:3], a[176:179]\n\t" \
    "ds_read_b64_tr_b16 v[72:73], v88 offset:0\n\t" \
    "v_mfma_f32_16x16x32_bf16 a[180:183], v[76:79], v[4:7], a[180:183]\n\t" \
    "ds_read_b64_tr_b16 v[74:75], v88 offset:1024\n\t" \
    "v_mfma_f32_16x16x32_bf16 a[184:187], v[76:79], v[8:11], a[184:187]\n\t" \
    "v_mfma_f32_16x16x32_bf16 a[188:191], v[76:79], v[12:15], a[188:191]\n\t" \
    "buffer_load_dwordx4 v107, s[12:15], s28 offen offset:1024 lds\n\t" \
    "s_waitcnt lgkmcnt(4)\n\t" \
    "v_mfma_f32_16x16x32_bf16 a[192:195], v[64:67], v[0:3], a[192:195]\n\t" \
    "ds_read_b64_tr_b16 v[76:77], v89 offset:0\n\t" \
    "v_cvt_pk_bf16_f32 v200, v200, v201\n\t" \
    "v_mfma_f32_16x16x32_bf16 a[196:199], v[64:67], v[4:7], a[196:199]\n\t" \
    "ds_read_b64_tr_b16 v[78:79], v89 offset:1024\n\t" \
    "v_cvt_pk_bf16_f32 v201, v202, v203\n\t" \
    "v_mfma_f32_16x16x32_bf16 a[200:203], v[64:67], v[8:11], a[200:203]\n\t" \
    "buffer_load_dwordx4 v[56:59], v80, s[16:19], s34 offen offset:3072 sc1\n\t" \
    "v_cvt_pk_bf16_f32 v202, v204, v205\n\t" \
    "v_mfma_f32_16x16x32_bf16 a[204:207], v[64:67], v[12:15], a[204:207]\n\t" \
    "v_cvt_pk_bf16_f32 v203, v206, v207\n\t" \
    "s_add_u32 s36, s23, 8192\n\t" \
    "s_waitcnt lgkmcnt(4)\n\t" \
    "v_mfma_f32_16x16x32_bf16 a[208:211], v[68:71], v[0:3], a[208:211]\n\t" \
    "ds_read_b64_tr_b16 v[64:65], v82 offset:8192\n\t" \
    "buffer_store_dwordx4 v[200:203], v110, s[16:19], s36 offen offset:1024 sc1\n\t" \
    "v_mfma_f32_16x16x32_bf16 a[212:215], v[68:71], v[4:7], a[212:215]\n\t" \
    "ds_read_b64_tr_b16 v[66:67], v82 offset:9216\n\t" \
    "v_mfma_f32_16x16x32_bf16 a[216:219], v[68:71], v[8:11], a[216:219]\n\t" \
    "v_mfma_f32_16x16x32_bf16 a[220:223], v[68:71], v[12:15], a[220:223]\n\t" \
    "buffer_load_dwordx4 v108, s[12:15], s28 offen offset:2048 lds\n\t" \
    "s_waitcnt lgkmcnt(4)\n\t" \
    "v_mfma_f32_16x16x32_bf16 a[224:227], v[72:75], v[0:3], a[224:227]\n\t" \
    "ds_read_b64_tr_b16 v[68:69], v83 offset:8192\n\t" \
    "v_mfma_f32_16x16x32_bf16 a[228:231], v[72:75], v[4:7], a[228:231]\n\t" \
    "ds_read_b64_tr_b16 v[70:71], v83 offset:9216\n\t" \
    "v_mfma_f32_16x16x32_bf16 a[232:235], v[72:75], v[8:11], a[232:235]\n\t" \
    "buffer_load_dwordx4 v[60:63], v80, s[16:19], s35 offen offset:3072 sc1\n\t" \
    "v_mfma_f32_16x16x32_bf16 a[236:239], v[72:75], v[12:15], a[236:239]\n\t" \
    "s_waitcnt lgkmcnt(4)\n\t" \
    "v_mfma_f32_16x16x32_bf16 a[240:243], v[76:79], v[0:3], a[240:243]\n\t" \
    "ds_read_b64_tr_b16 v[72:73], v84 offset:8192\n\t" \
    "v_mfma_f32_16x16x32_bf16 a[244:247], v[76:79], v[4:7], a[244:247]\n\t" \
    "ds_read_b64_tr_b16 v[74:75], v84 offset:9216\n\t" \
    "v_mfma_f32_16x16x32_bf16 a[248:251], v[76:79], v[8:11], a[248:251]\n\t" \
    "v_mfma_f32_16x16x32_bf16 a[252:255], v[76:79], v[12:15], a[252:255]\n\t" \
    "buffer_load_dwordx4 v109, s[12:15], s28 offen offset:3072 lds\n\t" \
    "s_waitcnt vmcnt(25) lgkmcnt(4)\n\t" \
    "v_mfma_f32_16x16x32_bf16 a[128:131], v[64:67], v[16:19], a[128:131]\n\t" \
    "ds_read_b64_tr_b16 v[76:77], v85 offset:8192\n\t" \
    "v_cvt_pk_bf16_f32 v208, v208, v209\n\t" \
    "s_waitcnt vmcnt(22)\n\t" \
    "v_mfma_f32_16x16x32_bf16 a[132:135], v[64:67], v[20:23], a[132:135]\n\t" \
    "ds_read_b64_tr_b16 v[78:79], v85 offset:9216\n\t" \
    "v_cvt_pk_bf16_f32 v209, v210, v211\n\t" \
    "s_add_u32 s32, s32, 4096\n\t" \
    "s_add_u32 s33, s33, 4096\n\t" \
    "s_add_u32 s34, s34, 4096\n\t" \
    "s_add_u32 s35, s35, 4096\n\t" \
    "s_waitcnt vmcnt(20)\n\t" \
    "v_mfma_f32_16x16x32_bf16 a[136:139], v[64:67], v[24:27], a[136:139]\n\t" \
    "buffer_load_dwordx4 v[0:3], v80, s[16:19], s32 offen offset:0 sc1\n\t" \
    "s_waitcnt vmcnt(18)\n\t" \
    "v_mfma_f32_16x16x32_bf16 a[140:143], v[64:67], v[28:31], a[140:143]\n\t" \
    "v_cvt_pk_bf16_f32 v210, v212, v213\n\t" \
    "v_cvt_pk_bf16_f32 v211, v214, v215\n\t" \
    "s_waitcnt lgkmcnt(4)\n\t" \
    "v_mfma_f32_16x16x32_bf16 a[144:147], v[68:71], v[16:19], a[144:147]\n\t" \
    "ds_read_b64_tr_b16 v[64:65], v86 offset:8192\n\t" \
    "s_add_u32 s36, s23, 16384\n\t" \
    "v_mfma_f32_16x16x32_bf16 a[148:151], v[68:71], v[20:23], a[148:151]\n\t" \
    "ds_read_b64_tr_b16 v[66:67], v86 offset:9216\n\t" \
    "buffer_store_dwordx4 v[208:211], v110, s[16:19], s36 offen offset:1024 sc1\n\t" \
    "v_mfma_f32_16x16x32_bf16 a[152:155], v[68:71], v[24:27], a[152:155]\n\t" \
    "v_mfma_f32_16x16x32_bf16 a[156:159], v[68:71], v[28:31], a[156:159]\n\t" \
    "s_waitcnt lgkmcnt(4)\n\t" \
    "v_mfma_f32_16x16x32_bf16 a[160:163], v[72:75], v[16:19], a[160:163]\n\t" \
    "ds_read_b64_tr_b16 v[68:69], v87 offset:8192\n\t" \
    "v_mfma_f32_16x16x32_bf16 a[164:167], v[72:75], v[20:23], a[164:167]\n\t" \
    "ds_read_b64_tr_b16 v[70:71], v87 offset:9216\n\t" \
    "v_mfma_f32_16x16x32_bf16 a[168:171], v[72:75], v[24:27], a[168:171]\n\t" \
    "buffer_load_dwordx4 v[4:7], v80, s[16:19], s33 offen offset:0 sc1\n\t" \
    "v_mfma_f32_16x16x32_bf16 a[172:175], v[72:75], v[28:31], a[172:175]\n\t" \
    "s_waitcnt lgkmcnt(4)\n\t" \
    "v_mfma_f32_16x16x32_bf16 a[176:179], v[76:79], v[16:19], a[176:179]\n\t" \
    "ds_read_b64_tr_b16 v[72:73], v88 offset:8192\n\t" \
    "v_mfma_f32_16x16x32_bf16 a[180:183], v[76:79], v[20:23], a[180:183]\n\t" \
    "ds_read_b64_tr_b16 v[74:75], v88 offset:9216\n\t" \
    "v_mfma_f32_16x16x32_bf16 a[184:187], v[76:79], v[24:27], a[184:187]\n\t" \
    "v_mfma_f32_16x16x32_bf16 a[188:191], v[76:79], v[28:31], a[188:191]\n\t" \
    "s_waitcnt lgkmcnt(4)\n\t" \
    "v_mfma_f32_16x16x32_bf16 a[192:195], v[64:67], v[16:19], a[192:195]\n\t" \
    "ds_read_b64_tr_b16 v[76:77], v89 offset:8192\n\t" \
    "v_cvt_pk_bf16_f32 v216, v216, v217\n\t" \
    "v_mfma_f32_16x16x32_bf16 a[196:199], v[64:67], v[20:23], a[196:199]\n\t" \
    "ds_read_b64_tr_b16 v[78:79], v89 offset:9216\n\t" \
    "v_cvt_pk_bf16_f32 v217, v218, v219\n\t" \
    "v_mfma_f32_16x16x32_bf16 a[200:203], v[64:67], v[24:27], a[200:203]\n\t" \
    "buffer_load_dwordx4 v[8:11], v80, s[16:19], s34 offen offset:0 sc1\n\t" \
    "v_cvt_pk_bf16_f32 v218, v220, v221\n\t" \
    "v_mfma_f32_16x16x32_bf16 a[204:207], v[64:67], v[28:31], a[204:207]\n\t" \
    "v_cvt_pk_bf16_f32 v219, v222, v223\n\t" \
    "s_add_u32 s36, s23, 24576\n\t" \
    "s_waitcnt vmcnt(58)\n\t" \
    "s_barrier\n\t" \
    "s_waitcnt lgkmcnt(4)\n\t" \
    "v_mfma_f32_16x16x32_bf16 a[208:211], v[68:71], v[16:19], a[208:211]\n\t" \
    "ds_read_b64_tr_b16 v[64:65], v82 offset:16384\n\t" \
    "v_mfma_f32_16x16x32_bf16 a[212:215], v[68:71], v[20:23], a[212:215]\n\t" \
    "ds_read_b64_tr_b16 v[66:67], v82 offset:17408\n\t" \
    "buffer_store_dwordx4 v[216:219], v110, s[16:19], s36 offen offset:1024 sc1\n\t" \
    "v_mfma_f32_16x16x32_bf16 a[216:219], v[68:71], v[24:27], a[216:219]\n\t" \
    "v_mfma_f32_16x16x32_bf16 a[220:223], v[68:71], v[28:31], a[220:223]\n\t" \
    "s_waitcnt lgkmcnt(4)\n\t" \
    "v_mfma_f32_16x16x32_bf16 a[224:227], v[72:75], v[16:19], a[224:227]\n\t" \
    "ds_read_b64_tr_b16 v[68:69], v83 offset:16384\n\t" \
    "v_mfma_f32_16x16x32_bf16 a[228:231], v[72:75], v[20:23], a[228:231]\n\t" \
    "ds_read_b64_tr_b16 v[70:71], v83 offset:17408\n\t" \
    "v_mfma_f32_16x16x32_bf16 a[232:235], v[72:75], v[24:27], a[232:235]\n\t" \
    "buffer_load_dwordx4 v[12:15], v80, s[16:19], s35 offen offset:0 sc1\n\t" \
    "v_mfma_f32_16x16x32_bf16 a[236:239], v[72:75], v[28:31], a[236:239]\n\t" \
    "s_waitcnt lgkmcnt(4)\n\t" \
    "v_mfma_f32_16x16x32_bf16 a[240:243], v[76:79], v[16:19], a[240:243]\n\t" \
    "ds_read_b64_tr_b16 v[72:73], v84 offset:16384\n\t" \
    "v_mfma_f32_16x16x32_bf16 a[244:247], v[76:79], v[20:23], a[244:247]\n\t" \
    "ds_read_b64_tr_b16 v[74:75], v84 offset:17408\n\t" \
    "v_mfma_f32_16x16x32_bf16 a[248:251], v[76:79], v[24:27], a[248:251]\n\t" \
    "v_mfma_f32_16x16x32_bf16 a[252:255], v[76:79], v[28:31], a[252:255]\n\t" \
    "s_waitcnt vmcnt(21) lgkmcnt(4)\n\t" \
    "v_mfma_f32_16x16x32_bf16 a[128:131], v[64:67], v[32:35], a[128:131]\n\t" \
    "ds_read_b64_tr_b16 v[76:77], v85 offset:16384\n\t" \
    "v_cvt_pk_bf16_f32 v224, v224, v225\n\t" \
    "s_waitcnt vmcnt(19)\n\t" \
    "v_mfma_f32_16x16x32_bf16 a[132:135], v[64:67], v[36:39], a[132:135]\n\t" \
    "ds_read_b64_tr_b16 v[78:79], v85 offset:17408\n\t" \
    "v_cvt_pk_bf16_f32 v225, v226, v227\n\t" \
    "s_waitcnt vmcnt(18)\n\t" \
    "v_mfma_f32_16x16x32_bf16 a[136:139], v[64:67], v[40:43], a[136:139]\n\t" \
    "buffer_load_dwordx4 v[16:19], v80, s[16:19], s32 offen offset:1024 sc1\n\t" \
    "v_cvt_pk_bf16_f32 v226, v228, v229\n\t" \
    "s_waitcnt vmcnt(17)\n\t" \
    "v_mfma_f32_16x16x32_bf16 a[140:143], v[64:67], v[44:47], a[140:143]\n\t" \
    "v_cvt_pk_bf16_f32 v227, v230, v231\n\t" \
    "s_add_u32 s36, s23, 0\n\t" \
    "s_waitcnt lgkmcnt(4)\n\t" \
    "v_mfma_f32_16x16x32_bf16 a[144:147], v[68:71], v[32:35], a[144:147]\n\t" \
    "ds_read_b64_tr_b16 v[64:65], v86 offset:16384\n\t" \
    "buffer_store_dwordx4 v[224:227], v110, s[16:19], s36 offen offset:1280 sc1\n\t" \
    "v_mfma_f32_16x16x32_bf16 a[148:151], v[68:71], v[36:39], a[148:151]\n\t" \
    "ds_read_b64_tr_b16 v[66:67], v86 offset:17408\n\t" \
    "v_mfma_f32_16x16x32_bf16 a[152:155], v[68:71], v[40:43], a[152:155]\n\t" \
    "s_mov_b32 s28, 3670016\n\t" \
    "s_add_u32 m0, s26, 81920\n\t" \
    "v_mfma_f32_16x16x32_bf16 a[156:159], v[68:71], v[44:47], a[156:159]\n\t" \
    "buffer_load_dwordx4 v106, s[12:15], s28 offen offset:0 lds\n\t" \
    "s_waitcnt lgkmcnt(4)\n\t" \
    "v_mfma_f32_16x16x32_bf16 a[160:163], v[72:75], v[32:35], a[160:163]\n\t" \
    "ds_read_b64_tr_b16 v[68:69], v87 offset:16384\n\t" \
    "v_mfma_f32_16x16x32_bf16 a[164:167], v[72:75], v[36:39], a[164:167]\n\t" \
    "ds_read_b64_tr_b16 v[70:71], v87 offset:17408\n\t" \
    "v_mfma_f32_16x16x32_bf16 a[168:171], v[72:75], v[40:43], a[168:171]\n\t" \
    "buffer_load_dwordx4 v[20:23], v80, s[16:19], s33 offen offset:1024 sc1\n\t" \
    "v_mfma_f32_16x16x32_bf16 a[172:175], v[72:75], v[44:47], a[172:175]\n\t" \
    "s_waitcnt lgkmcnt(4)\n\t" \
    "v_mfma_f32_16x16x32_bf16 a[176:179], v[76:79], v[32:35], a[176:179]\n\t" \
    "ds_read_b64_tr_b16 v[72:73], v88 offset:16384\n\t" \
    "v_mfma_f32_16x16x32_bf16 a[180:183], v[76:79], v[36:39], a[180:183]\n\t" \
    "ds_read_b64_tr_b16 v[74:75], v88 offset:17408\n\t" \
    "v_mfma_f32_16x16x32_bf16 a[184:187], v[76:79], v[40:43], a[184:187]\n\t" \
    "v_mfma_f32_16x16x32_bf16 a[188:191], v[76:79], v[44:47], a[188:191]\n\t" \
    "buffer_load_dwordx4 v107, s[12:15], s28 offen offset:1024 lds\n\t" \
    "s_waitcnt lgkmcnt(4)\n\t" \
    "v_mfma_f32_16x16x32_bf16 a[192:195], v[64:67], v[32:35], a[192:195]\n\t" \
    "ds_read_b64_tr_b16 v[76:77], v89 offset:16384\n\t" \
    "v_cvt_pk_bf16_f32 v232, v232, v233\n\t" \
    "v_mfma_f32_16x16x32_bf16 a[196:199], v[64:67], v[36:39], a[196:199]\n\t" \
    "ds_read_b64_tr_b16 v[78:79], v89 offset:17408\n\t" \
    "v_cvt_pk_bf16_f32 v233, v234, v235\n\t" \
    "v_mfma_f32_16x16x32_bf16 a[200:203], v[64:67], v[40:43], a[200:203]\n\t" \
    "buffer_load_dwordx4 v[24:27], v80, s[16:19], s34 offen offset:1024 sc1\n\t" \
    "v_cvt_pk_bf16_f32 v234, v236, v237\n\t" \
    "v_mfma_f32_16x16x32_bf16 a[204:207], v[64:67], v[44:47], a[204:207]\n\t" \
    "v_cvt_pk_bf16_f32 v235, v238, v239\n\t" \
    "s_add_u32 s36, s23, 8192\n\t" \
    "s_waitcnt lgkmcnt(4)\n\t" \
    "v_mfma_f32_16x16x32_bf16 a[208:211], v[68:71], v[32:35], a[208:211]\n\t" \
    "ds_read_b64_tr_b16 v[64:65], v82 offset:24576\n\t" \
    "buffer_store_dwordx4 v[232:235], v110, s[16:19], s36 offen offset:1280 sc1\n\t" \
    "v_mfma_f32_16x16x32_bf16 a[212:215], v[68:71], v[36:39], a[212:215]\n\t" \
    "ds_read_b64_tr_b16 v[66:67], v82 offset:25600\n\t" \
    "v_mfma_f32_16x16x32_bf16 a[216:219], v[68:71], v[40:43], a[216:219]\n\t" \
    "v_mfma_f32_16x16x32_bf16 a[220:223], v[68:71], v[44:47], a[220:223]\n\t" \
    "buffer_load_dwordx4 v108, s[12:15], s28 offen offset:2048 lds\n\t" \
    "s_waitcnt lgkmcnt(4)\n\t" \
    "v_mfma_f32_16x16x32_bf16 a[224:227], v[72:75], v[32:35], a[224:227]\n\t" \
    "ds_read_b64_tr_b16 v[68:69], v83 offset:24576\n\t" \
    "v_mfma_f32_16x16x32_bf16 a[228:231], v[72:75], v[36:39], a[228:231]\n\t" \
    "ds_read_b64_tr_b16 v[70:71], v83 offset:25600\n\t" \
    "v_mfma_f32_16x16x32_bf16 a[232:235], v[72:75], v[40:43], a[232:235]\n\t" \
    "buffer_load_dwordx4 v[28:31], v80, s[16:19], s35 offen offset:1024 sc1\n\t" \
    "v_mfma_f32_16x16x32_bf16 a[236:239], v[72:75], v[44:47], a[236:239]\n\t" \
    "s_waitcnt lgkmcnt(4)\n\t" \
    "v_mfma_f32_16x16x32_bf16 a[240:243], v[76:79], v[32:35], a[240:243]\n\t" \
    "ds_read_b64_tr_b16 v[72:73], v84 offset:24576\n\t" \
    "v_mfma_f32_16x16x32_bf16 a[244:247], v[76:79], v[36:39], a[244:247]\n\t" \
    "ds_read_b64_tr_b16 v[74:75], v84 offset:25600\n\t" \
    "v_mfma_f32_16x16x32_bf16 a[248:251], v[76:79], v[40:43], a[248:251]\n\t" \
    "v_mfma_f32_16x16x32_bf16 a[252:255], v[76:79], v[44:47], a[252:255]\n\t" \
    "buffer_load_dwordx4 v109, s[12:15], s28 offen offset:3072 lds\n\t" \
    "s_waitcnt vmcnt(25) lgkmcnt(4)\n\t" \
    "v_mfma_f32_16x16x32_bf16 a[128:131], v[64:67], v[48:51], a[128:131]\n\t" \
    "ds_read_b64_tr_b16 v[76:77], v85 offset:24576\n\t" \
    "v_cvt_pk_bf16_f32 v240, v240, v241\n\t" \
    "s_waitcnt vmcnt(22)\n\t" \
    "v_mfma_f32_16x16x32_bf16 a[132:135], v[64:67], v[52:55], a[132:135]\n\t" \
    "ds_read_b64_tr_b16 v[78:79], v85 offset:25600\n\t" \
    "v_cvt_pk_bf16_f32 v241, v242, v243\n\t" \
    "s_waitcnt vmcnt(20)\n\t" \
    "v_mfma_f32_16x16x32_bf16 a[136:139], v[64:67], v[56:59], a[136:139]\n\t" \
    "buffer_load_dwordx4 v[32:35], v80, s[16:19], s32 offen offset:2048 sc1\n\t" \
    "v_cvt_pk_bf16_f32 v242, v244, v245\n\t" \
    "s_waitcnt vmcnt(18)\n\t" \
    "v_mfma_f32_16x16x32_bf16 a[140:143], v[64:67], v[60:63], a[140:143]\n\t" \
    "v_cvt_pk_bf16_f32 v243, v246, v247\n\t" \
    "s_add_u32 s36, s23, 16384\n\t" \
    "s_waitcnt lgkmcnt(4)\n\t" \
    "v_mfma_f32_16x16x32_bf16 a[144:147], v[68:71], v[48:51], a[144:147]\n\t" \
    "ds_read_b64_tr_b16 v[64:65], v86 offset:24576\n\t" \
    "buffer_store_dwordx4 v[240:243], v110, s[16:19], s36 offen offset:1280 sc1\n\t" \
    "v_mfma_f32_16x16x32_bf16 a[148:151], v[68:71], v[52:55], a[148:151]\n\t" \
    "ds_read_b64_tr_b16 v[66:67], v86 offset:25600\n\t" \
    "v_mfma_f32_16x16x32_bf16 a[152:155], v[68:71], v[56:59], a[152:155]\n\t" \
    "v_mfma_f32_16x16x32_bf16 a[156:159], v[68:71], v[60:63], a[156:159]\n\t" \
    "s_waitcnt lgkmcnt(4)\n\t" \
    "v_mfma_f32_16x16x32_bf16 a[160:163], v[72:75], v[48:51], a[160:163]\n\t" \
    "ds_read_b64_tr_b16 v[68:69], v87 offset:24576\n\t" \
    "v_mfma_f32_16x16x32_bf16 a[164:167], v[72:75], v[52:55], a[164:167]\n\t" \
    "ds_read_b64_tr_b16 v[70:71], v87 offset:25600\n\t" \
    "v_mfma_f32_16x16x32_bf16 a[168:171], v[72:75], v[56:59], a[168:171]\n\t" \
    "buffer_load_dwordx4 v[36:39], v80, s[16:19], s33 offen offset:2048 sc1\n\t" \
    "v_mfma_f32_16x16x32_bf16 a[172:175], v[72:75], v[60:63], a[172:175]\n\t" \
    "s_waitcnt lgkmcnt(4)\n\t" \
    "v_mfma_f32_16x16x32_bf16 a[176:179], v[76:79], v[48:51], a[176:179]\n\t" \
    "ds_read_b64_tr_b16 v[72:73], v88 offset:24576\n\t" \
    "v_mfma_f32_16x16x32_bf16 a[180:183], v[76:79], v[52:55], a[180:183]\n\t" \
    "ds_read_b64_tr_b16 v[74:75], v88 offset:25600\n\t" \
    "v_mfma_f32_16x16x32_bf16 a[184:187], v[76:79], v[56:59], a[184:187]\n\t" \
    "v_mfma_f32_16x16x32_bf16 a[188:191], v[76:79], v[60:63], a[188:191]\n\t" \
    "s_waitcnt lgkmcnt(4)\n\t" \
    "v_mfma_f32_16x16x32_bf16 a[192:195], v[64:67], v[48:51], a[192:195]\n\t" \
    "ds_read_b64_tr_b16 v[76:77], v89 offset:24576\n\t" \
    "v_cvt_pk_bf16_f32 v248, v248, v249\n\t" \
    "v_mfma_f32_16x16x32_bf16 a[196:199], v[64:67], v[52:55], a[196:199]\n\t" \
    "ds_read_b64_tr_b16 v[78:79], v89 offset:25600\n\t" \
    "v_cvt_pk_bf16_f32 v249, v250, v251\n\t" \
    "v_mfma_f32_16x16x32_bf16 a[200:203], v[64:67], v[56:59], a[200:203]\n\t" \
    "buffer_load_dwordx4 v[40:43], v80, s[16:19], s34 offen offset:2048 sc1\n\t" \
    "v_cvt_pk_bf16_f32 v250, v252, v253\n\t" \
    "v_mfma_f32_16x16x32_bf16 a[204:207], v[64:67], v[60:63], a[204:207]\n\t" \
    "v_cvt_pk_bf16_f32 v251, v254, v255\n\t" \
    "s_add_u32 s36, s23, 24576\n\t" \
    "s_waitcnt vmcnt(62)\n\t" \
    "s_barrier\n\t" \
    "s_waitcnt lgkmcnt(4)\n\t" \
    "v_mfma_f32_16x16x32_bf16 a[208:211], v[68:71], v[48:51], a[208:211]\n\t" \
    "ds_read_b64_tr_b16 v[64:65], v82 offset:32768\n\t" \
    "v_mfma_f32_16x16x32_bf16 a[212:215], v[68:71], v[52:55], a[212:215]\n\t" \
    "ds_read_b64_tr_b16 v[66:67], v82 offset:33792\n\t" \
    "buffer_store_dwordx4 v[248:251], v110, s[16:19], s36 offen offset:1280 sc1\n\t" \
    "v_mfma_f32_16x16x32_bf16 a[216:219], v[68:71], v[56:59], a[216:219]\n\t" \
    "v_mfma_f32_16x16x32_bf16 a[220:223], v[68:71], v[60:63], a[220:223]\n\t" \
    "s_waitcnt lgkmcnt(4)\n\t" \
    "v_mfma_f32_16x16x32_bf16 a[224:227], v[72:75], v[48:51], a[224:227]\n\t" \
    "ds_read_b64_tr_b16 v[68:69], v83 offset:32768\n\t" \
    "v_mfma_f32_16x16x32_bf16 a[228:231], v[72:75], v[52:55], a[228:231]\n\t" \
    "ds_read_b64_tr_b16 v[70:71], v83 offset:33792\n\t" \
    "v_mfma_f32_16x16x32_bf16 a[232:235], v[72:75], v[56:59], a[232:235]\n\t" \
    "buffer_load_dwordx4 v[44:47], v80, s[16:19], s35 offen offset:2048 sc1\n\t" \
    "v_mfma_f32_16x16x32_bf16 a[236:239], v[72:75], v[60:63], a[236:239]\n\t" \
    "s_waitcnt lgkmcnt(4)\n\t" \
    "v_mfma_f32_16x16x32_bf16 a[240:243], v[76:79], v[48:51], a[240:243]\n\t" \
    "ds_read_b64_tr_b16 v[72:73], v84 offset:32768\n\t" \
    "v_mfma_f32_16x16x32_bf16 a[244:247], v[76:79], v[52:55], a[244:247]\n\t" \
    "ds_read_b64_tr_b16 v[74:75], v84 offset:33792\n\t" \
    "v_mfma_f32_16x16x32_bf16 a[248:251], v[76:79], v[56:59], a[248:251]\n\t" \
    "v_mfma_f32_16x16x32_bf16 a[252:255], v[76:79], v[60:63], a[252:255]\n\t" \
    "s_waitcnt vmcnt(21) lgkmcnt(4)\n\t" \
    "v_mfma_f32_16x16x32_bf16 a[128:131], v[64:67], v[0:3], a[128:131]\n\t" \
    "ds_read_b64_tr_b16 v[76:77], v85 offset:32768\n\t" \
    "v_accvgpr_read_b32 v116, a0\n\t" \
    "s_waitcnt vmcnt(19)\n\t" \
    "v_mfma_f32_16x16x32_bf16 a[132:135], v[64:67], v[4:7], a[132:135]\n\t" \
    "ds_read_b64_tr_b16 v[78:79], v85 offset:33792\n\t" \
    "v_accvgpr_read_b32 v117, a1\n\t" \
    "s_waitcnt vmcnt(18)\n\t" \
    "v_mfma_f32_16x16x32_bf16 a[136:139], v[64:67], v[8:11], a[136:139]\n\t" \
    "buffer_load_dwordx4 v[48:51], v80, s[16:19], s32 offen offset:3072 sc1\n\t" \
    "v_accvgpr_read_b32 v118, a2\n\t" \
    "s_waitcnt vmcnt(17)\n\t" \
    "v_mfma_f32_16x16x32_bf16 a[140:143], v[64:67], v[12:15], a[140:143]\n\t" \
    "v_accvgpr_read_b32 v119, a3\n\t" \
    "v_accvgpr_read_b32 v120, a4\n\t" \
    "s_waitcnt lgkmcnt(4)\n\t" \
    "v_mfma_f32_16x16x32_bf16 a[144:147], v[68:71], v[0:3], a[144:147]\n\t" \
    "ds_read_b64_tr_b16 v[64:65], v86 offset:32768\n\t" \
    "v_accvgpr_read_b32 v121, a5\n\t" \
    "v_mfma_f32_16x16x32_bf16 a[148:151], v[68:71], v[4:7], a[148:151]\n\t" \
    "ds_read_b64_tr_b16 v[66:67], v86 offset:33792\n\t" \
    "v_accvgpr_read_b32 v122, a6\n\t" \
    "v_mfma_f32_16x16x32_bf16 a[152:155], v[68:71], v[8:11], a[152:155]\n\t" \
    "v_accvgpr_read_b32 v123, a7\n\t" \
    "v_cvt_pk_bf16_f32 v116, v116, v117\n\t" \
    "v_mfma_f32_16x16x32_bf16 a[156:159], v[68:71], v[12:15], a[156:159]\n\t" \
    "v_cvt_pk_bf16_f32 v117, v118, v119\n\t" \
    "v_cvt_pk_bf16_f32 v118, v120, v121\n\t" \
    "s_waitcnt lgkmcnt(4)\n\t" \
    "v_mfma_f32_16x16x32_bf16 a[160:163], v[72:75], v[0:3], a[160:163]\n\t" \
    "ds_read_b64_tr_b16 v[68:69], v87 offset:32768\n\t" \
    "v_cvt_pk_bf16_f32 v119, v122, v123\n\t" \
    "v_mfma_f32_16x16x32_bf16 a[164:167], v[72:75], v[4:7], a[164:167]\n\t" \
    "ds_read_b64_tr_b16 v[70:71], v87 offset:33792\n\t" \
    "s_add_u32 s36, s23, 0\n\t" \
    "v_mfma_f32_16x16x32_bf16 a[168:171], v[72:75], v[8:11], a[168:171]\n\t" \
    "buffer_load_dwordx4 v[52:55], v80, s[16:19], s33 offen offset:3072 sc1\n\t" \
    "buffer_store_dwordx4 v[116:119], v110, s[16:19], s36 offen offset:2048 sc1\n\t" \
    "v_mfma_f32_16x16x32_bf16 a[172:175], v[72:75], v[12:15], a[172:175]\n\t" \
    "s_waitcnt lgkmcnt(4)\n\t" \
    "v_mfma_f32_16x16x32_bf16 a[176:179], v[76:79], v[0:3], a[176:179]\n\t" \
    "ds_read_b64_tr_b16 v[72:73], v88 offset:32768\n\t" \
    "v_mfma_f32_16x16x32_bf16 a[180:183], v[76:79], v[4:7], a[180:183]\n\t" \
    "ds_read_b64_tr_b16 v[74:75], v88 offset:33792\n\t" \
    "v_mfma_f32_16x16x32_bf16 a[184:187], v[76:79], v[8:11], a[184:187]\n\t" \
    "v_mfma_f32_16x16x32_bf16 a[188:191], v[76:79], v[12:15], a[188:191]\n\t" \
    "s_waitcnt lgkmcnt(4)\n\t" \
    "v_mfma_f32_16x16x32_bf16 a[192:195], v[64:67], v[0:3], a[192:195]\n\t" \
    "ds_read_b64_tr_b16 v[76:77], v89 offset:32768\n\t" \
    "v_accvgpr_read_b32 v98, a8\n\t" \
    "v_mfma_f32_16x16x32_bf16 a[196:199], v[64:67], v[4:7], a[196:199]\n\t" \
    "ds_read_b64_tr_b16 v[78:79], v89 offset:33792\n\t" \
    "v_accvgpr_read_b32 v99, a9\n\t" \
    "v_mfma_f32_16x16x32_bf16 a[200:203], v[64:67], v[8:11], a[200:203]\n\t" \
    "buffer_load_dwordx4 v[56:59], v80, s[16:19], s34 offen offset:3072 sc1\n\t" \
    "v_accvgpr_read_b32 v100, a10\n\t" \
    "v_mfma_f32_16x16x32_bf16 a[204:207], v[64:67], v[12:15], a[204:207]\n\t" \
    "v_accvgpr_read_b32 v101, a11\n\t" \
    "v_accvgpr_read_b32 v102, a12\n\t" \
    "s_waitcnt lgkmcnt(4)\n\t" \
    "v_mfma_f32_16x16x32_bf16 a[208:211], v[68:71], v[0:3], a[208:211]\n\t" \
    "ds_read_b64_tr_b16 v[64:65], v82 offset:40960\n\t" \
    "v_accvgpr_read_b32 v103, a13\n\t" \
    "v_mfma_f32_16x16x32_bf16 a[212:215], v[68:71], v[4:7], a[212:215]\n\t" \
    "ds_read_b64_tr_b16 v[66:67], v82 offset:41984\n\t" \
    "v_accvgpr_read_b32 v104, a14\n\t" \
    "v_mfma_f32_16x16x32_bf16 a[216:219], v[68:71], v[8:11], a[216:219]\n\t" \
    "v_accvgpr_read_b32 v105, a15\n\t" \
    "v_cvt_pk_bf16_f32 v98, v98, v99\n\t" \
    "v_mfma_f32_16x16x32_bf16 a[220:223], v[68:71], v[12:15], a[220:223]\n\t" \
    "v_cvt_pk_bf16_f32 v99, v100, v101\n\t" \
    "v_cvt_pk_bf16_f32 v100, v102, v103\n\t" \
    "s_waitcnt lgkmcnt(4)\n\t" \
    "v_mfma_f32_16x16x32_bf16 a[224:227], v[72:75], v[0:3], a[224:227]\n\t" \
    "ds_read_b64_tr_b16 v[68:69], v83 offset:40960\n\t" \
    "v_cvt_pk_bf16_f32 v101, v104, v105\n\t" \
    "v_mfma_f32_16x16x32_bf16 a[228:231], v[72:75], v[4:7], a[228:231]\n\t" \
    "ds_read_b64_tr_b16 v[70:71], v83 offset:41984\n\t" \
    "s_add_u32 s36, s23, 8192\n\t" \
    "v_mfma_f32_16x16x32_bf16 a[232:235], v[72:75], v[8:11], a[232:235]\n\t" \
    "buffer_load_dwordx4 v[60:63], v80, s[16:19], s35 offen offset:3072 sc1\n\t" \
    "buffer_store_dwordx4 v[98:101], v110, s[16:19], s36 offen offset:2048 sc1\n\t" \
    "v_mfma_f32_16x16x32_bf16 a[236:239], v[72:75], v[12:15], a[236:239]\n\t" \
    "s_waitcnt lgkmcnt(4)\n\t" \
    "v_mfma_f32_16x16x32_bf16 a[240:243], v[76:79], v[0:3], a[240:243]\n\t" \
    "ds_read_b64_tr_b16 v[72:73], v84 offset:40960\n\t" \
    "v_mfma_f32_16x16x32_bf16 a[244:247], v[76:79], v[4:7], a[244:247]\n\t" \
    "ds_read_b64_tr_b16 v[74:75], v84 offset:41984\n\t" \
    "v_mfma_f32_16x16x32_bf16 a[248:251], v[76:79], v[8:11], a[248:251]\n\t" \
    "v_mfma_f32_16x16x32_bf16 a[252:255], v[76:79], v[12:15], a[252:255]\n\t" \
    "s_waitcnt vmcnt(21) lgkmcnt(4)\n\t" \
    "v_mfma_f32_16x16x32_bf16 a[128:131], v[64:67], v[16:19], a[128:131]\n\t" \
    "ds_read_b64_tr_b16 v[76:77], v85 offset:40960\n\t" \
    "v_accvgpr_read_b32 v116, a16\n\t" \
    "s_waitcnt vmcnt(18)\n\t" \
    "v_mfma_f32_16x16x32_bf16 a[132:135], v[64:67], v[20:23], a[132:135]\n\t" \
    "ds_read_b64_tr_b16 v[78:79], v85 offset:41984\n\t" \
    "v_accvgpr_read_b32 v117, a17\n\t" \
    "s_add_u32 s32, s32, 4096\n\t" \
    "s_add_u32 s33, s33, 4096\n\t" \
    "s_add_u32 s34, s34, 4096\n\t" \
    "s_add_u32 s35, s35, 4096\n\t" \
    "s_waitcnt vmcnt(16)\n\t" \
    "v_mfma_f32_16x16x32_bf16 a[136:139], v[64:67], v[24:27], a[136:139]\n\t" \
    "buffer_load_dwordx4 v[0:3], v80, s[16:19], s32 offen offset:0 sc1\n\t" \
    "s_waitcnt vmcnt(14)\n\t" \
    "v_mfma_f32_16x16x32_bf16 a[140:143], v[64:67], v[28:31], a[140:143]\n\t" \
    "v_accvgpr_read_b32 v118, a18\n\t" \
    "v_accvgpr_read_b32 v119, a19\n\t" \
    "s_waitcnt lgkmcnt(4)\n\t" \
    "v_mfma_f32_16x16x32_bf16 a[144:147], v[68:71], v[16:19], a[144:147]\n\t" \
    "ds_read_b64_tr_b16 v[64:65], v86 offset:40960\n\t" \
    "v_accvgpr_read_b32 v120, a20\n\t" \
    "v_mfma_f32_16x16x32_bf16 a[148:151], v[68:71], v[20:23], a[148:151]\n\t" \
    "ds_read_b64_tr_b16 v[66:67], v86 offset:41984\n\t" \
    "v_accvgpr_read_b32 v121, a21\n\t" \
    "v_mfma_f32_16x16x32_bf16 a[152:155], v[68:71], v[24:27], a[152:155]\n\t" \
    "v_accvgpr_read_b32 v122, a22\n\t" \
    "v_accvgpr_read_b32 v123, a23\n\t" \
    "v_mfma_f32_16x16x32_bf16 a[156:159], v[68:71], v[28:31], a[156:159]\n\t" \
    "v_cvt_pk_bf16_f32 v116, v116, v117\n\t" \
    "v_cvt_pk_bf16_f32 v117, v118, v119\n\t" \
    "s_waitcnt lgkmcnt(4)\n\t" \
    "v_mfma_f32_16x16x32_bf16 a[160:163], v[72:75], v[16:19], a[160:163]\n\t" \
    "ds_read_b64_tr_b16 v[68:69], v87 offset:40960\n\t" \
    "v_cvt_pk_bf16_f32 v118, v120, v121\n\t" \
    "v_mfma_f32_16x16x32_bf16 a[164:167], v[72:75], v[20:23], a[164:167]\n\t" \
    "ds_read_b64_tr_b16 v[70:71], v87 offset:41984\n\t" \
    "v_cvt_pk_bf16_f32 v119, v122, v123\n\t" \
    "v_mfma_f32_16x16x32_bf16 a[168:171], v[72:75], v[24:27], a[168:171]\n\t" \
    "buffer_load_dwordx4 v[4:7], v80, s[16:19], s33 offen offset:0 sc1\n\t" \
    "s_add_u32 s36, s23, 16384\n\t" \
    "v_mfma_f32_16x16x32_bf16 a[172:175], v[72:75], v[28:31], a[172:175]\n\t" \
    "buffer_store_dwordx4 v[116:119], v110, s[16:19], s36 offen offset:2048 sc1\n\t" \
    "s_waitcnt lgkmcnt(4)\n\t" \
    "v_mfma_f32_16x16x32_bf16 a[176:179], v[76:79], v[16:19], a[176:179]\n\t" \
    "ds_read_b64_tr_b16 v[72:73], v88 offset:40960\n\t" \
    "v_mfma_f32_16x16x32_bf16 a[180:183], v[76:79], v[20:23], a[180:183]\n\t" \
    "ds_read_b64_tr_b16 v[74:75], v88 offset:41984\n\t" \
    "v_mfma_f32_16x16x32_bf16 a[184:187], v[76:79], v[24:27], a[184:187]\n\t" \
    "v_mfma_f32_16x16x32_bf16 a[188:191], v[76:79], v[28:31], a[188:191]\n\t" \
    "s_waitcnt lgkmcnt(4)\n\t" \
    "v_mfma_f32_16x16x32_bf16 a[192:195], v[64:67], v[16:19], a[192:195]\n\t" \
    "ds_read_b64_tr_b16 v[76:77], v89 offset:40960\n\t" \
    "v_accvgpr_read_b32 v98, a24\n\t" \
    "v_mfma_f32_16x16x32_bf16 a[196:199], v[64:67], v[20:23], a[196:199]\n\t" \
    "ds_read_b64_tr_b16 v[78:79], v89 offset:41984\n\t" \
    "v_accvgpr_read_b32 v99, a25\n\t" \
    "v_mfma_f32_16x16x32_bf16 a[200:203], v[64:67], v[24:27], a[200:203]\n\t" \
    "buffer_load_dwordx4 v[8:11], v80, s[16:19], s34 offen offset:0 sc1\n\t" \
    "v_accvgpr_read_b32 v100, a26\n\t" \
    "v_mfma_f32_16x16x32_bf16 a[204:207], v[64:67], v[28:31], a[204:207]\n\t" \
    "v_accvgpr_read_b32 v101, a27\n\t" \
    "v_accvgpr_read_b32 v102, a28\n\t" \
    "s_waitcnt vmcnt(48)\n\t" \
    "s_barrier\n\t" \
    "s_waitcnt lgkmcnt(4)\n\t" \
    "v_mfma_f32_16x16x32_bf16 a[208:211], v[68:71], v[16:19], a[208:211]\n\t" \
    "ds_read_b64_tr_b16 v[64:65], v82 offset:49152\n\t" \
    "v_accvgpr_read_b32 v103, a29\n\t" \
    "v_mfma_f32_16x16x32_bf16 a[212:215], v[68:71], v[20:23], a[212:215]\n\t" \
    "ds_read_b64_tr_b16 v[66:67], v82 offset:50176\n\t" \
    "v_accvgpr_read_b32 v104, a30\n\t" \
    "v_mfma_f32_16x16x32_bf16 a[216:219], v[68:71], v[24:27], a[216:219]\n\t" \
    "v_accvgpr_read_b32 v105, a31\n\t" \
    "v_cvt_pk_bf16_f32 v98, v98, v99\n\t" \
    "v_mfma_f32_16x16x32_bf16 a[220:223], v[68:71], v[28:31], a[220:223]\n\t" \
    "v_cvt_pk_bf16_f32 v99, v100, v101\n\t" \
    "v_cvt_pk_bf16_f32 v100, v102, v103\n\t" \
    "s_waitcnt lgkmcnt(4)\n\t" \
    "v_mfma_f32_16x16x32_bf16 a[224:227], v[72:75], v[16:19], a[224:227]\n\t" \
    "ds_read_b64_tr_b16 v[68:69], v83 offset:49152\n\t" \
    "v_cvt_pk_bf16_f32 v101, v104, v105\n\t" \
    "v_mfma_f32_16x16x32_bf16 a[228:231], v[72:75], v[20:23], a[228:231]\n\t" \
    "ds_read_b64_tr_b16 v[70:71], v83 offset:50176\n\t" \
    "s_add_u32 s36, s23, 24576\n\t" \
    "v_mfma_f32_16x16x32_bf16 a[232:235], v[72:75], v[24:27], a[232:235]\n\t" \
    "buffer_load_dwordx4 v[12:15], v80, s[16:19], s35 offen offset:0 sc1\n\t" \
    "buffer_store_dwordx4 v[98:101], v110, s[16:19], s36 offen offset:2048 sc1\n\t" \
    "v_mfma_f32_16x16x32_bf16 a[236:239], v[72:75], v[28:31], a[236:239]\n\t" \
    "s_waitcnt lgkmcnt(4)\n\t" \
    "v_mfma_f32_16x16x32_bf16 a[240:243], v[76:79], v[16:19], a[240:243]\n\t" \
    "ds_read_b64_tr_b16 v[72:73], v84 offset:49152\n\t" \
    "v_mfma_f32_16x16x32_bf16 a[244:247], v[76:79], v[20:23], a[244:247]\n\t" \
    "ds_read_b64_tr_b16 v[74:75], v84 offset:50176\n\t" \
    "v_mfma_f32_16x16x32_bf16 a[248:251], v[76:79], v[24:27], a[248:251]\n\t" \
    "v_mfma_f32_16x16x32_bf16 a[252:255], v[76:79], v[28:31], a[252:255]\n\t" \
    "s_waitcnt vmcnt(17) lgkmcnt(4)\n\t" \
    "v_mfma_f32_16x16x32_bf16 a[128:131], v[64:67], v[32:35], a[128:131]\n\t" \
    "ds_read_b64_tr_b16 v[76:77], v85 offset:49152\n\t" \
    "v_accvgpr_read_b32 v116, a32\n\t" \
    "s_waitcnt vmcnt(15)\n\t" \
    "v_mfma_f32_16x16x32_bf16 a[132:135], v[64:67], v[36:39], a[132:135]\n\t" \
    "ds_read_b64_tr_b16 v[78:79], v85 offset:50176\n\t" \
    "v_accvgpr_read_b32 v117, a33\n\t" \
    "s_waitcnt vmcnt(14)\n\t" \
    "v_mfma_f32_16x16x32_bf16 a[136:139], v[64:67], v[40:43], a[136:139]\n\t" \
    "buffer_load_dwordx4 v[16:19], v80, s[16:19], s32 offen offset:1024 sc1\n\t" \
    "v_accvgpr_read_b32 v118, a34\n\t" \
    "s_waitcnt vmcnt(13)\n\t" \
    "v_mfma_f32_16x16x32_bf16 a[140:143], v[64:67], v[44:47], a[140:143]\n\t" \
    "v_accvgpr_read_b32 v119, a35\n\t" \
    "v_accvgpr_read_b32 v120, a36\n\t" \
    "s_waitcnt lgkmcnt(4)\n\t" \
    "v_mfma_f32_16x16x32_bf16 a[144:147], v[68:71], v[32:35], a[144:147]\n\t" \
    "ds_read_b64_tr_b16 v[64:65], v86 offset:49152\n\t" \
    "v_accvgpr_read_b32 v121, a37\n\t" \
    "v_mfma_f32_16x16x32_bf16 a[148:151], v[68:71], v[36:39], a[148:151]\n\t" \
    "ds_read_b64_tr_b16 v[66:67], v86 offset:50176\n\t" \
    "v_accvgpr_read_b32 v122, a38\n\t" \
    "v_mfma_f32_16x16x32_bf16 a[152:155], v[68:71], v[40:43], a[152:155]\n\t" \
    "v_accvgpr_read_b32 v123, a39\n\t" \
    "v_cvt_pk_bf16_f32 v116, v116, v117\n\t" \
    "v_mfma_f32_16x16x32_bf16 a[156:159], v[68:71], v[44:47], a[156:159]\n\t" \
    "v_cvt_pk_bf16_f32 v117, v118, v119\n\t" \
    "v_cvt_pk_bf16_f32 v118, v120, v121\n\t" \
    "s_waitcnt lgkmcnt(4)\n\t" \
    "v_mfma_f32_16x16x32_bf16 a[160:163], v[72:75], v[32:35], a[160:163]\n\t" \
    "ds_read_b64_tr_b16 v[68:69], v87 offset:49152\n\t" \
    "v_cvt_pk_bf16_f32 v119, v122, v123\n\t" \
    "v_mfma_f32_16x16x32_bf16 a[164:167], v[72:75], v[36:39], a[164:167]\n\t" \
    "ds_read_b64_tr_b16 v[70:71], v87 offset:50176\n\t" \
    "s_add_u32 s36, s23, 0\n\t" \
    "v_mfma_f32_16x16x32_bf16 a[168:171], v[72:75], v[40:43], a[168:171]\n\t" \
    "buffer_load_dwordx4 v[20:23], v80, s[16:19], s33 offen offset:1024 sc1\n\t" \
    "buffer_store_dwordx4 v[116:119], v110, s[16:19], s36 offen offset:2304 sc1\n\t" \
    "v_mfma_f32_16x16x32_bf16 a[172:175], v[72:75], v[44:47], a[172:175]\n\t" \
    "s_waitcnt lgkmcnt(4)\n\t" \
    "v_mfma_f32_16x16x32_bf16 a[176:179], v[76:79], v[32:35], a[176:179]\n\t" \
    "ds_read_b64_tr_b16 v[72:73], v88 offset:49152\n\t" \
    "v_mfma_f32_16x16x32_bf16 a[180:183], v[76:79], v[36:39], a[180:183]\n\t" \
    "ds_read_b64_tr_b16 v[74:75], v88 offset:50176\n\t" \
    "v_mfma_f32_16x16x32_bf16 a[184:187], v[76:79], v[40:43], a[184:187]\n\t" \
    "v_mfma_f32_16x16x32_bf16 a[188:191], v[76:79], v[44:47], a[188:191]\n\t" \
    "s_waitcnt lgkmcnt(4)\n\t" \
    "v_mfma_f32_16x16x32_bf16 a[192:195], v[64:67], v[32:35], a[192:195]\n\t" \
    "ds_read_b64_tr_b16 v[76:77], v89 offset:49152\n\t" \
    "v_accvgpr_read_b32 v98, a40\n\t" \
    "v_mfma_f32_16x16x32_bf16 a[196:199], v[64:67], v[36:39], a[196:199]\n\t" \
    "ds_read_b64_tr_b16 v[78:79], v89 offset:50176\n\t" \
    "v_accvgpr_read_b32 v99, a41\n\t" \
    "v_mfma_f32_16x16x32_bf16 a[200:203], v[64:67], v[40:43], a[200:203]\n\t" \
    "buffer_load_dwordx4 v[24:27], v80, s[16:19], s34 offen offset:1024 sc1\n\t" \
    "v_accvgpr_read_b32 v100, a42\n\t" \
    "v_mfma_f32_16x16x32_bf16 a[204:207], v[64:67], v[44:47], a[204:207]\n\t" \
    "v_accvgpr_read_b32 v101, a43\n\t" \
    "v_accvgpr_read_b32 v102, a44\n\t" \
    "s_waitcnt lgkmcnt(4)\n\t" \
    "v_mfma_f32_16x16x32_bf16 a[208:211], v[68:71], v[32:35], a[208:211]\n\t" \
    "ds_read_b64_tr_b16 v[64:65], v82 offset:57344\n\t" \
    "v_accvgpr_read_b32 v103, a45\n\t" \
    "v_mfma_f32_16x16x32_bf16 a[212:215], v[68:71], v[36:39], a[212:215]\n\t" \
    "ds_read_b64_tr_b16 v[66:67], v82 offset:58368\n\t" \
    "v_accvgpr_read_b32 v104, a46\n\t" \
    "v_mfma_f32_16x16x32_bf16 a[216:219], v[68:71], v[40:43], a[216:219]\n\t" \
    "v_accvgpr_read_b32 v105, a47\n\t" \
    "v_cvt_pk_bf16_f32 v98, v98, v99\n\t" \
    "v_mfma_f32_16x16x32_bf16 a[220:223], v[68:71], v[44:47], a[220:223]\n\t" \
    "v_cvt_pk_bf16_f32 v99, v100, v101\n\t" \
    "v_cvt_pk_bf16_f32 v100, v102, v103\n\t" \
    "s_waitcnt lgkmcnt(4)\n\t" \
    "v_mfma_f32_16x16x32_bf16 a[224:227], v[72:75], v[32:35], a[224:227]\n\t" \
    "ds_read_b64_tr_b16 v[68:69], v83 offset:57344\n\t" \
    "v_cvt_pk_bf16_f32 v101, v104, v105\n\t" \
    "v_mfma_f32_16x16x32_bf16 a[228:231], v[72:75], v[36:39], a[228:231]\n\t" \
    "ds_read_b64_tr_b16 v[70:71], v83 offset:58368\n\t" \
    "s_add_u32 s36, s23, 8192\n\t" \
    "v_mfma_f32_16x16x32_bf16 a[232:235], v[72:75], v[40:43], a[232:235]\n\t" \
    "buffer_load_dwordx4 v[28:31], v80, s[16:19], s35 offen offset:1024 sc1\n\t" \
    "buffer_store_dwordx4 v[98:101], v110, s[16:19], s36 offen offset:2304 sc1\n\t" \
    "v_mfma_f32_16x16x32_bf16 a[236:239], v[72:75], v[44:47], a[236:239]\n\t" \
    "s_waitcnt lgkmcnt(4)\n\t" \
    "v_mfma_f32_16x16x32_bf16 a[240:243], v[76:79], v[32:35], a[240:243]\n\t" \
    "ds_read_b64_tr_b16 v[72:73], v84 offset:57344\n\t" \
    "v_mfma_f32_16x16x32_bf16 a[244:247], v[76:79], v[36:39], a[244:247]\n\t" \
    "ds_read_b64_tr_b16 v[74:75], v84 offset:58368\n\t" \
    "v_mfma_f32_16x16x32_bf16 a[248:251], v[76:79], v[40:43], a[248:251]\n\t" \
    "v_mfma_f32_16x16x32_bf16 a[252:255], v[76:79], v[44:47], a[252:255]\n\t" \
    "s_waitcnt vmcnt(17) lgkmcnt(4)\n\t" \
    "v_mfma_f32_16x16x32_bf16 a[128:131], v[64:67], v[48:51], a[128:131]\n\t" \
    "ds_read_b64_tr_b16 v[76:77], v85 offset:57344\n\t" \
    "v_accvgpr_read_b32 v116, a48\n\t" \
    "s_waitcnt vmcnt(16)\n\t" \
    "v_mfma_f32_16x16x32_bf16 a[132:135], v[64:67], v[52:55], a[132:135]\n\t" \
    "ds_read_b64_tr_b16 v[78:79], v85 offset:58368\n\t" \
    "v_accvgpr_read_b32 v117, a49\n\t" \
    "s_waitcnt vmcnt(14)\n\t" \
    "v_mfma_f32_16x16x32_bf16 a[136:139], v[64:67], v[56:59], a[136:139]\n\t" \
    "buffer_load_dwordx4 v[32:35], v80, s[16:19], s32 offen offset:2048 sc1\n\t" \
    "v_accvgpr_read_b32 v118, a50\n\t" \
    "s_waitcnt vmcnt(14)\n\t" \
    "v_mfma_f32_16x16x32_bf16 a[140:143], v[64:67], v[60:63], a[140:143]\n\t" \
    "v_accvgpr_read_b32 v119, a51\n\t" \
    "v_accvgpr_read_b32 v120, a52\n\t" \
    "s_waitcnt lgkmcnt(4)\n\t" \
    "v_mfma_f32_16x16x32_bf16 a[144:147], v[68:71], v[48:51], a[144:147]\n\t" \
    "ds_read_b64_tr_b16 v[64:65], v86 offset:57344\n\t" \
    "v_accvgpr_read_b32 v121, a53\n\t" \
    "v_mfma_f32_16x16x32_bf16 a[148:151], v[68:71], v[52:55], a[148:151]\n\t" \
    "ds_read_b64_tr_b16 v[66:67], v86 offset:58368\n\t" \
    "v_accvgpr_read_b32 v122, a54\n\t" \
    "v_mfma_f32_16x16x32_bf16 a[152:155], v[68:71], v[56:59], a[152:155]\n\t" \
    "v_accvgpr_read_b32 v123, a55\n\t" \
    "v_cvt_pk_bf16_f32 v116, v116, v117\n\t" \
    "v_mfma_f32_16x16x32_bf16 a[156:159], v[68:71], v[60:63], a[156:159]\n\t" \
    "v_cvt_pk_bf16_f32 v117, v118, v119\n\t" \
    "v_cvt_pk_bf16_f32 v118, v120, v121\n\t" \
    "s_waitcnt lgkmcnt(4)\n\t" \
    "v_mfma_f32_16x16x32_bf16 a[160:163], v[72:75], v[48:51], a[160:163]\n\t" \
    "ds_read_b64_tr_b16 v[68:69], v87 offset:57344\n\t" \
    "v_cvt_pk_bf16_f32 v119, v122, v123\n\t" \
    "v_mfma_f32_16x16x32_bf16 a[164:167], v[72:75], v[52:55], a[164:167]\n\t" \
    "ds_read_b64_tr_b16 v[70:71], v87 offset:58368\n\t" \
    "s_add_u32 s36, s23, 16384\n\t" \
    "v_mfma_f32_16x16x32_bf16 a[168:171], v[72:75], v[56:59], a[168:171]\n\t" \
    "buffer_load_dwordx4 v[36:39], v80, s[16:19], s33 offen offset:2048 sc1\n\t" \
    "buffer_store_dwordx4 v[116:119], v110, s[16:19], s36 offen offset:2304 sc1\n\t" \
    "v_mfma_f32_16x16x32_bf16 a[172:175], v[72:75], v[60:63], a[172:175]\n\t" \
    "s_waitcnt lgkmcnt(4)\n\t" \
    "v_mfma_f32_16x16x32_bf16 a[176:179], v[76:79], v[48:51], a[176:179]\n\t" \
    "ds_read_b64_tr_b16 v[72:73], v88 offset:57344\n\t" \
    "v_mfma_f32_16x16x32_bf16 a[180:183], v[76:79], v[52:55], a[180:183]\n\t" \
    "ds_read_b64_tr_b16 v[74:75], v88 offset:58368\n\t" \
    "v_mfma_f32_16x16x32_bf16 a[184:187], v[76:79], v[56:59], a[184:187]\n\t" \
    "v_mfma_f32_16x16x32_bf16 a[188:191], v[76:79], v[60:63], a[188:191]\n\t" \
    "s_waitcnt lgkmcnt(4)\n\t" \
    "v_mfma_f32_16x16x32_bf16 a[192:195], v[64:67], v[48:51], a[192:195]\n\t" \
    "ds_read_b64_tr_b16 v[76:77], v89 offset:57344\n\t" \
    "v_accvgpr_read_b32 v98, a56\n\t" \
    "v_mfma_f32_16x16x32_bf16 a[196:199], v[64:67], v[52:55], a[196:199]\n\t" \
    "ds_read_b64_tr_b16 v[78:79], v89 offset:58368\n\t" \
    "v_accvgpr_read_b32 v99, a57\n\t" \
    "v_mfma_f32_16x16x32_bf16 a[200:203], v[64:67], v[56:59], a[200:203]\n\t" \
    "buffer_load_dwordx4 v[40:43], v80, s[16:19], s34 offen offset:2048 sc1\n\t" \
    "v_accvgpr_read_b32 v100, a58\n\t" \
    "v_mfma_f32_16x16x32_bf16 a[204:207], v[64:67], v[60:63], a[204:207]\n\t" \
    "v_accvgpr_read_b32 v101, a59\n\t" \
    "v_accvgpr_read_b32 v102, a60\n\t" \
    "s_waitcnt vmcnt(44)\n\t" \
    "s_barrier\n\t" \
    "s_waitcnt lgkmcnt(4)\n\t" \
    "v_mfma_f32_16x16x32_bf16 a[208:211], v[68:71], v[48:51], a[208:211]\n\t" \
    "ds_read_b64_tr_b16 v[64:65], v90 offset:0\n\t" \
    "v_accvgpr_read_b32 v103, a61\n\t" \
    "v_mfma_f32_16x16x32_bf16 a[212:215], v[68:71], v[52:55], a[212:215]\n\t" \
    "ds_read_b64_tr_b16 v[66:67], v90 offset:1024\n\t" \
    "v_accvgpr_read_b32 v104, a62\n\t" \
    "v_mfma_f32_16x16x32_bf16 a[216:219], v[68:71], v[56:59], a[216:219]\n\t" \
    "v_accvgpr_read_b32 v105, a63\n\t" \
    "v_cvt_pk_bf16_f32 v98, v98, v99\n\t" \
    "v_mfma_f32_16x16x32_bf16 a[220:223], v[68:71], v[60:63], a[220:223]\n\t" \
    "v_cvt_pk_bf16_f32 v99, v100, v101\n\t" \
    "v_cvt_pk_bf16_f32 v100, v102, v103\n\t" \
    "s_waitcnt lgkmcnt(4)\n\t" \
    "v_mfma_f32_16x16x32_bf16 a[224:227], v[72:75], v[48:51], a[224:227]\n\t" \
    "ds_read_b64_tr_b16 v[68:69], v91 offset:0\n\t" \
    "v_cvt_pk_bf16_f32 v101, v104, v105\n\t" \
    "v_mfma_f32_16x16x32_bf16 a[228:231], v[72:75], v[52:55], a[228:231]\n\t" \
    "ds_read_b64_tr_b16 v[70:71], v91 offset:1024\n\t" \
    "s_add_u32 s36, s23, 24576\n\t" \
    "v_mfma_f32_16x16x32_bf16 a[232:235], v[72:75], v[56:59], a[232:235]\n\t" \
    "buffer_load_dwordx4 v[44:47], v80, s[16:19], s35 offen offset:2048 sc1\n\t" \
    "buffer_store_dwordx4 v[98:101], v110, s[16:19], s36 offen offset:2304 sc1\n\t" \
    "v_mfma_f32_16x16x32_bf16 a[236:239], v[72:75], v[60:63], a[236:239]\n\t" \
    "s_waitcnt lgkmcnt(4)\n\t" \
    "v_mfma_f32_16x16x32_bf16 a[240:243], v[76:79], v[48:51], a[240:243]\n\t" \
    "ds_read_b64_tr_b16 v[72:73], v92 offset:0\n\t" \
    "v_mfma_f32_16x16x32_bf16 a[244:247], v[76:79], v[52:55], a[244:247]\n\t" \
    "ds_read_b64_tr_b16 v[74:75], v92 offset:1024\n\t" \
    "v_mfma_f32_16x16x32_bf16 a[248:251], v[76:79], v[56:59], a[248:251]\n\t" \
    "v_mfma_f32_16x16x32_bf16 a[252:255], v[76:79], v[60:63], a[252:255]\n\t" \
    "s_waitcnt vmcnt(17) lgkmcnt(4)\n\t" \
    "v_mfma_f32_16x16x32_bf16 a[128:131], v[64:67], v[0:3], a[128:131]\n\t" \
    "ds_read_b64_tr_b16 v[76:77], v93 offset:0\n\t" \
    "v_accvgpr_read_b32 v116, a64\n\t" \
    "s_waitcnt vmcnt(16)\n\t" \
    "v_mfma_f32_16x16x32_bf16 a[132:135], v[64:67], v[4:7], a[132:135]\n\t" \
    "ds_read_b64_tr_b16 v[78:79], v93 offset:1024\n\t" \
    "v_accvgpr_read_b32 v117, a65\n\t" \
    "s_waitcnt vmcnt(14)\n\t" \
    "v_mfma_f32_16x16x32_bf16 a[136:139], v[64:67], v[8:11], a[136:139]\n\t" \
    "buffer_load_dwordx4 v[48:51], v80, s[16:19], s32 offen offset:3072 sc1\n\t" \
    "v_accvgpr_read_b32 v118, a66\n\t" \
    "s_waitcnt vmcnt(14)\n\t" \
    "v_mfma_f32_16x16x32_bf16 a[140:143], v[64:67], v[12:15], a[140:143]\n\t" \
    "v_accvgpr_read_b32 v119, a67\n\t" \
    "v_accvgpr_read_b32 v120, a68\n\t" \
    "s_waitcnt lgkmcnt(4)\n\t" \
    "v_mfma_f32_16x16x32_bf16 a[144:147], v[68:71], v[0:3], a[144:147]\n\t" \
    "ds_read_b64_tr_b16 v[64:65], v94 offset:0\n\t" \
    "v_accvgpr_read_b32 v121, a69\n\t" \
    "v_mfma_f32_16x16x32_bf16 a[148:151], v[68:71], v[4:7], a[148:151]\n\t" \
    "ds_read_b64_tr_b16 v[66:67], v94 offset:1024\n\t" \
    "v_accvgpr_read_b32 v122, a70\n\t" \
    "v_mfma_f32_16x16x32_bf16 a[152:155], v[68:71], v[8:11], a[152:155]\n\t" \
    "v_accvgpr_read_b32 v123, a71\n\t" \
    "v_cvt_pk_bf16_f32 v116, v116, v117\n\t" \
    "v_mfma_f32_16x16x32_bf16 a[156:159], v[68:71], v[12:15], a[156:159]\n\t" \
    "v_cvt_pk_bf16_f32 v117, v118, v119\n\t" \
    "v_cvt_pk_bf16_f32 v118, v120, v121\n\t" \
    "s_waitcnt lgkmcnt(4)\n\t" \
    "v_mfma_f32_16x16x32_bf16 a[160:163], v[72:75], v[0:3], a[160:163]\n\t" \
    "ds_read_b64_tr_b16 v[68:69], v95 offset:0\n\t" \
    "v_cvt_pk_bf16_f32 v119, v122, v123\n\t" \
    "v_mfma_f32_16x16x32_bf16 a[164:167], v[72:75], v[4:7], a[164:167]\n\t" \
    "ds_read_b64_tr_b16 v[70:71], v95 offset:1024\n\t" \
    "s_add_u32 s36, s23, 0\n\t" \
    "v_mfma_f32_16x16x32_bf16 a[168:171], v[72:75], v[8:11], a[168:171]\n\t" \
    "buffer_load_dwordx4 v[52:55], v80, s[16:19], s33 offen offset:3072 sc1\n\t" \
    "buffer_store_dwordx4 v[116:119], v110, s[16:19], s36 offen offset:3072 sc1\n\t" \
    "v_mfma_f32_16x16x32_bf16 a[172:175], v[72:75], v[12:15], a[172:175]\n\t" \
    "s_waitcnt lgkmcnt(4)\n\t" \
    "v_mfma_f32_16x16x32_bf16 a[176:179], v[76:79], v[0:3], a[176:179]\n\t" \
    "ds_read_b64_tr_b16 v[72:73], v96 offset:0\n\t" \
    "v_mfma_f32_16x16x32_bf16 a[180:183], v[76:79], v[4:7], a[180:183]\n\t" \
    "ds_read_b64_tr_b16 v[74:75], v96 offset:1024\n\t" \
    "v_mfma_f32_16x16x32_bf16 a[184:187], v[76:79], v[8:11], a[184:187]\n\t" \
    "v_mfma_f32_16x16x32_bf16 a[188:191], v[76:79], v[12:15], a[188:191]\n\t" \
    "s_waitcnt lgkmcnt(4)\n\t" \
    "v_mfma_f32_16x16x32_bf16 a[192:195], v[64:67], v[0:3], a[192:195]\n\t" \
    "ds_read_b64_tr_b16 v[76:77], v97 offset:0\n\t" \
    "v_accvgpr_read_b32 v98, a72\n\t" \
    "v_mfma_f32_16x16x32_bf16 a[196:199], v[64:67], v[4:7], a[196:199]\n\t" \
    "ds_read_b64_tr_b16 v[78:79], v97 offset:1024\n\t" \
    "v_accvgpr_read_b32 v99, a73\n\t" \
    "v_mfma_f32_16x16x32_bf16 a[200:203], v[64:67], v[8:11], a[200:203]\n\t" \
    "buffer_load_dwordx4 v[56:59], v80, s[16:19], s34 offen offset:3072 sc1\n\t" \
    "v_accvgpr_read_b32 v100, a74\n\t" \
    "v_mfma_f32_16x16x32_bf16 a[204:207], v[64:67], v[12:15], a[204:207]\n\t" \
    "v_accvgpr_read_b32 v101, a75\n\t" \
    "v_accvgpr_read_b32 v102, a76\n\t" \
    "s_waitcnt lgkmcnt(4)\n\t" \
    "v_mfma_f32_16x16x32_bf16 a[208:211], v[68:71], v[0:3], a[208:211]\n\t" \
    "ds_read_b64_tr_b16 v[64:65], v90 offset:8192\n\t" \
    "v_accvgpr_read_b32 v103, a77\n\t" \
    "v_mfma_f32_16x16x32_bf16 a[212:215], v[68:71], v[4:7], a[212:215]\n\t" \
    "ds_read_b64_tr_b16 v[66:67], v90 offset:9216\n\t" \
    "v_accvgpr_read_b32 v104, a78\n\t" \
    "v_mfma_f32_16x16x32_bf16 a[216:219], v[68:71], v[8:11], a[216:219]\n\t" \
    "v_accvgpr_read_b32 v105, a79\n\t" \
    "v_cvt_pk_bf16_f32 v98, v98, v99\n\t" \
    "v_mfma_f32_16x16x32_bf16 a[220:223], v[68:71], v[12:15], a[220:223]\n\t" \
    "v_cvt_pk_bf16_f32 v99, v100, v101\n\t" \
    "v_cvt_pk_bf16_f32 v100, v102, v103\n\t" \
    "s_waitcnt lgkmcnt(4)\n\t" \
    "v_mfma_f32_16x16x32_bf16 a[224:227], v[72:75], v[0:3], a[224:227]\n\t" \
    "ds_read_b64_tr_b16 v[68:69], v91 offset:8192\n\t" \
    "v_cvt_pk_bf16_f32 v101, v104, v105\n\t" \
    "v_mfma_f32_16x16x32_bf16 a[228:231], v[72:75], v[4:7], a[228:231]\n\t" \
    "ds_read_b64_tr_b16 v[70:71], v91 offset:9216\n\t" \
    "s_add_u32 s36, s23, 8192\n\t" \
    "v_mfma_f32_16x16x32_bf16 a[232:235], v[72:75], v[8:11], a[232:235]\n\t" \
    "buffer_load_dwordx4 v[60:63], v80, s[16:19], s35 offen offset:3072 sc1\n\t" \
    "buffer_store_dwordx4 v[98:101], v110, s[16:19], s36 offen offset:3072 sc1\n\t" \
    "v_mfma_f32_16x16x32_bf16 a[236:239], v[72:75], v[12:15], a[236:239]\n\t" \
    "s_waitcnt lgkmcnt(4)\n\t" \
    "v_mfma_f32_16x16x32_bf16 a[240:243], v[76:79], v[0:3], a[240:243]\n\t" \
    "ds_read_b64_tr_b16 v[72:73], v92 offset:8192\n\t" \
    "v_mfma_f32_16x16x32_bf16 a[244:247], v[76:79], v[4:7], a[244:247]\n\t" \
    "ds_read_b64_tr_b16 v[74:75], v92 offset:9216\n\t" \
    "v_mfma_f32_16x16x32_bf16 a[248:251], v[76:79], v[8:11], a[248:251]\n\t" \
    "v_mfma_f32_16x16x32_bf16 a[252:255], v[76:79], v[12:15], a[252:255]\n\t" \
    "s_waitcnt vmcnt(17) lgkmcnt(4)\n\t" \
    "v_mfma_f32_16x16x32_bf16 a[128:131], v[64:67], v[16:19], a[128:131]\n\t" \
    "ds_read_b64_tr_b16 v[76:77], v93 offset:8192\n\t" \
    "v_accvgpr_read_b32 v116, a80\n\t" \
    "s_waitcnt vmcnt(16)\n\t" \
    "v_mfma_f32_16x16x32_bf16 a[132:135], v[64:67], v[20:23], a[132:135]\n\t" \
    "ds_read_b64_tr_b16 v[78:79], v93 offset:9216\n\t" \
    "v_accvgpr_read_b32 v117, a81\n\t" \
    "s_waitcnt vmcnt(14)\n\t" \
    "v_mfma_f32_16x16x32_bf16 a[136:139], v[64:67], v[24:27], a[136:139]\n\t" \
    "v_accvgpr_read_b32 v118, a82\n\t" \
    "v_accvgpr_read_b32 v119, a83\n\t" \
    "s_waitcnt vmcnt(13)\n\t" \
    "v_mfma_f32_16x16x32_bf16 a[140:143], v[64:67], v[28:31], a[140:143]\n\t" \
    "v_accvgpr_read_b32 v120, a84\n\t" \
    "v_accvgpr_read_b32 v121, a85\n\t" \
    "s_waitcnt lgkmcnt(4)\n\t" \
    "v_mfma_f32_16x16x32_bf16 a[144:147], v[68:71], v[16:19], a[144:147]\n\t" \
    "ds_read_b64_tr_b16 v[64:65], v94 offset:8192\n\t" \
    "v_accvgpr_read_b32 v122, a86\n\t" \
    "v_mfma_f32_16x16x32_bf16 a[148:151], v[68:71], v[20:23], a[148:151]\n\t" \
    "ds_read_b64_tr_b16 v[66:67], v94 offset:9216\n\t" \
    "v_accvgpr_read_b32 v123, a87\n\t" \
    "v_mfma_f32_16x16x32_bf16 a[152:155], v[68:71], v[24:27], a[152:155]\n\t" \
    "v_cvt_pk_bf16_f32 v116, v116, v117\n\t" \
    "v_cvt_pk_bf16_f32 v117, v118, v119\n\t" \
    "v_mfma_f32_16x16x32_bf16 a[156:159], v[68:71], v[28:31], a[156:159]\n\t" \
    "v_cvt_pk_bf16_f32 v118, v120, v121\n\t" \
    "v_cvt_pk_bf16_f32 v119, v122, v123\n\t" \
    "s_waitcnt lgkmcnt(4)\n\t" \
    "v_mfma_f32_16x16x32_bf16 a[160:163], v[72:75], v[16:19], a[160:163]\n\t" \
    "ds_read_b64_tr_b16 v[68:69], v95 offset:8192\n\t" \
    "s_add_u32 s36, s23, 16384\n\t" \
    "v_mfma_f32_16x16x32_bf16 a[164:167], v[72:75], v[20:23], a[164:167]\n\t" \
    "ds_read_b64_tr_b16 v[70:71], v95 offset:9216\n\t" \
    "buffer_store_dwordx4 v[116:119], v110, s[16:19], s36 offen offset:3072 sc1\n\t" \
    "v_mfma_f32_16x16x32_bf16 a[168:171], v[72:75], v[24:27], a[168:171]\n\t" \
    "v_mfma_f32_16x16x32_bf16 a[172:175], v[72:75], v[28:31], a[172:175]\n\t" \
    "s_waitcnt lgkmcnt(4)\n\t" \
    "v_mfma_f32_16x16x32_bf16 a[176:179], v[76:79], v[16:19], a[176:179]\n\t" \
    "ds_read_b64_tr_b16 v[72:73], v96 offset:8192\n\t" \
    "v_mfma_f32_16x16x32_bf16 a[180:183], v[76:79], v[20:23], a[180:183]\n\t" \
    "ds_read_b64_tr_b16 v[74:75], v96 offset:9216\n\t" \
    "v_mfma_f32_16x16x32_bf16 a[184:187], v[76:79], v[24:27], a[184:187]\n\t" \
    "v_mfma_f32_16x16x32_bf16 a[188:191], v[76:79], v[28:31], a[188:191]\n\t" \
    "s_waitcnt lgkmcnt(4)\n\t" \
    "v_mfma_f32_16x16x32_bf16 a[192:195], v[64:67], v[16:19], a[192:195]\n\t" \
    "ds_read_b64_tr_b16 v[76:77], v97 offset:8192\n\t" \
    "v_accvgpr_read_b32 v98, a88\n\t" \
    "v_mfma_f32_16x16x32_bf16 a[196:199], v[64:67], v[20:23], a[196:199]\n\t" \
    "ds_read_b64_tr_b16 v[78:79], v97 offset:9216\n\t" \
    "v_accvgpr_read_b32 v99, a89\n\t" \
    "v_mfma_f32_16x16x32_bf16 a[200:203], v[64:67], v[24:27], a[200:203]\n\t" \
    "v_accvgpr_read_b32 v100, a90\n\t" \
    "v_accvgpr_read_b32 v101, a91\n\t" \
    "v_mfma_f32_16x16x32_bf16 a[204:207], v[64:67], v[28:31], a[204:207]\n\t" \
    "v_accvgpr_read_b32 v102, a92\n\t" \
    "v_accvgpr_read_b32 v103, a93\n\t" \
    "s_waitcnt vmcnt(37)\n\t" \
    "s_barrier\n\t" \
    "s_waitcnt lgkmcnt(4)\n\t" \
    "v_mfma_f32_16x16x32_bf16 a[208:211], v[68:71], v[16:19], a[208:211]\n\t" \
    "ds_read_b64_tr_b16 v[64:65], v90 offset:16384\n\t" \
    "v_accvgpr_read_b32 v104, a94\n\t" \
    "v_mfma_f32_16x16x32_bf16 a[212:215], v[68:71], v[20:23], a[212:215]\n\t" \
    "ds_read_b64_tr_b16 v[66:67], v90 offset:17408\n\t" \
    "v_accvgpr_read_b32 v105, a95\n\t" \
    "v_mfma_f32_16x16x32_bf16 a[216:219], v[68:71], v[24:27], a[216:219]\n\t" \
    "v_cvt_pk_bf16_f32 v98, v98, v99\n\t" \
    "v_cvt_pk_bf16_f32 v99, v100, v101\n\t" \
    "v_mfma_f32_16x16x32_bf16 a[220:223], v[68:71], v[28:31], a[220:223]\n\t" \
    "v_cvt_pk_bf16_f32 v100, v102, v103\n\t" \
    "v_cvt_pk_bf16_f32 v101, v104, v105\n\t" \
    "s_waitcnt lgkmcnt(4)\n\t" \
    "v_mfma_f32_16x16x32_bf16 a[224:227], v[72:75], v[16:19], a[224:227]\n\t" \
    "ds_read_b64_tr_b16 v[68:69], v91 offset:16384\n\t" \
    "s_add_u32 s36, s23, 24576\n\t" \
    "v_mfma_f32_16x16x32_bf16 a[228:231], v[72:75], v[20:23], a[228:231]\n\t" \
    "ds_read_b64_tr_b16 v[70:71], v91 offset:17408\n\t" \
    "buffer_store_dwordx4 v[98:101], v110, s[16:19], s36 offen offset:3072 sc1\n\t" \
    "v_mfma_f32_16x16x32_bf16 a[232:235], v[72:75], v[24:27], a[232:235]\n\t" \
    "v_mfma_f32_16x16x32_bf16 a[236:239], v[72:75], v[28:31], a[236:239]\n\t" \
    "s_waitcnt lgkmcnt(4)\n\t" \
    "v_mfma_f32_16x16x32_bf16 a[240:243], v[76:79], v[16:19], a[240:243]\n\t" \
    "ds_read_b64_tr_b16 v[72:73], v92 offset:16384\n\t" \
    "v_mfma_f32_16x16x32_bf16 a[244:247], v[76:79], v[20:23], a[244:247]\n\t" \
    "ds_read_b64_tr_b16 v[74:75], v92 offset:17408\n\t" \
    "v_mfma_f32_16x16x32_bf16 a[248:251], v[76:79], v[24:27], a[248:251]\n\t" \
    "v_mfma_f32_16x16x32_bf16 a[252:255], v[76:79], v[28:31], a[252:255]\n\t" \
    "s_waitcnt vmcnt(13) lgkmcnt(4)\n\t" \
    "v_mfma_f32_16x16x32_bf16 a[128:131], v[64:67], v[32:35], a[128:131]\n\t" \
    "ds_read_b64_tr_b16 v[76:77], v93 offset:16384\n\t" \
    "v_accvgpr_read_b32 v116, a96\n\t" \
    "s_waitcnt vmcnt(12)\n\t" \
    "v_mfma_f32_16x16x32_bf16 a[132:135], v[64:67], v[36:39], a[132:135]\n\t" \
    "ds_read_b64_tr_b16 v[78:79], v93 offset:17408\n\t" \
    "v_accvgpr_read_b32 v117, a97\n\t" \
    "s_waitcnt vmcnt(10)\n\t" \
    "v_mfma_f32_16x16x32_bf16 a[136:139], v[64:67], v[40:43], a[136:139]\n\t" \
    "v_accvgpr_read_b32 v118, a98\n\t" \
    "v_accvgpr_read_b32 v119, a99\n\t" \
    "s_waitcnt vmcnt(9)\n\t" \
    "v_mfma_f32_16x16x32_bf16 a[140:143], v[64:67], v[44:47], a[140:143]\n\t" \
    "v_accvgpr_read_b32 v120, a100\n\t" \
    "v_accvgpr_read_b32 v121, a101\n\t" \
    "s_waitcnt lgkmcnt(4)\n\t" \
    "v_mfma_f32_16x16x32_bf16 a[144:147], v[68:71], v[32:35], a[144:147]\n\t" \
    "ds_read_b64_tr_b16 v[64:65], v94 offset:16384\n\t" \
    "v_accvgpr_read_b32 v122, a102\n\t" \
    "v_mfma_f32_16x16x32_bf16 a[148:151], v[68:71], v[36:39], a[148:151]\n\t" \
    "ds_read_b64_tr_b16 v[66:67], v94 offset:17408\n\t" \
    "v_accvgpr_read_b32 v123, a103\n\t" \
    "v_mfma_f32_16x16x32_bf16 a[152:155], v[68:71], v[40:43], a[152:155]\n\t" \
    "v_cvt_pk_bf16_f32 v116, v116, v117\n\t" \
    "v_cvt_pk_bf16_f32 v117, v118, v119\n\t" \
    "v_mfma_f32_16x16x32_bf16 a[156:159], v[68:71], v[44:47], a[156:159]\n\t" \
    "v_cvt_pk_bf16_f32 v118, v120, v121\n\t" \
    "v_cvt_pk_bf16_f32 v119, v122, v123\n\t" \
    "s_waitcnt lgkmcnt(4)\n\t" \
    "v_mfma_f32_16x16x32_bf16 a[160:163], v[72:75], v[32:35], a[160:163]\n\t" \
    "ds_read_b64_tr_b16 v[68:69], v95 offset:16384\n\t" \
    "s_add_u32 s36, s23, 0\n\t" \
    "v_mfma_f32_16x16x32_bf16 a[164:167], v[72:75], v[36:39], a[164:167]\n\t" \
    "ds_read_b64_tr_b16 v[70:71], v95 offset:17408\n\t" \
    "buffer_store_dwordx4 v[116:119], v110, s[16:19], s36 offen offset:3328 sc1\n\t" \
    "v_mfma_f32_16x16x32_bf16 a[168:171], v[72:75], v[40:43], a[168:171]\n\t" \
    "v_mfma_f32_16x16x32_bf16 a[172:175], v[72:75], v[44:47], a[172:175]\n\t" \
    "s_waitcnt lgkmcnt(4)\n\t" \
    "v_mfma_f32_16x16x32_bf16 a[176:179], v[76:79], v[32:35], a[176:179]\n\t" \
    "ds_read_b64_tr_b16 v[72:73], v96 offset:16384\n\t" \
    "v_mfma_f32_16x16x32_bf16 a[180:183], v[76:79], v[36:39], a[180:183]\n\t" \
    "ds_read_b64_tr_b16 v[74:75], v96 offset:17408\n\t" \
    "v_mfma_f32_16x16x32_bf16 a[184:187], v[76:79], v[40:43], a[184:187]\n\t" \
    "v_mfma_f32_16x16x32_bf16 a[188:191], v[76:79], v[44:47], a[188:191]\n\t" \
    "s_waitcnt lgkmcnt(4)\n\t" \
    "v_mfma_f32_16x16x32_bf16 a[192:195], v[64:67], v[32:35], a[192:195]\n\t" \
    "ds_read_b64_tr_b16 v[76:77], v97 offset:16384\n\t" \
    "v_accvgpr_read_b32 v98, a104\n\t" \
    "v_mfma_f32_16x16x32_bf16 a[196:199], v[64:67], v[36:39], a[196:199]\n\t" \
    "ds_read_b64_tr_b16 v[78:79], v97 offset:17408\n\t" \
    "v_accvgpr_read_b32 v99, a105\n\t" \
    "v_mfma_f32_16x16x32_bf16 a[200:203], v[64:67], v[40:43], a[200:203]\n\t" \
    "v_accvgpr_read_b32 v100, a106\n\t" \
    "v_accvgpr_read_b32 v101, a107\n\t" \
    "v_mfma_f32_16x16x32_bf16 a[204:207], v[64:67], v[44:47], a[204:207]\n\t" \
    "v_accvgpr_read_b32 v102, a108\n\t" \
    "v_accvgpr_read_b32 v103, a109\n\t" \
    "s_waitcnt lgkmcnt(4)\n\t" \
    "v_mfma_f32_16x16x32_bf16 a[208:211], v[68:71], v[32:35], a[208:211]\n\t" \
    "ds_read_b64_tr_b16 v[64:65], v90 offset:24576\n\t" \
    "v_accvgpr_read_b32 v104, a110\n\t" \
    "v_mfma_f32_16x16x32_bf16 a[212:215], v[68:71], v[36:39], a[212:215]\n\t" \
    "ds_read_b64_tr_b16 v[66:67], v90 offset:25600\n\t" \
    "v_accvgpr_read_b32 v105, a111\n\t" \
    "v_mfma_f32_16x16x32_bf16 a[216:219], v[68:71], v[40:43], a[216:219]\n\t" \
    "v_cvt_pk_bf16_f32 v98, v98, v99\n\t" \
    "v_cvt_pk_bf16_f32 v99, v100, v101\n\t" \
    "v_mfma_f32_16x16x32_bf16 a[220:223], v[68:71], v[44:47], a[220:223]\n\t" \
    "v_cvt_pk_bf16_f32 v100, v102, v103\n\t" \
    "v_cvt_pk_bf16_f32 v101, v104, v105\n\t" \
    "s_waitcnt lgkmcnt(4)\n\t" \
    "v_mfma_f32_16x16x32_bf16 a[224:227], v[72:75], v[32:35], a[224:227]\n\t" \
    "ds_read_b64_tr_b16 v[68:69], v91 offset:24576\n\t" \
    "s_add_u32 s36, s23, 8192\n\t" \
    "v_mfma_f32_16x16x32_bf16 a[228:231], v[72:75], v[36:39], a[228:231]\n\t" \
    "ds_read_b64_tr_b16 v[70:71], v91 offset:25600\n\t" \
    "buffer_store_dwordx4 v[98:101], v110, s[16:19], s36 offen offset:3328 sc1\n\t" \
    "v_mfma_f32_16x16x32_bf16 a[232:235], v[72:75], v[40:43], a[232:235]\n\t" \
    "v_mfma_f32_16x16x32_bf16 a[236:239], v[72:75], v[44:47], a[236:239]\n\t" \
    "s_waitcnt lgkmcnt(4)\n\t" \
    "v_mfma_f32_16x16x32_bf16 a[240:243], v[76:79], v[32:35], a[240:243]\n\t" \
    "ds_read_b64_tr_b16 v[72:73], v92 offset:24576\n\t" \
    "v_mfma_f32_16x16x32_bf16 a[244:247], v[76:79], v[36:39], a[244:247]\n\t" \
    "ds_read_b64_tr_b16 v[74:75], v92 offset:25600\n\t" \
    "v_mfma_f32_16x16x32_bf16 a[248:251], v[76:79], v[40:43], a[248:251]\n\t" \
    "v_mfma_f32_16x16x32_bf16 a[252:255], v[76:79], v[44:47], a[252:255]\n\t" \
    "s_waitcnt vmcnt(9) lgkmcnt(4)\n\t" \
    "v_mfma_f32_16x16x32_bf16 a[128:131], v[64:67], v[48:51], a[128:131]\n\t" \
    "ds_read_b64_tr_b16 v[76:77], v93 offset:24576\n\t" \
    "v_accvgpr_read_b32 v116, a112\n\t" \
    "s_waitcnt vmcnt(8)\n\t" \
    "v_mfma_f32_16x16x32_bf16 a[132:135], v[64:67], v[52:55], a[132:135]\n\t" \
    "ds_read_b64_tr_b16 v[78:79], v93 offset:25600\n\t" \
    "v_accvgpr_read_b32 v117, a113\n\t" \
    "s_waitcnt vmcnt(6)\n\t" \
    "v_mfma_f32_16x16x32_bf16 a[136:139], v[64:67], v[56:59], a[136:139]\n\t" \
    "v_accvgpr_read_b32 v118, a114\n\t" \
    "v_accvgpr_read_b32 v119, a115\n\t" \
    "s_waitcnt vmcnt(5)\n\t" \
    "v_mfma_f32_16x16x32_bf16 a[140:143], v[64:67], v[60:63], a[140:143]\n\t" \
    "v_accvgpr_read_b32 v120, a116\n\t" \
    "v_accvgpr_read_b32 v121, a117\n\t" \
    "s_waitcnt lgkmcnt(4)\n\t" \
    "v_mfma_f32_16x16x32_bf16 a[144:147], v[68:71], v[48:51], a[144:147]\n\t" \
    "ds_read_b64_tr_b16 v[64:65], v94 offset:24576\n\t" \
    "v_accvgpr_read_b32 v122, a118\n\t" \
    "v_mfma_f32_16x16x32_bf16 a[148:151], v[68:71], v[52:55], a[148:151]\n\t" \
    "ds_read_b64_tr_b16 v[66:67], v94 offset:25600\n\t" \
    "v_accvgpr_read_b32 v123, a119\n\t" \
    "v_mfma_f32_16x16x32_bf16 a[152:155], v[68:71], v[56:59], a[152:155]\n\t" \
    "v_cvt_pk_bf16_f32 v116, v116, v117\n\t" \
    "v_cvt_pk_bf16_f32 v117, v118, v119\n\t" \
    "v_mfma_f32_16x16x32_bf16 a[156:159], v[68:71], v[60:63], a[156:159]\n\t" \
    "v_cvt_pk_bf16_f32 v118, v120, v121\n\t" \
    "v_cvt_pk_bf16_f32 v119, v122, v123\n\t" \
    "s_waitcnt lgkmcnt(4)\n\t" \
    "v_mfma_f32_16x16x32_bf16 a[160:163], v[72:75], v[48:51], a[160:163]\n\t" \
    "ds_read_b64_tr_b16 v[68:69], v95 offset:24576\n\t" \
    "s_add_u32 s36, s23, 16384\n\t" \
    "v_mfma_f32_16x16x32_bf16 a[164:167], v[72:75], v[52:55], a[164:167]\n\t" \
    "ds_read_b64_tr_b16 v[70:71], v95 offset:25600\n\t" \
    "buffer_store_dwordx4 v[116:119], v110, s[16:19], s36 offen offset:3328 sc1\n\t" \
    "v_mfma_f32_16x16x32_bf16 a[168:171], v[72:75], v[56:59], a[168:171]\n\t" \
    "v_mfma_f32_16x16x32_bf16 a[172:175], v[72:75], v[60:63], a[172:175]\n\t" \
    "s_waitcnt lgkmcnt(4)\n\t" \
    "v_mfma_f32_16x16x32_bf16 a[176:179], v[76:79], v[48:51], a[176:179]\n\t" \
    "ds_read_b64_tr_b16 v[72:73], v96 offset:24576\n\t" \
    "v_mfma_f32_16x16x32_bf16 a[180:183], v[76:79], v[52:55], a[180:183]\n\t" \
    "ds_read_b64_tr_b16 v[74:75], v96 offset:25600\n\t" \
    "v_mfma_f32_16x16x32_bf16 a[184:187], v[76:79], v[56:59], a[184:187]\n\t" \
    "v_mfma_f32_16x16x32_bf16 a[188:191], v[76:79], v[60:63], a[188:191]\n\t" \
    "s_waitcnt lgkmcnt(4)\n\t" \
    "v_mfma_f32_16x16x32_bf16 a[192:195], v[64:67], v[48:51], a[192:195]\n\t" \
    "ds_read_b64_tr_b16 v[76:77], v97 offset:24576\n\t" \
    "v_accvgpr_read_b32 v98, a120\n\t" \
    "v_mfma_f32_16x16x32_bf16 a[196:199], v[64:67], v[52:55], a[196:199]\n\t" \
    "ds_read_b64_tr_b16 v[78:79], v97 offset:25600\n\t" \
    "v_accvgpr_read_b32 v99, a121\n\t" \
    "v_mfma_f32_16x16x32_bf16 a[200:203], v[64:67], v[56:59], a[200:203]\n\t" \
    "v_accvgpr_read_b32 v100, a122\n\t" \
    "v_accvgpr_read_b32 v101, a123\n\t" \
    "v_mfma_f32_16x16x32_bf16 a[204:207], v[64:67], v[60:63], a[204:207]\n\t" \
    "v_accvgpr_read_b32 v102, a124\n\t" \
    "v_accvgpr_read_b32 v103, a125\n\t" \
    "s_waitcnt lgkmcnt(4)\n\t" \
    "v_mfma_f32_16x16x32_bf16 a[208:211], v[68:71], v[48:51], a[208:211]\n\t" \
    "v_accvgpr_read_b32 v104, a126\n\t" \
    "v_accvgpr_read_b32 v105, a127\n\t" \
    "v_mfma_f32_16x16x32_bf16 a[212:215], v[68:71], v[52:55], a[212:215]\n\t" \
    "v_cvt_pk_bf16_f32 v98, v98, v99\n\t" \
    "v_cvt_pk_bf16_f32 v99, v100, v101\n\t" \
    "v_mfma_f32_16x16x32_bf16 a[216:219], v[68:71], v[56:59], a[216:219]\n\t" \
    "v_cvt_pk_bf16_f32 v100, v102, v103\n\t" \
    "v_cvt_pk_bf16_f32 v101, v104, v105\n\t" \
    "v_mfma_f32_16x16x32_bf16 a[220:223], v[68:71], v[60:63], a[220:223]\n\t" \
    "s_add_u32 s36, s23, 24576\n\t" \
    "s_waitcnt lgkmcnt(2)\n\t" \
    "v_mfma_f32_16x16x32_bf16 a[224:227], v[72:75], v[48:51], a[224:227]\n\t" \
    "buffer_store_dwordx4 v[98:101], v110, s[16:19], s36 offen offset:3328 sc1\n\t" \
    "v_mfma_f32_16x16x32_bf16 a[228:231], v[72:75], v[52:55], a[228:231]\n\t" \
    "v_mfma_f32_16x16x32_bf16 a[232:235], v[72:75], v[56:59], a[232:235]\n\t" \
    "v_mfma_f32_16x16x32_bf16 a[236:239], v[72:75], v[60:63], a[236:239]\n\t" \
    "s_waitcnt lgkmcnt(0)\n\t" \
    "v_mfma_f32_16x16x32_bf16 a[240:243], v[76:79], v[48:51], a[240:243]\n\t" \
    "v_mfma_f32_16x16x32_bf16 a[244:247], v[76:79], v[52:55], a[244:247]\n\t" \
    "v_mfma_f32_16x16x32_bf16 a[248:251], v[76:79], v[56:59], a[248:251]\n\t" \
    "v_mfma_f32_16x16x32_bf16 a[252:255], v[76:79], v[60:63], a[252:255]\n\t" \
    "QEND_%=:\n\t" \
    "s_waitcnt vmcnt(0)\n\t" \
    "s_barrier\n\t" \
    "s_cmp_eq_u32 s20, 0\n\t" \
    "s_cselect_b32 s23, 1, 0\n\t" \
    "v_mov_b32 v112, s23\n\t" \
    "s_lshl_b32 s29, s21, 7\n\t" \
    "s_add_u32 s29, s29, 0x81000\n\t" \
    "s_mov_b64 exec, 1\n\t" \
    "buffer_atomic_add v112, off, s[16:19], s29 sc1\n\t" \
    "s_mov_b64 exec, -1\n\t" \
    "v_lshrrev_b32 v111, 5, v115\n\t" \
    "v_lshlrev_b32 v111, 10, v111\n\t" \
    "v_bfe_u32 v112, v115, 4, 1\n\t" \
    "v_lshlrev_b32 v112, 9, v112\n\t" \
    "v_add_u32 v111, v111, v112\n\t" \
    "v_and_b32 v112, 15, v115\n\t" \
    "v_lshlrev_b32 v112, 4, v112\n\t" \
    "v_add_u32 v111, v111, v112\n\t" \
    "s_lshl_b32 s23, s20, 14\n\t" \
    "s_add_u32 s23, s23, 0x18000\n\t" \
    "v_add_u32 v111, s23, v111\n\t" \
    "v_accvgpr_read_b32 v0, a128\n\t" \
    "v_accvgpr_read_b32 v1, a129\n\t" \
    "v_accvgpr_read_b32 v2, a130\n\t" \
    "v_accvgpr_read_b32 v3, a131\n\t" \
    "v_accvgpr_read_b32 v4, a144\n\t" \
    "v_accvgpr_read_b32 v5, a145\n\t" \
    "v_accvgpr_read_b32 v6, a146\n\t" \
    "v_accvgpr_read_b32 v7, a147\n\t" \
    "v_cvt_pk_bf16_f32 v8, v0, v1\n\t" \
    "v_cvt_pk_bf16_f32 v9, v2, v3\n\t" \
    "v_cvt_pk_bf16_f32 v10, v4, v5\n\t" \
    "v_cvt_pk_bf16_f32 v11, v6, v7\n\t" \
    "ds_write_b128 v111, v[8:11] offset:0\n\t" \
    "v_accvgpr_read_b32 v16, a132\n\t" \
    "v_accvgpr_read_b32 v17, a133\n\t" \
    "v_accvgpr_read_b32 v18, a134\n\t" \
    "v_accvgpr_read_b32 v19, a135\n\t" \
    "v_accvgpr_read_b32 v20, a148\n\t" \
    "v_accvgpr_read_b32 v21, a149\n\t" \
    "v_accvgpr_read_b32 v22, a150\n\t" \
    "v_accvgpr_read_b32 v23, a151\n\t" \
    "v_cvt_pk_bf16_f32 v24, v16, v17\n\t" \
    "v_cvt_pk_bf16_f32 v25, v18, v19\n\t" \
    "v_cvt_pk_bf16_f32 v26, v20, v21\n\t" \
    "v_cvt_pk_bf16_f32 v27, v22, v23\n\t" \
    "ds_write_b128 v111, v[24:27] offset:256\n\t" \
    "v_accvgpr_read_b32 v32, a136\n\t" \
    "v_accvgpr_read_b32 v33, a137\n\t" \
    "v_accvgpr_read_b32 v34, a138\n\t" \
    "v_accvgpr_read_b32 v35, a139\n\t" \
    "v_accvgpr_read_b32 v36, a152\n\t" \
    "v_accvgpr_read_b32 v37, a153\n\t" \
    "v_accvgpr_read_b32 v38, a154\n\t" \
    "v_accvgpr_read_b32 v39, a155\n\t" \
    "v_cvt_pk_bf16_f32 v40, v32, v33\n\t" \
    "v_cvt_pk_bf16_f32 v41, v34, v35\n\t" \
    "v_cvt_pk_bf16_f32 v42, v36, v37\n\t" \
    "v_cvt_pk_bf16_f32 v43, v38, v39\n\t" \
    "ds_write_b128 v111, v[40:43] offset:8192\n\t" \
    "v_accvgpr_read_b32 v48, a140\n\t" \
    "v_accvgpr_read_b32 v49, a141\n\t" \
    "v_accvgpr_read_b32 v50, a142\n\t" \
    "v_accvgpr_read_b32 v51, a143\n\t" \
    "v_accvgpr_read_b32 v52, a156\n\t" \
    "v_accvgpr_read_b32 v53, a157\n\t" \
    "v_accvgpr_read_b32 v54, a158\n\t" \
    "v_accvgpr_read_b32 v55, a159\n\t" \
    "v_cvt_pk_bf16_f32 v56, v48, v49\n\t" \
    "v_cvt_pk_bf16_f32 v57, v50, v51\n\t" \
    "v_cvt_pk_bf16_f32 v58, v52, v53\n\t" \
    "v_cvt_pk_bf16_f32 v59, v54, v55\n\t" \
    "ds_write_b128 v111, v[56:59] offset:8448\n\t" \
    "v_accvgpr_read_b32 v0, a160\n\t" \
    "v_accvgpr_read_b32 v1, a161\n\t" \
    "v_accvgpr_read_b32 v2, a162\n\t" \
    "v_accvgpr_read_b32 v3, a163\n\t" \
    "v_accvgpr_read_b32 v4, a176\n\t" \
    "v_accvgpr_read_b32 v5, a177\n\t" \
    "v_accvgpr_read_b32 v6, a178\n\t" \
    "v_accvgpr_read_b32 v7, a179\n\t" \
    "v_cvt_pk_bf16_f32 v8, v0, v1\n\t" \
    "v_cvt_pk_bf16_f32 v9, v2, v3\n\t" \
    "v_cvt_pk_bf16_f32 v10, v4, v5\n\t" \
    "v_cvt_pk_bf16_f32 v11, v6, v7\n\t" \
    "ds_write_b128 v111, v[8:11] offset:2048\n\t" \
    "v_accvgpr_read_b32 v16, a164\n\t" \
    "v_accvgpr_read_b32 v17, a165\n\t" \
    "v_accvgpr_read_b32 v18, a166\n\t" \
    "v_accvgpr_read_b32 v19, a167\n\t" \
    "v_accvgpr_read_b32 v20, a180\n\t" \
    "v_accvgpr_read_b32 v21, a181\n\t" \
    "v_accvgpr_read_b32 v22, a182\n\t" \
    "v_accvgpr_read_b32 v23, a183\n\t" \
    "v_cvt_pk_bf16_f32 v24, v16, v17\n\t" \
    "v_cvt_pk_bf16_f32 v25, v18, v19\n\t" \
    "v_cvt_pk_bf16_f32 v26, v20, v21\n\t" \
    "v_cvt_pk_bf16_f32 v27, v22, v23\n\t" \
    "ds_write_b128 v111, v[24:27] offset:2304\n\t" \
    "v_accvgpr_read_b32 v32, a168\n\t" \
    "v_accvgpr_read_b32 v33, a169\n\t" \
    "v_accvgpr_read_b32 v34, a170\n\t" \
    "v_accvgpr_read_b32 v35, a171\n\t" \
    "v_accvgpr_read_b32 v36, a184\n\t" \
    "v_accvgpr_read_b32 v37, a185\n\t" \
    "v_accvgpr_read_b32 v38, a186\n\t" \
    "v_accvgpr_read_b32 v39, a187\n\t" \
    "v_cvt_pk_bf16_f32 v40, v32, v33\n\t" \
    "v_cvt_pk_bf16_f32 v41, v34, v35\n\t" \
    "v_cvt_pk_bf16_f32 v42, v36, v37\n\t" \
    "v_cvt_pk_bf16_f32 v43, v38, v39\n\t" \
    "ds_write_b128 v111, v[40:43] offset:10240\n\t" \
    "v_accvgpr_read_b32 v48, a172\n\t" \
    "v_accvgpr_read_b32 v49, a173\n\t" \
    "v_accvgpr_read_b32 v50, a174\n\t" \
    "v_accvgpr_read_b32 v51, a175\n\t" \
    "v_accvgpr_read_b32 v52, a188\n\t" \
    "v_accvgpr_read_b32 v53, a189\n\t" \
    "v_accvgpr_read_b32 v54, a190\n\t" \
    "v_accvgpr_read_b32 v55, a191\n\t" \
    "v_cvt_pk_bf16_f32 v56, v48, v49\n\t" \
    "v_cvt_pk_bf16_f32 v57, v50, v51\n\t" \
    "v_cvt_pk_bf16_f32 v58, v52, v53\n\t" \
    "v_cvt_pk_bf16_f32 v59, v54, v55\n\t" \
    "ds_write_b128 v111, v[56:59] offset:10496\n\t" \
    "v_accvgpr_read_b32 v0, a192\n\t" \
    "v_accvgpr_read_b32 v1, a193\n\t" \
    "v_accvgpr_read_b32 v2, a194\n\t" \
    "v_accvgpr_read_b32 v3, a195\n\t" \
    "v_accvgpr_read_b32 v4, a208\n\t" \
    "v_accvgpr_read_b32 v5, a209\n\t" \
    "v_accvgpr_read_b32 v6, a210\n\t" \
    "v_accvgpr_read_b32 v7, a211\n\t" \
    "v_cvt_pk_bf16_f32 v8, v0, v1\n\t" \
    "v_cvt_pk_bf16_f32 v9, v2, v3\n\t" \
    "v_cvt_pk_bf16_f32 v10, v4, v5\n\t" \
    "v_cvt_pk_bf16_f32 v11, v6, v7\n\t" \
    "ds_write_b128 v111, v[8:11] offset:4096\n\t" \
    "v_accvgpr_read_b32 v16, a196\n\t" \
    "v_accvgpr_read_b32 v17, a197\n\t" \
    "v_accvgpr_read_b32 v18, a198\n\t" \
    "v_accvgpr_read_b32 v19, a199\n\t" \
    "v_accvgpr_read_b32 v20, a212\n\t" \
    "v_accvgpr_read_b32 v21, a213\n\t" \
    "v_accvgpr_read_b32 v22, a214\n\t" \
    "v_accvgpr_read_b32 v23, a215\n\t" \
    "v_cvt_pk_bf16_f32 v24, v16, v17\n\t" \
    "v_cvt_pk_bf16_f32 v25, v18, v19\n\t" \
    "v_cvt_pk_bf16_f32 v26, v20, v21\n\t" \
    "v_cvt_pk_bf16_f32 v27, v22, v23\n\t" \
    "ds_write_b128 v111, v[24:27] offset:4352\n\t" \
    "v_accvgpr_read_b32 v32, a200\n\t" \
    "v_accvgpr_read_b32 v33, a201\n\t" \
    "v_accvgpr_read_b32 v34, a202\n\t" \
    "v_accvgpr_read_b32 v35, a203\n\t" \
    "v_accvgpr_read_b32 v36, a216\n\t" \
    "v_accvgpr_read_b32 v37, a217\n\t" \
    "v_accvgpr_read_b32 v38, a218\n\t" \
    "v_accvgpr_read_b32 v39, a219\n\t" \
    "v_cvt_pk_bf16_f32 v40, v32, v33\n\t" \
    "v_cvt_pk_bf16_f32 v41, v34, v35\n\t" \
    "v_cvt_pk_bf16_f32 v42, v36, v37\n\t" \
    "v_cvt_pk_bf16_f32 v43, v38, v39\n\t" \
    "ds_write_b128 v111, v[40:43] offset:12288\n\t" \
    "v_accvgpr_read_b32 v48, a204\n\t" \
    "v_accvgpr_read_b32 v49, a205\n\t" \
    "v_accvgpr_read_b32 v50, a206\n\t" \
    "v_accvgpr_read_b32 v51, a207\n\t" \
    "v_accvgpr_read_b32 v52, a220\n\t" \
    "v_accvgpr_read_b32 v53, a221\n\t" \
    "v_accvgpr_read_b32 v54, a222\n\t" \
    "v_accvgpr_read_b32 v55, a223\n\t" \
    "v_cvt_pk_bf16_f32 v56, v48, v49\n\t" \
    "v_cvt_pk_bf16_f32 v57, v50, v51\n\t" \
    "v_cvt_pk_bf16_f32 v58, v52, v53\n\t" \
    "v_cvt_pk_bf16_f32 v59, v54, v55\n\t" \
    "ds_write_b128 v111, v[56:59] offset:12544\n\t" \
    "v_accvgpr_read_b32 v0, a224\n\t" \
    "v_accvgpr_read_b32 v1, a225\n\t" \
    "v_accvgpr_read_b32 v2, a226\n\t" \
    "v_accvgpr_read_b32 v3, a227\n\t" \
    "v_accvgpr_read_b32 v4, a240\n\t" \
    "v_accvgpr_read_b32 v5, a241\n\t" \
    "v_accvgpr_read_b32 v6, a242\n\t" \
    "v_accvgpr_read_b32 v7, a243\n\t" \
    "v_cvt_pk_bf16_f32 v8, v0, v1\n\t" \
    "v_cvt_pk_bf16_f32 v9, v2, v3\n\t" \
    "v_cvt_pk_bf16_f32 v10, v4, v5\n\t" \
    "v_cvt_pk_bf16_f32 v11, v6, v7\n\t" \
    "ds_write_b128 v111, v[8:11] offset:6144\n\t" \
    "v_accvgpr_read_b32 v16, a228\n\t" \
    "v_accvgpr_read_b32 v17, a229\n\t" \
    "v_accvgpr_read_b32 v18, a230\n\t" \
    "v_accvgpr_read_b32 v19, a231\n\t" \
    "v_accvgpr_read_b32 v20, a244\n\t" \
    "v_accvgpr_read_b32 v21, a245\n\t" \
    "v_accvgpr_read_b32 v22, a246\n\t" \
    "v_accvgpr_read_b32 v23, a247\n\t" \
    "v_cvt_pk_bf16_f32 v24, v16, v17\n\t" \
    "v_cvt_pk_bf16_f32 v25, v18, v19\n\t" \
    "v_cvt_pk_bf16_f32 v26, v20, v21\n\t" \
    "v_cvt_pk_bf16_f32 v27, v22, v23\n\t" \
    "ds_write_b128 v111, v[24:27] offset:6400\n\t" \
    "v_accvgpr_read_b32 v32, a232\n\t" \
    "v_accvgpr_read_b32 v33, a233\n\t" \
    "v_accvgpr_read_b32 v34, a234\n\t" \
    "v_accvgpr_read_b32 v35, a235\n\t" \
    "v_accvgpr_read_b32 v36, a248\n\t" \
    "v_accvgpr_read_b32 v37, a249\n\t" \
    "v_accvgpr_read_b32 v38, a250\n\t" \
    "v_accvgpr_read_b32 v39, a251\n\t" \
    "v_cvt_pk_bf16_f32 v40, v32, v33\n\t" \
    "v_cvt_pk_bf16_f32 v41, v34, v35\n\t" \
    "v_cvt_pk_bf16_f32 v42, v36, v37\n\t" \
    "v_cvt_pk_bf16_f32 v43, v38, v39\n\t" \
    "ds_write_b128 v111, v[40:43] offset:14336\n\t" \
    "v_accvgpr_read_b32 v48, a236\n\t" \
    "v_accvgpr_read_b32 v49, a237\n\t" \
    "v_accvgpr_read_b32 v50, a238\n\t" \
    "v_accvgpr_read_b32 v51, a239\n\t" \
    "v_accvgpr_read_b32 v52, a252\n\t" \
    "v_accvgpr_read_b32 v53, a253\n\t" \
    "v_accvgpr_read_b32 v54, a254\n\t" \
    "v_accvgpr_read_b32 v55, a255\n\t" \
    "v_cvt_pk_bf16_f32 v56, v48, v49\n\t" \
    "v_cvt_pk_bf16_f32 v57, v50, v51\n\t" \
    "v_cvt_pk_bf16_f32 v58, v52, v53\n\t" \
    "v_cvt_pk_bf16_f32 v59, v54, v55\n\t" \
    "ds_write_b128 v111, v[56:59] offset:14592\n\t" \
    "s_cmp_lg_u32 s20, 0\n\t" \
    "s_cbranch_scc1 KVOK_%=\n\t" \
    "s_mov_b32 s31, 0\n\t" \
    "KVL_%=:\n\t" \
    "buffer_load_dword v112, off, s[16:19], s29 sc1\n\t" \
    "s_waitcnt vmcnt(0)\n\t" \
    "v_readfirstlane_b32 s23, v112\n\t" \
    "s_nop 0\n\t" \
    "s_cmp_eq_u32 s23, 8\n\t" \
    "s_cbranch_scc1 KVI_%=\n\t" \
    "s_add_u32 s31, s31, 1\n\t" \
    "s_cmp_gt_u32 s31, 0x40000\n\t" \
    "s_cbranch_scc1 KVI_%=\n\t" \
    "s_sleep 1\n\t" \
    "s_branch KVL_%=\n\t" \
    "KVI_%=:\n\t" \
    "buffer_inv sc1\n\t" \
    "KVOK_%=:\n\t" \
    "s_mov_b32 s8, %[wslo]\n\t" \
    "s_mov_b32 s9, %[wshi]\n\t" \
    "s_mov_b32 s10, 0x10000000\n\t" \
    "s_mov_b32 s11, 0x00020000\n\t" \
    "s_mov_b32 s22, %[hh]\n\t" \
    "s_mov_b32 s23, %[gg]\n\t" \
    "s_mov_b32 s24, %[wv]\n\t" \
    "v_mbcnt_lo_u32_b32 v235, -1, 0\n\t" \
    "v_mbcnt_hi_u32_b32 v235, -1, v235\n\t" \
    "v_lshlrev_b32 v232, 4, v235\n\t" \
    "s_lshl_b32 s25, s24, 13\n\t" \
    "v_add_u32 v228, s25, v232\n\t" \
    "v_add_u32 v229, 4096, v228\n\t" \
    "s_mov_b32 s16, s25\n\t" \
    "s_add_u32 s17, s25, 0x10000\n\t" \
    "s_lshl_b32 s26, s22, 19\n\t" \
    "s_add_u32 s27, s26, 0x400000\n\t" \
    "s_add_u32 s28, s26, 0x1400000\n\t" \
    "s_sub_u32 s19, 15, s23\n\t" \
    "s_lshl_b32 s26, s19, 15\n\t" \
    "s_add_u32 s13, s27, s26\n\t" \
    "s_add_u32 s15, s28, s26\n\t" \
    "s_lshl_b32 s26, s23, 15\n\t" \
    "s_add_u32 s29, s27, s26\n\t" \
    "s_add_u32 s30, s28, s26\n\t" \
    "s_and_b32 s25, s24, 1\n\t" \
    "s_lshr_b32 s26, s24, 1\n\t" \
    "s_lshl_b32 s36, s25, 14\n\t" \
    "v_add_u32 v226, s36, v232\n\t" \
    "v_add_u32 v227, 0x18000, v226\n\t" \
    "s_lshl_b32 s36, s26, 14\n\t" \
    "s_lshl_b32 s37, s25, 13\n\t" \
    "s_add_u32 s36, s36, s37\n\t" \
    "s_add_u32 s36, s36, 0x18000\n\t" \
    "v_add_u32 v235, s36, v232\n\t" \
    "v_xor_b32 v236, 0x2000, v235\n\t" \
    "s_waitcnt lgkmcnt(0)\n\t" \
    "s_barrier\n\t" \
    "ds_read_b128 a[0:3], v235 offset:0\n\t" \
    "ds_read_b128 a[4:7], v235 offset:1024\n\t" \
    "ds_read_b128 a[8:11], v235 offset:2048\n\t" \
    "ds_read_b128 a[12:15], v235 offset:3072\n\t" \
    "ds_read_b128 a[16:19], v235 offset:4096\n\t" \
    "ds_read_b128 a[20:23], v235 offset:5120\n\t" \
    "ds_read_b128 a[24:27], v235 offset:6144\n\t" \
    "ds_read_b128 a[28:31], v235 offset:7168\n\t" \
    "ds_read_b128 a[32:35], v236 offset:0\n\t" \
    "ds_read_b128 a[36:39], v236 offset:1024\n\t" \
    "ds_read_b128 a[40:43], v236 offset:2048\n\t" \
    "ds_read_b128 a[44:47], v236 offset:3072\n\t" \
    "ds_read_b128 a[48:51], v236 offset:4096\n\t" \
    "ds_read_b128 a[52:55], v236 offset:5120\n\t" \
    "ds_read_b128 a[56:59], v236 offset:6144\n\t" \
    "ds_read_b128 a[60:63], v236 offset:7168\n\t" \
    "ds_read_b128 a[64:67], v235 offset:32768\n\t" \
    "ds_read_b128 a[68:71], v235 offset:33792\n\t" \
    "ds_read_b128 a[72:75], v235 offset:34816\n\t" \
    "ds_read_b128 a[76:79], v235 offset:35840\n\t" \
    "ds_read_b128 a[80:83], v235 offset:36864\n\t" \
    "ds_read_b128 a[84:87], v235 offset:37888\n\t" \
    "ds_read_b128 a[88:91], v235 offset:38912\n\t" \
    "ds_read_b128 a[92:95], v235 offset:39936\n\t" \
    "ds_read_b128 a[96:99], v236 offset:32768\n\t" \
    "ds_read_b128 a[100:103], v236 offset:33792\n\t" \
    "ds_read_b128 a[104:107], v236 offset:34816\n\t" \
    "ds_read_b128 a[108:111], v236 offset:35840\n\t" \
    "ds_read_b128 a[112:115], v236 offset:36864\n\t" \
    "ds_read_b128 a[116:119], v236 offset:37888\n\t" \
    "ds_read_b128 a[120:123], v236 offset:38912\n\t" \
    "ds_read_b128 a[124:127], v236 offset:39936\n\t" \
    "v_mbcnt_lo_u32_b32 v238, -1, 0\n\t" \
    "v_mbcnt_hi_u32_b32 v238, -1, v238\n\t" \
    "v_and_b32 v237, 31, v238\n\t" \
    "v_lshrrev_b32 v233, 5, v238\n\t" \
    "v_lshlrev_b32 v233, 3, v233\n\t" \
    "v_sub_u32 v237, v237, v233\n\t" \
    "s_sub_i32 s36, s26, s25\n\t" \
    "s_lshl_b32 s36, s36, 6\n\t" \
    "s_lshl_b32 s37, s25, 5\n\t" \
    "s_add_i32 s37, s36, s37\n\t" \
    "v_add_u32 v230, s37, v237\n\t" \
    "s_xor_b32 s37, s25, 1\n\t" \
    "s_lshl_b32 s37, s37, 5\n\t" \
    "s_add_i32 s37, s36, s37\n\t" \
    "v_add_u32 v231, s37, v237\n\t" \
    "v_lshlrev_b32 v234, 2, v238\n\t" \
    "s_lshl_b32 s36, s24, 8\n\t" \
    "v_add_u32 v234, s36, v234\n\t" \
    "s_mov_b32 s20, 0\n\t" \
    "s_mov_b64 s[38:39], 0\n\t" \
    "v_lshlrev_b32 v246, 2, v232\n\t" \
    "s_waitcnt vmcnt(0) lgkmcnt(0)\n\t" \
    "s_barrier\n\t" \
    "s_mov_b32 s12, s27\n\t" \
    "s_mov_b32 s14, s28\n\t" \
    "s_mov_b32 m0, s16\n\t" \
    "s_nop 0\n\t" \
    "buffer_load_dwordx4 v228, s[8:11], s12 offen offset:0 lds\n\t" \
    "buffer_load_dwordx4 v228, s[8:11], s12 offen offset:1024 lds\n\t" \
    "buffer_load_dwordx4 v228, s[8:11], s12 offen offset:2048 lds\n\t" \
    "buffer_load_dwordx4 v228, s[8:11], s12 offen offset:3072 lds\n\t" \
    "s_add_u32 m0, s16, 4096\n\t" \
    "s_nop 0\n\t" \
    "buffer_load_dwordx4 v229, s[8:11], s12 offen offset:0 lds\n\t" \
    "buffer_load_dwordx4 v229, s[8:11], s12 offen offset:1024 lds\n\t" \
    "buffer_load_dwordx4 v229, s[8:11], s12 offen offset:2048 lds\n\t" \
    "buffer_load_dwordx4 v229, s[8:11], s12 offen offset:3072 lds\n\t" \
    "s_add_u32 s12, s12, 0x8000\n\t" \
    "s_cmp_gt_u32 s12, s13\n\t" \
    "s_cselect_b32 s12, s27, s12\n\t" \
    "s_cselect_b32 s13, s29, s13\n\t" \
    "s_xor_b32 s16, s16, 0x8000\n\t" \
    "s_mov_b32 m0, s17\n\t" \
    "s_nop 0\n\t" \
    "buffer_load_dwordx4 v228, s[8:11], s14 offen offset:0 lds\n\t" \
    "buffer_load_dwordx4 v228, s[8:11], s14 offen offset:1024 lds\n\t" \
    "buffer_load_dwordx4 v228, s[8:11], s14 offen offset:2048 lds\n\t" \
    "buffer_load_dwordx4 v228, s[8:11], s14 offen offset:3072 lds\n\t" \
    "s_add_u32 m0, s17, 4096\n\t" \
    "s_nop 0\n\t" \
    "buffer_load_dwordx4 v229, s[8:11], s14 offen offset:0 lds\n\t" \
    "buffer_load_dwordx4 v229, s[8:11], s14 offen offset:1024 lds\n\t" \
    "buffer_load_dwordx4 v229, s[8:11], s14 offen offset:2048 lds\n\t" \
    "buffer_load_dwordx4 v229, s[8:11], s14 offen offset:3072 lds\n\t" \
    "s_add_u32 s14, s14, 0x8000\n\t" \
    "s_cmp_gt_u32 s14, s15\n\t" \
    "s_cselect_b32 s14, s28, s14\n\t" \
    "s_cselect_b32 s15, s30, s15\n\t" \
    "s_xor_b32 s17, s17, 0x8000\n\t" \
    "s_mov_b32 m0, s16\n\t" \
    "s_nop 0\n\t" \
    "buffer_load_dwordx4 v228, s[8:11], s12 offen offset:0 lds\n\t" \
    "buffer_load_dwordx4 v228, s[8:11], s12 offen offset:1024 lds\n\t" \
    "buffer_load_dwordx4 v228, s[8:11], s12 offen offset:2048 lds\n\t" \
    "buffer_load_dwordx4 v228, s[8:11], s12 offen offset:3072 lds\n\t" \
    "s_add_u32 m0, s16, 4096\n\t" \
    "s_nop 0\n\t" \
    "buffer_load_dwordx4 v229, s[8:11], s12 offen offset:0 lds\n\t" \
    "buffer_load_dwordx4 v229, s[8:11], s12 offen offset:1024 lds\n\t" \
    "buffer_load_dwordx4 v229, s[8:11], s12 offen offset:2048 lds\n\t" \
    "buffer_load_dwordx4 v229, s[8:11], s12 offen offset:3072 lds\n\t" \
    "s_add_u32 s12, s12, 0x8000\n\t" \
    "s_cmp_gt_u32 s12, s13\n\t" \
    "s_cselect_b32 s12, s27, s12\n\t" \
    "s_cselect_b32 s13, s29, s13\n\t" \
    "s_xor_b32 s16, s16, 0x8000\n\t" \
    "s_waitcnt vmcnt(16)\n\t" \
    "s_barrier\n\t" \
    "BLOCK_%=:\n\t" \
    "ds_read_b128 a[128:131], v226 offset:0\n\t" \
    "ds_read_b128 a[132:135], v226 offset:1024\n\t" \
    "ds_read_b128 a[136:139], v226 offset:2048\n\t" \
    "ds_read_b128 a[140:143], v226 offset:3072\n\t" \
    "v_mov_b32 v224, 0\n\t" \
    "v_mov_b32 v225, 0\n\t" \
    "s_cmp_eq_u32 s19, 0\n\t" \
    "s_cbranch_scc1 FL_%=\n\t" \
    "s_waitcnt lgkmcnt(3)\n\t" \
    "v_mfma_f32_32x32x16_bf16 v[0:15], a[128:131], a[0:3], 0\n\t" \
    "ds_read_b128 a[144:147], v226 offset:4096\n\t" \
    "v_mfma_f32_32x32x16_bf16 v[16:31], a[128:131], a[32:35], 0\n\t" \
    "s_waitcnt lgkmcnt(3)\n\t" \
    "v_mfma_f32_32x32x16_bf16 v[0:15], a[132:135], a[4:7], v[0:15]\n\t" \
    "ds_read_b128 a[148:151], v226 offset:5120\n\t" \
    "v_mfma_f32_32x32x16_bf16 v[16:31], a[132:135], a[36:39], v[16:31]\n\t" \
    "s_waitcnt lgkmcnt(3)\n\t" \
    "v_mfma_f32_32x32x16_bf16 v[0:15], a[136:139], a[8:11], v[0:15]\n\t" \
    "ds_read_b128 a[152:155], v226 offset:6144\n\t" \
    "v_mfma_f32_32x32x16_bf16 v[16:31], a[136:139], a[40:43], v[16:31]\n\t" \
    "s_waitcnt lgkmcnt(3)\n\t" \
    "v_mfma_f32_32x32x16_bf16 v[0:15], a[140:143], a[12:15], v[0:15]\n\t" \
    "ds_read_b128 a[156:159], v226 offset:7168\n\t" \
    "v_mfma_f32_32x32x16_bf16 v[16:31], a[140:143], a[44:47], v[16:31]\n\t" \
    "s_waitcnt lgkmcnt(3)\n\t" \
    "v_mfma_f32_32x32x16_bf16 v[0:15], a[144:147], a[16:19], v[0:15]\n\t" \
    "v_mfma_f32_32x32x16_bf16 v[16:31], a[144:147], a[48:51], v[16:31]\n\t" \
    "s_waitcnt lgkmcnt(2)\n\t" \
    "v_mfma_f32_32x32x16_bf16 v[0:15], a[148:151], a[20:23], v[0:15]\n\t" \
    "v_mfma_f32_32x32x16_bf16 v[16:31], a[148:151], a[52:55], v[16:31]\n\t" \
    "s_waitcnt lgkmcnt(1)\n\t" \
    "v_mfma_f32_32x32x16_bf16 v[0:15], a[152:155], a[24:27], v[0:15]\n\t" \
    "v_mfma_f32_32x32x16_bf16 v[16:31], a[152:155], a[56:59], v[16:31]\n\t" \
    "s_waitcnt lgkmcnt(0)\n\t" \
    "v_mfma_f32_32x32x16_bf16 v[0:15], a[156:159], a[28:31], v[0:15]\n\t" \
    "v_mfma_f32_32x32x16_bf16 v[16:31], a[156:159], a[60:63], v[16:31]\n\t" \
    "ds_read_b128 a[128:131], v226 offset:8192\n\t" \
    "s_nop 7\n\t" \
    "s_nop 1\n\t" \
    "v_exp_f32 v0, v0\n\t" \
    "ds_read_b128 a[132:135], v226 offset:9216\n\t" \
    "v_exp_f32 v1, v1\n\t" \
    "v_add_f32 v224, v224, v0\n\t" \
    "v_exp_f32 v2, v2\n\t" \
    "v_add_f32 v224, v224, v1\n\t" \
    "v_cvt_pk_bf16_f32 v192, v0, v1\n\t" \
    "ds_read_b128 a[136:139], v226 offset:10240\n\t" \
    "v_exp_f32 v3, v3\n\t" \
    "v_add_f32 v224, v224, v2\n\t" \
    "v_exp_f32 v4, v4\n\t" \
    "v_add_f32 v224, v224, v3\n\t" \
    "v_cvt_pk_bf16_f32 v193, v2, v3\n\t" \
    "ds_read_b128 a[140:143], v226 offset:11264\n\t" \
    "v_exp_f32 v5, v5\n\t" \
    "v_add_f32 v224, v224, v4\n\t" \
    "v_exp_f32 v6, v6\n\t" \
    "v_add_f32 v224, v224, v5\n\t" \
    "v_cvt_pk_bf16_f32 v194, v4, v5\n\t" \
    "ds_read_b128 a[144:147], v226 offset:12288\n\t" \
    "v_exp_f32 v7, v7\n\t" \
    "v_add_f32 v224, v224, v6\n\t" \
    "v_exp_f32 v16, v16\n\t" \
    "v_add_f32 v224, v224, v7\n\t" \
    "v_cvt_pk_bf16_f32 v195, v6, v7\n\t" \
    "ds_read_b128 a[148:151], v226 offset:13312\n\t" \
    "v_exp_f32 v17, v17\n\t" \
    "v_add_f32 v225, v225, v16\n\t" \
    "v_exp_f32 v18, v18\n\t" \
    "v_add_f32 v225, v225, v17\n\t" \
    "v_cvt_pk_bf16_f32 v196, v16, v17\n\t" \
    "ds_read_b128 a[152:155], v226 offset:14336\n\t" \
    "v_exp_f32 v19, v19\n\t" \
    "v_add_f32 v225, v225, v18\n\t" \
    "v_exp_f32 v20, v20\n\t" \
    "v_add_f32 v225, v225, v19\n\t" \
    "v_cvt_pk_bf16_f32 v197, v18, v19\n\t" \
    "v_xor_b32 v227, 0x8000, v227\n\t" \
    "ds_read_b128 a[156:159], v226 offset:15360\n\t" \
    "v_exp_f32 v21, v21\n\t" \
    "v_add_f32 v225, v225, v20\n\t" \
    "s_waitcnt lgkmcnt(7)\n\t" \
    "v_mfma_f32_32x32x16_bf16 v[32:47], a[128:131], a[0:3], 0\n\t" \
    "v_exp_f32 v22, v22\n\t" \
    "v_add_f32 v225, v225, v21\n\t" \
    "v_cvt_pk_bf16_f32 v198, v20, v21\n\t" \
    "v_mfma_f32_32x32x16_bf16 v[48:63], a[128:131], a[32:35], 0\n\t" \
    "v_exp_f32 v23, v23\n\t" \
    "v_add_f32 v225, v225, v22\n\t" \
    "s_waitcnt lgkmcnt(6)\n\t" \
    "v_mfma_f32_32x32x16_bf16 v[32:47], a[132:135], a[4:7], v[32:47]\n\t" \
    "v_exp_f32 v8, v8\n\t" \
    "v_add_f32 v225, v225, v23\n\t" \
    "v_cvt_pk_bf16_f32 v199, v22, v23\n\t" \
    "v_mfma_f32_32x32x16_bf16 v[48:63], a[132:135], a[36:39], v[48:63]\n\t" \
    "v_exp_f32 v9, v9\n\t" \
    "v_add_f32 v224, v224, v8\n\t" \
    "s_waitcnt lgkmcnt(5)\n\t" \
    "v_mfma_f32_32x32x16_bf16 v[32:47], a[136:139], a[8:11], v[32:47]\n\t" \
    "v_exp_f32 v10, v10\n\t" \
    "v_add_f32 v224, v224, v9\n\t" \
    "v_cvt_pk_bf16_f32 v200, v8, v9\n\t" \
    "v_mfma_f32_32x32x16_bf16 v[48:63], a[136:139], a[40:43], v[48:63]\n\t" \
    "v_exp_f32 v11, v11\n\t" \
    "v_add_f32 v224, v224, v10\n\t" \
    "s_waitcnt lgkmcnt(4)\n\t" \
    "v_mfma_f32_32x32x16_bf16 v[32:47], a[140:143], a[12:15], v[32:47]\n\t" \
    "v_exp_f32 v12, v12\n\t" \
    "v_add_f32 v224, v224, v11\n\t" \
    "v_cvt_pk_bf16_f32 v201, v10, v11\n\t" \
    "v_mfma_f32_32x32x16_bf16 v[48:63], a[140:143], a[44:47], v[48:63]\n\t" \
    "v_exp_f32 v13, v13\n\t" \
    "v_add_f32 v224, v224, v12\n\t" \
    "s_waitcnt vmcnt(0) lgkmcnt(0)\n\t" \
    "s_barrier\n\t" \
    "v_mfma_f32_32x32x16_bf16 v[32:47], a[144:147], a[16:19], v[32:47]\n\t" \
    "ds_read_b128 a[128:131], v227 offset:0\n\t" \
    "v_exp_f32 v14, v14\n\t" \
    "v_add_f32 v224, v224, v13\n\t" \
    "v_cvt_pk_bf16_f32 v202, v12, v13\n\t" \
    "s_mov_b32 m0, s16\n\t" \
    "v_mfma_f32_32x32x16_bf16 v[48:63], a[144:147], a[48:51], v[48:63]\n\t" \
    "buffer_load_dwordx4 v228, s[8:11], s12 offen offset:0 lds\n\t" \
    "v_exp_f32 v15, v15\n\t" \
    "v_add_f32 v224, v224, v14\n\t" \
    "v_mfma_f32_32x32x16_bf16 v[32:47], a[148:151], a[20:23], v[32:47]\n\t" \
    "ds_read_b128 a[132:135], v227 offset:1024\n\t" \
    "v_exp_f32 v24, v24\n\t" \
    "v_add_f32 v224, v224, v15\n\t" \
    "v_cvt_pk_bf16_f32 v203, v14, v15\n\t" \
    "v_mfma_f32_32x32x16_bf16 v[48:63], a[148:151], a[52:55], v[48:63]\n\t" \
    "buffer_load_dwordx4 v228, s[8:11], s12 offen offset:1024 lds\n\t" \
    "v_exp_f32 v25, v25\n\t" \
    "v_add_f32 v225, v225, v24\n\t" \
    "v_mfma_f32_32x32x16_bf16 v[32:47], a[152:155], a[24:27], v[32:47]\n\t" \
    "ds_read_b128 a[136:139], v227 offset:2048\n\t" \
    "v_exp_f32 v26, v26\n\t" \
    "v_add_f32 v225, v225, v25\n\t" \
    "v_cvt_pk_bf16_f32 v204, v24, v25\n\t" \
    "v_xor_b32 v226, 0x8000, v226\n\t" \
    "v_mfma_f32_32x32x16_bf16 v[48:63], a[152:155], a[56:59], v[48:63]\n\t" \
    "buffer_load_dwordx4 v228, s[8:11], s12 offen offset:2048 lds\n\t" \
    "v_exp_f32 v27, v27\n\t" \
    "v_add_f32 v225, v225, v26\n\t" \
    "v_mfma_f32_32x32x16_bf16 v[32:47], a[156:159], a[28:31], v[32:47]\n\t" \
    "ds_read_b128 a[140:143], v227 offset:3072\n\t" \
    "v_exp_f32 v28, v28\n\t" \
    "v_add_f32 v225, v225, v27\n\t" \
    "v_cvt_pk_bf16_f32 v205, v26, v27\n\t" \
    "v_mfma_f32_32x32x16_bf16 v[48:63], a[156:159], a[60:63], v[48:63]\n\t" \
    "buffer_load_dwordx4 v228, s[8:11], s12 offen offset:3072 lds\n\t" \
    "v_exp_f32 v29, v29\n\t" \
    "v_add_f32 v225, v225, v28\n\t" \
    "s_waitcnt lgkmcnt(3)\n\t" \
    "v_mfma_f32_32x32x16_bf16 v[64:79], a[128:131], v[192:195], 0\n\t" \
    "ds_read_b128 a[144:147], v227 offset:4096\n\t" \
    "v_exp_f32 v30, v30\n\t" \
    "v_add_f32 v225, v225, v29\n\t" \
    "v_cvt_pk_bf16_f32 v206, v28, v29\n\t" \
    "s_add_u32 m0, s16, 4096\n\t" \
    "v_mfma_f32_32x32x16_bf16 v[80:95], a[128:131], v[196:199], 0\n\t" \
    "buffer_load_dwordx4 v229, s[8:11], s12 offen offset:0 lds\n\t" \
    "v_exp_f32 v31, v31\n\t" \
    "v_add_f32 v225, v225, v30\n\t" \
    "s_waitcnt lgkmcnt(3)\n\t" \
    "v_mfma_f32_32x32x16_bf16 v[96:111], a[132:135], v[192:195], 0\n\t" \
    "ds_read_b128 a[148:151], v227 offset:5120\n\t" \
    "v_exp_f32 v32, v32\n\t" \
    "v_add_f32 v225, v225, v31\n\t" \
    "v_cvt_pk_bf16_f32 v207, v30, v31\n\t" \
    "v_mfma_f32_32x32x16_bf16 v[112:127], a[132:135], v[196:199], 0\n\t" \
    "buffer_load_dwordx4 v229, s[8:11], s12 offen offset:1024 lds\n\t" \
    "v_exp_f32 v33, v33\n\t" \
    "v_add_f32 v224, v224, v32\n\t" \
    "s_waitcnt lgkmcnt(3)\n\t" \
    "v_mfma_f32_32x32x16_bf16 v[128:143], a[136:139], v[192:195], 0\n\t" \
    "ds_read_b128 a[152:155], v227 offset:6144\n\t" \
    "v_exp_f32 v34, v34\n\t" \
    "v_add_f32 v224, v224, v33\n\t" \
    "v_cvt_pk_bf16_f32 v208, v32, v33\n\t" \
    "v_mfma_f32_32x32x16_bf16 v[144:159], a[136:139], v[196:199], 0\n\t" \
    "buffer_load_dwordx4 v229, s[8:11], s12 offen offset:2048 lds\n\t" \
    "v_exp_f32 v35, v35\n\t" \
    "v_add_f32 v224, v224, v34\n\t" \
    "s_waitcnt lgkmcnt(3)\n\t" \
    "v_mfma_f32_32x32x16_bf16 v[160:175], a[140:143], v[192:195], 0\n\t" \
    "ds_read_b128 a[156:159], v227 offset:7168\n\t" \
    "v_exp_f32 v36, v36\n\t" \
    "v_add_f32 v224, v224, v35\n\t" \
    "v_cvt_pk_bf16_f32 v209, v34, v35\n\t" \
    "v_mfma_f32_32x32x16_bf16 v[176:191], a[140:143], v[196:199], 0\n\t" \
    "buffer_load_dwordx4 v229, s[8:11], s12 offen offset:3072 lds\n\t" \
    "v_exp_f32 v37, v37\n\t" \
    "v_add_f32 v224, v224, v36\n\t" \
    "s_waitcnt lgkmcnt(3)\n\t" \
    "v_mfma_f32_32x32x16_bf16 v[64:79], a[144:147], v[200:203], v[64:79]\n\t" \
    "ds_read_b128 a[128:131], v226 offset:0\n\t" \
    "v_exp_f32 v38, v38\n\t" \
    "v_add_f32 v224, v224, v37\n\t" \
    "v_cvt_pk_bf16_f32 v210, v36, v37\n\t" \
    "s_add_u32 s12, s12, 0x8000\n\t" \
    "s_cmp_gt_u32 s12, s13\n\t" \
    "s_cselect_b32 s12, s27, s12\n\t" \
    "s_cselect_b32 s13, s29, s13\n\t" \
    "s_mov_b32 m0, s17\n\t" \
    "v_mfma_f32_32x32x16_bf16 v[80:95], a[144:147], v[204:207], v[80:95]\n\t" \
    "buffer_load_dwordx4 v228, s[8:11], s14 offen offset:0 lds\n\t" \
    "v_exp_f32 v39, v39\n\t" \
    "v_add_f32 v224, v224, v38\n\t" \
    "s_waitcnt lgkmcnt(3)\n\t" \
    "v_mfma_f32_32x32x16_bf16 v[96:111], a[148:151], v[200:203], v[96:111]\n\t" \
    "ds_read_b128 a[132:135], v226 offset:1024\n\t" \
    "v_exp_f32 v48, v48\n\t" \
    "v_add_f32 v224, v224, v39\n\t" \
    "v_cvt_pk_bf16_f32 v211, v38, v39\n\t" \
    "s_xor_b32 s16, s16, 0x8000\n\t" \
    "v_mfma_f32_32x32x16_bf16 v[112:127], a[148:151], v[204:207], v[112:127]\n\t" \
    "buffer_load_dwordx4 v228, s[8:11], s14 offen offset:1024 lds\n\t" \
    "v_exp_f32 v49, v49\n\t" \
    "v_add_f32 v225, v225, v48\n\t" \
    "s_waitcnt lgkmcnt(3)\n\t" \
    "v_mfma_f32_32x32x16_bf16 v[128:143], a[152:155], v[200:203], v[128:143]\n\t" \
    "ds_read_b128 a[136:139], v226 offset:2048\n\t" \
    "v_exp_f32 v50, v50\n\t" \
    "v_add_f32 v225, v225, v49\n\t" \
    "v_cvt_pk_bf16_f32 v212, v48, v49\n\t" \
    "v_mfma_f32_32x32x16_bf16 v[144:159], a[152:155], v[204:207], v[144:159]\n\t" \
    "buffer_load_dwordx4 v228, s[8:11], s14 offen offset:2048 lds\n\t" \
    "v_exp_f32 v51, v51\n\t" \
    "v_add_f32 v225, v225, v50\n\t" \
    "s_waitcnt lgkmcnt(3)\n\t" \
    "v_mfma_f32_32x32x16_bf16 v[160:175], a[156:159], v[200:203], v[160:175]\n\t" \
    "ds_read_b128 a[140:143], v226 offset:3072\n\t" \
    "v_exp_f32 v52, v52\n\t" \
    "v_add_f32 v225, v225, v51\n\t" \
    "v_cvt_pk_bf16_f32 v213, v50, v51\n\t" \
    "v_mfma_f32_32x32x16_bf16 v[176:191], a[156:159], v[204:207], v[176:191]\n\t" \
    "buffer_load_dwordx4 v228, s[8:11], s14 offen offset:3072 lds\n\t" \
    "v_exp_f32 v53, v53\n\t" \
    "v_add_f32 v225, v225, v52\n\t" \
    "s_sub_u32 s18, s19, 1\n\t" \
    "s_cmp_eq_u32 s18, 0\n\t" \
    "s_cbranch_scc1 LAST_%=\n\t" \
    "MID_%=:\n\t" \
    "s_waitcnt lgkmcnt(3)\n\t" \
    "v_mfma_f32_32x32x16_bf16 v[0:15], a[128:131], a[0:3], 0\n\t" \
    "ds_read_b128 a[144:147], v226 offset:4096\n\t" \
    "v_exp_f32 v54, v54\n\t" \
    "v_add_f32 v225, v225, v53\n\t" \
    "v_cvt_pk_bf16_f32 v214, v52, v53\n\t" \
    "s_add_u32 m0, s17, 4096\n\t" \
    "v_mfma_f32_32x32x16_bf16 v[16:31], a[128:131], a[32:35], 0\n\t" \
    "buffer_load_dwordx4 v229, s[8:11], s14 offen offset:0 lds\n\t" \
    "v_exp_f32 v55, v55\n\t" \
    "v_add_f32 v225, v225, v54\n\t" \
    "s_waitcnt lgkmcnt(3)\n\t" \
    "v_mfma_f32_32x32x16_bf16 v[0:15], a[132:135], a[4:7], v[0:15]\n\t" \
    "ds_read_b128 a[148:151], v226 offset:5120\n\t" \
    "v_exp_f32 v40, v40\n\t" \
    "v_add_f32 v225, v225, v55\n\t" \
    "v_cvt_pk_bf16_f32 v215, v54, v55\n\t" \
    "v_mfma_f32_32x32x16_bf16 v[16:31], a[132:135], a[36:39], v[16:31]\n\t" \
    "buffer_load_dwordx4 v229, s[8:11], s14 offen offset:1024 lds\n\t" \
    "v_exp_f32 v41, v41\n\t" \
    "v_add_f32 v224, v224, v40\n\t" \
    "s_waitcnt lgkmcnt(3)\n\t" \
    "v_mfma_f32_32x32x16_bf16 v[0:15], a[136:139], a[8:11], v[0:15]\n\t" \
    "ds_read_b128 a[152:155], v226 offset:6144\n\t" \
    "v_exp_f32 v42, v42\n\t" \
    "v_add_f32 v224, v224, v41\n\t" \
    "v_cvt_pk_bf16_f32 v216, v40, v41\n\t" \
    "v_mfma_f32_32x32x16_bf16 v[16:31], a[136:139], a[40:43], v[16:31]\n\t" \
    "buffer_load_dwordx4 v229, s[8:11], s14 offen offset:2048 lds\n\t" \
    "v_exp_f32 v43, v43\n\t" \
    "v_add_f32 v224, v224, v42\n\t" \
    "s_waitcnt lgkmcnt(3)\n\t" \
    "v_mfma_f32_32x32x16_bf16 v[0:15], a[140:143], a[12:15], v[0:15]\n\t" \
    "ds_read_b128 a[156:159], v226 offset:7168\n\t" \
    "v_exp_f32 v44, v44\n\t" \
    "v_add_f32 v224, v224, v43\n\t" \
    "v_cvt_pk_bf16_f32 v217, v42, v43\n\t" \
    "v_mfma_f32_32x32x16_bf16 v[16:31], a[140:143], a[44:47], v[16:31]\n\t" \
    "buffer_load_dwordx4 v229, s[8:11], s14 offen offset:3072 lds\n\t" \
    "v_exp_f32 v45, v45\n\t" \
    "v_add_f32 v224, v224, v44\n\t" \
    "s_waitcnt lgkmcnt(3)\n\t" \
    "v_mfma_f32_32x32x16_bf16 v[0:15], a[144:147], a[16:19], v[0:15]\n\t" \
    "ds_read_b128 a[128:131], v227 offset:8192\n\t" \
    "v_exp_f32 v46, v46\n\t" \
    "v_add_f32 v224, v224, v45\n\t" \
    "v_cvt_pk_bf16_f32 v218, v44, v45\n\t" \
    "s_add_u32 s14, s14, 0x8000\n\t" \
    "s_cmp_gt_u32 s14, s15\n\t" \
    "s_cselect_b32 s14, s28, s14\n\t" \
    "s_cselect_b32 s15, s30, s15\n\t" \
    "v_mfma_f32_32x32x16_bf16 v[16:31], a[144:147], a[48:51], v[16:31]\n\t" \
    "v_exp_f32 v47, v47\n\t" \
    "v_add_f32 v224, v224, v46\n\t" \
    "s_xor_b32 s17, s17, 0x8000\n\t" \
    "s_waitcnt lgkmcnt(3)\n\t" \
    "v_mfma_f32_32x32x16_bf16 v[0:15], a[148:151], a[20:23], v[0:15]\n\t" \
    "ds_read_b128 a[132:135], v227 offset:9216\n\t" \
    "v_exp_f32 v56, v56\n\t" \
    "v_add_f32 v224, v224, v47\n\t" \
    "v_cvt_pk_bf16_f32 v219, v46, v47\n\t" \
    "v_mfma_f32_32x32x16_bf16 v[16:31], a[148:151], a[52:55], v[16:31]\n\t" \
    "v_exp_f32 v57, v57\n\t" \
    "v_add_f32 v225, v225, v56\n\t" \
    "s_waitcnt lgkmcnt(3)\n\t" \
    "v_mfma_f32_32x32x16_bf16 v[0:15], a[152:155], a[24:27], v[0:15]\n\t" \
    "ds_read_b128 a[136:139], v227 offset:10240\n\t" \
    "v_exp_f32 v58, v58\n\t" \
    "v_add_f32 v225, v225, v57\n\t" \
    "v_cvt_pk_bf16_f32 v220, v56, v57\n\t" \
    "v_mfma_f32_32x32x16_bf16 v[16:31], a[152:155], a[56:59], v[16:31]\n\t" \
    "v_exp_f32 v59, v59\n\t" \
    "v_add_f32 v225, v225, v58\n\t" \
    "s_waitcnt lgkmcnt(3)\n\t" \
    "v_mfma_f32_32x32x16_bf16 v[0:15], a[156:159], a[28:31], v[0:15]\n\t" \
    "ds_read_b128 a[140:143], v227 offset:11264\n\t" \
    "v_exp_f32 v60, v60\n\t" \
    "v_add_f32 v225, v225, v59\n\t" \
    "v_cvt_pk_bf16_f32 v221, v58, v59\n\t" \
    "v_mfma_f32_32x32x16_bf16 v[16:31], a[156:159], a[60:63], v[16:31]\n\t" \
    "v_exp_f32 v61, v61\n\t" \
    "v_add_f32 v225, v225, v60\n\t" \
    "s_waitcnt lgkmcnt(3)\n\t" \
    "v_mfma_f32_32x32x16_bf16 v[64:79], a[128:131], v[208:211], v[64:79]\n\t" \
    "ds_read_b128 a[144:147], v227 offset:12288\n\t" \
    "v_exp_f32 v62, v62\n\t" \
    "v_add_f32 v225, v225, v61\n\t" \
    "v_cvt_pk_bf16_f32 v222, v60, v61\n\t" \
    "v_mfma_f32_32x32x16_bf16 v[80:95], a[128:131], v[212:215], v[80:95]\n\t" \
    "ds_read_b128 a[128:131], v226 offset:8192\n\t" \
    "v_exp_f32 v63, v63\n\t" \
    "v_add_f32 v225, v225, v62\n\t" \
    "s_waitcnt lgkmcnt(4)\n\t" \
    "v_mfma_f32_32x32x16_bf16 v[96:111], a[132:135], v[208:211], v[96:111]\n\t" \
    "ds_read_b128 a[148:151], v227 offset:13312\n\t" \
    "v_exp_f32 v0, v0\n\t" \
    "v_add_f32 v225, v225, v63\n\t" \
    "v_cvt_pk_bf16_f32 v223, v62, v63\n\t" \
    "v_mfma_f32_32x32x16_bf16 v[112:127], a[132:135], v[212:215], v[112:127]\n\t" \
    "ds_read_b128 a[132:135], v226 offset:9216\n\t" \
    "v_exp_f32 v1, v1\n\t" \
    "v_add_f32 v224, v224, v0\n\t" \
    "s_waitcnt lgkmcnt(5)\n\t" \
    "v_mfma_f32_32x32x16_bf16 v[128:143], a[136:139], v[208:211], v[128:143]\n\t" \
    "ds_read_b128 a[152:155], v227 offset:14336\n\t" \
    "v_exp_f32 v2, v2\n\t" \
    "v_add_f32 v224, v224, v1\n\t" \
    "v_cvt_pk_bf16_f32 v192, v0, v1\n\t" \
    "v_mfma_f32_32x32x16_bf16 v[144:159], a[136:139], v[212:215], v[144:159]\n\t" \
    "ds_read_b128 a[136:139], v226 offset:10240\n\t" \
    "v_exp_f32 v3, v3\n\t" \
    "v_add_f32 v224, v224, v2\n\t" \
    "s_waitcnt lgkmcnt(6)\n\t" \
    "v_mfma_f32_32x32x16_bf16 v[160:175], a[140:143], v[208:211], v[160:175]\n\t" \
    "ds_read_b128 a[156:159], v227 offset:15360\n\t" \
    "v_exp_f32 v4, v4\n\t" \
    "v_add_f32 v224, v224, v3\n\t" \
    "v_cvt_pk_bf16_f32 v193, v2, v3\n\t" \
    "v_mfma_f32_32x32x16_bf16 v[176:191], a[140:143], v[212:215], v[176:191]\n\t" \
    "ds_read_b128 a[140:143], v226 offset:11264\n\t" \
    "v_exp_f32 v5, v5\n\t" \
    "v_add_f32 v224, v224, v4\n\t" \
    "s_waitcnt lgkmcnt(7)\n\t" \
    "v_mfma_f32_32x32x16_bf16 v[64:79], a[144:147], v[216:219], v[64:79]\n\t" \
    "v_exp_f32 v6, v6\n\t" \
    "v_add_f32 v224, v224, v5\n\t" \
    "v_cvt_pk_bf16_f32 v194, v4, v5\n\t" \
    "v_mfma_f32_32x32x16_bf16 v[80:95], a[144:147], v[220:223], v[80:95]\n\t" \
    "ds_read_b128 a[144:147], v226 offset:12288\n\t" \
    "v_exp_f32 v7, v7\n\t" \
    "v_add_f32 v224, v224, v6\n\t" \
    "s_waitcnt lgkmcnt(6)\n\t" \
    "v_mfma_f32_32x32x16_bf16 v[96:111], a[148:151], v[216:219], v[96:111]\n\t" \
    "v_exp_f32 v16, v16\n\t" \
    "v_add_f32 v224, v224, v7\n\t" \
    "v_cvt_pk_bf16_f32 v195, v6, v7\n\t" \
    "v_mfma_f32_32x32x16_bf16 v[112:127], a[148:151], v[220:223], v[112:127]\n\t" \
    "ds_read_b128 a[148:151], v226 offset:13312\n\t" \
    "v_exp_f32 v17, v17\n\t" \
    "v_add_f32 v225, v225, v16\n\t" \
    "s_waitcnt lgkmcnt(5)\n\t" \
    "v_mfma_f32_32x32x16_bf16 v[128:143], a[152:155], v[216:219], v[128:143]\n\t" \
    "v_exp_f32 v18, v18\n\t" \
    "v_add_f32 v225, v225, v17\n\t" \
    "v_cvt_pk_bf16_f32 v196, v16, v17\n\t" \
    "v_mfma_f32_32x32x16_bf16 v[144:159], a[152:155], v[220:223], v[144:159]\n\t" \
    "ds_read_b128 a[152:155], v226 offset:14336\n\t" \
    "v_exp_f32 v19, v19\n\t" \
    "v_add_f32 v225, v225, v18\n\t" \
    "s_waitcnt lgkmcnt(4)\n\t" \
    "v_mfma_f32_32x32x16_bf16 v[160:175], a[156:159], v[216:219], v[160:175]\n\t" \
    "v_exp_f32 v20, v20\n\t" \
    "v_add_f32 v225, v225, v19\n\t" \
    "v_cvt_pk_bf16_f32 v197, v18, v19\n\t" \
    "v_xor_b32 v227, 0x8000, v227\n\t" \
    "v_mfma_f32_32x32x16_bf16 v[176:191], a[156:159], v[220:223], v[176:191]\n\t" \
    "ds_read_b128 a[156:159], v226 offset:15360\n\t" \
    "v_exp_f32 v21, v21\n\t" \
    "v_add_f32 v225, v225, v20\n\t" \
    "v_mfma_f32_32x32x16_bf16 v[32:47], a[128:131], a[0:3], 0\n\t" \
    "v_exp_f32 v22, v22\n\t" \
    "v_add_f32 v225, v225, v21\n\t" \
    "v_cvt_pk_bf16_f32 v198, v20, v21\n\t" \
    "v_mfma_f32_32x32x16_bf16 v[48:63], a[128:131], a[32:35], 0\n\t" \
    "v_exp_f32 v23, v23\n\t" \
    "v_add_f32 v225, v225, v22\n\t" \
    "v_mfma_f32_32x32x16_bf16 v[32:47], a[132:135], a[4:7], v[32:47]\n\t" \
    "v_exp_f32 v8, v8\n\t" \
    "v_add_f32 v225, v225, v23\n\t" \
    "v_cvt_pk_bf16_f32 v199, v22, v23\n\t" \
    "v_mfma_f32_32x32x16_bf16 v[48:63], a[132:135], a[36:39], v[48:63]\n\t" \
    "v_exp_f32 v9, v9\n\t" \
    "v_add_f32 v224, v224, v8\n\t" \
    "v_mfma_f32_32x32x16_bf16 v[32:47], a[136:139], a[8:11], v[32:47]\n\t" \
    "v_exp_f32 v10, v10\n\t" \
    "v_add_f32 v224, v224, v9\n\t" \
    "v_cvt_pk_bf16_f32 v200, v8, v9\n\t" \
    "v_mfma_f32_32x32x16_bf16 v[48:63], a[136:139], a[40:43], v[48:63]\n\t" \
    "v_exp_f32 v11, v11\n\t" \
    "v_add_f32 v224, v224, v10\n\t" \
    "s_waitcnt lgkmcnt(4)\n\t" \
    "v_mfma_f32_32x32x16_bf16 v[32:47], a[140:143], a[12:15], v[32:47]\n\t" \
    "v_exp_f32 v12, v12\n\t" \
    "v_add_f32 v224, v224, v11\n\t" \
    "v_cvt_pk_bf16_f32 v201, v10, v11\n\t" \
    "v_mfma_f32_32x32x16_bf16 v[48:63], a[140:143], a[44:47], v[48:63]\n\t" \
    "v_exp_f32 v13, v13\n\t" \
    "v_add_f32 v224, v224, v12\n\t" \
    "s_waitcnt vmcnt(0) lgkmcnt(0)\n\t" \
    "s_barrier\n\t" \
    "v_mfma_f32_32x32x16_bf16 v[32:47], a[144:147], a[16:19], v[32:47]\n\t" \
    "ds_read_b128 a[128:131], v227 offset:0\n\t" \
    "v_exp_f32 v14, v14\n\t" \
    "v_add_f32 v224, v224, v13\n\t" \
    "v_cvt_pk_bf16_f32 v202, v12, v13\n\t" \
    "s_mov_b32 m0, s16\n\t" \
    "v_mfma_f32_32x32x16_bf16 v[48:63], a[144:147], a[48:51], v[48:63]\n\t" \
    "buffer_load_dwordx4 v228, s[8:11], s12 offen offset:0 lds\n\t" \
    "v_exp_f32 v15, v15\n\t" \
    "v_add_f32 v224, v224, v14\n\t" \
    "v_mfma_f32_32x32x16_bf16 v[32:47], a[148:151], a[20:23], v[32:47]\n\t" \
    "ds_read_b128 a[132:135], v227 offset:1024\n\t" \
    "v_exp_f32 v24, v24\n\t" \
    "v_add_f32 v224, v224, v15\n\t" \
    "v_cvt_pk_bf16_f32 v203, v14, v15\n\t" \
    "v_mfma_f32_32x32x16_bf16 v[48:63], a[148:151], a[52:55], v[48:63]\n\t" \
    "buffer_load_dwordx4 v228, s[8:11], s12 offen offset:1024 lds\n\t" \
    "v_exp_f32 v25, v25\n\t" \
    "v_add_f32 v225, v225, v24\n\t" \
    "v_mfma_f32_32x32x16_bf16 v[32:47], a[152:155], a[24:27], v[32:47]\n\t" \
    "ds_read_b128 a[136:139], v227 offset:2048\n\t" \
    "v_exp_f32 v26, v26\n\t" \
    "v_add_f32 v225, v225, v25\n\t" \
    "v_cvt_pk_bf16_f32 v204, v24, v25\n\t" \
    "v_xor_b32 v226, 0x8000, v226\n\t" \
    "v_mfma_f32_32x32x16_bf16 v[48:63], a[152:155], a[56:59], v[48:63]\n\t" \
    "buffer_load_dwordx4 v228, s[8:11], s12 offen offset:2048 lds\n\t" \
    "v_exp_f32 v27, v27\n\t" \
    "v_add_f32 v225, v225, v26\n\t" \
    "v_mfma_f32_32x32x16_bf16 v[32:47], a[156:159], a[28:31], v[32:47]\n\t" \
    "ds_read_b128 a[140:143], v227 offset:3072\n\t" \
    "v_exp_f32 v28, v28\n\t" \
    "v_add_f32 v225, v225, v27\n\t" \
    "v_cvt_pk_bf16_f32 v205, v26, v27\n\t" \
    "v_mfma_f32_32x32x16_bf16 v[48:63], a[156:159], a[60:63], v[48:63]\n\t" \
    "buffer_load_dwordx4 v228, s[8:11], s12 offen offset:3072 lds\n\t" \
    "v_exp_f32 v29, v29\n\t" \
    "v_add_f32 v225, v225, v28\n\t" \
    "s_waitcnt lgkmcnt(3)\n\t" \
    "v_mfma_f32_32x32x16_bf16 v[64:79], a[128:131], v[192:195], v[64:79]\n\t" \
    "ds_read_b128 a[144:147], v227 offset:4096\n\t" \
    "v_exp_f32 v30, v30\n\t" \
    "v_add_f32 v225, v225, v29\n\t" \
    "v_cvt_pk_bf16_f32 v206, v28, v29\n\t" \
    "s_add_u32 m0, s16, 4096\n\t" \
    "v_mfma_f32_32x32x16_bf16 v[80:95], a[128:131], v[196:199], v[80:95]\n\t" \
    "buffer_load_dwordx4 v229, s[8:11], s12 offen offset:0 lds\n\t" \
    "v_exp_f32 v31, v31\n\t" \
    "v_add_f32 v225, v225, v30\n\t" \
    "s_waitcnt lgkmcnt(3)\n\t" \
    "v_mfma_f32_32x32x16_bf16 v[96:111], a[132:135], v[192:195], v[96:111]\n\t" \
    "ds_read_b128 a[148:151], v227 offset:5120\n\t" \
    "v_exp_f32 v32, v32\n\t" \
    "v_add_f32 v225, v225, v31\n\t" \
    "v_cvt_pk_bf16_f32 v207, v30, v31\n\t" \
    "v_mfma_f32_32x32x16_bf16 v[112:127], a[132:135], v[196:199], v[112:127]\n\t" \
    "buffer_load_dwordx4 v229, s[8:11], s12 offen offset:1024 lds\n\t" \
    "v_exp_f32 v33, v33\n\t" \
    "v_add_f32 v224, v224, v32\n\t" \
    "s_waitcnt lgkmcnt(3)\n\t" \
    "v_mfma_f32_32x32x16_bf16 v[128:143], a[136:139], v[192:195], v[128:143]\n\t" \
    "ds_read_b128 a[152:155], v227 offset:6144\n\t" \
    "v_exp_f32 v34, v34\n\t" \
    "v_add_f32 v224, v224, v33\n\t" \
    "v_cvt_pk_bf16_f32 v208, v32, v33\n\t" \
    "v_mfma_f32_32x32x16_bf16 v[144:159], a[136:139], v[196:199], v[144:159]\n\t" \
    "buffer_load_dwordx4 v229, s[8:11], s12 offen offset:2048 lds\n\t" \
    "v_exp_f32 v35, v35\n\t" \
    "v_add_f32 v224, v224, v34\n\t" \
    "s_waitcnt lgkmcnt(3)\n\t" \
    "v_mfma_f32_32x32x16_bf16 v[160:175], a[140:143], v[192:195], v[160:175]\n\t" \
    "ds_read_b128 a[156:159], v227 offset:7168\n\t" \
    "v_exp_f32 v36, v36\n\t" \
    "v_add_f32 v224, v224, v35\n\t" \
    "v_cvt_pk_bf16_f32 v209, v34, v35\n\t" \
    "v_mfma_f32_32x32x16_bf16 v[176:191], a[140:143], v[196:199], v[176:191]\n\t" \
    "buffer_load_dwordx4 v229, s[8:11], s12 offen offset:3072 lds\n\t" \
    "v_exp_f32 v37, v37\n\t" \
    "v_add_f32 v224, v224, v36\n\t" \
    "s_waitcnt lgkmcnt(3)\n\t" \
    "v_mfma_f32_32x32x16_bf16 v[64:79], a[144:147], v[200:203], v[64:79]\n\t" \
    "ds_read_b128 a[128:131], v226 offset:0\n\t" \
    "v_exp_f32 v38, v38\n\t" \
    "v_add_f32 v224, v224, v37\n\t" \
    "v_cvt_pk_bf16_f32 v210, v36, v37\n\t" \
    "s_add_u32 s12, s12, 0x8000\n\t" \
    "s_cmp_gt_u32 s12, s13\n\t" \
    "s_cselect_b32 s12, s27, s12\n\t" \
    "s_cselect_b32 s13, s29, s13\n\t" \
    "s_mov_b32 m0, s17\n\t" \
    "v_mfma_f32_32x32x16_bf16 v[80:95], a[144:147], v[204:207], v[80:95]\n\t" \
    "buffer_load_dwordx4 v228, s[8:11], s14 offen offset:0 lds\n\t" \
    "v_exp_f32 v39, v39\n\t" \
    "v_add_f32 v224, v224, v38\n\t" \
    "s_waitcnt lgkmcnt(3)\n\t" \
    "v_mfma_f32_32x32x16_bf16 v[96:111], a[148:151], v[200:203], v[96:111]\n\t" \
    "ds_read_b128 a[132:135], v226 offset:1024\n\t" \
    "v_exp_f32 v48, v48\n\t" \
    "v_add_f32 v224, v224, v39\n\t" \
    "v_cvt_pk_bf16_f32 v211, v38, v39\n\t" \
    "s_xor_b32 s16, s16, 0x8000\n\t" \
    "v_mfma_f32_32x32x16_bf16 v[112:127], a[148:151], v[204:207], v[112:127]\n\t" \
    "buffer_load_dwordx4 v228, s[8:11], s14 offen offset:1024 lds\n\t" \
    "v_exp_f32 v49, v49\n\t" \
    "v_add_f32 v225, v225, v48\n\t" \
    "s_waitcnt lgkmcnt(3)\n\t" \
    "v_mfma_f32_32x32x16_bf16 v[128:143], a[152:155], v[200:203], v[128:143]\n\t" \
    "ds_read_b128 a[136:139], v226 offset:2048\n\t" \
    "v_exp_f32 v50, v50\n\t" \
    "v_add_f32 v225, v225, v49\n\t" \
    "v_cvt_pk_bf16_f32 v212, v48, v49\n\t" \
    "v_mfma_f32_32x32x16_bf16 v[144:159], a[152:155], v[204:207], v[144:159]\n\t" \
    "buffer_load_dwordx4 v228, s[8:11], s14 offen offset:2048 lds\n\t" \
    "v_exp_f32 v51, v51\n\t" \
    "v_add_f32 v225, v225, v50\n\t" \
    "s_waitcnt lgkmcnt(3)\n\t" \
    "v_mfma_f32_32x32x16_bf16 v[160:175], a[156:159], v[200:203], v[160:175]\n\t" \
    "ds_read_b128 a[140:143], v226 offset:3072\n\t" \
    "v_exp_f32 v52, v52\n\t" \
    "v_add_f32 v225, v225, v51\n\t" \
    "v_cvt_pk_bf16_f32 v213, v50, v51\n\t" \
    "v_mfma_f32_32x32x16_bf16 v[176:191], a[156:159], v[204:207], v[176:191]\n\t" \
    "buffer_load_dwordx4 v228, s[8:11], s14 offen offset:3072 lds\n\t" \
    "v_exp_f32 v53, v53\n\t" \
    "v_add_f32 v225, v225, v52\n\t" \
    "s_sub_u32 s18, s18, 1\n\t" \
    "s_cmp_lg_u32 s18, 0\n\t" \
    "s_cbranch_scc1 MID_%=\n\t" \
    "LAST_%=:\n\t" \
    "s_waitcnt lgkmcnt(3)\n\t" \
    "v_mfma_f32_32x32x16_bf16 v[0:15], a[128:131], a[0:3], 0\n\t" \
    "ds_read_b128 a[144:147], v226 offset:4096\n\t" \
    "v_exp_f32 v54, v54\n\t" \
    "v_add_f32 v225, v225, v53\n\t" \
    "v_cvt_pk_bf16_f32 v214, v52, v53\n\t" \
    "s_add_u32 m0, s17, 4096\n\t" \
    "v_mfma_f32_32x32x16_bf16 v[16:31], a[128:131], a[32:35], 0\n\t" \
    "buffer_load_dwordx4 v229, s[8:11], s14 offen offset:0 lds\n\t" \
    "v_exp_f32 v55, v55\n\t" \
    "v_add_f32 v225, v225, v54\n\t" \
    "s_waitcnt lgkmcnt(3)\n\t" \
    "v_mfma_f32_32x32x16_bf16 v[0:15], a[132:135], a[4:7], v[0:15]\n\t" \
    "ds_read_b128 a[148:151], v226 offset:5120\n\t" \
    "v_exp_f32 v40, v40\n\t" \
    "v_add_f32 v225, v225, v55\n\t" \
    "v_cvt_pk_bf16_f32 v215, v54, v55\n\t" \
    "v_mfma_f32_32x32x16_bf16 v[16:31], a[132:135], a[36:39], v[16:31]\n\t" \
    "buffer_load_dwordx4 v229, s[8:11], s14 offen offset:1024 lds\n\t" \
    "v_exp_f32 v41, v41\n\t" \
    "v_add_f32 v224, v224, v40\n\t" \
    "s_waitcnt lgkmcnt(3)\n\t" \
    "v_mfma_f32_32x32x16_bf16 v[0:15], a[136:139], a[8:11], v[0:15]\n\t" \
    "ds_read_b128 a[152:155], v226 offset:6144\n\t" \
    "v_exp_f32 v42, v42\n\t" \
    "v_add_f32 v224, v224, v41\n\t" \
    "v_cvt_pk_bf16_f32 v216, v40, v41\n\t" \
    "v_mfma_f32_32x32x16_bf16 v[16:31], a[136:139], a[40:43], v[16:31]\n\t" \
    "buffer_load_dwordx4 v229, s[8:11], s14 offen offset:2048 lds\n\t" \
    "v_exp_f32 v43, v43\n\t" \
    "v_add_f32 v224, v224, v42\n\t" \
    "s_waitcnt lgkmcnt(3)\n\t" \
    "v_mfma_f32_32x32x16_bf16 v[0:15], a[140:143], a[12:15], v[0:15]\n\t" \
    "ds_read_b128 a[156:159], v226 offset:7168\n\t" \
    "v_exp_f32 v44, v44\n\t" \
    "v_add_f32 v224, v224, v43\n\t" \
    "v_cvt_pk_bf16_f32 v217, v42, v43\n\t" \
    "v_mfma_f32_32x32x16_bf16 v[16:31], a[140:143], a[44:47], v[16:31]\n\t" \
    "buffer_load_dwordx4 v229, s[8:11], s14 offen offset:3072 lds\n\t" \
    "v_exp_f32 v45, v45\n\t" \
    "v_add_f32 v224, v224, v44\n\t" \
    "s_waitcnt lgkmcnt(3)\n\t" \
    "v_mfma_f32_32x32x16_bf16 v[0:15], a[144:147], a[16:19], v[0:15]\n\t" \
    "ds_read_b128 a[128:131], v227 offset:8192\n\t" \
    "v_exp_f32 v46, v46\n\t" \
    "v_add_f32 v224, v224, v45\n\t" \
    "v_cvt_pk_bf16_f32 v218, v44, v45\n\t" \
    "s_add_u32 s14, s14, 0x8000\n\t" \
    "s_cmp_gt_u32 s14, s15\n\t" \
    "s_cselect_b32 s14, s28, s14\n\t" \
    "s_cselect_b32 s15, s30, s15\n\t" \
    "v_mfma_f32_32x32x16_bf16 v[16:31], a[144:147], a[48:51], v[16:31]\n\t" \
    "v_exp_f32 v47, v47\n\t" \
    "v_add_f32 v224, v224, v46\n\t" \
    "s_xor_b32 s17, s17, 0x8000\n\t" \
    "s_waitcnt lgkmcnt(3)\n\t" \
    "v_mfma_f32_32x32x16_bf16 v[0:15], a[148:151], a[20:23], v[0:15]\n\t" \
    "ds_read_b128 a[132:135], v227 offset:9216\n\t" \
    "v_exp_f32 v56, v56\n\t" \
    "v_add_f32 v224, v224, v47\n\t" \
    "v_cvt_pk_bf16_f32 v219, v46, v47\n\t" \
    "v_mfma_f32_32x32x16_bf16 v[16:31], a[148:151], a[52:55], v[16:31]\n\t" \
    "v_exp_f32 v57, v57\n\t" \
    "v_add_f32 v225, v225, v56\n\t" \
    "s_waitcnt lgkmcnt(3)\n\t" \
    "v_mfma_f32_32x32x16_bf16 v[0:15], a[152:155], a[24:27], v[0:15]\n\t" \
    "ds_read_b128 a[136:139], v227 offset:10240\n\t" \
    "v_exp_f32 v58, v58\n\t" \
    "v_add_f32 v225, v225, v57\n\t" \
    "v_cvt_pk_bf16_f32 v220, v56, v57\n\t" \
    "v_mfma_f32_32x32x16_bf16 v[16:31], a[152:155], a[56:59], v[16:31]\n\t" \
    "v_exp_f32 v59, v59\n\t" \
    "v_add_f32 v225, v225, v58\n\t" \
    "s_waitcnt lgkmcnt(3)\n\t" \
    "v_mfma_f32_32x32x16_bf16 v[0:15], a[156:159], a[28:31], v[0:15]\n\t" \
    "ds_read_b128 a[140:143], v227 offset:11264\n\t" \
    "v_exp_f32 v60, v60\n\t" \
    "v_add_f32 v225, v225, v59\n\t" \
    "v_cvt_pk_bf16_f32 v221, v58, v59\n\t" \
    "v_mfma_f32_32x32x16_bf16 v[16:31], a[156:159], a[60:63], v[16:31]\n\t" \
    "v_exp_f32 v61, v61\n\t" \
    "v_add_f32 v225, v225, v60\n\t" \
    "s_waitcnt lgkmcnt(3)\n\t" \
    "v_mfma_f32_32x32x16_bf16 v[64:79], a[128:131], v[208:211], v[64:79]\n\t" \
    "ds_read_b128 a[144:147], v227 offset:12288\n\t" \
    "v_exp_f32 v62, v62\n\t" \
    "v_add_f32 v225, v225, v61\n\t" \
    "v_cvt_pk_bf16_f32 v222, v60, v61\n\t" \
    "v_mfma_f32_32x32x16_bf16 v[80:95], a[128:131], v[212:215], v[80:95]\n\t" \
    "ds_read_b128 a[128:131], v226 offset:8192\n\t" \
    "v_exp_f32 v63, v63\n\t" \
    "v_add_f32 v225, v225, v62\n\t" \
    "s_waitcnt lgkmcnt(4)\n\t" \
    "v_mfma_f32_32x32x16_bf16 v[96:111], a[132:135], v[208:211], v[96:111]\n\t" \
    "ds_read_b128 a[148:151], v227 offset:13312\n\t" \
    "v_exp_f32 v0, v0\n\t" \
    "v_cmp_le_i32_e64 s[32:33], 0, v230\n\t" \
    "v_add_f32 v225, v225, v63\n\t" \
    "v_cvt_pk_bf16_f32 v223, v62, v63\n\t" \
    "v_mfma_f32_32x32x16_bf16 v[112:127], a[132:135], v[212:215], v[112:127]\n\t" \
    "ds_read_b128 a[132:135], v226 offset:9216\n\t" \
    "v_cndmask_b32_e64 v0, 0, v0, s[32:33]\n\t" \
    "v_exp_f32 v1, v1\n\t" \
    "v_cmp_le_i32_e64 s[34:35], 1, v230\n\t" \
    "v_add_f32 v224, v224, v0\n\t" \
    "s_waitcnt lgkmcnt(5)\n\t" \
    "v_mfma_f32_32x32x16_bf16 v[128:143], a[136:139], v[208:211], v[128:143]\n\t" \
    "ds_read_b128 a[152:155], v227 offset:14336\n\t" \
    "v_cndmask_b32_e64 v1, 0, v1, s[34:35]\n\t" \
    "v_exp_f32 v2, v2\n\t" \
    "v_cmp_le_i32_e64 s[32:33], 2, v230\n\t" \
    "v_add_f32 v224, v224, v1\n\t" \
    "v_cvt_pk_bf16_f32 v192, v0, v1\n\t" \
    "v_mfma_f32_32x32x16_bf16 v[144:159], a[136:139], v[212:215], v[144:159]\n\t" \
    "ds_read_b128 a[136:139], v226 offset:10240\n\t" \
    "v_cndmask_b32_e64 v2, 0, v2, s[32:33]\n\t" \
    "v_exp_f32 v3, v3\n\t" \
    "v_cmp_le_i32_e64 s[34:35], 3, v230\n\t" \
    "v_add_f32 v224, v224, v2\n\t" \
    "s_waitcnt lgkmcnt(6)\n\t" \
    "v_mfma_f32_32x32x16_bf16 v[160:175], a[140:143], v[208:211], v[160:175]\n\t" \
    "ds_read_b128 a[156:159], v227 offset:15360\n\t" \
    "v_cndmask_b32_e64 v3, 0, v3, s[34:35]\n\t" \
    "v_exp_f32 v4, v4\n\t" \
    "v_cmp_le_i32_e64 s[32:33], 4, v230\n\t" \
    "v_add_f32 v224, v224, v3\n\t" \
    "v_cvt_pk_bf16_f32 v193, v2, v3\n\t" \
    "v_mfma_f32_32x32x16_bf16 v[176:191], a[140:143], v[212:215], v[176:191]\n\t" \
    "ds_read_b128 a[140:143], v226 offset:11264\n\t" \
    "v_cndmask_b32_e64 v4, 0, v4, s[32:33]\n\t" \
    "v_exp_f32 v5, v5\n\t" \
    "v_cmp_le_i32_e64 s[34:35], 5, v230\n\t" \
    "v_add_f32 v224, v224, v4\n\t" \
    "s_waitcnt lgkmcnt(7)\n\t" \
    "v_mfma_f32_32x32x16_bf16 v[64:79], a[144:147], v[216:219], v[64:79]\n\t" \
    "v_cndmask_b32_e64 v5, 0, v5, s[34:35]\n\t" \
    "v_exp_f32 v6, v6\n\t" \
    "v_cmp_le_i32_e64 s[32:33], 6, v230\n\t" \
    "v_add_f32 v224, v224, v5\n\t" \
    "v_cvt_pk_bf16_f32 v194, v4, v5\n\t" \
    "v_mfma_f32_32x32x16_bf16 v[80:95], a[144:147], v[220:223], v[80:95]\n\t" \
    "ds_read_b128 a[144:147], v226 offset:12288\n\t" \
    "v_cndmask_b32_e64 v6, 0, v6, s[32:33]\n\t" \
    "v_exp_f32 v7, v7\n\t" \
    "v_cmp_le_i32_e64 s[34:35], 7, v230\n\t" \
    "v_add_f32 v224, v224, v6\n\t" \
    "s_waitcnt lgkmcnt(6)\n\t" \
    "v_mfma_f32_32x32x16_bf16 v[96:111], a[148:151], v[216:219], v[96:111]\n\t" \
    "v_cndmask_b32_e64 v7, 0, v7, s[34:35]\n\t" \
    "v_exp_f32 v16, v16\n\t" \
    "v_cmp_le_i32_e64 s[32:33], 0, v231\n\t" \
    "v_add_f32 v224, v224, v7\n\t" \
    "v_cvt_pk_bf16_f32 v195, v6, v7\n\t" \
    "v_mfma_f32_32x32x16_bf16 v[112:127], a[148:151], v[220:223], v[112:127]\n\t" \
    "ds_read_b128 a[148:151], v226 offset:13312\n\t" \
    "v_cndmask_b32_e64 v16, 0, v16, s[32:33]\n\t" \
    "v_exp_f32 v17, v17\n\t" \
    "v_cmp_le_i32_e64 s[34:35], 1, v231\n\t" \
    "v_add_f32 v225, v225, v16\n\t" \
    "s_waitcnt lgkmcnt(5)\n\t" \
    "v_mfma_f32_32x32x16_bf16 v[128:143], a[152:155], v[216:219], v[128:143]\n\t" \
    "v_cndmask_b32_e64 v17, 0, v17, s[34:35]\n\t" \
    "v_exp_f32 v18, v18\n\t" \
    "v_cmp_le_i32_e64 s[32:33], 2, v231\n\t" \
    "v_add_f32 v225, v225, v17\n\t" \
    "v_cvt_pk_bf16_f32 v196, v16, v17\n\t" \
    "v_mfma_f32_32x32x16_bf16 v[144:159], a[152:155], v[220:223], v[144:159]\n\t" \
    "ds_read_b128 a[152:155], v226 offset:14336\n\t" \
    "v_cndmask_b32_e64 v18, 0, v18, s[32:33]\n\t" \
    "v_exp_f32 v19, v19\n\t" \
    "v_cmp_le_i32_e64 s[34:35], 3, v231\n\t" \
    "v_add_f32 v225, v225, v18\n\t" \
    "s_waitcnt lgkmcnt(4)\n\t" \
    "v_mfma_f32_32x32x16_bf16 v[160:175], a[156:159], v[216:219], v[160:175]\n\t" \
    "v_cndmask_b32_e64 v19, 0, v19, s[34:35]\n\t" \
    "v_exp_f32 v20, v20\n\t" \
    "v_cmp_le_i32_e64 s[32:33], 4, v231\n\t" \
    "v_add_f32 v225, v225, v19\n\t" \
    "v_cvt_pk_bf16_f32 v197, v18, v19\n\t" \
    "v_xor_b32 v227, 0x8000, v227\n\t" \
    "v_mfma_f32_32x32x16_bf16 v[176:191], a[156:159], v[220:223], v[176:191]\n\t" \
    "ds_read_b128 a[156:159], v226 offset:15360\n\t" \
    "v_cndmask_b32_e64 v20, 0, v20, s[32:33]\n\t" \
    "v_exp_f32 v21, v21\n\t" \
    "v_cmp_le_i32_e64 s[34:35], 5, v231\n\t" \
    "v_add_f32 v225, v225, v20\n\t" \
    "v_mfma_f32_32x32x16_bf16 v[32:47], a[128:131], a[0:3], 0\n\t" \
    "v_cndmask_b32_e64 v21, 0, v21, s[34:35]\n\t" \
    "v_exp_f32 v22, v22\n\t" \
    "v_cmp_le_i32_e64 s[32:33], 6, v231\n\t" \
    "v_add_f32 v225, v225, v21\n\t" \
    "v_cvt_pk_bf16_f32 v198, v20, v21\n\t" \
    "v_mfma_f32_32x32x16_bf16 v[48:63], a[128:131], a[32:35], 0\n\t" \
    "v_cndmask_b32_e64 v22, 0, v22, s[32:33]\n\t" \
    "v_exp_f32 v23, v23\n\t" \
    "v_cmp_le_i32_e64 s[34:35], 7, v231\n\t" \
    "v_add_f32 v225, v225, v22\n\t" \
    "v_mfma_f32_32x32x16_bf16 v[32:47], a[132:135], a[4:7], v[32:47]\n\t" \
    "v_cndmask_b32_e64 v23, 0, v23, s[34:35]\n\t" \
    "v_exp_f32 v8, v8\n\t" \
    "v_cmp_le_i32_e64 s[32:33], 16, v230\n\t" \
    "v_add_f32 v225, v225, v23\n\t" \
    "v_cvt_pk_bf16_f32 v199, v22, v23\n\t" \
    "v_mfma_f32_32x32x16_bf16 v[48:63], a[132:135], a[36:39], v[48:63]\n\t" \
    "v_cndmask_b32_e64 v8, 0, v8, s[32:33]\n\t" \
    "v_exp_f32 v9, v9\n\t" \
    "v_cmp_le_i32_e64 s[34:35], 17, v230\n\t" \
    "v_add_f32 v224, v224, v8\n\t" \
    "v_mfma_f32_32x32x16_bf16 v[32:47], a[136:139], a[8:11], v[32:47]\n\t" \
    "v_cndmask_b32_e64 v9, 0, v9, s[34:35]\n\t" \
    "v_exp_f32 v10, v10\n\t" \
    "v_cmp_le_i32_e64 s[32:33], 18, v230\n\t" \
    "v_add_f32 v224, v224, v9\n\t" \
    "v_cvt_pk_bf16_f32 v200, v8, v9\n\t" \
    "v_mfma_f32_32x32x16_bf16 v[48:63], a[136:139], a[40:43], v[48:63]\n\t" \
    "v_cndmask_b32_e64 v10, 0, v10, s[32:33]\n\t" \
    "v_exp_f32 v11, v11\n\t" \
    "v_cmp_le_i32_e64 s[34:35], 19, v230\n\t" \
    "v_add_f32 v224, v224, v10\n\t" \
    "s_waitcnt lgkmcnt(4)\n\t" \
    "v_mfma_f32_32x32x16_bf16 v[32:47], a[140:143], a[12:15], v[32:47]\n\t" \
    "v_cndmask_b32_e64 v11, 0, v11, s[34:35]\n\t" \
    "v_exp_f32 v12, v12\n\t" \
    "v_cmp_le_i32_e64 s[32:33], 20, v230\n\t" \
    "v_add_f32 v224, v224, v11\n\t" \
    "v_cvt_pk_bf16_f32 v201, v10, v11\n\t" \
    "v_mfma_f32_32x32x16_bf16 v[48:63], a[140:143], a[44:47], v[48:63]\n\t" \
    "v_cndmask_b32_e64 v12, 0, v12, s[32:33]\n\t" \
    "v_exp_f32 v13, v13\n\t" \
    "v_cmp_le_i32_e64 s[34:35], 21, v230\n\t" \
    "v_add_f32 v224, v224, v12\n\t" \
    "s_waitcnt vmcnt(0) lgkmcnt(0)\n\t" \
    "s_barrier\n\t" \
    "v_mfma_f32_32x32x16_bf16 v[32:47], a[144:147], a[16:19], v[32:47]\n\t" \
    "ds_read_b128 a[128:131], v227 offset:0\n\t" \
    "v_cndmask_b32_e64 v13, 0, v13, s[34:35]\n\t" \
    "v_exp_f32 v14, v14\n\t" \
    "v_cmp_le_i32_e64 s[32:33], 22, v230\n\t" \
    "v_add_f32 v224, v224, v13\n\t" \
    "v_cvt_pk_bf16_f32 v202, v12, v13\n\t" \
    "v_mfma_f32_32x32x16_bf16 v[48:63], a[144:147], a[48:51], v[48:63]\n\t" \
    "v_cndmask_b32_e64 v14, 0, v14, s[32:33]\n\t" \
    "v_exp_f32 v15, v15\n\t" \
    "v_cmp_le_i32_e64 s[34:35], 23, v230\n\t" \
    "v_add_f32 v224, v224, v14\n\t" \
    "v_mfma_f32_32x32x16_bf16 v[32:47], a[148:151], a[20:23], v[32:47]\n\t" \
    "ds_read_b128 a[132:135], v227 offset:1024\n\t" \
    "v_cndmask_b32_e64 v15, 0, v15, s[34:35]\n\t" \
    "v_exp_f32 v24, v24\n\t" \
    "v_cmp_le_i32_e64 s[32:33], 16, v231\n\t" \
    "v_add_f32 v224, v224, v15\n\t" \
    "v_cvt_pk_bf16_f32 v203, v14, v15\n\t" \
    "v_mfma_f32_32x32x16_bf16 v[48:63], a[148:151], a[52:55], v[48:63]\n\t" \
    "v_cndmask_b32_e64 v24, 0, v24, s[32:33]\n\t" \
    "v_exp_f32 v25, v25\n\t" \
    "v_cmp_le_i32_e64 s[34:35], 17, v231\n\t" \
    "v_add_f32 v225, v225, v24\n\t" \
    "v_mfma_f32_32x32x16_bf16 v[32:47], a[152:155], a[24:27], v[32:47]\n\t" \
    "ds_read_b128 a[136:139], v227 offset:2048\n\t" \
    "v_cndmask_b32_e64 v25, 0, v25, s[34:35]\n\t" \
    "v_exp_f32 v26, v26\n\t" \
    "v_cmp_le_i32_e64 s[32:33], 18, v231\n\t" \
    "v_add_f32 v225, v225, v25\n\t" \
    "v_cvt_pk_bf16_f32 v204, v24, v25\n\t" \
    "v_xor_b32 v226, 0x8000, v226\n\t" \
    "v_mfma_f32_32x32x16_bf16 v[48:63], a[152:155], a[56:59], v[48:63]\n\t" \
    "v_cndmask_b32_e64 v26, 0, v26, s[32:33]\n\t" \
    "v_exp_f32 v27, v27\n\t" \
    "v_cmp_le_i32_e64 s[34:35], 19, v231\n\t" \
    "v_add_f32 v225, v225, v26\n\t" \
    "v_mfma_f32_32x32x16_bf16 v[32:47], a[156:159], a[28:31], v[32:47]\n\t" \
    "ds_read_b128 a[140:143], v227 offset:3072\n\t" \
    "v_cndmask_b32_e64 v27, 0, v27, s[34:35]\n\t" \
    "v_exp_f32 v28, v28\n\t" \
    "v_cmp_le_i32_e64 s[32:33], 20, v231\n\t" \
    "v_add_f32 v225, v225, v27\n\t" \
    "v_cvt_pk_bf16_f32 v205, v26, v27\n\t" \
    "v_mfma_f32_32x32x16_bf16 v[48:63], a[156:159], a[60:63], v[48:63]\n\t" \
    "v_cndmask_b32_e64 v28, 0, v28, s[32:33]\n\t" \
    "v_exp_f32 v29, v29\n\t" \
    "v_cmp_le_i32_e64 s[34:35], 21, v231\n\t" \
    "v_add_f32 v225, v225, v28\n\t" \
    "s_waitcnt lgkmcnt(3)\n\t" \
    "v_mfma_f32_32x32x16_bf16 v[64:79], a[128:131], v[192:195], v[64:79]\n\t" \
    "ds_read_b128 a[144:147], v227 offset:4096\n\t" \
    "v_cndmask_b32_e64 v29, 0, v29, s[34:35]\n\t" \
    "v_exp_f32 v30, v30\n\t" \
    "v_cmp_le_i32_e64 s[32:33], 22, v231\n\t" \
    "v_add_f32 v225, v225, v29\n\t" \
    "v_cvt_pk_bf16_f32 v206, v28, v29\n\t" \
    "v_mfma_f32_32x32x16_bf16 v[80:95], a[128:131], v[196:199], v[80:95]\n\t" \
    "v_cndmask_b32_e64 v30, 0, v30, s[32:33]\n\t" \
    "v_exp_f32 v31, v31\n\t" \
    "v_cmp_le_i32_e64 s[34:35], 23, v231\n\t" \
    "v_add_f32 v225, v225, v30\n\t" \
    "s_waitcnt lgkmcnt(3)\n\t" \
    "v_mfma_f32_32x32x16_bf16 v[96:111], a[132:135], v[192:195], v[96:111]\n\t" \
    "ds_read_b128 a[148:151], v227 offset:5120\n\t" \
    "v_cndmask_b32_e64 v31, 0, v31, s[34:35]\n\t" \
    "v_exp_f32 v32, v32\n\t" \
    "v_cmp_le_i32_e64 s[32:33], 32, v230\n\t" \
    "v_add_f32 v225, v225, v31\n\t" \
    "v_cvt_pk_bf16_f32 v207, v30, v31\n\t" \
    "v_mfma_f32_32x32x16_bf16 v[112:127], a[132:135], v[196:199], v[112:127]\n\t" \
    "v_cndmask_b32_e64 v32, 0, v32, s[32:33]\n\t" \
    "v_exp_f32 v33, v33\n\t" \
    "v_cmp_le_i32_e64 s[34:35], 33, v230\n\t" \
    "v_add_f32 v224, v224, v32\n\t" \
    "s_waitcnt lgkmcnt(3)\n\t" \
    "v_mfma_f32_32x32x16_bf16 v[128:143], a[136:139], v[192:195], v[128:143]\n\t" \
    "ds_read_b128 a[152:155], v227 offset:6144\n\t" \
    "v_cndmask_b32_e64 v33, 0, v33, s[34:35]\n\t" \
    "v_exp_f32 v34, v34\n\t" \
    "v_cmp_le_i32_e64 s[32:33], 34, v230\n\t" \
    "v_add_f32 v224, v224, v33\n\t" \
    "v_cvt_pk_bf16_f32 v208, v32, v33\n\t" \
    "v_mfma_f32_32x32x16_bf16 v[144:159], a[136:139], v[196:199], v[144:159]\n\t" \
    "v_cndmask_b32_e64 v34, 0, v34, s[32:33]\n\t" \
    "v_exp_f32 v35, v35\n\t" \
    "v_cmp_le_i32_e64 s[34:35], 35, v230\n\t" \
    "v_add_f32 v224, v224, v34\n\t" \
    "s_waitcnt lgkmcnt(3)\n\t" \
    "v_mfma_f32_32x32x16_bf16 v[160:175], a[140:143], v[192:195], v[160:175]\n\t" \
    "ds_read_b128 a[156:159], v227 offset:7168\n\t" \
    "v_cndmask_b32_e64 v35, 0, v35, s[34:35]\n\t" \
    "v_exp_f32 v36, v36\n\t" \
    "v_cmp_le_i32_e64 s[32:33], 36, v230\n\t" \
    "v_add_f32 v224, v224, v35\n\t" \
    "v_cvt_pk_bf16_f32 v209, v34, v35\n\t" \
    "v_mfma_f32_32x32x16_bf16 v[176:191], a[140:143], v[196:199], v[176:191]\n\t" \
    "v_cndmask_b32_e64 v36, 0, v36, s[32:33]\n\t" \
    "v_exp_f32 v37, v37\n\t" \
    "v_cmp_le_i32_e64 s[34:35], 37, v230\n\t" \
    "v_add_f32 v224, v224, v36\n\t" \
    "s_waitcnt lgkmcnt(3)\n\t" \
    "v_mfma_f32_32x32x16_bf16 v[64:79], a[144:147], v[200:203], v[64:79]\n\t" \
    "v_cndmask_b32_e64 v37, 0, v37, s[34:35]\n\t" \
    "v_exp_f32 v38, v38\n\t" \
    "v_cmp_le_i32_e64 s[32:33], 38, v230\n\t" \
    "v_add_f32 v224, v224, v37\n\t" \
    "v_cvt_pk_bf16_f32 v210, v36, v37\n\t" \
    "v_mfma_f32_32x32x16_bf16 v[80:95], a[144:147], v[204:207], v[80:95]\n\t" \
    "v_cndmask_b32_e64 v38, 0, v38, s[32:33]\n\t" \
    "v_exp_f32 v39, v39\n\t" \
    "v_cmp_le_i32_e64 s[34:35], 39, v230\n\t" \
    "v_add_f32 v224, v224, v38\n\t" \
    "s_waitcnt lgkmcnt(2)\n\t" \
    "v_mfma_f32_32x32x16_bf16 v[96:111], a[148:151], v[200:203], v[96:111]\n\t" \
    "v_cndmask_b32_e64 v39, 0, v39, s[34:35]\n\t" \
    "v_exp_f32 v48, v48\n\t" \
    "v_cmp_le_i32_e64 s[32:33], 32, v231\n\t" \
    "v_add_f32 v224, v224, v39\n\t" \
    "v_cvt_pk_bf16_f32 v211, v38, v39\n\t" \
    "v_mfma_f32_32x32x16_bf16 v[112:127], a[148:151], v[204:207], v[112:127]\n\t" \
    "v_cndmask_b32_e64 v48, 0, v48, s[32:33]\n\t" \
    "v_exp_f32 v49, v49\n\t" \
    "v_cmp_le_i32_e64 s[34:35], 33, v231\n\t" \
    "v_add_f32 v225, v225, v48\n\t" \
    "s_waitcnt lgkmcnt(1)\n\t" \
    "v_mfma_f32_32x32x16_bf16 v[128:143], a[152:155], v[200:203], v[128:143]\n\t" \
    "v_cndmask_b32_e64 v49, 0, v49, s[34:35]\n\t" \
    "v_exp_f32 v50, v50\n\t" \
    "v_cmp_le_i32_e64 s[32:33], 34, v231\n\t" \
    "v_add_f32 v225, v225, v49\n\t" \
    "v_cvt_pk_bf16_f32 v212, v48, v49\n\t" \
    "v_mfma_f32_32x32x16_bf16 v[144:159], a[152:155], v[204:207], v[144:159]\n\t" \
    "v_cndmask_b32_e64 v50, 0, v50, s[32:33]\n\t" \
    "v_exp_f32 v51, v51\n\t" \
    "v_cmp_le_i32_e64 s[34:35], 35, v231\n\t" \
    "v_add_f32 v225, v225, v50\n\t" \
    "s_waitcnt lgkmcnt(0)\n\t" \
    "v_mfma_f32_32x32x16_bf16 v[160:175], a[156:159], v[200:203], v[160:175]\n\t" \
    "v_cndmask_b32_e64 v51, 0, v51, s[34:35]\n\t" \
    "v_exp_f32 v52, v52\n\t" \
    "v_cmp_le_i32_e64 s[32:33], 36, v231\n\t" \
    "v_add_f32 v225, v225, v51\n\t" \
    "v_cvt_pk_bf16_f32 v213, v50, v51\n\t" \
    "v_mfma_f32_32x32x16_bf16 v[176:191], a[156:159], v[204:207], v[176:191]\n\t" \
    "v_cndmask_b32_e64 v52, 0, v52, s[32:33]\n\t" \
    "v_exp_f32 v53, v53\n\t" \
    "v_cmp_le_i32_e64 s[34:35], 37, v231\n\t" \
    "v_add_f32 v225, v225, v52\n\t" \
    "s_branch DRAIN_%=\n\t" \
    "FL_%=:\n\t" \
    "s_waitcnt lgkmcnt(3)\n\t" \
    "v_mfma_f32_32x32x16_bf16 v[0:15], a[128:131], a[0:3], 0\n\t" \
    "ds_read_b128 a[144:147], v226 offset:4096\n\t" \
    "v_mfma_f32_32x32x16_bf16 v[16:31], a[128:131], a[32:35], 0\n\t" \
    "s_waitcnt lgkmcnt(3)\n\t" \
    "v_mfma_f32_32x32x16_bf16 v[0:15], a[132:135], a[4:7], v[0:15]\n\t" \
    "ds_read_b128 a[148:151], v226 offset:5120\n\t" \
    "v_mfma_f32_32x32x16_bf16 v[16:31], a[132:135], a[36:39], v[16:31]\n\t" \
    "s_waitcnt lgkmcnt(3)\n\t" \
    "v_mfma_f32_32x32x16_bf16 v[0:15], a[136:139], a[8:11], v[0:15]\n\t" \
    "ds_read_b128 a[152:155], v226 offset:6144\n\t" \
    "v_mfma_f32_32x32x16_bf16 v[16:31], a[136:139], a[40:43], v[16:31]\n\t" \
    "s_waitcnt lgkmcnt(3)\n\t" \
    "v_mfma_f32_32x32x16_bf16 v[0:15], a[140:143], a[12:15], v[0:15]\n\t" \
    "ds_read_b128 a[156:159], v226 offset:7168\n\t" \
    "v_mfma_f32_32x32x16_bf16 v[16:31], a[140:143], a[44:47], v[16:31]\n\t" \
    "s_waitcnt lgkmcnt(3)\n\t" \
    "v_mfma_f32_32x32x16_bf16 v[0:15], a[144:147], a[16:19], v[0:15]\n\t" \
    "v_mfma_f32_32x32x16_bf16 v[16:31], a[144:147], a[48:51], v[16:31]\n\t" \
    "s_waitcnt lgkmcnt(2)\n\t" \
    "v_mfma_f32_32x32x16_bf16 v[0:15], a[148:151], a[20:23], v[0:15]\n\t" \
    "v_mfma_f32_32x32x16_bf16 v[16:31], a[148:151], a[52:55], v[16:31]\n\t" \
    "s_waitcnt lgkmcnt(1)\n\t" \
    "v_mfma_f32_32x32x16_bf16 v[0:15], a[152:155], a[24:27], v[0:15]\n\t" \
    "v_mfma_f32_32x32x16_bf16 v[16:31], a[152:155], a[56:59], v[16:31]\n\t" \
    "s_waitcnt lgkmcnt(0)\n\t" \
    "v_mfma_f32_32x32x16_bf16 v[0:15], a[156:159], a[28:31], v[0:15]\n\t" \
    "v_mfma_f32_32x32x16_bf16 v[16:31], a[156:159], a[60:63], v[16:31]\n\t" \
    "ds_read_b128 a[128:131], v226 offset:8192\n\t" \
    "s_nop 7\n\t" \
    "s_nop 1\n\t" \
    "v_exp_f32 v0, v0\n\t" \
    "v_cmp_le_i32_e64 s[32:33], 0, v230\n\t" \
    "ds_read_b128 a[132:135], v226 offset:9216\n\t" \
    "s_nop 0\n\t" \
    "v_cndmask_b32_e64 v0, 0, v0, s[32:33]\n\t" \
    "v_exp_f32 v1, v1\n\t" \
    "v_cmp_le_i32_e64 s[34:35], 1, v230\n\t" \
    "v_add_f32 v224, v224, v0\n\t" \
    "s_nop 0\n\t" \
    "v_cndmask_b32_e64 v1, 0, v1, s[34:35]\n\t" \
    "v_exp_f32 v2, v2\n\t" \
    "v_cmp_le_i32_e64 s[32:33], 2, v230\n\t" \
    "v_add_f32 v224, v224, v1\n\t" \
    "v_cvt_pk_bf16_f32 v192, v0, v1\n\t" \
    "ds_read_b128 a[136:139], v226 offset:10240\n\t" \
    "v_cndmask_b32_e64 v2, 0, v2, s[32:33]\n\t" \
    "v_exp_f32 v3, v3\n\t" \
    "v_cmp_le_i32_e64 s[34:35], 3, v230\n\t" \
    "v_add_f32 v224, v224, v2\n\t" \
    "s_nop 0\n\t" \
    "v_cndmask_b32_e64 v3, 0, v3, s[34:35]\n\t" \
    "v_exp_f32 v4, v4\n\t" \
    "v_cmp_le_i32_e64 s[32:33], 4, v230\n\t" \
    "v_add_f32 v224, v224, v3\n\t" \
    "v_cvt_pk_bf16_f32 v193, v2, v3\n\t" \
    "ds_read_b128 a[140:143], v226 offset:11264\n\t" \
    "v_cndmask_b32_e64 v4, 0, v4, s[32:33]\n\t" \
    "v_exp_f32 v5, v5\n\t" \
    "v_cmp_le_i32_e64 s[34:35], 5, v230\n\t" \
    "v_add_f32 v224, v224, v4\n\t" \
    "s_nop 0\n\t" \
    "v_cndmask_b32_e64 v5, 0, v5, s[34:35]\n\t" \
    "v_exp_f32 v6, v6\n\t" \
    "v_cmp_le_i32_e64 s[32:33], 6, v230\n\t" \
    "v_add_f32 v224, v224, v5\n\t" \
    "v_cvt_pk_bf16_f32 v194, v4, v5\n\t" \
    "ds_read_b128 a[144:147], v226 offset:12288\n\t" \
    "v_cndmask_b32_e64 v6, 0, v6, s[32:33]\n\t" \
    "v_exp_f32 v7, v7\n\t" \
    "v_cmp_le_i32_e64 s[34:35], 7, v230\n\t" \
    "v_add_f32 v224, v224, v6\n\t" \
    "s_nop 0\n\t" \
    "v_cndmask_b32_e64 v7, 0, v7, s[34:35]\n\t" \
    "v_exp_f32 v16, v16\n\t" \
    "v_cmp_le_i32_e64 s[32:33], 0, v231\n\t" \
    "v_add_f32 v224, v224, v7\n\t" \
    "v_cvt_pk_bf16_f32 v195, v6, v7\n\t" \
    "ds_read_b128 a[148:151], v226 offset:13312\n\t" \
    "v_cndmask_b32_e64 v16, 0, v16, s[32:33]\n\t" \
    "v_exp_f32 v17, v17\n\t" \
    "v_cmp_le_i32_e64 s[34:35], 1, v231\n\t" \
    "v_add_f32 v225, v225, v16\n\t" \
    "s_nop 0\n\t" \
    "v_cndmask_b32_e64 v17, 0, v17, s[34:35]\n\t" \
    "v_exp_f32 v18, v18\n\t" \
    "v_cmp_le_i32_e64 s[32:33], 2, v231\n\t" \
    "v_add_f32 v225, v225, v17\n\t" \
    "v_cvt_pk_bf16_f32 v196, v16, v17\n\t" \
    "ds_read_b128 a[152:155], v226 offset:14336\n\t" \
    "v_cndmask_b32_e64 v18, 0, v18, s[32:33]\n\t" \
    "v_exp_f32 v19, v19\n\t" \
    "v_cmp_le_i32_e64 s[34:35], 3, v231\n\t" \
    "v_add_f32 v225, v225, v18\n\t" \
    "s_nop 0\n\t" \
    "v_cndmask_b32_e64 v19, 0, v19, s[34:35]\n\t" \
    "v_exp_f32 v20, v20\n\t" \
    "v_cmp_le_i32_e64 s[32:33], 4, v231\n\t" \
    "v_add_f32 v225, v225, v19\n\t" \
    "v_cvt_pk_bf16_f32 v197, v18, v19\n\t" \
    "v_xor_b32 v227, 0x8000, v227\n\t" \
    "ds_read_b128 a[156:159], v226 offset:15360\n\t" \
    "v_cndmask_b32_e64 v20, 0, v20, s[32:33]\n\t" \
    "v_exp_f32 v21, v21\n\t" \
    "v_cmp_le_i32_e64 s[34:35], 5, v231\n\t" \
    "v_add_f32 v225, v225, v20\n\t" \
    "s_waitcnt lgkmcnt(7)\n\t" \
    "v_mfma_f32_32x32x16_bf16 v[32:47], a[128:131], a[0:3], 0\n\t" \
    "v_cndmask_b32_e64 v21, 0, v21, s[34:35]\n\t" \
    "v_exp_f32 v22, v22\n\t" \
    "v_cmp_le_i32_e64 s[32:33], 6, v231\n\t" \
    "v_add_f32 v225, v225, v21\n\t" \
    "v_cvt_pk_bf16_f32 v198, v20, v21\n\t" \
    "v_mfma_f32_32x32x16_bf16 v[48:63], a[128:131], a[32:35], 0\n\t" \
    "v_cndmask_b32_e64 v22, 0, v22, s[32:33]\n\t" \
    "v_exp_f32 v23, v23\n\t" \
    "v_cmp_le_i32_e64 s[34:35], 7, v231\n\t" \
    "v_add_f32 v225, v225, v22\n\t" \
    "s_waitcnt lgkmcnt(6)\n\t" \
    "v_mfma_f32_32x32x16_bf16 v[32:47], a[132:135], a[4:7], v[32:47]\n\t" \
    "v_cndmask_b32_e64 v23, 0, v23, s[34:35]\n\t" \
    "v_exp_f32 v8, v8\n\t" \
    "v_cmp_le_i32_e64 s[32:33], 16, v230\n\t" \
    "v_add_f32 v225, v225, v23\n\t" \
    "v_cvt_pk_bf16_f32 v199, v22, v23\n\t" \
    "v_mfma_f32_32x32x16_bf16 v[48:63], a[132:135], a[36:39], v[48:63]\n\t" \
    "v_cndmask_b32_e64 v8, 0, v8, s[32:33]\n\t" \
    "v_exp_f32 v9, v9\n\t" \
    "v_cmp_le_i32_e64 s[34:35], 17, v230\n\t" \
    "v_add_f32 v224, v224, v8\n\t" \
    "s_waitcnt lgkmcnt(5)\n\t" \
    "v_mfma_f32_32x32x16_bf16 v[32:47], a[136:139], a[8:11], v[32:47]\n\t" \
    "v_cndmask_b32_e64 v9, 0, v9, s[34:35]\n\t" \
    "v_exp_f32 v10, v10\n\t" \
    "v_cmp_le_i32_e64 s[32:33], 18, v230\n\t" \
    "v_add_f32 v224, v224, v9\n\t" \
    "v_cvt_pk_bf16_f32 v200, v8, v9\n\t" \
    "v_mfma_f32_32x32x16_bf16 v[48:63], a[136:139], a[40:43], v[48:63]\n\t" \
    "v_cndmask_b32_e64 v10, 0, v10, s[32:33]\n\t" \
    "v_exp_f32 v11, v11\n\t" \
    "v_cmp_le_i32_e64 s[34:35], 19, v230\n\t" \
    "v_add_f32 v224, v224, v10\n\t" \
    "s_waitcnt lgkmcnt(4)\n\t" \
    "v_mfma_f32_32x32x16_bf16 v[32:47], a[140:143], a[12:15], v[32:47]\n\t" \
    "v_cndmask_b32_e64 v11, 0, v11, s[34:35]\n\t" \
    "v_exp_f32 v12, v12\n\t" \
    "v_cmp_le_i32_e64 s[32:33], 20, v230\n\t" \
    "v_add_f32 v224, v224, v11\n\t" \
    "v_cvt_pk_bf16_f32 v201, v10, v11\n\t" \
    "v_mfma_f32_32x32x16_bf16 v[48:63], a[140:143], a[44:47], v[48:63]\n\t" \
    "v_cndmask_b32_e64 v12, 0, v12, s[32:33]\n\t" \
    "v_exp_f32 v13, v13\n\t" \
    "v_cmp_le_i32_e64 s[34:35], 21, v230\n\t" \
    "v_add_f32 v224, v224, v12\n\t" \
    "s_waitcnt vmcnt(0) lgkmcnt(0)\n\t" \
    "s_barrier\n\t" \
    "v_mfma_f32_32x32x16_bf16 v[32:47], a[144:147], a[16:19], v[32:47]\n\t" \
    "ds_read_b128 a[128:131], v227 offset:0\n\t" \
    "v_cndmask_b32_e64 v13, 0, v13, s[34:35]\n\t" \
    "v_exp_f32 v14, v14\n\t" \
    "v_cmp_le_i32_e64 s[32:33], 22, v230\n\t" \
    "v_add_f32 v224, v224, v13\n\t" \
    "v_cvt_pk_bf16_f32 v202, v12, v13\n\t" \
    "v_mfma_f32_32x32x16_bf16 v[48:63], a[144:147], a[48:51], v[48:63]\n\t" \
    "v_cndmask_b32_e64 v14, 0, v14, s[32:33]\n\t" \
    "v_exp_f32 v15, v15\n\t" \
    "v_cmp_le_i32_e64 s[34:35], 23, v230\n\t" \
    "v_add_f32 v224, v224, v14\n\t" \
    "v_mfma_f32_32x32x16_bf16 v[32:47], a[148:151], a[20:23], v[32:47]\n\t" \
    "ds_read_b128 a[132:135], v227 offset:1024\n\t" \
    "v_cndmask_b32_e64 v15, 0, v15, s[34:35]\n\t" \
    "v_exp_f32 v24, v24\n\t" \
    "v_cmp_le_i32_e64 s[32:33], 16, v231\n\t" \
    "v_add_f32 v224, v224, v15\n\t" \
    "v_cvt_pk_bf16_f32 v203, v14, v15\n\t" \
    "v_mfma_f32_32x32x16_bf16 v[48:63], a[148:151], a[52:55], v[48:63]\n\t" \
    "v_cndmask_b32_e64 v24, 0, v24, s[32:33]\n\t" \
    "v_exp_f32 v25, v25\n\t" \
    "v_cmp_le_i32_e64 s[34:35], 17, v231\n\t" \
    "v_add_f32 v225, v225, v24\n\t" \
    "v_mfma_f32_32x32x16_bf16 v[32:47], a[152:155], a[24:27], v[32:47]\n\t" \
    "ds_read_b128 a[136:139], v227 offset:2048\n\t" \
    "v_cndmask_b32_e64 v25, 0, v25, s[34:35]\n\t" \
    "v_exp_f32 v26, v26\n\t" \
    "v_cmp_le_i32_e64 s[32:33], 18, v231\n\t" \
    "v_add_f32 v225, v225, v25\n\t" \
    "v_cvt_pk_bf16_f32 v204, v24, v25\n\t" \
    "v_xor_b32 v226, 0x8000, v226\n\t" \
    "v_mfma_f32_32x32x16_bf16 v[48:63], a[152:155], a[56:59], v[48:63]\n\t" \
    "v_cndmask_b32_e64 v26, 0, v26, s[32:33]\n\t" \
    "v_exp_f32 v27, v27\n\t" \
    "v_cmp_le_i32_e64 s[34:35], 19, v231\n\t" \
    "v_add_f32 v225, v225, v26\n\t" \
    "v_mfma_f32_32x32x16_bf16 v[32:47], a[156:159], a[28:31], v[32:47]\n\t" \
    "ds_read_b128 a[140:143], v227 offset:3072\n\t" \
    "v_cndmask_b32_e64 v27, 0, v27, s[34:35]\n\t" \
    "v_exp_f32 v28, v28\n\t" \
    "v_cmp_le_i32_e64 s[32:33], 20, v231\n\t" \
    "v_add_f32 v225, v225, v27\n\t" \
    "v_cvt_pk_bf16_f32 v205, v26, v27\n\t" \
    "v_mfma_f32_32x32x16_bf16 v[48:63], a[156:159], a[60:63], v[48:63]\n\t" \
    "v_cndmask_b32_e64 v28, 0, v28, s[32:33]\n\t" \
    "v_exp_f32 v29, v29\n\t" \
    "v_cmp_le_i32_e64 s[34:35], 21, v231\n\t" \
    "v_add_f32 v225, v225, v28\n\t" \
    "s_waitcnt lgkmcnt(3)\n\t" \
    "v_mfma_f32_32x32x16_bf16 v[64:79], a[128:131], v[192:195], 0\n\t" \
    "ds_read_b128 a[144:147], v227 offset:4096\n\t" \
    "v_cndmask_b32_e64 v29, 0, v29, s[34:35]\n\t" \
    "v_exp_f32 v30, v30\n\t" \
    "v_cmp_le_i32_e64 s[32:33], 22, v231\n\t" \
    "v_add_f32 v225, v225, v29\n\t" \
    "v_cvt_pk_bf16_f32 v206, v28, v29\n\t" \
    "v_mfma_f32_32x32x16_bf16 v[80:95], a[128:131], v[196:199], 0\n\t" \
    "v_cndmask_b32_e64 v30, 0, v30, s[32:33]\n\t" \
    "v_exp_f32 v31, v31\n\t" \
    "v_cmp_le_i32_e64 s[34:35], 23, v231\n\t" \
    "v_add_f32 v225, v225, v30\n\t" \
    "s_waitcnt lgkmcnt(3)\n\t" \
    "v_mfma_f32_32x32x16_bf16 v[96:111], a[132:135], v[192:195], 0\n\t" \
    "ds_read_b128 a[148:151], v227 offset:5120\n\t" \
    "v_cndmask_b32_e64 v31, 0, v31, s[34:35]\n\t" \
    "v_exp_f32 v32, v32\n\t" \
    "v_cmp_le_i32_e64 s[32:33], 32, v230\n\t" \
    "v_add_f32 v225, v225, v31\n\t" \
    "v_cvt_pk_bf16_f32 v207, v30, v31\n\t" \
    "v_mfma_f32_32x32x16_bf16 v[112:127], a[132:135], v[196:199], 0\n\t" \
    "v_cndmask_b32_e64 v32, 0, v32, s[32:33]\n\t" \
    "v_exp_f32 v33, v33\n\t" \
    "v_cmp_le_i32_e64 s[34:35], 33, v230\n\t" \
    "v_add_f32 v224, v224, v32\n\t" \
    "s_waitcnt lgkmcnt(3)\n\t" \
    "v_mfma_f32_32x32x16_bf16 v[128:143], a[136:139], v[192:195], 0\n\t" \
    "ds_read_b128 a[152:155], v227 offset:6144\n\t" \
    "v_cndmask_b32_e64 v33, 0, v33, s[34:35]\n\t" \
    "v_exp_f32 v34, v34\n\t" \
    "v_cmp_le_i32_e64 s[32:33], 34, v230\n\t" \
    "v_add_f32 v224, v224, v33\n\t" \
    "v_cvt_pk_bf16_f32 v208, v32, v33\n\t" \
    "v_mfma_f32_32x32x16_bf16 v[144:159], a[136:139], v[196:199], 0\n\t" \
    "v_cndmask_b32_e64 v34, 0, v34, s[32:33]\n\t" \
    "v_exp_f32 v35, v35\n\t" \
    "v_cmp_le_i32_e64 s[34:35], 35, v230\n\t" \
    "v_add_f32 v224, v224, v34\n\t" \
    "s_waitcnt lgkmcnt(3)\n\t" \
    "v_mfma_f32_32x32x16_bf16 v[160:175], a[140:143], v[192:195], 0\n\t" \
    "ds_read_b128 a[156:159], v227 offset:7168\n\t" \
    "v_cndmask_b32_e64 v35, 0, v35, s[34:35]\n\t" \
    "v_exp_f32 v36, v36\n\t" \
    "v_cmp_le_i32_e64 s[32:33], 36, v230\n\t" \
    "v_add_f32 v224, v224, v35\n\t" \
    "v_cvt_pk_bf16_f32 v209, v34, v35\n\t" \
    "v_mfma_f32_32x32x16_bf16 v[176:191], a[140:143], v[196:199], 0\n\t" \
    "v_cndmask_b32_e64 v36, 0, v36, s[32:33]\n\t" \
    "v_exp_f32 v37, v37\n\t" \
    "v_cmp_le_i32_e64 s[34:35], 37, v230\n\t" \
    "v_add_f32 v224, v224, v36\n\t" \
    "s_waitcnt lgkmcnt(3)\n\t" \
    "v_mfma_f32_32x32x16_bf16 v[64:79], a[144:147], v[200:203], v[64:79]\n\t" \
    "v_cndmask_b32_e64 v37, 0, v37, s[34:35]\n\t" \
    "v_exp_f32 v38, v38\n\t" \
    "v_cmp_le_i32_e64 s[32:33], 38, v230\n\t" \
    "v_add_f32 v224, v224, v37\n\t" \
    "v_cvt_pk_bf16_f32 v210, v36, v37\n\t" \
    "v_mfma_f32_32x32x16_bf16 v[80:95], a[144:147], v[204:207], v[80:95]\n\t" \
    "v_cndmask_b32_e64 v38, 0, v38, s[32:33]\n\t" \
    "v_exp_f32 v39, v39\n\t" \
    "v_cmp_le_i32_e64 s[34:35], 39, v230\n\t" \
    "v_add_f32 v224, v224, v38\n\t" \
    "s_waitcnt lgkmcnt(2)\n\t" \
    "v_mfma_f32_32x32x16_bf16 v[96:111], a[148:151], v[200:203], v[96:111]\n\t" \
    "v_cndmask_b32_e64 v39, 0, v39, s[34:35]\n\t" \
    "v_exp_f32 v48, v48\n\t" \
    "v_cmp_le_i32_e64 s[32:33], 32, v231\n\t" \
    "v_add_f32 v224, v224, v39\n\t" \
    "v_cvt_pk_bf16_f32 v211, v38, v39\n\t" \
    "v_mfma_f32_32x32x16_bf16 v[112:127], a[148:151], v[204:207], v[112:127]\n\t" \
    "v_cndmask_b32_e64 v48, 0, v48, s[32:33]\n\t" \
    "v_exp_f32 v49, v49\n\t" \
    "v_cmp_le_i32_e64 s[34:35], 33, v231\n\t" \
    "v_add_f32 v225, v225, v48\n\t" \
    "s_waitcnt lgkmcnt(1)\n\t" \
    "v_mfma_f32_32x32x16_bf16 v[128:143], a[152:155], v[200:203], v[128:143]\n\t" \
    "v_cndmask_b32_e64 v49, 0, v49, s[34:35]\n\t" \
    "v_exp_f32 v50, v50\n\t" \
    "v_cmp_le_i32_e64 s[32:33], 34, v231\n\t" \
    "v_add_f32 v225, v225, v49\n\t" \
    "v_cvt_pk_bf16_f32 v212, v48, v49\n\t" \
    "v_mfma_f32_32x32x16_bf16 v[144:159], a[152:155], v[204:207], v[144:159]\n\t" \
    "v_cndmask_b32_e64 v50, 0, v50, s[32:33]\n\t" \
    "v_exp_f32 v51, v51\n\t" \
    "v_cmp_le_i32_e64 s[34:35], 35, v231\n\t" \
    "v_add_f32 v225, v225, v50\n\t" \
    "s_waitcnt lgkmcnt(0)\n\t" \
    "v_mfma_f32_32x32x16_bf16 v[160:175], a[156:159], v[200:203], v[160:175]\n\t" \
    "v_cndmask_b32_e64 v51, 0, v51, s[34:35]\n\t" \
    "v_exp_f32 v52, v52\n\t" \
    "v_cmp_le_i32_e64 s[32:33], 36, v231\n\t" \
    "v_add_f32 v225, v225, v51\n\t" \
    "v_cvt_pk_bf16_f32 v213, v50, v51\n\t" \
    "v_mfma_f32_32x32x16_bf16 v[176:191], a[156:159], v[204:207], v[176:191]\n\t" \
    "v_cndmask_b32_e64 v52, 0, v52, s[32:33]\n\t" \
    "v_exp_f32 v53, v53\n\t" \
    "v_cmp_le_i32_e64 s[34:35], 37, v231\n\t" \
    "v_add_f32 v225, v225, v52\n\t" \
    "DRAIN_%=:\n\t" \
    "s_cmp_lg_u32 s20, 0\n\t" \
    "s_cbranch_scc1 NOPF_%=\n\t" \
    "global_load_dword v244, v246, %[pfa]\n\t" \
    "global_load_dword v245, v246, %[pfb]\n\t" \
    "NOPF_%=:\n\t" \
    "v_cndmask_b32_e64 v53, 0, v53, s[34:35]\n\t" \
    "v_exp_f32 v54, v54\n\t" \
    "v_cmp_le_i32_e64 s[32:33], 38, v231\n\t" \
    "v_add_f32 v225, v225, v53\n\t" \
    "v_cvt_pk_bf16_f32 v214, v52, v53\n\t" \
    "v_cndmask_b32_e64 v54, 0, v54, s[32:33]\n\t" \
    "v_exp_f32 v55, v55\n\t" \
    "v_cmp_le_i32_e64 s[34:35], 39, v231\n\t" \
    "v_add_f32 v225, v225, v54\n\t" \
    "s_nop 0\n\t" \
    "v_cndmask_b32_e64 v55, 0, v55, s[34:35]\n\t" \
    "v_exp_f32 v40, v40\n\t" \
    "v_cmp_le_i32_e64 s[32:33], 48, v230\n\t" \
    "v_add_f32 v225, v225, v55\n\t" \
    "v_cvt_pk_bf16_f32 v215, v54, v55\n\t" \
    "v_accvgpr_mov_b32 a0, a64\n\t" \
    "v_accvgpr_mov_b32 a1, a65\n\t" \
    "v_accvgpr_mov_b32 a2, a66\n\t" \
    "v_cndmask_b32_e64 v40, 0, v40, s[32:33]\n\t" \
    "v_exp_f32 v41, v41\n\t" \
    "v_cmp_le_i32_e64 s[34:35], 49, v230\n\t" \
    "v_add_f32 v224, v224, v40\n\t" \
    "v_accvgpr_mov_b32 a3, a67\n\t" \
    "v_accvgpr_mov_b32 a4, a68\n\t" \
    "v_accvgpr_mov_b32 a5, a69\n\t" \
    "v_cndmask_b32_e64 v41, 0, v41, s[34:35]\n\t" \
    "v_exp_f32 v42, v42\n\t" \
    "v_cmp_le_i32_e64 s[32:33], 50, v230\n\t" \
    "v_add_f32 v224, v224, v41\n\t" \
    "v_cvt_pk_bf16_f32 v216, v40, v41\n\t" \
    "v_accvgpr_mov_b32 a6, a70\n\t" \
    "v_accvgpr_mov_b32 a7, a71\n\t" \
    "v_accvgpr_mov_b32 a8, a72\n\t" \
    "v_cndmask_b32_e64 v42, 0, v42, s[32:33]\n\t" \
    "v_exp_f32 v43, v43\n\t" \
    "v_cmp_le_i32_e64 s[34:35], 51, v230\n\t" \
    "v_add_f32 v224, v224, v42\n\t" \
    "v_accvgpr_mov_b32 a9, a73\n\t" \
    "v_accvgpr_mov_b32 a10, a74\n\t" \
    "v_accvgpr_mov_b32 a11, a75\n\t" \
    "v_cndmask_b32_e64 v43, 0, v43, s[34:35]\n\t" \
    "v_exp_f32 v44, v44\n\t" \
    "v_cmp_le_i32_e64 s[32:33], 52, v230\n\t" \
    "v_add_f32 v224, v224, v43\n\t" \
    "v_cvt_pk_bf16_f32 v217, v42, v43\n\t" \
    "v_accvgpr_mov_b32 a12, a76\n\t" \
    "v_accvgpr_mov_b32 a13, a77\n\t" \
    "v_accvgpr_mov_b32 a14, a78\n\t" \
    "v_cndmask_b32_e64 v44, 0, v44, s[32:33]\n\t" \
    "v_exp_f32 v45, v45\n\t" \
    "v_cmp_le_i32_e64 s[34:35], 53, v230\n\t" \
    "v_add_f32 v224, v224, v44\n\t" \
    "v_accvgpr_mov_b32 a15, a79\n\t" \
    "v_accvgpr_mov_b32 a16, a80\n\t" \
    "v_accvgpr_mov_b32 a17, a81\n\t" \
    "ds_read_b128 a[128:131], v227 offset:8192\n\t" \
    "v_cndmask_b32_e64 v45, 0, v45, s[34:35]\n\t" \
    "v_exp_f32 v46, v46\n\t" \
    "v_cmp_le_i32_e64 s[32:33], 54, v230\n\t" \
    "v_add_f32 v224, v224, v45\n\t" \
    "v_cvt_pk_bf16_f32 v218, v44, v45\n\t" \
    "v_accvgpr_mov_b32 a18, a82\n\t" \
    "v_accvgpr_mov_b32 a19, a83\n\t" \
    "v_accvgpr_mov_b32 a20, a84\n\t" \
    "v_cndmask_b32_e64 v46, 0, v46, s[32:33]\n\t" \
    "v_exp_f32 v47, v47\n\t" \
    "v_cmp_le_i32_e64 s[34:35], 55, v230\n\t" \
    "v_add_f32 v224, v224, v46\n\t" \
    "v_accvgpr_mov_b32 a21, a85\n\t" \
    "v_accvgpr_mov_b32 a22, a86\n\t" \
    "v_accvgpr_mov_b32 a23, a87\n\t" \
    "ds_read_b128 a[132:135], v227 offset:9216\n\t" \
    "v_cndmask_b32_e64 v47, 0, v47, s[34:35]\n\t" \
    "v_exp_f32 v56, v56\n\t" \
    "v_cmp_le_i32_e64 s[32:33], 48, v231\n\t" \
    "v_add_f32 v224, v224, v47\n\t" \
    "v_cvt_pk_bf16_f32 v219, v46, v47\n\t" \
    "v_accvgpr_mov_b32 a24, a88\n\t" \
    "v_accvgpr_mov_b32 a25, a89\n\t" \
    "v_accvgpr_mov_b32 a26, a90\n\t" \
    "v_cndmask_b32_e64 v56, 0, v56, s[32:33]\n\t" \
    "v_exp_f32 v57, v57\n\t" \
    "v_cmp_le_i32_e64 s[34:35], 49, v231\n\t" \
    "v_add_f32 v225, v225, v56\n\t" \
    "v_accvgpr_mov_b32 a27, a91\n\t" \
    "v_accvgpr_mov_b32 a28, a92\n\t" \
    "v_accvgpr_mov_b32 a29, a93\n\t" \
    "ds_read_b128 a[136:139], v227 offset:10240\n\t" \
    "v_cndmask_b32_e64 v57, 0, v57, s[34:35]\n\t" \
    "v_exp_f32 v58, v58\n\t" \
    "v_cmp_le_i32_e64 s[32:33], 50, v231\n\t" \
    "v_add_f32 v225, v225, v57\n\t" \
    "v_cvt_pk_bf16_f32 v220, v56, v57\n\t" \
    "v_accvgpr_mov_b32 a30, a94\n\t" \
    "v_accvgpr_mov_b32 a31, a95\n\t" \
    "v_accvgpr_mov_b32 a32, a96\n\t" \
    "v_cndmask_b32_e64 v58, 0, v58, s[32:33]\n\t" \
    "v_exp_f32 v59, v59\n\t" \
    "v_cmp_le_i32_e64 s[34:35], 51, v231\n\t" \
    "v_add_f32 v225, v225, v58\n\t" \
    "v_accvgpr_mov_b32 a33, a97\n\t" \
    "v_accvgpr_mov_b32 a34, a98\n\t" \
    "v_accvgpr_mov_b32 a35, a99\n\t" \
    "ds_read_b128 a[140:143], v227 offset:11264\n\t" \
    "v_cndmask_b32_e64 v59, 0, v59, s[34:35]\n\t" \
    "v_exp_f32 v60, v60\n\t" \
    "v_cmp_le_i32_e64 s[32:33], 52, v231\n\t" \
    "v_add_f32 v225, v225, v59\n\t" \
    "v_cvt_pk_bf16_f32 v221, v58, v59\n\t" \
    "v_accvgpr_mov_b32 a36, a100\n\t" \
    "v_accvgpr_mov_b32 a37, a101\n\t" \
    "v_accvgpr_mov_b32 a38, a102\n\t" \
    "v_cndmask_b32_e64 v60, 0, v60, s[32:33]\n\t" \
    "v_exp_f32 v61, v61\n\t" \
    "v_cmp_le_i32_e64 s[34:35], 53, v231\n\t" \
    "v_add_f32 v225, v225, v60\n\t" \
    "v_accvgpr_mov_b32 a39, a103\n\t" \
    "v_accvgpr_mov_b32 a40, a104\n\t" \
    "v_accvgpr_mov_b32 a41, a105\n\t" \
    "s_waitcnt lgkmcnt(3)\n\t" \
    "v_mfma_f32_32x32x16_bf16 v[64:79], a[128:131], v[208:211], v[64:79]\n\t" \
    "ds_read_b128 a[144:147], v227 offset:12288\n\t" \
    "v_cndmask_b32_e64 v61, 0, v61, s[34:35]\n\t" \
    "v_exp_f32 v62, v62\n\t" \
    "v_cmp_le_i32_e64 s[32:33], 54, v231\n\t" \
    "v_add_f32 v225, v225, v61\n\t" \
    "v_cvt_pk_bf16_f32 v222, v60, v61\n\t" \
    "v_accvgpr_mov_b32 a42, a106\n\t" \
    "v_accvgpr_mov_b32 a43, a107\n\t" \
    "v_accvgpr_mov_b32 a44, a108\n\t" \
    "v_mfma_f32_32x32x16_bf16 v[80:95], a[128:131], v[212:215], v[80:95]\n\t" \
    "v_cndmask_b32_e64 v62, 0, v62, s[32:33]\n\t" \
    "v_exp_f32 v63, v63\n\t" \
    "v_cmp_le_i32_e64 s[34:35], 55, v231\n\t" \
    "v_add_f32 v225, v225, v62\n\t" \
    "v_accvgpr_mov_b32 a45, a109\n\t" \
    "v_accvgpr_mov_b32 a46, a110\n\t" \
    "v_accvgpr_mov_b32 a47, a111\n\t" \
    "s_waitcnt lgkmcnt(3)\n\t" \
    "v_mfma_f32_32x32x16_bf16 v[96:111], a[132:135], v[208:211], v[96:111]\n\t" \
    "ds_read_b128 a[148:151], v227 offset:13312\n\t" \
    "v_cndmask_b32_e64 v63, 0, v63, s[34:35]\n\t" \
    "v_add_f32 v225, v225, v63\n\t" \
    "v_cvt_pk_bf16_f32 v223, v62, v63\n\t" \
    "v_accvgpr_mov_b32 a48, a112\n\t" \
    "v_accvgpr_mov_b32 a49, a113\n\t" \
    "v_accvgpr_mov_b32 a50, a114\n\t" \
    "v_mfma_f32_32x32x16_bf16 v[112:127], a[132:135], v[212:215], v[112:127]\n\t" \
    "v_accvgpr_mov_b32 a51, a115\n\t" \
    "v_accvgpr_mov_b32 a52, a116\n\t" \
    "v_accvgpr_mov_b32 a53, a117\n\t" \
    "s_waitcnt lgkmcnt(3)\n\t" \
    "v_mfma_f32_32x32x16_bf16 v[128:143], a[136:139], v[208:211], v[128:143]\n\t" \
    "ds_read_b128 a[152:155], v227 offset:14336\n\t" \
    "v_accvgpr_mov_b32 a54, a118\n\t" \
    "v_accvgpr_mov_b32 a55, a119\n\t" \
    "v_accvgpr_mov_b32 a56, a120\n\t" \
    "v_mfma_f32_32x32x16_bf16 v[144:159], a[136:139], v[212:215], v[144:159]\n\t" \
    "v_accvgpr_mov_b32 a57, a121\n\t" \
    "v_accvgpr_mov_b32 a58, a122\n\t" \
    "v_accvgpr_mov_b32 a59, a123\n\t" \
    "s_waitcnt lgkmcnt(3)\n\t" \
    "v_mfma_f32_32x32x16_bf16 v[160:175], a[140:143], v[208:211], v[160:175]\n\t" \
    "ds_read_b128 a[156:159], v227 offset:15360\n\t" \
    "v_accvgpr_mov_b32 a60, a124\n\t" \
    "v_accvgpr_mov_b32 a61, a125\n\t" \
    "v_accvgpr_mov_b32 a62, a126\n\t" \
    "v_mfma_f32_32x32x16_bf16 v[176:191], a[140:143], v[212:215], v[176:191]\n\t" \
    "v_accvgpr_mov_b32 a63, a127\n\t" \
    "s_waitcnt lgkmcnt(3)\n\t" \
    "v_mfma_f32_32x32x16_bf16 v[64:79], a[144:147], v[216:219], v[64:79]\n\t" \
    "v_mfma_f32_32x32x16_bf16 v[80:95], a[144:147], v[220:223], v[80:95]\n\t" \
    "s_waitcnt lgkmcnt(2)\n\t" \
    "v_mfma_f32_32x32x16_bf16 v[96:111], a[148:151], v[216:219], v[96:111]\n\t" \
    "v_mfma_f32_32x32x16_bf16 v[112:127], a[148:151], v[220:223], v[112:127]\n\t" \
    "s_waitcnt lgkmcnt(1)\n\t" \
    "v_mfma_f32_32x32x16_bf16 v[128:143], a[152:155], v[216:219], v[128:143]\n\t" \
    "v_mfma_f32_32x32x16_bf16 v[144:159], a[152:155], v[220:223], v[144:159]\n\t" \
    "s_waitcnt lgkmcnt(0)\n\t" \
    "v_mfma_f32_32x32x16_bf16 v[160:175], a[156:159], v[216:219], v[160:175]\n\t" \
    "v_mfma_f32_32x32x16_bf16 v[176:191], a[156:159], v[220:223], v[176:191]\n\t" \
    "s_lshr_b32 s36, s24, 1\n\t" \
    "s_cmp_eq_u32 s36, 0\n\t" \
    "v_xor_b32 v233, 0x8000, v227\n\t" \
    "v_add_u32 v235, 0x10000, v226\n\t" \
    "v_and_b32 v235, 0xffff7fff, v235\n\t" \
    "v_add_u32 v235, 0x10000, v235\n\t" \
    "s_cselect_b64 vcc, -1, 0\n\t" \
    "s_nop 1\n\t" \
    "v_cndmask_b32 v233, v233, v235, vcc\n\t" \
    "s_and_b32 s36, s16, 0x8000\n\t" \
    "v_add_u32 v236, s36, v234\n\t" \
    "ds_write_b128 v233, v[80:83] offset:0\n\t" \
    "ds_write_b128 v233, v[84:87] offset:1024\n\t" \
    "ds_write_b128 v233, v[88:91] offset:2048\n\t" \
    "ds_write_b128 v233, v[92:95] offset:3072\n\t" \
    "ds_write_b128 v233, v[112:115] offset:4096\n\t" \
    "ds_write_b128 v233, v[116:119] offset:5120\n\t" \
    "ds_write_b128 v233, v[120:123] offset:6144\n\t" \
    "ds_write_b128 v233, v[124:127] offset:7168\n\t" \
    "ds_write_b128 v233, v[144:147] offset:8192\n\t" \
    "ds_write_b128 v233, v[148:151] offset:9216\n\t" \
    "ds_write_b128 v233, v[152:155] offset:10240\n\t" \
    "ds_write_b128 v233, v[156:159] offset:11264\n\t" \
    "ds_write_b128 v233, v[176:179] offset:12288\n\t" \
    "ds_write_b128 v233, v[180:183] offset:13312\n\t" \
    "ds_write_b128 v233, v[184:187] offset:14336\n\t" \
    "ds_write_b128 v233, v[188:191] offset:15360\n\t" \
    "v_mov_b32 v237, v224\n\t" \
    "s_nop 1\n\t" \
    "v_permlane32_swap_b32 v237, v224\n\t" \
    "v_add_f32 v224, v224, v237\n\t" \
    "v_mov_b32 v237, v225\n\t" \
    "s_nop 1\n\t" \
    "v_permlane32_swap_b32 v237, v225\n\t" \
    "v_add_f32 v225, v225, v237\n\t" \
    "ds_write_b32 v236, v225\n\t" \
    "s_waitcnt lgkmcnt(0)\n\t" \
    "s_barrier\n\t" \
    "v_xor_b32 v233, 0x4000, v233\n\t" \
    "v_xor_b32 v236, 0x100, v236\n\t" \
    "ds_read_b32 v237, v236\n\t" \
    "ds_read_b128 v[0:3], v233 offset:0\n\t" \
    "ds_read_b128 v[4:7], v233 offset:1024\n\t" \
    "ds_read_b128 v[8:11], v233 offset:2048\n\t" \
    "ds_read_b128 v[12:15], v233 offset:3072\n\t" \
    "ds_read_b128 v[16:19], v233 offset:4096\n\t" \
    "ds_read_b128 v[20:23], v233 offset:5120\n\t" \
    "ds_read_b128 v[24:27], v233 offset:6144\n\t" \
    "ds_read_b128 v[28:31], v233 offset:7168\n\t" \
    "ds_read_b128 v[32:35], v233 offset:8192\n\t" \
    "ds_read_b128 v[36:39], v233 offset:9216\n\t" \
    "ds_read_b128 v[40:43], v233 offset:10240\n\t" \
    "ds_read_b128 v[44:47], v233 offset:11264\n\t" \
    "ds_read_b128 v[48:51], v233 offset:12288\n\t" \
    "ds_read_b128 v[52:55], v233 offset:13312\n\t" \
    "ds_read_b128 v[56:59], v233 offset:14336\n\t" \
    "ds_read_b128 v[60:63], v233 offset:15360\n\t" \
    "s_lshl_b32 s36, s19, 2\n\t" \
    "s_add_u32 s36, s36, s24\n\t" \
    "s_lshl_b32 s36, s36, 18\n\t" \
    "s_lshl_b32 s37, s22, 13\n\t" \
    "s_add_u32 s36, s36, s37\n\t" \
    "s_add_u32 s36, s36, 0x3400000\n\t" \
    "s_add_u32 s37, s36, 4096\n\t" \
    "s_waitcnt lgkmcnt(15)\n\t" \
    "v_add_f32 v237, v237, v224\n\t" \
    "v_rcp_f32 v236, v237\n\t" \
    "v_mov_b32 v235, 0x5f800000\n\t" \
    "v_cmp_nlt_f32 vcc, v237, v235\n\t" \
    "s_nop 0\n\t" \
    "s_or_b64 s[38:39], s[38:39], vcc\n\t" \
    "v_mov_b32 v235, 0x1f800000\n\t" \
    "v_cmp_ngt_f32 vcc, v237, v235\n\t" \
    "s_nop 0\n\t" \
    "s_or_b64 s[38:39], s[38:39], vcc\n\t" \
    "v_pk_add_f32 v[64:65], v[64:65], v[0:1]\n\t" \
    "v_pk_add_f32 v[66:67], v[66:67], v[2:3]\n\t" \
    "s_waitcnt lgkmcnt(14)\n\t" \
    "v_pk_add_f32 v[68:69], v[68:69], v[4:5]\n\t" \
    "v_pk_add_f32 v[70:71], v[70:71], v[6:7]\n\t" \
    "s_waitcnt lgkmcnt(13)\n\t" \
    "v_pk_add_f32 v[72:73], v[72:73], v[8:9]\n\t" \
    "v_pk_add_f32 v[74:75], v[74:75], v[10:11]\n\t" \
    "s_waitcnt lgkmcnt(12)\n\t" \
    "v_pk_add_f32 v[76:77], v[76:77], v[12:13]\n\t" \
    "v_pk_add_f32 v[78:79], v[78:79], v[14:15]\n\t" \
    "v_mul_f32 v64, v64, v236\n\t" \
    "v_mul_f32 v65, v65, v236\n\t" \
    "v_mul_f32 v66, v66, v236\n\t" \
    "v_mul_f32 v67, v67, v236\n\t" \
    "v_mul_f32 v68, v68, v236\n\t" \
    "v_mul_f32 v69, v69, v236\n\t" \
    "v_mul_f32 v70, v70, v236\n\t" \
    "v_mul_f32 v71, v71, v236\n\t" \
    "v_mul_f32 v72, v72, v236\n\t" \
    "v_mul_f32 v73, v73, v236\n\t" \
    "v_mul_f32 v74, v74, v236\n\t" \
    "v_mul_f32 v75, v75, v236\n\t" \
    "v_mul_f32 v76, v76, v236\n\t" \
    "v_mul_f32 v77, v77, v236\n\t" \
    "v_mul_f32 v78, v78, v236\n\t" \
    "v_mul_f32 v79, v79, v236\n\t" \
    "v_cvt_pk_bf16_f32 v192, v64, v65\n\t" \
    "v_cvt_pk_bf16_f32 v193, v66, v67\n\t" \
    "v_cvt_pk_bf16_f32 v194, v68, v69\n\t" \
    "v_cvt_pk_bf16_f32 v195, v70, v71\n\t" \
    "v_cvt_pk_bf16_f32 v196, v72, v73\n\t" \
    "v_cvt_pk_bf16_f32 v197, v74, v75\n\t" \
    "v_cvt_pk_bf16_f32 v198, v76, v77\n\t" \
    "v_cvt_pk_bf16_f32 v199, v78, v79\n\t" \
    "buffer_store_dwordx4 v[192:195], v232, s[8:11], s36 offen offset:0 sc1\n\t" \
    "buffer_store_dwordx4 v[196:199], v232, s[8:11], s36 offen offset:1024 sc1\n\t" \
    "s_waitcnt lgkmcnt(11)\n\t" \
    "v_pk_add_f32 v[96:97], v[96:97], v[16:17]\n\t" \
    "v_pk_add_f32 v[98:99], v[98:99], v[18:19]\n\t" \
    "s_waitcnt lgkmcnt(10)\n\t" \
    "v_pk_add_f32 v[100:101], v[100:101], v[20:21]\n\t" \
    "v_pk_add_f32 v[102:103], v[102:103], v[22:23]\n\t" \
    "s_waitcnt lgkmcnt(9)\n\t" \
    "v_pk_add_f32 v[104:105], v[104:105], v[24:25]\n\t" \
    "v_pk_add_f32 v[106:107], v[106:107], v[26:27]\n\t" \
    "s_waitcnt lgkmcnt(8)\n\t" \
    "v_pk_add_f32 v[108:109], v[108:109], v[28:29]\n\t" \
    "v_pk_add_f32 v[110:111], v[110:111], v[30:31]\n\t" \
    "v_mul_f32 v96, v96, v236\n\t" \
    "v_mul_f32 v97, v97, v236\n\t" \
    "v_mul_f32 v98, v98, v236\n\t" \
    "v_mul_f32 v99, v99, v236\n\t" \
    "v_mul_f32 v100, v100, v236\n\t" \
    "v_mul_f32 v101, v101, v236\n\t" \
    "v_mul_f32 v102, v102, v236\n\t" \
    "v_mul_f32 v103, v103, v236\n\t" \
    "v_mul_f32 v104, v104, v236\n\t" \
    "v_mul_f32 v105, v105, v236\n\t" \
    "v_mul_f32 v106, v106, v236\n\t" \
    "v_mul_f32 v107, v107, v236\n\t" \
    "v_mul_f32 v108, v108, v236\n\t" \
    "v_mul_f32 v109, v109, v236\n\t" \
    "v_mul_f32 v110, v110, v236\n\t" \
    "v_mul_f32 v111, v111, v236\n\t" \
    "v_cvt_pk_bf16_f32 v200, v96, v97\n\t" \
    "v_cvt_pk_bf16_f32 v201, v98, v99\n\t" \
    "v_cvt_pk_bf16_f32 v202, v100, v101\n\t" \
    "v_cvt_pk_bf16_f32 v203, v102, v103\n\t" \
    "v_cvt_pk_bf16_f32 v204, v104, v105\n\t" \
    "v_cvt_pk_bf16_f32 v205, v106, v107\n\t" \
    "v_cvt_pk_bf16_f32 v206, v108, v109\n\t" \
    "v_cvt_pk_bf16_f32 v207, v110, v111\n\t" \
    "buffer_store_dwordx4 v[200:203], v232, s[8:11], s36 offen offset:2048 sc1\n\t" \
    "buffer_store_dwordx4 v[204:207], v232, s[8:11], s36 offen offset:3072 sc1\n\t" \
    "s_waitcnt lgkmcnt(7)\n\t" \
    "v_pk_add_f32 v[128:129], v[128:129], v[32:33]\n\t" \
    "v_pk_add_f32 v[130:131], v[130:131], v[34:35]\n\t" \
    "s_waitcnt lgkmcnt(6)\n\t" \
    "v_pk_add_f32 v[132:133], v[132:133], v[36:37]\n\t" \
    "v_pk_add_f32 v[134:135], v[134:135], v[38:39]\n\t" \
    "s_waitcnt lgkmcnt(5)\n\t" \
    "v_pk_add_f32 v[136:137], v[136:137], v[40:41]\n\t" \
    "v_pk_add_f32 v[138:139], v[138:139], v[42:43]\n\t" \
    "s_waitcnt lgkmcnt(4)\n\t" \
    "v_pk_add_f32 v[140:141], v[140:141], v[44:45]\n\t" \
    "v_pk_add_f32 v[142:143], v[142:143], v[46:47]\n\t" \
    "v_mul_f32 v128, v128, v236\n\t" \
    "v_mul_f32 v129, v129, v236\n\t" \
    "v_mul_f32 v130, v130, v236\n\t" \
    "v_mul_f32 v131, v131, v236\n\t" \
    "v_mul_f32 v132, v132, v236\n\t" \
    "v_mul_f32 v133, v133, v236\n\t" \
    "v_mul_f32 v134, v134, v236\n\t" \
    "v_mul_f32 v135, v135, v236\n\t" \
    "v_mul_f32 v136, v136, v236\n\t" \
    "v_mul_f32 v137, v137, v236\n\t" \
    "v_mul_f32 v138, v138, v236\n\t" \
    "v_mul_f32 v139, v139, v236\n\t" \
    "v_mul_f32 v140, v140, v236\n\t" \
    "v_mul_f32 v141, v141, v236\n\t" \
    "v_mul_f32 v142, v142, v236\n\t" \
    "v_mul_f32 v143, v143, v236\n\t" \
    "v_cvt_pk_bf16_f32 v208, v128, v129\n\t" \
    "v_cvt_pk_bf16_f32 v209, v130, v131\n\t" \
    "v_cvt_pk_bf16_f32 v210, v132, v133\n\t" \
    "v_cvt_pk_bf16_f32 v211, v134, v135\n\t" \
    "v_cvt_pk_bf16_f32 v212, v136, v137\n\t" \
    "v_cvt_pk_bf16_f32 v213, v138, v139\n\t" \
    "v_cvt_pk_bf16_f32 v214, v140, v141\n\t" \
    "v_cvt_pk_bf16_f32 v215, v142, v143\n\t" \
    "buffer_store_dwordx4 v[208:211], v232, s[8:11], s37 offen offset:0 sc1\n\t" \
    "buffer_store_dwordx4 v[212:215], v232, s[8:11], s37 offen offset:1024 sc1\n\t" \
    "s_waitcnt lgkmcnt(3)\n\t" \
    "v_pk_add_f32 v[160:161], v[160:161], v[48:49]\n\t" \
    "v_pk_add_f32 v[162:163], v[162:163], v[50:51]\n\t" \
    "s_waitcnt lgkmcnt(2)\n\t" \
    "v_pk_add_f32 v[164:165], v[164:165], v[52:53]\n\t" \
    "v_pk_add_f32 v[166:167], v[166:167], v[54:55]\n\t" \
    "s_waitcnt lgkmcnt(1)\n\t" \
    "v_pk_add_f32 v[168:169], v[168:169], v[56:57]\n\t" \
    "v_pk_add_f32 v[170:171], v[170:171], v[58:59]\n\t" \
    "s_waitcnt lgkmcnt(0)\n\t" \
    "v_pk_add_f32 v[172:173], v[172:173], v[60:61]\n\t" \
    "v_pk_add_f32 v[174:175], v[174:175], v[62:63]\n\t" \
    "v_mul_f32 v160, v160, v236\n\t" \
    "v_mul_f32 v161, v161, v236\n\t" \
    "v_mul_f32 v162, v162, v236\n\t" \
    "v_mul_f32 v163, v163, v236\n\t" \
    "v_mul_f32 v164, v164, v236\n\t" \
    "v_mul_f32 v165, v165, v236\n\t" \
    "v_mul_f32 v166, v166, v236\n\t" \
    "v_mul_f32 v167, v167, v236\n\t" \
    "v_mul_f32 v168, v168, v236\n\t" \
    "v_mul_f32 v169, v169, v236\n\t" \
    "v_mul_f32 v170, v170, v236\n\t" \
    "v_mul_f32 v171, v171, v236\n\t" \
    "v_mul_f32 v172, v172, v236\n\t" \
    "v_mul_f32 v173, v173, v236\n\t" \
    "v_mul_f32 v174, v174, v236\n\t" \
    "v_mul_f32 v175, v175, v236\n\t" \
    "v_cvt_pk_bf16_f32 v216, v160, v161\n\t" \
    "v_cvt_pk_bf16_f32 v217, v162, v163\n\t" \
    "v_cvt_pk_bf16_f32 v218, v164, v165\n\t" \
    "v_cvt_pk_bf16_f32 v219, v166, v167\n\t" \
    "v_cvt_pk_bf16_f32 v220, v168, v169\n\t" \
    "v_cvt_pk_bf16_f32 v221, v170, v171\n\t" \
    "v_cvt_pk_bf16_f32 v222, v172, v173\n\t" \
    "v_cvt_pk_bf16_f32 v223, v174, v175\n\t" \
    "buffer_store_dwordx4 v[216:219], v232, s[8:11], s37 offen offset:2048 sc1\n\t" \
    "buffer_store_dwordx4 v[220:223], v232, s[8:11], s37 offen offset:3072 sc1\n\t" \
    "v_mov_b32 v0, 0\n\t" \
    "v_mov_b32 v1, 0\n\t" \
    "v_mov_b32 v2, 0\n\t" \
    "v_mov_b32 v3, 0\n\t" \
    "s_nop 1\n\t" \
    "v_mfma_f32_32x32x16_bf16 v[16:31], v[0:3], v[192:195], 0\n\t" \
    "v_mfma_f32_32x32x16_bf16 v[16:31], v[0:3], v[196:199], v[16:31]\n\t" \
    "v_mfma_f32_32x32x16_bf16 v[16:31], v[0:3], v[200:203], v[16:31]\n\t" \
    "v_mfma_f32_32x32x16_bf16 v[16:31], v[0:3], v[204:207], v[16:31]\n\t" \
    "v_mfma_f32_32x32x16_bf16 v[16:31], v[0:3], v[208:211], v[16:31]\n\t" \
    "v_mfma_f32_32x32x16_bf16 v[16:31], v[0:3], v[212:215], v[16:31]\n\t" \
    "v_mfma_f32_32x32x16_bf16 v[16:31], v[0:3], v[216:219], v[16:31]\n\t" \
    "v_mfma_f32_32x32x16_bf16 v[16:31], v[0:3], v[220:223], v[16:31]\n\t" \
    "s_nop 7\n\t" \
    "s_nop 3\n\t" \
    "v_cmp_u_f32 vcc, v16, v16\n\t" \
    "s_nop 0\n\t" \
    "s_or_b64 s[38:39], s[38:39], vcc\n\t" \
    "s_cmp_lg_u32 s20, 0\n\t" \
    "s_cbranch_scc1 END_%=\n\t" \
    "s_barrier\n\t" \
    "s_mov_b32 m0, s16\n\t" \
    "s_nop 0\n\t" \
    "buffer_load_dwordx4 v228, s[8:11], s12 offen offset:0 lds\n\t" \
    "buffer_load_dwordx4 v228, s[8:11], s12 offen offset:1024 lds\n\t" \
    "buffer_load_dwordx4 v228, s[8:11], s12 offen offset:2048 lds\n\t" \
    "buffer_load_dwordx4 v228, s[8:11], s12 offen offset:3072 lds\n\t" \
    "s_add_u32 m0, s16, 4096\n\t" \
    "s_nop 0\n\t" \
    "buffer_load_dwordx4 v229, s[8:11], s12 offen offset:0 lds\n\t" \
    "buffer_load_dwordx4 v229, s[8:11], s12 offen offset:1024 lds\n\t" \
    "buffer_load_dwordx4 v229, s[8:11], s12 offen offset:2048 lds\n\t" \
    "buffer_load_dwordx4 v229, s[8:11], s12 offen offset:3072 lds\n\t" \
    "s_add_u32 s12, s12, 0x8000\n\t" \
    "s_cmp_gt_u32 s12, s13\n\t" \
    "s_cselect_b32 s12, s27, s12\n\t" \
    "s_cselect_b32 s13, s29, s13\n\t" \
    "s_xor_b32 s16, s16, 0x8000\n\t" \
    "s_mov_b32 m0, s17\n\t" \
    "s_nop 0\n\t" \
    "buffer_load_dwordx4 v228, s[8:11], s14 offen offset:0 lds\n\t" \
    "buffer_load_dwordx4 v228, s[8:11], s14 offen offset:1024 lds\n\t" \
    "buffer_load_dwordx4 v228, s[8:11], s14 offen offset:2048 lds\n\t" \
    "buffer_load_dwordx4 v228, s[8:11], s14 offen offset:3072 lds\n\t" \
    "s_add_u32 m0, s17, 4096\n\t" \
    "s_nop 0\n\t" \
    "buffer_load_dwordx4 v229, s[8:11], s14 offen offset:0 lds\n\t" \
    "buffer_load_dwordx4 v229, s[8:11], s14 offen offset:1024 lds\n\t" \
    "buffer_load_dwordx4 v229, s[8:11], s14 offen offset:2048 lds\n\t" \
    "buffer_load_dwordx4 v229, s[8:11], s14 offen offset:3072 lds\n\t" \
    "s_add_u32 s14, s14, 0x8000\n\t" \
    "s_cmp_gt_u32 s14, s15\n\t" \
    "s_cselect_b32 s14, s28, s14\n\t" \
    "s_cselect_b32 s15, s30, s15\n\t" \
    "s_xor_b32 s17, s17, 0x8000\n\t" \
    "s_mov_b32 s20, 1\n\t" \
    "s_mov_b32 s19, s23\n\t" \
    "s_branch BLOCK_%=\n\t" \
    "END_%=:\n\t" \
    "s_waitcnt vmcnt(0) lgkmcnt(0)\n\t" \
    "s_or_b32 %[bad], s38, s39\n\t" \
    "s_branch FIN_%=\n\t" \
    "HSPIN0_%=:\n\t" \
    "s_mov_b32 s31, 0\n\t" \
    "HSL0_%=:\n\t" \
    "buffer_load_dword v111, off, s[16:19], s29 sc1\n\t" \
    "s_waitcnt vmcnt(0)\n\t" \
    "v_readfirstlane_b32 s23, v111\n\t" \
    "s_nop 0\n\t" \
    "s_cmp_eq_u32 s23, 16\n\t" \
    "s_cbranch_scc1 HOK0_%=\n\t" \
    "s_add_u32 s31, s31, 1\n\t" \
    "s_cmp_gt_u32 s31, 0x40000\n\t" \
    "s_cbranch_scc1 HOK0_%=\n\t" \
    "s_sleep 2\n\t" \
    "s_branch HSL0_%=\n\t" \
    "HSPIN1_%=:\n\t" \
    "s_mov_b32 s31, 0\n\t" \
    "HSL1_%=:\n\t" \
    "buffer_load_dword v111, off, s[16:19], s29 sc1\n\t" \
    "s_waitcnt vmcnt(0)\n\t" \
    "v_readfirstlane_b32 s23, v111\n\t" \
    "s_nop 0\n\t" \
    "s_cmp_eq_u32 s23, 16\n\t" \
    "s_cbranch_scc1 HOK1_%=\n\t" \
    "s_add_u32 s31, s31, 1\n\t" \
    "s_cmp_gt_u32 s31, 0x40000\n\t" \
    "s_cbranch_scc1 HOK1_%=\n\t" \
    "s_sleep 2\n\t" \
    "s_branch HSL1_%=\n\t" \
    "FIN_%=:\n\t"
#define A_CLOB "v0", "v1", "v2", "v3", "v4", "v5", "v6", "v7", "v8", "v9", "v10", "v11", "v12", "v13", "v14", "v15", "v16", "v17", "v18", "v19", "v20", "v21", "v22", "v23", "v24", "v25", "v26", "v27", "v28", "v29", "v30", "v31", "v32", "v33", "v34", "v35", "v36", "v37", "v38", "v39", "v40", "v41", "v42", "v43", "v44", "v45", "v46", "v47", "v48", "v49", "v50", "v51", "v52", "v53", "v54", "v55", "v56", "v57", "v58", "v59", "v60", "v61", "v62", "v63", "v64", "v65", "v66", "v67", "v68", "v69", "v70", "v71", "v72", "v73", "v74", "v75", "v76", "v77", "v78", "v79", "v80", "v81", "v82", "v83", "v84", "v85", "v86", "v87", "v88", "v89", "v90", "v91", "v92", "v93", "v94", "v95", "v96", "v97", "v98", "v99", "v100", "v101", "v102", "v103", "v104", "v105", "v106", "v107", "v108", "v109", "v110", "v111", "v112", "v113", "v114", "v115", "v116", "v117", "v118", "v119", "v120", "v121", "v122", "v123", "v124", "v125", "v126", "v127", "v128", "v129", "v130", "v131", "v132", "v133", "v134", "v135", "v136", "v137", "v138", "v139", "v140", "v141", "v142", "v143", "v144", "v145", "v146", "v147", "v148", "v149", "v150", "v151", "v152", "v153", "v154", "v155", "v156", "v157", "v158", "v159", "v160", "v161", "v162", "v163", "v164", "v165", "v166", "v167", "v168", "v169", "v170", "v171", "v172", "v173", "v174", "v175", "v176", "v177", "v178", "v179", "v180", "v181", "v182", "v183", "v184", "v185", "v186", "v187", "v188", "v189", "v190", "v191", "v192", "v193", "v194", "v195", "v196", "v197", "v198", "v199", "v200", "v201", "v202", "v203", "v204", "v205", "v206", "v207", "v208", "v209", "v210", "v211", "v212", "v213", "v214", "v215", "v216", "v217", "v218", "v219", "v220", "v221", "v222", "v223", "v224", "v225", "v226", "v227", "v228", "v229", "v230", "v231", "v232", "v233", "v234", "v235", "v236", "v237", "v238", "v239", "v240", "v241", "v242", "v243", "v244", "v245", "v246", "v247", "v248", "v249", "v250", "v251", "v252", "v253", "v254", "v255", "a0", "a1", "a2", "a3", "a4", "a5", "a6", "a7", "a8", "a9", "a10", "a11", "a12", "a13", "a14", "a15", "a16", "a17", "a18", "a19", "a20", "a21", "a22", "a23", "a24", "a25", "a26", "a27", "a28", "a29", "a30", "a31", "a32", "a33", "a34", "a35", "a36", "a37", "a38", "a39", "a40", "a41", "a42", "a43", "a44", "a45", "a46", "a47", "a48", "a49", "a50", "a51", "a52", "a53", "a54", "a55", "a56", "a57", "a58", "a59", "a60", "a61", "a62", "a63", "a64", "a65", "a66", "a67", "a68", "a69", "a70", "a71", "a72", "a73", "a74", "a75", "a76", "a77", "a78", "a79", "a80", "a81", "a82", "a83", "a84", "a85", "a86", "a87", "a88", "a89", "a90", "a91", "a92", "a93", "a94", "a95", "a96", "a97", "a98", "a99", "a100", "a101", "a102", "a103", "a104", "a105", "a106", "a107", "a108", "a109", "a110", "a111", "a112", "a113", "a114", "a115", "a116", "a117", "a118", "a119", "a120", "a121", "a122", "a123", "a124", "a125", "a126", "a127", "a128", "a129", "a130", "a131", "a132", "a133", "a134", "a135", "a136", "a137", "a138", "a139", "a140", "a141", "a142", "a143", "a144", "a145", "a146", "a147", "a148", "a149", "a150", "a151", "a152", "a153", "a154", "a155", "a156", "a157", "a158", "a159", "a160", "a161", "a162", "a163", "a164", "a165", "a166", "a167", "a168", "a169", "a170", "a171", "a172", "a173", "a174", "a175", "a176", "a177", "a178", "a179", "a180", "a181", "a182", "a183", "a184", "a185", "a186", "a187", "a188", "a189", "a190", "a191", "a192", "a193", "a194", "a195", "a196", "a197", "a198", "a199", "a200", "a201", "a202", "a203", "a204", "a205", "a206", "a207", "a208", "a209", "a210", "a211", "a212", "a213", "a214", "a215", "a216", "a217", "a218", "a219", "a220", "a221", "a222", "a223", "a224", "a225", "a226", "a227", "a228", "a229", "a230", "a231", "a232", "a233", "a234", "a235", "a236", "a237", "a238", "a239", "a240", "a241", "a242", "a243", "a244", "a245", "a246", "a247", "a248", "a249", "a250", "a251", "a252", "a253", "a254", "a255", "s8", "s9", "s10", "s11", "s12", "s13", "s14", "s15", "s16", "s17", "s18", "s19", "s20", "s21", "s22", "s23", "s24", "s25", "s26", "s27", "s28", "s29", "s30", "s31", "s32", "s33", "s34", "s35", "s36", "s37", "s38", "s39", "s40", "s41", "s42", "s43", "s44", "s45", "s46", "s47", "m0", "vcc", "scc", "memory"

__device__ __forceinline__ void attn_exact(const u16* himg, const u16* wq, const u16* Kimg, const u16* Vimg, u16* ctx, int h, int rb, int lane) {
  int i = lane & 31, hh = lane >> 5;
  u32x4 q[8];
  {
    int row = 32 * rb + i;
    int dsw = (i & 7) | ((i >> 4) << 3) | (((i >> 3) & 1) << 4);
#pragma unroll
    for (int dt = 0; dt < 4; dt++) {
      f32x16 c = {};
#pragma unroll 1
      for (int ks = 0; ks < 32; ks++) {
        int k0 = 16 * ks + 8 * hh;
        const u16* wp = wq + (size_t)k0 * 4096 + h * 128 + 32 * dt + dsw;
        u32x4 a;
#pragma unroll
        for (int j = 0; j < 4; j++) a[j] = wp[(2 * j) * 4096] | ((uint32_t)wp[(2 * j + 1) * 4096] << 16);
        u32x4 bq = *(const u32x4*)(himg + ((size_t)((row >> 4) * 16 + (k0 >> 5)) * 64 + ((k0 >> 3) & 3) * 16 + (row & 15)) * 8);
        c = mfma(a, bq, c);
      }
      q[2 * dt] = pack8(c, 0, 1.f);
      q[2 * dt + 1] = pack8(c, 1, 1.f);
    }
  }
  f32x16 o[4] = {};
  float m = -INFINITY, l = 0.f;
  for (int kb = 0; kb <= rb; kb++) {
    f32x16 s = {};
#pragma unroll
    for (int ks = 0; ks < 8; ks++) s = mfma(*((const u32x4*)Kimg + ((size_t)(h * 64 + kb) * 8 + ks) * 64 + lane), q[ks], s);
    if (kb == rb) {
#pragma unroll
      for (int r = 0; r < 16; r++) {
        int key = 16 * (r >> 3) + 8 * hh + (r & 7);
        if (key > i) s[r] = -INFINITY;
      }
    }
    float mx = s[0];
#pragma unroll
    for (int r = 1; r < 16; r++) mx = fmaxf(mx, s[r]);
    mx = fmaxf(mx, __shfl_xor(mx, 32));
    float mn = fmaxf(m, mx), al = exp2f(m - mn);
    m = mn;
    float ps = 0.f;
#pragma unroll
    for (int r = 0; r < 16; r++) { s[r] = exp2f(s[r] - mn); ps += s[r]; }
    l = l * al + ps;
#pragma unroll
    for (int dt = 0; dt < 4; dt++)
#pragma unroll
      for (int r = 0; r < 16; r++) o[dt][r] *= al;
#pragma unroll
    for (int s2 = 0; s2 < 2; s2++) {
      u32x4 p = pack8(s, s2, 1.f);
#pragma unroll
      for (int dt = 0; dt < 4; dt++)
        o[dt] = mfma(*((const u32x4*)Vimg + ((size_t)(h * 128 + 2 * kb + s2) * 4 + dt) * 64 + lane), p, o[dt]);
    }
  }
  l += __shfl_xor(l, 32);
  float inv = 1.f / l;
#pragma unroll
  for (int dt = 0; dt < 4; dt++)
#pragma unroll
    for (int s2 = 0; s2 < 2; s2++)
      *((u32x4*)ctx + ((size_t)rb * 256 + 8 * h + 2 * dt + s2) * 64 + lane) = pack8(o[dt], s2, inv);
}

#define F_ASM \
    "v_accvgpr_write_b32 a232, %[vx]\n\t" \
    "v_accvgpr_write_b32 a233, %[vw]\n\t" \
    "v_accvgpr_write_b32 a234, %[vwa]\n\t" \
    "v_accvgpr_write_b32 a235, %[vst]\n\t" \
    "v_accvgpr_write_b32 a236, %[vp]\n\t" \
    "v_accvgpr_write_b32 a237, %[vm]\n\t" \
    "v_accvgpr_write_b32 a238, %[vy]\n\t" \
    "v_accvgpr_write_b32 a239, %[w10]\n\t" \
    "v_accvgpr_write_b32 a240, %[w11]\n\t" \
    "v_accvgpr_write_b32 a241, %[w12]\n\t" \
    "v_accvgpr_write_b32 a242, %[vbo]\n\t" \
    "v_accvgpr_write_b32 a243, %[vre]\n\t" \
    "v_accvgpr_write_b32 a244, %[vro]\n\t" \
    "v_accvgpr_write_b32 a245, %[w2]\n\t" \
    "v_accvgpr_write_b32 a246, %[vr2]\n\t" \
    "v_accvgpr_write_b32 a247, %[vx1]\n\t" \
    "v_accvgpr_write_b32 a248, %[vb2]\n\t" \
    "v_accvgpr_write_b32 a249, %[vo]\n\t" \
    "s_mov_b32 s8, %[wslo]\n\t" \
    "s_mov_b32 s9, %[wshi]\n\t" \
    "s_mov_b32 s10, 0x10000000\n\t" \
    "s_mov_b32 s11, 0x00020000\n\t" \
    "s_mov_b32 s12, %[wolo]\n\t" \
    "s_mov_b32 s13, %[wohi]\n\t" \
    "s_mov_b32 s14, 0x400000\n\t" \
    "s_mov_b32 s15, 0x00020000\n\t" \
    "s_mov_b32 s16, %[w1lo]\n\t" \
    "s_mov_b32 s17, %[w1hi]\n\t" \
    "s_mov_b32 s18, 0x158000\n\t" \
    "s_mov_b32 s19, 0x00020000\n\t" \
    "s_mov_b32 s20, %[w2lo]\n\t" \
    "s_mov_b32 s21, %[w2hi]\n\t" \
    "s_mov_b32 s22, 0x158000\n\t" \
    "s_mov_b32 s23, 0x00020000\n\t" \
    "s_mov_b32 s24, %[xlo]\n\t" \
    "s_mov_b32 s25, %[xhi]\n\t" \
    "s_mov_b32 s26, 0x200000\n\t" \
    "s_mov_b32 s27, 0x00020000\n\t" \
    "v_mbcnt_lo_u32_b32 v16, -1, 0\n\t" \
    "v_mbcnt_hi_u32_b32 v16, -1, v16\n\t" \
    "v_lshlrev_b32 v16, 4, v16\n\t" \
    "s_waitcnt vmcnt(0) lgkmcnt(0)\n\t" \
    "s_barrier\n\t" \
    "s_cmp_lg_u32 %[wv], 0\n\t" \
    "s_cbranch_scc1 SG_%=\n\t" \
    "v_mov_b32 v17, 1\n\t" \
    "s_mov_b64 exec, 1\n\t" \
    "buffer_atomic_add v17, off, s[8:11], %[cs1] sc1\n\t" \
    "buffer_atomic_add v17, off, s[8:11], %[cs2] sc1\n\t" \
    "s_mov_b64 exec, -1\n\t" \
    "SG_%=:\n\t" \
    "v_accvgpr_read_b32 v25, a232\n\t" \
    "v_accvgpr_read_b32 v26, a233\n\t" \
    "v_add_u32 v27, 3072, v26\n\t" \
    "v_xor_b32 v28, 128, v26\n\t" \
    "v_add_u32 v29, 9216, v28\n\t" \
    "v_add_u32 v28, 6144, v28\n\t" \
    "s_mov_b32 s31, %[sa]\n\t" \
    "s_mov_b32 s32, %[sw]\n\t" \
    "s_lshl_b32 s33, %[wv], 12\n\t" \
    "s_add_u32 m0, s33, 0\n\t" \
    "s_nop 0\n\t" \
    "buffer_load_dwordx4 v26, s[12:15], s32 offen lds\n\t" \
    "buffer_load_dwordx4 v27, s[12:15], s32 offen offset:1024 lds\n\t" \
    "buffer_load_dwordx4 v28, s[12:15], s32 offen offset:2048 lds\n\t" \
    "buffer_load_dwordx4 v29, s[12:15], s32 offen offset:3072 lds\n\t" \
    "s_add_u32 s32, s32, 65536\n\t" \
    "s_add_u32 m0, s33, 16384\n\t" \
    "s_nop 0\n\t" \
    "buffer_load_dwordx4 v26, s[12:15], s32 offen lds\n\t" \
    "buffer_load_dwordx4 v27, s[12:15], s32 offen offset:1024 lds\n\t" \
    "buffer_load_dwordx4 v28, s[12:15], s32 offen offset:2048 lds\n\t" \
    "buffer_load_dwordx4 v29, s[12:15], s32 offen offset:3072 lds\n\t" \
    "s_add_u32 s32, s32, 65536\n\t" \
    "s_add_u32 m0, s33, 32768\n\t" \
    "s_nop 0\n\t" \
    "buffer_load_dwordx4 v26, s[12:15], s32 offen lds\n\t" \
    "buffer_load_dwordx4 v27, s[12:15], s32 offen offset:1024 lds\n\t" \
    "buffer_load_dwordx4 v28, s[12:15], s32 offen offset:2048 lds\n\t" \
    "buffer_load_dwordx4 v29, s[12:15], s32 offen offset:3072 lds\n\t" \
    "s_add_u32 s32, s32, 65536\n\t" \
    "s_mov_b32 s30, 0\n\t" \
    "CP_%=:\n\t" \
    "buffer_load_dword v17, off, s[8:11], %[cp] sc1\n\t" \
    "s_waitcnt vmcnt(0)\n\t" \
    "v_readfirstlane_b32 s28, v17\n\t" \
    "s_nop 0\n\t" \
    "s_cmp_ge_u32 s28, 8\n\t" \
    "s_cbranch_scc1 COK_%=\n\t" \
    "s_add_u32 s30, s30, 1\n\t" \
    "s_cmp_gt_u32 s30, 0x40000\n\t" \
    "s_cbranch_scc1 COK_%=\n\t" \
    "s_sleep 1\n\t" \
    "s_branch CP_%=\n\t" \
    "COK_%=:\n\t" \
    "buffer_load_dwordx4 a[0:3], v25, s[8:11], s31 offen offset:0 sc1\n\t" \
    "buffer_load_dwordx4 a[4:7], v25, s[8:11], s31 offen offset:256 sc1\n\t" \
    "buffer_load_dwordx4 a[8:11], v25, s[8:11], s31 offen offset:2048 sc1\n\t" \
    "buffer_load_dwordx4 a[12:15], v25, s[8:11], s31 offen offset:2304 sc1\n\t" \
    "s_add_u32 s31, s31, 4096\n\t" \
    "buffer_load_dwordx4 a[16:19], v25, s[8:11], s31 offen offset:0 sc1\n\t" \
    "buffer_load_dwordx4 a[20:23], v25, s[8:11], s31 offen offset:256 sc1\n\t" \
    "buffer_load_dwordx4 a[24:27], v25, s[8:11], s31 offen offset:2048 sc1\n\t" \
    "buffer_load_dwordx4 a[28:31], v25, s[8:11], s31 offen offset:2304 sc1\n\t" \
    "s_add_u32 s31, s31, 4096\n\t" \
    "buffer_load_dwordx4 a[32:35], v25, s[8:11], s31 offen offset:0 sc1\n\t" \
    "buffer_load_dwordx4 a[36:39], v25, s[8:11], s31 offen offset:256 sc1\n\t" \
    "buffer_load_dwordx4 a[40:43], v25, s[8:11], s31 offen offset:2048 sc1\n\t" \
    "buffer_load_dwordx4 a[44:47], v25, s[8:11], s31 offen offset:2304 sc1\n\t" \
    "s_add_u32 s31, s31, 4096\n\t" \
    "v_accvgpr_read_b32 v32, a234\n\t" \
    "v_xor_b32 v33, 32, v32\n\t" \
    "v_xor_b32 v34, 64, v32\n\t" \
    "v_xor_b32 v35, 96, v32\n\t" \
    "v_xor_b32 v36, 128, v32\n\t" \
    "v_xor_b32 v37, 160, v32\n\t" \
    "v_xor_b32 v38, 192, v32\n\t" \
    "v_xor_b32 v39, 224, v32\n\t" \
    "v_add_u32 v40, 0x10000, v32\n\t" \
    "v_add_u32 v41, 0x10000, v33\n\t" \
    "v_add_u32 v42, 0x10000, v34\n\t" \
    "v_add_u32 v43, 0x10000, v35\n\t" \
    "v_add_u32 v44, 0x10000, v36\n\t" \
    "v_add_u32 v45, 0x10000, v37\n\t" \
    "v_add_u32 v46, 0x10000, v38\n\t" \
    "v_add_u32 v47, 0x10000, v39\n\t" \
    "v_accvgpr_read_b32 v48, a235\n\t" \
    "v_accvgpr_read_b32 v18, a239\n\t" \
    "v_accvgpr_read_b32 v19, a240\n\t" \
    "v_accvgpr_read_b32 v20, a241\n\t" \
    "v_accvgpr_read_b32 v21, a245\n\t" \
    "v_add_u32 v22, 7168, v21\n\t" \
    "v_add_u32 v23, 14336, v21\n\t" \
    "v_add_u32 v24, 21504, v21\n\t" \
    "v_accvgpr_read_b32 v49, a236\n\t" \
    "v_accvgpr_read_b32 v50, a237\n\t" \
    "v_accvgpr_read_b32 v51, a242\n\t" \
    "s_waitcnt vmcnt(20)\n\t" \
    "s_barrier\n\t" \
    "ds_read_b64_tr_b16 v[64:65], v32 offset:0\n\t" \
    "ds_read_b64_tr_b16 v[66:67], v32 offset:1024\n\t" \
    "ds_read_b64_tr_b16 v[68:69], v33 offset:0\n\t" \
    "ds_read_b64_tr_b16 v[70:71], v33 offset:1024\n\t" \
    "ds_read_b64_tr_b16 v[72:73], v34 offset:0\n\t" \
    "ds_read_b64_tr_b16 v[74:75], v34 offset:1024\n\t" \
    "ds_read_b64_tr_b16 v[76:77], v35 offset:0\n\t" \
    "ds_read_b64_tr_b16 v[78:79], v35 offset:1024\n\t" \
    "ds_read_b64_tr_b16 v[80:81], v36 offset:0\n\t" \
    "ds_read_b64_tr_b16 v[82:83], v36 offset:1024\n\t" \
    "ds_read_b64_tr_b16 v[84:85], v37 offset:0\n\t" \
    "ds_read_b64_tr_b16 v[86:87], v37 offset:1024\n\t" \
    "s_waitcnt vmcnt(11) lgkmcnt(10)\n\t" \
    "v_mfma_f32_16x16x32_bf16 v[128:131], v[64:67], a[0:3], 0\n\t" \
    "ds_read_b64_tr_b16 v[88:89], v38 offset:0\n\t" \
    "s_waitcnt vmcnt(10)\n\t" \
    "v_mfma_f32_16x16x32_bf16 v[132:135], v[64:67], a[4:7], 0\n\t" \
    "ds_read_b64_tr_b16 v[90:91], v38 offset:1024\n\t" \
    "s_waitcnt lgkmcnt(10)\n\t" \
    "v_mfma_f32_16x16x32_bf16 v[136:139], v[68:71], a[0:3], 0\n\t" \
    "ds_read_b64_tr_b16 v[92:93], v39 offset:0\n\t" \
    "v_mfma_f32_16x16x32_bf16 v[140:143], v[68:71], a[4:7], 0\n\t" \
    "ds_read_b64_tr_b16 v[94:95], v39 offset:1024\n\t" \
    "s_waitcnt lgkmcnt(10)\n\t" \
    "v_mfma_f32_16x16x32_bf16 v[144:147], v[72:75], a[0:3], 0\n\t" \
    "ds_read_b64_tr_b16 v[64:65], v32 offset:8192\n\t" \
    "v_mfma_f32_16x16x32_bf16 v[148:151], v[72:75], a[4:7], 0\n\t" \
    "ds_read_b64_tr_b16 v[66:67], v32 offset:9216\n\t" \
    "s_waitcnt lgkmcnt(10)\n\t" \
    "v_mfma_f32_16x16x32_bf16 v[152:155], v[76:79], a[0:3], 0\n\t" \
    "ds_read_b64_tr_b16 v[68:69], v33 offset:8192\n\t" \
    "v_mfma_f32_16x16x32_bf16 v[156:159], v[76:79], a[4:7], 0\n\t" \
    "ds_read_b64_tr_b16 v[70:71], v33 offset:9216\n\t" \
    "s_waitcnt lgkmcnt(10)\n\t" \
    "v_mfma_f32_16x16x32_bf16 v[160:163], v[80:83], a[0:3], 0\n\t" \
    "ds_read_b64_tr_b16 v[72:73], v34 offset:8192\n\t" \
    "v_mfma_f32_16x16x32_bf16 v[164:167], v[80:83], a[4:7], 0\n\t" \
    "ds_read_b64_tr_b16 v[74:75], v34 offset:9216\n\t" \
    "s_waitcnt lgkmcnt(10)\n\t" \
    "v_mfma_f32_16x16x32_bf16 v[168:171], v[84:87], a[0:3], 0\n\t" \
    "ds_read_b64_tr_b16 v[76:77], v35 offset:8192\n\t" \
    "v_mfma_f32_16x16x32_bf16 v[172:175], v[84:87], a[4:7], 0\n\t" \
    "ds_read_b64_tr_b16 v[78:79], v35 offset:9216\n\t" \
    "s_waitcnt lgkmcnt(10)\n\t" \
    "v_mfma_f32_16x16x32_bf16 v[176:179], v[88:91], a[0:3], 0\n\t" \
    "ds_read_b64_tr_b16 v[80:81], v36 offset:8192\n\t" \
    "v_mfma_f32_16x16x32_bf16 v[180:183], v[88:91], a[4:7], 0\n\t" \
    "ds_read_b64_tr_b16 v[82:83], v36 offset:9216\n\t" \
    "s_waitcnt lgkmcnt(10)\n\t" \
    "v_mfma_f32_16x16x32_bf16 v[184:187], v[92:95], a[0:3], 0\n\t" \
    "ds_read_b64_tr_b16 v[84:85], v37 offset:8192\n\t" \
    "v_mfma_f32_16x16x32_bf16 v[188:191], v[92:95], a[4:7], 0\n\t" \
    "ds_read_b64_tr_b16 v[86:87], v37 offset:9216\n\t" \
    "s_waitcnt vmcnt(16)\n\t" \
    "s_barrier\n\t" \
    "s_waitcnt vmcnt(9) lgkmcnt(10)\n\t" \
    "v_mfma_f32_16x16x32_bf16 v[128:131], v[64:67], a[8:11], v[128:131]\n\t" \
    "ds_read_b64_tr_b16 v[88:89], v38 offset:8192\n\t" \
    "s_add_u32 m0, s33, 49152\n\t" \
    "s_waitcnt vmcnt(8)\n\t" \
    "v_mfma_f32_16x16x32_bf16 v[132:135], v[64:67], a[12:15], v[132:135]\n\t" \
    "ds_read_b64_tr_b16 v[90:91], v38 offset:9216\n\t" \
    "buffer_load_dwordx4 v26, s[12:15], s32 offen lds\n\t" \
    "s_waitcnt lgkmcnt(10)\n\t" \
    "v_mfma_f32_16x16x32_bf16 v[136:139], v[68:71], a[8:11], v[136:139]\n\t" \
    "ds_read_b64_tr_b16 v[92:93], v39 offset:8192\n\t" \
    "buffer_load_dwordx4 v27, s[12:15], s32 offen offset:1024 lds\n\t" \
    "v_mfma_f32_16x16x32_bf16 v[140:143], v[68:71], a[12:15], v[140:143]\n\t" \
    "ds_read_b64_tr_b16 v[94:95], v39 offset:9216\n\t" \
    "s_waitcnt lgkmcnt(10)\n\t" \
    "v_mfma_f32_16x16x32_bf16 v[144:147], v[72:75], a[8:11], v[144:147]\n\t" \
    "ds_read_b64_tr_b16 v[64:65], v32 offset:16384\n\t" \
    "buffer_load_dwordx4 v28, s[12:15], s32 offen offset:2048 lds\n\t" \
    "v_mfma_f32_16x16x32_bf16 v[148:151], v[72:75], a[12:15], v[148:151]\n\t" \
    "ds_read_b64_tr_b16 v[66:67], v32 offset:17408\n\t" \
    "s_waitcnt lgkmcnt(10)\n\t" \
    "v_mfma_f32_16x16x32_bf16 v[152:155], v[76:79], a[8:11], v[152:155]\n\t" \
    "ds_read_b64_tr_b16 v[68:69], v33 offset:16384\n\t" \
    "buffer_load_dwordx4 v29, s[12:15], s32 offen offset:3072 lds\n\t" \
    "v_mfma_f32_16x16x32_bf16 v[156:159], v[76:79], a[12:15], v[156:159]\n\t" \
    "ds_read_b64_tr_b16 v[70:71], v33 offset:17408\n\t" \
    "s_waitcnt lgkmcnt(10)\n\t" \
    "v_mfma_f32_16x16x32_bf16 v[160:163], v[80:83], a[8:11], v[160:163]\n\t" \
    "ds_read_b64_tr_b16 v[72:73], v34 offset:16384\n\t" \
    "buffer_load_dwordx4 a[48:51], v25, s[8:11], s31 offen offset:0 sc1\n\t" \
    "v_mfma_f32_16x16x32_bf16 v[164:167], v[80:83], a[12:15], v[164:167]\n\t" \
    "ds_read_b64_tr_b16 v[74:75], v34 offset:17408\n\t" \
    "s_waitcnt lgkmcnt(10)\n\t" \
    "v_mfma_f32_16x16x32_bf16 v[168:171], v[84:87], a[8:11], v[168:171]\n\t" \
    "ds_read_b64_tr_b16 v[76:77], v35 offset:16384\n\t" \
    "buffer_load_dwordx4 a[52:55], v25, s[8:11], s31 offen offset:256 sc1\n\t" \
    "v_mfma_f32_16x16x32_bf16 v[172:175], v[84:87], a[12:15], v[172:175]\n\t" \
    "ds_read_b64_tr_b16 v[78:79], v35 offset:17408\n\t" \
    "s_waitcnt lgkmcnt(10)\n\t" \
    "v_mfma_f32_16x16x32_bf16 v[176:179], v[88:91], a[8:11], v[176:179]\n\t" \
    "ds_read_b64_tr_b16 v[80:81], v36 offset:16384\n\t" \
    "buffer_load_dwordx4 a[56:59], v25, s[8:11], s31 offen offset:2048 sc1\n\t" \
    "v_mfma_f32_16x16x32_bf16 v[180:183], v[88:91], a[12:15], v[180:183]\n\t" \
    "ds_read_b64_tr_b16 v[82:83], v36 offset:17408\n\t" \
    "s_waitcnt lgkmcnt(10)\n\t" \
    "v_mfma_f32_16x16x32_bf16 v[184:187], v[92:95], a[8:11], v[184:187]\n\t" \
    "ds_read_b64_tr_b16 v[84:85], v37 offset:16384\n\t" \
    "buffer_load_dwordx4 a[60:63], v25, s[8:11], s31 offen offset:2304 sc1\n\t" \
    "s_add_u32 s31, s31, 4096\n\t" \
    "s_add_u32 s32, s32, 65536\n\t" \
    "v_mfma_f32_16x16x32_bf16 v[188:191], v[92:95], a[12:15], v[188:191]\n\t" \
    "ds_read_b64_tr_b16 v[86:87], v37 offset:17408\n\t" \
    "s_add_u32 m0, s33, 65536\n\t" \
    "s_waitcnt vmcnt(15) lgkmcnt(10)\n\t" \
    "v_mfma_f32_16x16x32_bf16 v[128:131], v[64:67], a[16:19], v[128:131]\n\t" \
    "ds_read_b64_tr_b16 v[88:89], v38 offset:16384\n\t" \
    "buffer_load_dwordx4 v26, s[12:15], s32 offen lds\n\t" \
    "s_waitcnt vmcnt(15)\n\t" \
    "v_mfma_f32_16x16x32_bf16 v[132:135], v[64:67], a[20:23], v[132:135]\n\t" \
    "ds_read_b64_tr_b16 v[90:91], v38 offset:17408\n\t" \
    "buffer_load_dwordx4 v27, s[12:15], s32 offen offset:1024 lds\n\t" \
    "s_waitcnt lgkmcnt(10)\n\t" \
    "v_mfma_f32_16x16x32_bf16 v[136:139], v[68:71], a[16:19], v[136:139]\n\t" \
    "ds_read_b64_tr_b16 v[92:93], v39 offset:16384\n\t" \
    "v_mfma_f32_16x16x32_bf16 v[140:143], v[68:71], a[20:23], v[140:143]\n\t" \
    "ds_read_b64_tr_b16 v[94:95], v39 offset:17408\n\t" \
    "buffer_load_dwordx4 v28, s[12:15], s32 offen offset:2048 lds\n\t" \
    "s_waitcnt lgkmcnt(10)\n\t" \
    "v_mfma_f32_16x16x32_bf16 v[144:147], v[72:75], a[16:19], v[144:147]\n\t" \
    "ds_read_b64_tr_b16 v[64:65], v32 offset:24576\n\t" \
    "v_mfma_f32_16x16x32_bf16 v[148:151], v[72:75], a[20:23], v[148:151]\n\t" \
    "ds_read_b64_tr_b16 v[66:67], v32 offset:25600\n\t" \
    "buffer_load_dwordx4 v29, s[12:15], s32 offen offset:3072 lds\n\t" \
    "s_waitcnt lgkmcnt(10)\n\t" \
    "v_mfma_f32_16x16x32_bf16 v[152:155], v[76:79], a[16:19], v[152:155]\n\t" \
    "ds_read_b64_tr_b16 v[68:69], v33 offset:24576\n\t" \
    "v_mfma_f32_16x16x32_bf16 v[156:159], v[76:79], a[20:23], v[156:159]\n\t" \
    "ds_read_b64_tr_b16 v[70:71], v33 offset:25600\n\t" \
    "buffer_load_dwordx4 a[64:67], v25, s[8:11], s31 offen offset:0 sc1\n\t" \
    "s_waitcnt lgkmcnt(10)\n\t" \
    "v_mfma_f32_16x16x32_bf16 v[160:163], v[80:83], a[16:19], v[160:163]\n\t" \
    "ds_read_b64_tr_b16 v[72:73], v34 offset:24576\n\t" \
    "v_mfma_f32_16x16x32_bf16 v[164:167], v[80:83], a[20:23], v[164:167]\n\t" \
    "ds_read_b64_tr_b16 v[74:75], v34 offset:25600\n\t" \
    "buffer_load_dwordx4 a[68:71], v25, s[8:11], s31 offen offset:256 sc1\n\t" \
    "s_waitcnt lgkmcnt(10)\n\t" \
    "v_mfma_f32_16x16x32_bf16 v[168:171], v[84:87], a[16:19], v[168:171]\n\t" \
    "ds_read_b64_tr_b16 v[76:77], v35 offset:24576\n\t" \
    "v_mfma_f32_16x16x32_bf16 v[172:175], v[84:87], a[20:23], v[172:175]\n\t" \
    "ds_read_b64_tr_b16 v[78:79], v35 offset:25600\n\t" \
    "buffer_load_dwordx4 a[72:75], v25, s[8:11], s31 offen offset:2048 sc1\n\t" \
    "s_waitcnt lgkmcnt(10)\n\t" \
    "v_mfma_f32_16x16x32_bf16 v[176:179], v[88:91], a[16:19], v[176:179]\n\t" \
    "ds_read_b64_tr_b16 v[80:81], v36 offset:24576\n\t" \
    "v_mfma_f32_16x16x32_bf16 v[180:183], v[88:91], a[20:23], v[180:183]\n\t" \
    "ds_read_b64_tr_b16 v[82:83], v36 offset:25600\n\t" \
    "buffer_load_dwordx4 a[76:79], v25, s[8:11], s31 offen offset:2304 sc1\n\t" \
    "s_add_u32 s31, s31, 4096\n\t" \
    "s_add_u32 s32, s32, 65536\n\t" \
    "s_waitcnt lgkmcnt(10)\n\t" \
    "v_mfma_f32_16x16x32_bf16 v[184:187], v[92:95], a[16:19], v[184:187]\n\t" \
    "ds_read_b64_tr_b16 v[84:85], v37 offset:24576\n\t" \
    "v_mfma_f32_16x16x32_bf16 v[188:191], v[92:95], a[20:23], v[188:191]\n\t" \
    "ds_read_b64_tr_b16 v[86:87], v37 offset:25600\n\t" \
    "s_waitcnt vmcnt(28)\n\t" \
    "s_barrier\n\t" \
    "s_waitcnt vmcnt(21) lgkmcnt(10)\n\t" \
    "v_mfma_f32_16x16x32_bf16 v[128:131], v[64:67], a[24:27], v[128:131]\n\t" \
    "ds_read_b64_tr_b16 v[88:89], v38 offset:24576\n\t" \
    "s_add_u32 m0, s33, 81920\n\t" \
    "s_waitcnt vmcnt(20)\n\t" \
    "v_mfma_f32_16x16x32_bf16 v[132:135], v[64:67], a[28:31], v[132:135]\n\t" \
    "ds_read_b64_tr_b16 v[90:91], v38 offset:25600\n\t" \
    "buffer_load_dwordx4 v26, s[12:15], s32 offen lds\n\t" \
    "s_waitcnt lgkmcnt(10)\n\t" \
    "v_mfma_f32_16x16x32_bf16 v[136:139], v[68:71], a[24:27], v[136:139]\n\t" \
    "ds_read_b64_tr_b16 v[92:93], v39 offset:24576\n\t" \
    "buffer_load_dwordx4 v27, s[12:15], s32 offen offset:1024 lds\n\t" \
    "v_mfma_f32_16x16x32_bf16 v[140:143], v[68:71], a[28:31], v[140:143]\n\t" \
    "ds_read_b64_tr_b16 v[94:95], v39 offset:25600\n\t" \
    "s_waitcnt lgkmcnt(10)\n\t" \
    "v_mfma_f32_16x16x32_bf16 v[144:147], v[72:75], a[24:27], v[144:147]\n\t" \
    "ds_read_b64_tr_b16 v[64:65], v32 offset:32768\n\t" \
    "buffer_load_dwordx4 v28, s[12:15], s32 offen offset:2048 lds\n\t" \
    "v_mfma_f32_16x16x32_bf16 v[148:151], v[72:75], a[28:31], v[148:151]\n\t" \
    "ds_read_b64_tr_b16 v[66:67], v32 offset:33792\n\t" \
    "s_waitcnt lgkmcnt(10)\n\t" \
    "v_mfma_f32_16x16x32_bf16 v[152:155], v[76:79], a[24:27], v[152:155]\n\t" \
    "ds_read_b64_tr_b16 v[68:69], v33 offset:32768\n\t" \
    "buffer_load_dwordx4 v29, s[12:15], s32 offen offset:3072 lds\n\t" \
    "v_mfma_f32_16x16x32_bf16 v[156:159], v[76:79], a[28:31], v[156:159]\n\t" \
    "ds_read_b64_tr_b16 v[70:71], v33 offset:33792\n\t" \
    "s_waitcnt lgkmcnt(10)\n\t" \
    "v_mfma_f32_16x16x32_bf16 v[160:163], v[80:83], a[24:27], v[160:163]\n\t" \
    "ds_read_b64_tr_b16 v[72:73], v34 offset:32768\n\t" \
    "buffer_load_dwordx4 a[80:83], v25, s[8:11], s31 offen offset:0 sc1\n\t" \
    "v_mfma_f32_16x16x32_bf16 v[164:167], v[80:83], a[28:31], v[164:167]\n\t" \
    "ds_read_b64_tr_b16 v[74:75], v34 offset:33792\n\t" \
    "s_waitcnt lgkmcnt(10)\n\t" \
    "v_mfma_f32_16x16x32_bf16 v[168:171], v[84:87], a[24:27], v[168:171]\n\t" \
    "ds_read_b64_tr_b16 v[76:77], v35 offset:32768\n\t" \
    "buffer_load_dwordx4 a[84:87], v25, s[8:11], s31 offen offset:256 sc1\n\t" \
    "v_mfma_f32_16x16x32_bf16 v[172:175], v[84:87], a[28:31], v[172:175]\n\t" \
    "ds_read_b64_tr_b16 v[78:79], v35 offset:33792\n\t" \
    "s_waitcnt lgkmcnt(10)\n\t" \
    "v_mfma_f32_16x16x32_bf16 v[176:179], v[88:91], a[24:27], v[176:179]\n\t" \
    "ds_read_b64_tr_b16 v[80:81], v36 offset:32768\n\t" \
    "buffer_load_dwordx4 a[88:91], v25, s[8:11], s31 offen offset:2048 sc1\n\t" \
    "v_mfma_f32_16x16x32_bf16 v[180:183], v[88:91], a[28:31], v[180:183]\n\t" \
    "ds_read_b64_tr_b16 v[82:83], v36 offset:33792\n\t" \
    "s_waitcnt lgkmcnt(10)\n\t" \
    "v_mfma_f32_16x16x32_bf16 v[184:187], v[92:95], a[24:27], v[184:187]\n\t" \
    "ds_read_b64_tr_b16 v[84:85], v37 offset:32768\n\t" \
    "buffer_load_dwordx4 a[92:95], v25, s[8:11], s31 offen offset:2304 sc1\n\t" \
    "s_add_u32 s31, s31, 4096\n\t" \
    "s_add_u32 s32, s32, 65536\n\t" \
    "v_mfma_f32_16x16x32_bf16 v[188:191], v[92:95], a[28:31], v[188:191]\n\t" \
    "ds_read_b64_tr_b16 v[86:87], v37 offset:33792\n\t" \
    "s_add_u32 m0, s33, 98304\n\t" \
    "s_waitcnt vmcnt(27) lgkmcnt(10)\n\t" \
    "v_mfma_f32_16x16x32_bf16 v[128:131], v[64:67], a[32:35], v[128:131]\n\t" \
    "ds_read_b64_tr_b16 v[88:89], v38 offset:32768\n\t" \
    "buffer_load_dwordx4 v26, s[12:15], s32 offen lds\n\t" \
    "s_waitcnt vmcnt(27)\n\t" \
    "v_mfma_f32_16x16x32_bf16 v[132:135], v[64:67], a[36:39], v[132:135]\n\t" \
    "ds_read_b64_tr_b16 v[90:91], v38 offset:33792\n\t" \
    "buffer_load_dwordx4 v27, s[12:15], s32 offen offset:1024 lds\n\t" \
    "s_waitcnt lgkmcnt(10)\n\t" \
    "v_mfma_f32_16x16x32_bf16 v[136:139], v[68:71], a[32:35], v[136:139]\n\t" \
    "ds_read_b64_tr_b16 v[92:93], v39 offset:32768\n\t" \
    "v_mfma_f32_16x16x32_bf16 v[140:143], v[68:71], a[36:39], v[140:143]\n\t" \
    "ds_read_b64_tr_b16 v[94:95], v39 offset:33792\n\t" \
    "buffer_load_dwordx4 v28, s[12:15], s32 offen offset:2048 lds\n\t" \
    "s_waitcnt lgkmcnt(10)\n\t" \
    "v_mfma_f32_16x16x32_bf16 v[144:147], v[72:75], a[32:35], v[144:147]\n\t" \
    "ds_read_b64_tr_b16 v[64:65], v32 offset:40960\n\t" \
    "v_mfma_f32_16x16x32_bf16 v[148:151], v[72:75], a[36:39], v[148:151]\n\t" \
    "ds_read_b64_tr_b16 v[66:67], v32 offset:41984\n\t" \
    "buffer_load_dwordx4 v29, s[12:15], s32 offen offset:3072 lds\n\t" \
    "s_waitcnt lgkmcnt(10)\n\t" \
    "v_mfma_f32_16x16x32_bf16 v[152:155], v[76:79], a[32:35], v[152:155]\n\t" \
    "ds_read_b64_tr_b16 v[68:69], v33 offset:40960\n\t" \
    "v_mfma_f32_16x16x32_bf16 v[156:159], v[76:79], a[36:39], v[156:159]\n\t" \
    "ds_read_b64_tr_b16 v[70:71], v33 offset:41984\n\t" \
    "buffer_load_dwordx4 a[96:99], v25, s[8:11], s31 offen offset:0 sc1\n\t" \
    "s_waitcnt lgkmcnt(10)\n\t" \
    "v_mfma_f32_16x16x32_bf16 v[160:163], v[80:83], a[32:35], v[160:163]\n\t" \
    "ds_read_b64_tr_b16 v[72:73], v34 offset:40960\n\t" \
    "v_mfma_f32_16x16x32_bf16 v[164:167], v[80:83], a[36:39], v[164:167]\n\t" \
    "ds_read_b64_tr_b16 v[74:75], v34 offset:41984\n\t" \
    "buffer_load_dwordx4 a[100:103], v25, s[8:11], s31 offen offset:256 sc1\n\t" \
    "s_waitcnt lgkmcnt(10)\n\t" \
    "v_mfma_f32_16x16x32_bf16 v[168:171], v[84:87], a[32:35], v[168:171]\n\t" \
    "ds_read_b64_tr_b16 v[76:77], v35 offset:40960\n\t" \
    "v_mfma_f32_16x16x32_bf16 v[172:175], v[84:87], a[36:39], v[172:175]\n\t" \
    "ds_read_b64_tr_b16 v[78:79], v35 offset:41984\n\t" \
    "buffer_load_dwordx4 a[104:107], v25, s[8:11], s31 offen offset:2048 sc1\n\t" \
    "s_waitcnt lgkmcnt(10)\n\t" \
    "v_mfma_f32_16x16x32_bf16 v[176:179], v[88:91], a[32:35], v[176:179]\n\t" \
    "ds_read_b64_tr_b16 v[80:81], v36 offset:40960\n\t" \
    "v_mfma_f32_16x16x32_bf16 v[180:183], v[88:91], a[36:39], v[180:183]\n\t" \
    "ds_read_b64_tr_b16 v[82:83], v36 offset:41984\n\t" \
    "buffer_load_dwordx4 a[108:111], v25, s[8:11], s31 offen offset:2304 sc1\n\t" \
    "s_add_u32 s31, s31, 4096\n\t" \
    "s_add_u32 s32, s32, 65536\n\t" \
    "s_waitcnt lgkmcnt(10)\n\t" \
    "v_mfma_f32_16x16x32_bf16 v[184:187], v[92:95], a[32:35], v[184:187]\n\t" \
    "ds_read_b64_tr_b16 v[84:85], v37 offset:40960\n\t" \
    "v_mfma_f32_16x16x32_bf16 v[188:191], v[92:95], a[36:39], v[188:191]\n\t" \
    "ds_read_b64_tr_b16 v[86:87], v37 offset:41984\n\t" \
    "s_waitcnt vmcnt(28)\n\t" \
    "s_barrier\n\t" \
    "s_waitcnt lgkmcnt(10)\n\t" \
    "v_mfma_f32_16x16x32_bf16 v[128:131], v[64:67], a[40:43], v[128:131]\n\t" \
    "ds_read_b64_tr_b16 v[88:89], v38 offset:40960\n\t" \
    "s_add_u32 m0, s33, 114688\n\t" \
    "v_mfma_f32_16x16x32_bf16 v[132:135], v[64:67], a[44:47], v[132:135]\n\t" \
    "ds_read_b64_tr_b16 v[90:91], v38 offset:41984\n\t" \
    "buffer_load_dwordx4 v26, s[12:15], s32 offen lds\n\t" \
    "s_waitcnt lgkmcnt(10)\n\t" \
    "v_mfma_f32_16x16x32_bf16 v[136:139], v[68:71], a[40:43], v[136:139]\n\t" \
    "ds_read_b64_tr_b16 v[92:93], v39 offset:40960\n\t" \
    "buffer_load_dwordx4 v27, s[12:15], s32 offen offset:1024 lds\n\t" \
    "v_mfma_f32_16x16x32_bf16 v[140:143], v[68:71], a[44:47], v[140:143]\n\t" \
    "ds_read_b64_tr_b16 v[94:95], v39 offset:41984\n\t" \
    "s_waitcnt lgkmcnt(10)\n\t" \
    "v_mfma_f32_16x16x32_bf16 v[144:147], v[72:75], a[40:43], v[144:147]\n\t" \
    "ds_read_b64_tr_b16 v[64:65], v32 offset:49152\n\t" \
    "buffer_load_dwordx4 v28, s[12:15], s32 offen offset:2048 lds\n\t" \
    "v_mfma_f32_16x16x32_bf16 v[148:151], v[72:75], a[44:47], v[148:151]\n\t" \
    "ds_read_b64_tr_b16 v[66:67], v32 offset:50176\n\t" \
    "s_waitcnt lgkmcnt(10)\n\t" \
    "v_mfma_f32_16x16x32_bf16 v[152:155], v[76:79], a[40:43], v[152:155]\n\t" \
    "ds_read_b64_tr_b16 v[68:69], v33 offset:49152\n\t" \
    "buffer_load_dwordx4 v29, s[12:15], s32 offen offset:3072 lds\n\t" \
    "v_mfma_f32_16x16x32_bf16 v[156:159], v[76:79], a[44:47], v[156:159]\n\t" \
    "ds_read_b64_tr_b16 v[70:71], v33 offset:50176\n\t" \
    "s_waitcnt lgkmcnt(10)\n\t" \
    "v_mfma_f32_16x16x32_bf16 v[160:163], v[80:83], a[40:43], v[160:163]\n\t" \
    "ds_read_b64_tr_b16 v[72:73], v34 offset:49152\n\t" \
    "buffer_load_dwordx4 a[112:115], v25, s[8:11], s31 offen offset:0 sc1\n\t" \
    "v_mfma_f32_16x16x32_bf16 v[164:167], v[80:83], a[44:47], v[164:167]\n\t" \
    "ds_read_b64_tr_b16 v[74:75], v34 offset:50176\n\t" \
    "s_waitcnt lgkmcnt(10)\n\t" \
    "v_mfma_f32_16x16x32_bf16 v[168:171], v[84:87], a[40:43], v[168:171]\n\t" \
    "ds_read_b64_tr_b16 v[76:77], v35 offset:49152\n\t" \
    "buffer_load_dwordx4 a[116:119], v25, s[8:11], s31 offen offset:256 sc1\n\t" \
    "v_mfma_f32_16x16x32_bf16 v[172:175], v[84:87], a[44:47], v[172:175]\n\t" \
    "ds_read_b64_tr_b16 v[78:79], v35 offset:50176\n\t" \
    "s_waitcnt lgkmcnt(10)\n\t" \
    "v_mfma_f32_16x16x32_bf16 v[176:179], v[88:91], a[40:43], v[176:179]\n\t" \
    "ds_read_b64_tr_b16 v[80:81], v36 offset:49152\n\t" \
    "buffer_load_dwordx4 a[120:123], v25, s[8:11], s31 offen offset:2048 sc1\n\t" \
    "v_mfma_f32_16x16x32_bf16 v[180:183], v[88:91], a[44:47], v[180:183]\n\t" \
    "ds_read_b64_tr_b16 v[82:83], v36 offset:50176\n\t" \
    "s_waitcnt lgkmcnt(10)\n\t" \
    "v_mfma_f32_16x16x32_bf16 v[184:187], v[92:95], a[40:43], v[184:187]\n\t" \
    "ds_read_b64_tr_b16 v[84:85], v37 offset:49152\n\t" \
    "buffer_load_dwordx4 a[124:127], v25, s[8:11], s31 offen offset:2304 sc1\n\t" \
    "s_add_u32 s31, s31, 4096\n\t" \
    "s_add_u32 s32, s32, 65536\n\t" \
    "v_mfma_f32_16x16x32_bf16 v[188:191], v[92:95], a[44:47], v[188:191]\n\t" \
    "ds_read_b64_tr_b16 v[86:87], v37 offset:50176\n\t" \
    "s_add_u32 m0, s33, 0\n\t" \
    "s_waitcnt vmcnt(35) lgkmcnt(10)\n\t" \
    "v_mfma_f32_16x16x32_bf16 v[128:131], v[64:67], a[48:51], v[128:131]\n\t" \
    "ds_read_b64_tr_b16 v[88:89], v38 offset:49152\n\t" \
    "buffer_load_dwordx4 v26, s[12:15], s32 offen lds\n\t" \
    "s_waitcnt vmcnt(35)\n\t" \
    "v_mfma_f32_16x16x32_bf16 v[132:135], v[64:67], a[52:55], v[132:135]\n\t" \
    "ds_read_b64_tr_b16 v[90:91], v38 offset:50176\n\t" \
    "buffer_load_dwordx4 v27, s[12:15], s32 offen offset:1024 lds\n\t" \
    "s_waitcnt lgkmcnt(10)\n\t" \
    "v_mfma_f32_16x16x32_bf16 v[136:139], v[68:71], a[48:51], v[136:139]\n\t" \
    "ds_read_b64_tr_b16 v[92:93], v39 offset:49152\n\t" \
    "v_mfma_f32_16x16x32_bf16 v[140:143], v[68:71], a[52:55], v[140:143]\n\t" \
    "ds_read_b64_tr_b16 v[94:95], v39 offset:50176\n\t" \
    "buffer_load_dwordx4 v28, s[12:15], s32 offen offset:2048 lds\n\t" \
    "s_waitcnt lgkmcnt(10)\n\t" \
    "v_mfma_f32_16x16x32_bf16 v[144:147], v[72:75], a[48:51], v[144:147]\n\t" \
    "ds_read_b64_tr_b16 v[64:65], v32 offset:57344\n\t" \
    "v_mfma_f32_16x16x32_bf16 v[148:151], v[72:75], a[52:55], v[148:151]\n\t" \
    "ds_read_b64_tr_b16 v[66:67], v32 offset:58368\n\t" \
    "buffer_load_dwordx4 v29, s[12:15], s32 offen offset:3072 lds\n\t" \
    "s_waitcnt lgkmcnt(10)\n\t" \
    "v_mfma_f32_16x16x32_bf16 v[152:155], v[76:79], a[48:51], v[152:155]\n\t" \
    "ds_read_b64_tr_b16 v[68:69], v33 offset:57344\n\t" \
    "v_mfma_f32_16x16x32_bf16 v[156:159], v[76:79], a[52:55], v[156:159]\n\t" \
    "ds_read_b64_tr_b16 v[70:71], v33 offset:58368\n\t" \
    "buffer_load_dwordx4 a[0:3], v25, s[8:11], s31 offen offset:0 sc1\n\t" \
    "s_waitcnt lgkmcnt(10)\n\t" \
    "v_mfma_f32_16x16x32_bf16 v[160:163], v[80:83], a[48:51], v[160:163]\n\t" \
    "ds_read_b64_tr_b16 v[72:73], v34 offset:57344\n\t" \
    "v_mfma_f32_16x16x32_bf16 v[164:167], v[80:83], a[52:55], v[164:167]\n\t" \
    "ds_read_b64_tr_b16 v[74:75], v34 offset:58368\n\t" \
    "buffer_load_dwordx4 a[4:7], v25, s[8:11], s31 offen offset:256 sc1\n\t" \
    "s_waitcnt lgkmcnt(10)\n\t" \
    "v_mfma_f32_16x16x32_bf16 v[168:171], v[84:87], a[48:51], v[168:171]\n\t" \
    "ds_read_b64_tr_b16 v[76:77], v35 offset:57344\n\t" \
    "v_mfma_f32_16x16x32_bf16 v[172:175], v[84:87], a[52:55], v[172:175]\n\t" \
    "ds_read_b64_tr_b16 v[78:79], v35 offset:58368\n\t" \
    "buffer_load_dwordx4 a[8:11], v25, s[8:11], s31 offen offset:2048 sc1\n\t" \
    "s_waitcnt lgkmcnt(10)\n\t" \
    "v_mfma_f32_16x16x32_bf16 v[176:179], v[88:91], a[48:51], v[176:179]\n\t" \
    "ds_read_b64_tr_b16 v[80:81], v36 offset:57344\n\t" \
    "v_mfma_f32_16x16x32_bf16 v[180:183], v[88:91], a[52:55], v[180:183]\n\t" \
    "ds_read_b64_tr_b16 v[82:83], v36 offset:58368\n\t" \
    "buffer_load_dwordx4 a[12:15], v25, s[8:11], s31 offen offset:2304 sc1\n\t" \
    "s_add_u32 s31, s31, 4096\n\t" \
    "s_add_u32 s32, s32, 65536\n\t" \
    "s_waitcnt lgkmcnt(10)\n\t" \
    "v_mfma_f32_16x16x32_bf16 v[184:187], v[92:95], a[48:51], v[184:187]\n\t" \
    "ds_read_b64_tr_b16 v[84:85], v37 offset:57344\n\t" \
    "v_mfma_f32_16x16x32_bf16 v[188:191], v[92:95], a[52:55], v[188:191]\n\t" \
    "ds_read_b64_tr_b16 v[86:87], v37 offset:58368\n\t" \
    "s_waitcnt vmcnt(36)\n\t" \
    "s_barrier\n\t" \
    "s_waitcnt lgkmcnt(10)\n\t" \
    "v_mfma_f32_16x16x32_bf16 v[128:131], v[64:67], a[56:59], v[128:131]\n\t" \
    "ds_read_b64_tr_b16 v[88:89], v38 offset:57344\n\t" \
    "s_add_u32 m0, s33, 16384\n\t" \
    "v_mfma_f32_16x16x32_bf16 v[132:135], v[64:67], a[60:63], v[132:135]\n\t" \
    "ds_read_b64_tr_b16 v[90:91], v38 offset:58368\n\t" \
    "buffer_load_dwordx4 v26, s[12:15], s32 offen lds\n\t" \
    "s_waitcnt lgkmcnt(10)\n\t" \
    "v_mfma_f32_16x16x32_bf16 v[136:139], v[68:71], a[56:59], v[136:139]\n\t" \
    "ds_read_b64_tr_b16 v[92:93], v39 offset:57344\n\t" \
    "buffer_load_dwordx4 v27, s[12:15], s32 offen offset:1024 lds\n\t" \
    "v_mfma_f32_16x16x32_bf16 v[140:143], v[68:71], a[60:63], v[140:143]\n\t" \
    "ds_read_b64_tr_b16 v[94:95], v39 offset:58368\n\t" \
    "s_waitcnt lgkmcnt(10)\n\t" \
    "v_mfma_f32_16x16x32_bf16 v[144:147], v[72:75], a[56:59], v[144:147]\n\t" \
    "ds_read_b64_tr_b16 v[64:65], v40 offset:0\n\t" \
    "buffer_load_dwordx4 v28, s[12:15], s32 offen offset:2048 lds\n\t" \
    "v_mfma_f32_16x16x32_bf16 v[148:151], v[72:75], a[60:63], v[148:151]\n\t" \
    "ds_read_b64_tr_b16 v[66:67], v40 offset:1024\n\t" \
    "s_waitcnt lgkmcnt(10)\n\t" \
    "v_mfma_f32_16x16x32_bf16 v[152:155], v[76:79], a[56:59], v[152:155]\n\t" \
    "ds_read_b64_tr_b16 v[68:69], v41 offset:0\n\t" \
    "buffer_load_dwordx4 v29, s[12:15], s32 offen offset:3072 lds\n\t" \
    "v_mfma_f32_16x16x32_bf16 v[156:159], v[76:79], a[60:63], v[156:159]\n\t" \
    "ds_read_b64_tr_b16 v[70:71], v41 offset:1024\n\t" \
    "s_waitcnt lgkmcnt(10)\n\t" \
    "v_mfma_f32_16x16x32_bf16 v[160:163], v[80:83], a[56:59], v[160:163]\n\t" \
    "ds_read_b64_tr_b16 v[72:73], v42 offset:0\n\t" \
    "buffer_load_dwordx4 a[16:19], v25, s[8:11], s31 offen offset:0 sc1\n\t" \
    "v_mfma_f32_16x16x32_bf16 v[164:167], v[80:83], a[60:63], v[164:167]\n\t" \
    "ds_read_b64_tr_b16 v[74:75], v42 offset:1024\n\t" \
    "s_waitcnt lgkmcnt(10)\n\t" \
    "v_mfma_f32_16x16x32_bf16 v[168:171], v[84:87], a[56:59], v[168:171]\n\t" \
    "ds_read_b64_tr_b16 v[76:77], v43 offset:0\n\t" \
    "buffer_load_dwordx4 a[20:23], v25, s[8:11], s31 offen offset:256 sc1\n\t" \
    "v_mfma_f32_16x16x32_bf16 v[172:175], v[84:87], a[60:63], v[172:175]\n\t" \
    "ds_read_b64_tr_b16 v[78:79], v43 offset:1024\n\t" \
    "s_waitcnt lgkmcnt(10)\n\t" \
    "v_mfma_f32_16x16x32_bf16 v[176:179], v[88:91], a[56:59], v[176:179]\n\t" \
    "ds_read_b64_tr_b16 v[80:81], v44 offset:0\n\t" \
    "buffer_load_dwordx4 a[24:27], v25, s[8:11], s31 offen offset:2048 sc1\n\t" \
    "v_mfma_f32_16x16x32_bf16 v[180:183], v[88:91], a[60:63], v[180:183]\n\t" \
    "ds_read_b64_tr_b16 v[82:83], v44 offset:1024\n\t" \
    "s_waitcnt lgkmcnt(10)\n\t" \
    "v_mfma_f32_16x16x32_bf16 v[184:187], v[92:95], a[56:59], v[184:187]\n\t" \
    "ds_read_b64_tr_b16 v[84:85], v45 offset:0\n\t" \
    "buffer_load_dwordx4 a[28:31], v25, s[8:11], s31 offen offset:2304 sc1\n\t" \
    "s_add_u32 s31, s31, 4096\n\t" \
    "s_add_u32 s32, s32, 65536\n\t" \
    "v_mfma_f32_16x16x32_bf16 v[188:191], v[92:95], a[60:63], v[188:191]\n\t" \
    "ds_read_b64_tr_b16 v[86:87], v45 offset:1024\n\t" \
    "s_add_u32 m0, s33, 32768\n\t" \
    "s_waitcnt vmcnt(43) lgkmcnt(10)\n\t" \
    "v_mfma_f32_16x16x32_bf16 v[128:131], v[64:67], a[64:67], v[128:131]\n\t" \
    "ds_read_b64_tr_b16 v[88:89], v46 offset:0\n\t" \
    "buffer_load_dwordx4 v26, s[12:15], s32 offen lds\n\t" \
    "s_waitcnt vmcnt(43)\n\t" \
    "v_mfma_f32_16x16x32_bf16 v[132:135], v[64:67], a[68:71], v[132:135]\n\t" \
    "ds_read_b64_tr_b16 v[90:91], v46 offset:1024\n\t" \
    "buffer_load_dwordx4 v27, s[12:15], s32 offen offset:1024 lds\n\t" \
    "s_waitcnt lgkmcnt(10)\n\t" \
    "v_mfma_f32_16x16x32_bf16 v[136:139], v[68:71], a[64:67], v[136:139]\n\t" \
    "ds_read_b64_tr_b16 v[92:93], v47 offset:0\n\t" \
    "v_mfma_f32_16x16x32_bf16 v[140:143], v[68:71], a[68:71], v[140:143]\n\t" \
    "ds_read_b64_tr_b16 v[94:95], v47 offset:1024\n\t" \
    "buffer_load_dwordx4 v28, s[12:15], s32 offen offset:2048 lds\n\t" \
    "s_waitcnt lgkmcnt(10)\n\t" \
    "v_mfma_f32_16x16x32_bf16 v[144:147], v[72:75], a[64:67], v[144:147]\n\t" \
    "ds_read_b64_tr_b16 v[64:65], v40 offset:8192\n\t" \
    "v_mfma_f32_16x16x32_bf16 v[148:151], v[72:75], a[68:71], v[148:151]\n\t" \
    "ds_read_b64_tr_b16 v[66:67], v40 offset:9216\n\t" \
    "buffer_load_dwordx4 v29, s[12:15], s32 offen offset:3072 lds\n\t" \
    "s_waitcnt lgkmcnt(10)\n\t" \
    "v_mfma_f32_16x16x32_bf16 v[152:155], v[76:79], a[64:67], v[152:155]\n\t" \
    "ds_read_b64_tr_b16 v[68:69], v41 offset:8192\n\t" \
    "v_mfma_f32_16x16x32_bf16 v[156:159], v[76:79], a[68:71], v[156:159]\n\t" \
    "ds_read_b64_tr_b16 v[70:71], v41 offset:9216\n\t" \
    "buffer_load_dwordx4 a[32:35], v25, s[8:11], s31 offen offset:0 sc1\n\t" \
    "s_waitcnt lgkmcnt(10)\n\t" \
    "v_mfma_f32_16x16x32_bf16 v[160:163], v[80:83], a[64:67], v[160:163]\n\t" \
    "ds_read_b64_tr_b16 v[72:73], v42 offset:8192\n\t" \
    "v_mfma_f32_16x16x32_bf16 v[164:167], v[80:83], a[68:71], v[164:167]\n\t" \
    "ds_read_b64_tr_b16 v[74:75], v42 offset:9216\n\t" \
    "buffer_load_dwordx4 a[36:39], v25, s[8:11], s31 offen offset:256 sc1\n\t" \
    "s_waitcnt lgkmcnt(10)\n\t" \
    "v_mfma_f32_16x16x32_bf16 v[168:171], v[84:87], a[64:67], v[168:171]\n\t" \
    "ds_read_b64_tr_b16 v[76:77], v43 offset:8192\n\t" \
    "v_mfma_f32_16x16x32_bf16 v[172:175], v[84:87], a[68:71], v[172:175]\n\t" \
    "ds_read_b64_tr_b16 v[78:79], v43 offset:9216\n\t" \
    "buffer_load_dwordx4 a[40:43], v25, s[8:11], s31 offen offset:2048 sc1\n\t" \
    "s_waitcnt lgkmcnt(10)\n\t" \
    "v_mfma_f32_16x16x32_bf16 v[176:179], v[88:91], a[64:67], v[176:179]\n\t" \
    "ds_read_b64_tr_b16 v[80:81], v44 offset:8192\n\t" \
    "v_mfma_f32_16x16x32_bf16 v[180:183], v[88:91], a[68:71], v[180:183]\n\t" \
    "ds_read_b64_tr_b16 v[82:83], v44 offset:9216\n\t" \
    "buffer_load_dwordx4 a[44:47], v25, s[8:11], s31 offen offset:2304 sc1\n\t" \
    "s_add_u32 s31, s31, 4096\n\t" \
    "s_add_u32 s32, s32, 65536\n\t" \
    "s_waitcnt lgkmcnt(10)\n\t" \
    "v_mfma_f32_16x16x32_bf16 v[184:187], v[92:95], a[64:67], v[184:187]\n\t" \
    "ds_read_b64_tr_b16 v[84:85], v45 offset:8192\n\t" \
    "v_mfma_f32_16x16x32_bf16 v[188:191], v[92:95], a[68:71], v[188:191]\n\t" \
    "ds_read_b64_tr_b16 v[86:87], v45 offset:9216\n\t" \
    "s_waitcnt vmcnt(44)\n\t" \
    "s_barrier\n\t" \
    "s_waitcnt lgkmcnt(10)\n\t" \
    "v_mfma_f32_16x16x32_bf16 v[128:131], v[64:67], a[72:75], v[128:131]\n\t" \
    "ds_read_b64_tr_b16 v[88:89], v46 offset:8192\n\t" \
    "s_add_u32 m0, s33, 49152\n\t" \
    "v_mfma_f32_16x16x32_bf16 v[132:135], v[64:67], a[76:79], v[132:135]\n\t" \
    "ds_read_b64_tr_b16 v[90:91], v46 offset:9216\n\t" \
    "buffer_load_dwordx4 v26, s[12:15], s32 offen lds\n\t" \
    "s_waitcnt lgkmcnt(10)\n\t" \
    "v_mfma_f32_16x16x32_bf16 v[136:139], v[68:71], a[72:75], v[136:139]\n\t" \
    "ds_read_b64_tr_b16 v[92:93], v47 offset:8192\n\t" \
    "v_mfma_f32_16x16x32_bf16 v[140:143], v[68:71], a[76:79], v[140:143]\n\t" \
    "ds_read_b64_tr_b16 v[94:95], v47 offset:9216\n\t" \
    "s_waitcnt lgkmcnt(10)\n\t" \
    "v_mfma_f32_16x16x32_bf16 v[144:147], v[72:75], a[72:75], v[144:147]\n\t" \
    "ds_read_b64_tr_b16 v[64:65], v40 offset:16384\n\t" \
    "buffer_load_dwordx4 v27, s[12:15], s32 offen offset:1024 lds\n\t" \
    "v_mfma_f32_16x16x32_bf16 v[148:151], v[72:75], a[76:79], v[148:151]\n\t" \
    "ds_read_b64_tr_b16 v[66:67], v40 offset:17408\n\t" \
    "s_waitcnt lgkmcnt(10)\n\t" \
    "v_mfma_f32_16x16x32_bf16 v[152:155], v[76:79], a[72:75], v[152:155]\n\t" \
    "ds_read_b64_tr_b16 v[68:69], v41 offset:16384\n\t" \
    "v_mfma_f32_16x16x32_bf16 v[156:159], v[76:79], a[76:79], v[156:159]\n\t" \
    "ds_read_b64_tr_b16 v[70:71], v41 offset:17408\n\t" \
    "s_waitcnt lgkmcnt(10)\n\t" \
    "v_mfma_f32_16x16x32_bf16 v[160:163], v[80:83], a[72:75], v[160:163]\n\t" \
    "ds_read_b64_tr_b16 v[72:73], v42 offset:16384\n\t" \
    "buffer_load_dwordx4 v28, s[12:15], s32 offen offset:2048 lds\n\t" \
    "v_mfma_f32_16x16x32_bf16 v[164:167], v[80:83], a[76:79], v[164:167]\n\t" \
    "ds_read_b64_tr_b16 v[74:75], v42 offset:17408\n\t" \
    "s_waitcnt lgkmcnt(10)\n\t" \
    "v_mfma_f32_16x16x32_bf16 v[168:171], v[84:87], a[72:75], v[168:171]\n\t" \
    "ds_read_b64_tr_b16 v[76:77], v43 offset:16384\n\t" \
    "v_mfma_f32_16x16x32_bf16 v[172:175], v[84:87], a[76:79], v[172:175]\n\t" \
    "ds_read_b64_tr_b16 v[78:79], v43 offset:17408\n\t" \
    "s_waitcnt lgkmcnt(10)\n\t" \
    "v_mfma_f32_16x16x32_bf16 v[176:179], v[88:91], a[72:75], v[176:179]\n\t" \
    "ds_read_b64_tr_b16 v[80:81], v44 offset:16384\n\t" \
    "buffer_load_dwordx4 v29, s[12:15], s32 offen offset:3072 lds\n\t" \
    "v_mfma_f32_16x16x32_bf16 v[180:183], v[88:91], a[76:79], v[180:183]\n\t" \
    "ds_read_b64_tr_b16 v[82:83], v44 offset:17408\n\t" \
    "s_waitcnt lgkmcnt(10)\n\t" \
    "v_mfma_f32_16x16x32_bf16 v[184:187], v[92:95], a[72:75], v[184:187]\n\t" \
    "ds_read_b64_tr_b16 v[84:85], v45 offset:16384\n\t" \
    "v_mfma_f32_16x16x32_bf16 v[188:191], v[92:95], a[76:79], v[188:191]\n\t" \
    "ds_read_b64_tr_b16 v[86:87], v45 offset:17408\n\t" \
    "s_waitcnt vmcnt(47) lgkmcnt(10)\n\t" \
    "v_mfma_f32_16x16x32_bf16 v[128:131], v[64:67], a[80:83], v[128:131]\n\t" \
    "ds_read_b64_tr_b16 v[88:89], v46 offset:16384\n\t" \
    "buffer_load_dwordx4 a[48:51], v25, s[8:11], s31 offen offset:0 sc1\n\t" \
    "s_waitcnt vmcnt(47)\n\t" \
    "v_mfma_f32_16x16x32_bf16 v[132:135], v[64:67], a[84:87], v[132:135]\n\t" \
    "ds_read_b64_tr_b16 v[90:91], v46 offset:17408\n\t" \
    "s_waitcnt lgkmcnt(10)\n\t" \
    "v_mfma_f32_16x16x32_bf16 v[136:139], v[68:71], a[80:83], v[136:139]\n\t" \
    "ds_read_b64_tr_b16 v[92:93], v47 offset:16384\n\t" \
    "v_mfma_f32_16x16x32_bf16 v[140:143], v[68:71], a[84:87], v[140:143]\n\t" \
    "ds_read_b64_tr_b16 v[94:95], v47 offset:17408\n\t" \
    "buffer_load_dwordx4 a[52:55], v25, s[8:11], s31 offen offset:256 sc1\n\t" \
    "s_waitcnt lgkmcnt(10)\n\t" \
    "v_mfma_f32_16x16x32_bf16 v[144:147], v[72:75], a[80:83], v[144:147]\n\t" \
    "ds_read_b64_tr_b16 v[64:65], v40 offset:24576\n\t" \
    "v_mfma_f32_16x16x32_bf16 v[148:151], v[72:75], a[84:87], v[148:151]\n\t" \
    "ds_read_b64_tr_b16 v[66:67], v40 offset:25600\n\t" \
    "s_waitcnt lgkmcnt(10)\n\t" \
    "v_mfma_f32_16x16x32_bf16 v[152:155], v[76:79], a[80:83], v[152:155]\n\t" \
    "ds_read_b64_tr_b16 v[68:69], v41 offset:24576\n\t" \
    "v_mfma_f32_16x16x32_bf16 v[156:159], v[76:79], a[84:87], v[156:159]\n\t" \
    "ds_read_b64_tr_b16 v[70:71], v41 offset:25600\n\t" \
    "buffer_load_dwordx4 a[56:59], v25, s[8:11], s31 offen offset:2048 sc1\n\t" \
    "s_waitcnt lgkmcnt(10)\n\t" \
    "v_mfma_f32_16x16x32_bf16 v[160:163], v[80:83], a[80:83], v[160:163]\n\t" \
    "ds_read_b64_tr_b16 v[72:73], v42 offset:24576\n\t" \
    "v_mfma_f32_16x16x32_bf16 v[164:167], v[80:83], a[84:87], v[164:167]\n\t" \
    "ds_read_b64_tr_b16 v[74:75], v42 offset:25600\n\t" \
    "s_waitcnt lgkmcnt(10)\n\t" \
    "v_mfma_f32_16x16x32_bf16 v[168:171], v[84:87], a[80:83], v[168:171]\n\t" \
    "ds_read_b64_tr_b16 v[76:77], v43 offset:24576\n\t" \
    "v_mfma_f32_16x16x32_bf16 v[172:175], v[84:87], a[84:87], v[172:175]\n\t" \
    "ds_read_b64_tr_b16 v[78:79], v43 offset:25600\n\t" \
    "buffer_load_dwordx4 a[60:63], v25, s[8:11], s31 offen offset:2304 sc1\n\t" \
    "s_add_u32 s31, s31, 4096\n\t" \
    "s_add_u32 s32, s32, 65536\n\t" \
    "s_waitcnt lgkmcnt(10)\n\t" \
    "v_mfma_f32_16x16x32_bf16 v[176:179], v[88:91], a[80:83], v[176:179]\n\t" \
    "ds_read_b64_tr_b16 v[80:81], v44 offset:24576\n\t" \
    "v_mfma_f32_16x16x32_bf16 v[180:183], v[88:91], a[84:87], v[180:183]\n\t" \
    "ds_read_b64_tr_b16 v[82:83], v44 offset:25600\n\t" \
    "s_waitcnt lgkmcnt(10)\n\t" \
    "v_mfma_f32_16x16x32_bf16 v[184:187], v[92:95], a[80:83], v[184:187]\n\t" \
    "ds_read_b64_tr_b16 v[84:85], v45 offset:24576\n\t" \
    "v_mfma_f32_16x16x32_bf16 v[188:191], v[92:95], a[84:87], v[188:191]\n\t" \
    "ds_read_b64_tr_b16 v[86:87], v45 offset:25600\n\t" \
    "s_waitcnt vmcnt(44)\n\t" \
    "s_barrier\n\t" \
    "s_waitcnt lgkmcnt(10)\n\t" \
    "v_mfma_f32_16x16x32_bf16 v[128:131], v[64:67], a[88:91], v[128:131]\n\t" \
    "ds_read_b64_tr_b16 v[88:89], v46 offset:24576\n\t" \
    "s_add_u32 m0, s33, 65536\n\t" \
    "v_mfma_f32_16x16x32_bf16 v[132:135], v[64:67], a[92:95], v[132:135]\n\t" \
    "ds_read_b64_tr_b16 v[90:91], v46 offset:25600\n\t" \
    "buffer_load_dwordx4 v26, s[12:15], s32 offen lds\n\t" \
    "s_waitcnt lgkmcnt(10)\n\t" \
    "v_mfma_f32_16x16x32_bf16 v[136:139], v[68:71], a[88:91], v[136:139]\n\t" \
    "ds_read_b64_tr_b16 v[92:93], v47 offset:24576\n\t" \
    "v_mfma_f32_16x16x32_bf16 v[140:143], v[68:71], a[92:95], v[140:143]\n\t" \
    "ds_read_b64_tr_b16 v[94:95], v47 offset:25600\n\t" \
    "s_waitcnt lgkmcnt(10)\n\t" \
    "v_mfma_f32_16x16x32_bf16 v[144:147], v[72:75], a[88:91], v[144:147]\n\t" \
    "ds_read_b64_tr_b16 v[64:65], v40 offset:32768\n\t" \
    "buffer_load_dwordx4 v27, s[12:15], s32 offen offset:1024 lds\n\t" \
    "v_mfma_f32_16x16x32_bf16 v[148:151], v[72:75], a[92:95], v[148:151]\n\t" \
    "ds_read_b64_tr_b16 v[66:67], v40 offset:33792\n\t" \
    "s_waitcnt lgkmcnt(10)\n\t" \
    "v_mfma_f32_16x16x32_bf16 v[152:155], v[76:79], a[88:91], v[152:155]\n\t" \
    "ds_read_b64_tr_b16 v[68:69], v41 offset:32768\n\t" \
    "v_mfma_f32_16x16x32_bf16 v[156:159], v[76:79], a[92:95], v[156:159]\n\t" \
    "ds_read_b64_tr_b16 v[70:71], v41 offset:33792\n\t" \
    "s_waitcnt lgkmcnt(10)\n\t" \
    "v_mfma_f32_16x16x32_bf16 v[160:163], v[80:83], a[88:91], v[160:163]\n\t" \
    "ds_read_b64_tr_b16 v[72:73], v42 offset:32768\n\t" \
    "buffer_load_dwordx4 v28, s[12:15], s32 offen offset:2048 lds\n\t" \
    "v_mfma_f32_16x16x32_bf16 v[164:167], v[80:83], a[92:95], v[164:167]\n\t" \
    "ds_read_b64_tr_b16 v[74:75], v42 offset:33792\n\t" \
    "s_waitcnt lgkmcnt(10)\n\t" \
    "v_mfma_f32_16x16x32_bf16 v[168:171], v[84:87], a[88:91], v[168:171]\n\t" \
    "ds_read_b64_tr_b16 v[76:77], v43 offset:32768\n\t" \
    "v_mfma_f32_16x16x32_bf16 v[172:175], v[84:87], a[92:95], v[172:175]\n\t" \
    "ds_read_b64_tr_b16 v[78:79], v43 offset:33792\n\t" \
    "s_waitcnt lgkmcnt(10)\n\t" \
    "v_mfma_f32_16x16x32_bf16 v[176:179], v[88:91], a[88:91], v[176:179]\n\t" \
    "ds_read_b64_tr_b16 v[80:81], v44 offset:32768\n\t" \
    "buffer_load_dwordx4 v29, s[12:15], s32 offen offset:3072 lds\n\t" \
    "v_mfma_f32_16x16x32_bf16 v[180:183], v[88:91], a[92:95], v[180:183]\n\t" \
    "ds_read_b64_tr_b16 v[82:83], v44 offset:33792\n\t" \
    "s_waitcnt lgkmcnt(10)\n\t" \
    "v_mfma_f32_16x16x32_bf16 v[184:187], v[92:95], a[88:91], v[184:187]\n\t" \
    "ds_read_b64_tr_b16 v[84:85], v45 offset:32768\n\t" \
    "v_mfma_f32_16x16x32_bf16 v[188:191], v[92:95], a[92:95], v[188:191]\n\t" \
    "ds_read_b64_tr_b16 v[86:87], v45 offset:33792\n\t" \
    "s_waitcnt vmcnt(47) lgkmcnt(10)\n\t" \
    "v_mfma_f32_16x16x32_bf16 v[128:131], v[64:67], a[96:99], v[128:131]\n\t" \
    "ds_read_b64_tr_b16 v[88:89], v46 offset:32768\n\t" \
    "buffer_load_dwordx4 a[64:67], v25, s[8:11], s31 offen offset:0 sc1\n\t" \
    "s_waitcnt vmcnt(47)\n\t" \
    "v_mfma_f32_16x16x32_bf16 v[132:135], v[64:67], a[100:103], v[132:135]\n\t" \
    "ds_read_b64_tr_b16 v[90:91], v46 offset:33792\n\t" \
    "s_waitcnt lgkmcnt(10)\n\t" \
    "v_mfma_f32_16x16x32_bf16 v[136:139], v[68:71], a[96:99], v[136:139]\n\t" \
    "ds_read_b64_tr_b16 v[92:93], v47 offset:32768\n\t" \
    "v_mfma_f32_16x16x32_bf16 v[140:143], v[68:71], a[100:103], v[140:143]\n\t" \
    "ds_read_b64_tr_b16 v[94:95], v47 offset:33792\n\t" \
    "buffer_load_dwordx4 a[68:71], v25, s[8:11], s31 offen offset:256 sc1\n\t" \
    "s_waitcnt lgkmcnt(10)\n\t" \
    "v_mfma_f32_16x16x32_bf16 v[144:147], v[72:75], a[96:99], v[144:147]\n\t" \
    "ds_read_b64_tr_b16 v[64:65], v40 offset:40960\n\t" \
    "v_mfma_f32_16x16x32_bf16 v[148:151], v[72:75], a[100:103], v[148:151]\n\t" \
    "ds_read_b64_tr_b16 v[66:67], v40 offset:41984\n\t" \
    "s_waitcnt lgkmcnt(10)\n\t" \
    "v_mfma_f32_16x16x32_bf16 v[152:155], v[76:79], a[96:99], v[152:155]\n\t" \
    "ds_read_b64_tr_b16 v[68:69], v41 offset:40960\n\t" \
    "v_mfma_f32_16x16x32_bf16 v[156:159], v[76:79], a[100:103], v[156:159]\n\t" \
    "ds_read_b64_tr_b16 v[70:71], v41 offset:41984\n\t" \
    "buffer_load_dwordx4 a[72:75], v25, s[8:11], s31 offen offset:2048 sc1\n\t" \
    "s_waitcnt lgkmcnt(10)\n\t" \
    "v_mfma_f32_16x16x32_bf16 v[160:163], v[80:83], a[96:99], v[160:163]\n\t" \
    "ds_read_b64_tr_b16 v[72:73], v42 offset:40960\n\t" \
    "v_mfma_f32_16x16x32_bf16 v[164:167], v[80:83], a[100:103], v[164:167]\n\t" \
    "ds_read_b64_tr_b16 v[74:75], v42 offset:41984\n\t" \
    "s_waitcnt lgkmcnt(10)\n\t" \
    "v_mfma_f32_16x16x32_bf16 v[168:171], v[84:87], a[96:99], v[168:171]\n\t" \
    "ds_read_b64_tr_b16 v[76:77], v43 offset:40960\n\t" \
    "v_mfma_f32_16x16x32_bf16 v[172:175], v[84:87], a[100:103], v[172:175]\n\t" \
    "ds_read_b64_tr_b16 v[78:79], v43 offset:41984\n\t" \
    "buffer_load_dwordx4 a[76:79], v25, s[8:11], s31 offen offset:2304 sc1\n\t" \
    "s_add_u32 s31, s31, 4096\n\t" \
    "s_add_u32 s32, s32, 65536\n\t" \
    "s_waitcnt lgkmcnt(10)\n\t" \
    "v_mfma_f32_16x16x32_bf16 v[176:179], v[88:91], a[96:99], v[176:179]\n\t" \
    "ds_read_b64_tr_b16 v[80:81], v44 offset:40960\n\t" \
    "v_mfma_f32_16x16x32_bf16 v[180:183], v[88:91], a[100:103], v[180:183]\n\t" \
    "ds_read_b64_tr_b16 v[82:83], v44 offset:41984\n\t" \
    "s_waitcnt lgkmcnt(10)\n\t" \
    "v_mfma_f32_16x16x32_bf16 v[184:187], v[92:95], a[96:99], v[184:187]\n\t" \
    "ds_read_b64_tr_b16 v[84:85], v45 offset:40960\n\t" \
    "v_mfma_f32_16x16x32_bf16 v[188:191], v[92:95], a[100:103], v[188:191]\n\t" \
    "ds_read_b64_tr_b16 v[86:87], v45 offset:41984\n\t" \
    "s_waitcnt vmcnt(44)\n\t" \
    "s_barrier\n\t" \
    "s_waitcnt lgkmcnt(10)\n\t" \
    "v_mfma_f32_16x16x32_bf16 v[128:131], v[64:67], a[104:107], v[128:131]\n\t" \
    "ds_read_b64_tr_b16 v[88:89], v46 offset:40960\n\t" \
    "s_add_u32 m0, s33, 81920\n\t" \
    "v_mfma_f32_16x16x32_bf16 v[132:135], v[64:67], a[108:111], v[132:135]\n\t" \
    "ds_read_b64_tr_b16 v[90:91], v46 offset:41984\n\t" \
    "buffer_load_dwordx4 v26, s[12:15], s32 offen lds\n\t" \
    "s_waitcnt lgkmcnt(10)\n\t" \
    "v_mfma_f32_16x16x32_bf16 v[136:139], v[68:71], a[104:107], v[136:139]\n\t" \
    "ds_read_b64_tr_b16 v[92:93], v47 offset:40960\n\t" \
    "v_mfma_f32_16x16x32_bf16 v[140:143], v[68:71], a[108:111], v[140:143]\n\t" \
    "ds_read_b64_tr_b16 v[94:95], v47 offset:41984\n\t" \
    "s_waitcnt lgkmcnt(10)\n\t" \
    "v_mfma_f32_16x16x32_bf16 v[144:147], v[72:75], a[104:107], v[144:147]\n\t" \
    "ds_read_b64_tr_b16 v[64:65], v40 offset:49152\n\t" \
    "buffer_load_dwordx4 v27, s[12:15], s32 offen offset:1024 lds\n\t" \
    "v_mfma_f32_16x16x32_bf16 v[148:151], v[72:75], a[108:111], v[148:151]\n\t" \
    "ds_read_b64_tr_b16 v[66:67], v40 offset:50176\n\t" \
    "s_waitcnt lgkmcnt(10)\n\t" \
    "v_mfma_f32_16x16x32_bf16 v[152:155], v[76:79], a[104:107], v[152:155]\n\t" \
    "ds_read_b64_tr_b16 v[68:69], v41 offset:49152\n\t" \
    "v_mfma_f32_16x16x32_bf16 v[156:159], v[76:79], a[108:111], v[156:159]\n\t" \
    "ds_read_b64_tr_b16 v[70:71], v41 offset:50176\n\t" \
    "s_waitcnt lgkmcnt(10)\n\t" \
    "v_mfma_f32_16x16x32_bf16 v[160:163], v[80:83], a[104:107], v[160:163]\n\t" \
    "ds_read_b64_tr_b16 v[72:73], v42 offset:49152\n\t" \
    "buffer_load_dwordx4 v28, s[12:15], s32 offen offset:2048 lds\n\t" \
    "v_mfma_f32_16x16x32_bf16 v[164:167], v[80:83], a[108:111], v[164:167]\n\t" \
    "ds_read_b64_tr_b16 v[74:75], v42 offset:50176\n\t" \
    "s_waitcnt lgkmcnt(10)\n\t" \
    "v_mfma_f32_16x16x32_bf16 v[168:171], v[84:87], a[104:107], v[168:171]\n\t" \
    "ds_read_b64_tr_b16 v[76:77], v43 offset:49152\n\t" \
    "v_mfma_f32_16x16x32_bf16 v[172:175], v[84:87], a[108:111], v[172:175]\n\t" \
    "ds_read_b64_tr_b16 v[78:79], v43 offset:50176\n\t" \
    "s_waitcnt lgkmcnt(10)\n\t" \
    "v_mfma_f32_16x16x32_bf16 v[176:179], v[88:91], a[104:107], v[176:179]\n\t" \
    "ds_read_b64_tr_b16 v[80:81], v44 offset:49152\n\t" \
    "buffer_load_dwordx4 v29, s[12:15], s32 offen offset:3072 lds\n\t" \
    "v_mfma_f32_16x16x32_bf16 v[180:183], v[88:91], a[108:111], v[180:183]\n\t" \
    "ds_read_b64_tr_b16 v[82:83], v44 offset:50176\n\t" \
    "s_waitcnt lgkmcnt(10)\n\t" \
    "v_mfma_f32_16x16x32_bf16 v[184:187], v[92:95], a[104:107], v[184:187]\n\t" \
    "ds_read_b64_tr_b16 v[84:85], v45 offset:49152\n\t" \
    "v_mfma_f32_16x16x32_bf16 v[188:191], v[92:95], a[108:111], v[188:191]\n\t" \
    "ds_read_b64_tr_b16 v[86:87], v45 offset:50176\n\t" \
    "s_waitcnt vmcnt(47) lgkmcnt(10)\n\t" \
    "v_mfma_f32_16x16x32_bf16 v[128:131], v[64:67], a[112:115], v[128:131]\n\t" \
    "ds_read_b64_tr_b16 v[88:89], v46 offset:49152\n\t" \
    "buffer_load_dwordx4 a[80:83], v25, s[8:11], s31 offen offset:0 sc1\n\t" \
    "s_waitcnt vmcnt(47)\n\t" \
    "v_mfma_f32_16x16x32_bf16 v[132:135], v[64:67], a[116:119], v[132:135]\n\t" \
    "ds_read_b64_tr_b16 v[90:91], v46 offset:50176\n\t" \
    "s_waitcnt lgkmcnt(10)\n\t" \
    "v_mfma_f32_16x16x32_bf16 v[136:139], v[68:71], a[112:115], v[136:139]\n\t" \
    "ds_read_b64_tr_b16 v[92:93], v47 offset:49152\n\t" \
    "v_mfma_f32_16x16x32_bf16 v[140:143], v[68:71], a[116:119], v[140:143]\n\t" \
    "ds_read_b64_tr_b16 v[94:95], v47 offset:50176\n\t" \
    "buffer_load_dwordx4 a[84:87], v25, s[8:11], s31 offen offset:256 sc1\n\t" \
    "s_waitcnt lgkmcnt(10)\n\t" \
    "v_mfma_f32_16x16x32_bf16 v[144:147], v[72:75], a[112:115], v[144:147]\n\t" \
    "ds_read_b64_tr_b16 v[64:65], v40 offset:57344\n\t" \
    "v_mfma_f32_16x16x32_bf16 v[148:151], v[72:75], a[116:119], v[148:151]\n\t" \
    "ds_read_b64_tr_b16 v[66:67], v40 offset:58368\n\t" \
    "s_waitcnt lgkmcnt(10)\n\t" \
    "v_mfma_f32_16x16x32_bf16 v[152:155], v[76:79], a[112:115], v[152:155]\n\t" \
    "ds_read_b64_tr_b16 v[68:69], v41 offset:57344\n\t" \
    "v_mfma_f32_16x16x32_bf16 v[156:159], v[76:79], a[116:119], v[156:159]\n\t" \
    "ds_read_b64_tr_b16 v[70:71], v41 offset:58368\n\t" \
    "buffer_load_dwordx4 a[88:91], v25, s[8:11], s31 offen offset:2048 sc1\n\t" \
    "s_waitcnt lgkmcnt(10)\n\t" \
    "v_mfma_f32_16x16x32_bf16 v[160:163], v[80:83], a[112:115], v[160:163]\n\t" \
    "ds_read_b64_tr_b16 v[72:73], v42 offset:57344\n\t" \
    "v_mfma_f32_16x16x32_bf16 v[164:167], v[80:83], a[116:119], v[164:167]\n\t" \
    "ds_read_b64_tr_b16 v[74:75], v42 offset:58368\n\t" \
    "s_waitcnt lgkmcnt(10)\n\t" \
    "v_mfma_f32_16x16x32_bf16 v[168:171], v[84:87], a[112:115], v[168:171]\n\t" \
    "ds_read_b64_tr_b16 v[76:77], v43 offset:57344\n\t" \
    "v_mfma_f32_16x16x32_bf16 v[172:175], v[84:87], a[116:119], v[172:175]\n\t" \
    "ds_read_b64_tr_b16 v[78:79], v43 offset:58368\n\t" \
    "buffer_load_dwordx4 a[92:95], v25, s[8:11], s31 offen offset:2304 sc1\n\t" \
    "s_add_u32 s31, s31, 4096\n\t" \
    "s_add_u32 s32, s32, 65536\n\t" \
    "s_waitcnt lgkmcnt(10)\n\t" \
    "v_mfma_f32_16x16x32_bf16 v[176:179], v[88:91], a[112:115], v[176:179]\n\t" \
    "ds_read_b64_tr_b16 v[80:81], v44 offset:57344\n\t" \
    "v_mfma_f32_16x16x32_bf16 v[180:183], v[88:91], a[116:119], v[180:183]\n\t" \
    "ds_read_b64_tr_b16 v[82:83], v44 offset:58368\n\t" \
    "s_waitcnt lgkmcnt(10)\n\t" \
    "v_mfma_f32_16x16x32_bf16 v[184:187], v[92:95], a[112:115], v[184:187]\n\t" \
    "ds_read_b64_tr_b16 v[84:85], v45 offset:57344\n\t" \
    "v_mfma_f32_16x16x32_bf16 v[188:191], v[92:95], a[116:119], v[188:191]\n\t" \
    "ds_read_b64_tr_b16 v[86:87], v45 offset:58368\n\t" \
    "s_waitcnt vmcnt(44)\n\t" \
    "s_barrier\n\t" \
    "s_waitcnt lgkmcnt(10)\n\t" \
    "v_mfma_f32_16x16x32_bf16 v[128:131], v[64:67], a[120:123], v[128:131]\n\t" \
    "ds_read_b64_tr_b16 v[88:89], v46 offset:57344\n\t" \
    "s_add_u32 m0, s33, 98304\n\t" \
    "v_mfma_f32_16x16x32_bf16 v[132:135], v[64:67], a[124:127], v[132:135]\n\t" \
    "ds_read_b64_tr_b16 v[90:91], v46 offset:58368\n\t" \
    "buffer_load_dwordx4 v26, s[12:15], s32 offen lds\n\t" \
    "s_waitcnt lgkmcnt(10)\n\t" \
    "v_mfma_f32_16x16x32_bf16 v[136:139], v[68:71], a[120:123], v[136:139]\n\t" \
    "ds_read_b64_tr_b16 v[92:93], v47 offset:57344\n\t" \
    "v_mfma_f32_16x16x32_bf16 v[140:143], v[68:71], a[124:127], v[140:143]\n\t" \
    "ds_read_b64_tr_b16 v[94:95], v47 offset:58368\n\t" \
    "s_waitcnt lgkmcnt(10)\n\t" \
    "v_mfma_f32_16x16x32_bf16 v[144:147], v[72:75], a[120:123], v[144:147]\n\t" \
    "ds_read_b64_tr_b16 v[64:65], v32 offset:0\n\t" \
    "buffer_load_dwordx4 v27, s[12:15], s32 offen offset:1024 lds\n\t" \
    "v_mfma_f32_16x16x32_bf16 v[148:151], v[72:75], a[124:127], v[148:151]\n\t" \
    "ds_read_b64_tr_b16 v[66:67], v32 offset:1024\n\t" \
    "s_waitcnt lgkmcnt(10)\n\t" \
    "v_mfma_f32_16x16x32_bf16 v[152:155], v[76:79], a[120:123], v[152:155]\n\t" \
    "ds_read_b64_tr_b16 v[68:69], v33 offset:0\n\t" \
    "v_mfma_f32_16x16x32_bf16 v[156:159], v[76:79], a[124:127], v[156:159]\n\t" \
    "ds_read_b64_tr_b16 v[70:71], v33 offset:1024\n\t" \
    "s_waitcnt lgkmcnt(10)\n\t" \
    "v_mfma_f32_16x16x32_bf16 v[160:163], v[80:83], a[120:123], v[160:163]\n\t" \
    "ds_read_b64_tr_b16 v[72:73], v34 offset:0\n\t" \
    "buffer_load_dwordx4 v28, s[12:15], s32 offen offset:2048 lds\n\t" \
    "v_mfma_f32_16x16x32_bf16 v[164:167], v[80:83], a[124:127], v[164:167]\n\t" \
    "ds_read_b64_tr_b16 v[74:75], v34 offset:1024\n\t" \
    "s_waitcnt lgkmcnt(10)\n\t" \
    "v_mfma_f32_16x16x32_bf16 v[168:171], v[84:87], a[120:123], v[168:171]\n\t" \
    "ds_read_b64_tr_b16 v[76:77], v35 offset:0\n\t" \
    "v_mfma_f32_16x16x32_bf16 v[172:175], v[84:87], a[124:127], v[172:175]\n\t" \
    "ds_read_b64_tr_b16 v[78:79], v35 offset:1024\n\t" \
    "s_waitcnt lgkmcnt(10)\n\t" \
    "v_mfma_f32_16x16x32_bf16 v[176:179], v[88:91], a[120:123], v[176:179]\n\t" \
    "ds_read_b64_tr_b16 v[80:81], v36 offset:0\n\t" \
    "buffer_load_dwordx4 v29, s[12:15], s32 offen offset:3072 lds\n\t" \
    "v_mfma_f32_16x16x32_bf16 v[180:183], v[88:91], a[124:127], v[180:183]\n\t" \
    "ds_read_b64_tr_b16 v[82:83], v36 offset:1024\n\t" \
    "s_waitcnt lgkmcnt(10)\n\t" \
    "v_mfma_f32_16x16x32_bf16 v[184:187], v[92:95], a[120:123], v[184:187]\n\t" \
    "ds_read_b64_tr_b16 v[84:85], v37 offset:0\n\t" \
    "v_mfma_f32_16x16x32_bf16 v[188:191], v[92:95], a[124:127], v[188:191]\n\t" \
    "ds_read_b64_tr_b16 v[86:87], v37 offset:1024\n\t" \
    "s_waitcnt vmcnt(47) lgkmcnt(10)\n\t" \
    "v_mfma_f32_16x16x32_bf16 v[128:131], v[64:67], a[0:3], v[128:131]\n\t" \
    "ds_read_b64_tr_b16 v[88:89], v38 offset:0\n\t" \
    "buffer_load_dwordx4 a[96:99], v25, s[8:11], s31 offen offset:0 sc1\n\t" \
    "s_waitcnt vmcnt(47)\n\t" \
    "v_mfma_f32_16x16x32_bf16 v[132:135], v[64:67], a[4:7], v[132:135]\n\t" \
    "ds_read_b64_tr_b16 v[90:91], v38 offset:1024\n\t" \
    "s_waitcnt lgkmcnt(10)\n\t" \
    "v_mfma_f32_16x16x32_bf16 v[136:139], v[68:71], a[0:3], v[136:139]\n\t" \
    "ds_read_b64_tr_b16 v[92:93], v39 offset:0\n\t" \
    "v_mfma_f32_16x16x32_bf16 v[140:143], v[68:71], a[4:7], v[140:143]\n\t" \
    "ds_read_b64_tr_b16 v[94:95], v39 offset:1024\n\t" \
    "buffer_load_dwordx4 a[100:103], v25, s[8:11], s31 offen offset:256 sc1\n\t" \
    "s_waitcnt lgkmcnt(10)\n\t" \
    "v_mfma_f32_16x16x32_bf16 v[144:147], v[72:75], a[0:3], v[144:147]\n\t" \
    "ds_read_b64_tr_b16 v[64:65], v32 offset:8192\n\t" \
    "v_mfma_f32_16x16x32_bf16 v[148:151], v[72:75], a[4:7], v[148:151]\n\t" \
    "ds_read_b64_tr_b16 v[66:67], v32 offset:9216\n\t" \
    "s_waitcnt lgkmcnt(10)\n\t" \
    "v_mfma_f32_16x16x32_bf16 v[152:155], v[76:79], a[0:3], v[152:155]\n\t" \
    "ds_read_b64_tr_b16 v[68:69], v33 offset:8192\n\t" \
    "v_mfma_f32_16x16x32_bf16 v[156:159], v[76:79], a[4:7], v[156:159]\n\t" \
    "ds_read_b64_tr_b16 v[70:71], v33 offset:9216\n\t" \
    "buffer_load_dwordx4 a[104:107], v25, s[8:11], s31 offen offset:2048 sc1\n\t" \
    "s_waitcnt lgkmcnt(10)\n\t" \
    "v_mfma_f32_16x16x32_bf16 v[160:163], v[80:83], a[0:3], v[160:163]\n\t" \
    "ds_read_b64_tr_b16 v[72:73], v34 offset:8192\n\t" \
    "v_mfma_f32_16x16x32_bf16 v[164:167], v[80:83], a[4:7], v[164:167]\n\t" \
    "ds_read_b64_tr_b16 v[74:75], v34 offset:9216\n\t" \
    "s_waitcnt lgkmcnt(10)\n\t" \
    "v_mfma_f32_16x16x32_bf16 v[168:171], v[84:87], a[0:3], v[168:171]\n\t" \
    "ds_read_b64_tr_b16 v[76:77], v35 offset:8192\n\t" \
    "v_mfma_f32_16x16x32_bf16 v[172:175], v[84:87], a[4:7], v[172:175]\n\t" \
    "ds_read_b64_tr_b16 v[78:79], v35 offset:9216\n\t" \
    "buffer_load_dwordx4 a[108:111], v25, s[8:11], s31 offen offset:2304 sc1\n\t" \
    "s_add_u32 s31, s31, 4096\n\t" \
    "s_add_u32 s32, s32, 65536\n\t" \
    "s_waitcnt lgkmcnt(10)\n\t" \
    "v_mfma_f32_16x16x32_bf16 v[176:179], v[88:91], a[0:3], v[176:179]\n\t" \
    "ds_read_b64_tr_b16 v[80:81], v36 offset:8192\n\t" \
    "v_mfma_f32_16x16x32_bf16 v[180:183], v[88:91], a[4:7], v[180:183]\n\t" \
    "ds_read_b64_tr_b16 v[82:83], v36 offset:9216\n\t" \
    "s_waitcnt lgkmcnt(10)\n\t" \
    "v_mfma_f32_16x16x32_bf16 v[184:187], v[92:95], a[0:3], v[184:187]\n\t" \
    "ds_read_b64_tr_b16 v[84:85], v37 offset:8192\n\t" \
    "v_mfma_f32_16x16x32_bf16 v[188:191], v[92:95], a[4:7], v[188:191]\n\t" \
    "ds_read_b64_tr_b16 v[86:87], v37 offset:9216\n\t" \
    "s_waitcnt vmcnt(44)\n\t" \
    "s_barrier\n\t" \
    "s_waitcnt lgkmcnt(10)\n\t" \
    "v_mfma_f32_16x16x32_bf16 v[128:131], v[64:67], a[8:11], v[128:131]\n\t" \
    "ds_read_b64_tr_b16 v[88:89], v38 offset:8192\n\t" \
    "s_add_u32 m0, s33, 114688\n\t" \
    "v_mfma_f32_16x16x32_bf16 v[132:135], v[64:67], a[12:15], v[132:135]\n\t" \
    "ds_read_b64_tr_b16 v[90:91], v38 offset:9216\n\t" \
    "buffer_load_dwordx4 v26, s[12:15], s32 offen lds\n\t" \
    "s_waitcnt lgkmcnt(10)\n\t" \
    "v_mfma_f32_16x16x32_bf16 v[136:139], v[68:71], a[8:11], v[136:139]\n\t" \
    "ds_read_b64_tr_b16 v[92:93], v39 offset:8192\n\t" \
    "v_mfma_f32_16x16x32_bf16 v[140:143], v[68:71], a[12:15], v[140:143]\n\t" \
    "ds_read_b64_tr_b16 v[94:95], v39 offset:9216\n\t" \
    "s_waitcnt lgkmcnt(10)\n\t" \
    "v_mfma_f32_16x16x32_bf16 v[144:147], v[72:75], a[8:11], v[144:147]\n\t" \
    "ds_read_b64_tr_b16 v[64:65], v32 offset:16384\n\t" \
    "buffer_load_dwordx4 v27, s[12:15], s32 offen offset:1024 lds\n\t" \
    "v_mfma_f32_16x16x32_bf16 v[148:151], v[72:75], a[12:15], v[148:151]\n\t" \
    "ds_read_b64_tr_b16 v[66:67], v32 offset:17408\n\t" \
    "s_waitcnt lgkmcnt(10)\n\t" \
    "v_mfma_f32_16x16x32_bf16 v[152:155], v[76:79], a[8:11], v[152:155]\n\t" \
    "ds_read_b64_tr_b16 v[68:69], v33 offset:16384\n\t" \
    "v_mfma_f32_16x16x32_bf16 v[156:159], v[76:79], a[12:15], v[156:159]\n\t" \
    "ds_read_b64_tr_b16 v[70:71], v33 offset:17408\n\t" \
    "s_waitcnt lgkmcnt(10)\n\t" \
    "v_mfma_f32_16x16x32_bf16 v[160:163], v[80:83], a[8:11], v[160:163]\n\t" \
    "ds_read_b64_tr_b16 v[72:73], v34 offset:16384\n\t" \
    "buffer_load_dwordx4 v28, s[12:15], s32 offen offset:2048 lds\n\t" \
    "v_mfma_f32_16x16x32_bf16 v[164:167], v[80:83], a[12:15], v[164:167]\n\t" \
    "ds_read_b64_tr_b16 v[74:75], v34 offset:17408\n\t" \
    "s_waitcnt lgkmcnt(10)\n\t" \
    "v_mfma_f32_16x16x32_bf16 v[168:171], v[84:87], a[8:11], v[168:171]\n\t" \
    "ds_read_b64_tr_b16 v[76:77], v35 offset:16384\n\t" \
    "v_mfma_f32_16x16x32_bf16 v[172:175], v[84:87], a[12:15], v[172:175]\n\t" \
    "ds_read_b64_tr_b16 v[78:79], v35 offset:17408\n\t" \
    "s_waitcnt lgkmcnt(10)\n\t" \
    "v_mfma_f32_16x16x32_bf16 v[176:179], v[88:91], a[8:11], v[176:179]\n\t" \
    "ds_read_b64_tr_b16 v[80:81], v36 offset:16384\n\t" \
    "buffer_load_dwordx4 v29, s[12:15], s32 offen offset:3072 lds\n\t" \
    "v_mfma_f32_16x16x32_bf16 v[180:183], v[88:91], a[12:15], v[180:183]\n\t" \
    "ds_read_b64_tr_b16 v[82:83], v36 offset:17408\n\t" \
    "s_waitcnt lgkmcnt(10)\n\t" \
    "v_mfma_f32_16x16x32_bf16 v[184:187], v[92:95], a[8:11], v[184:187]\n\t" \
    "ds_read_b64_tr_b16 v[84:85], v37 offset:16384\n\t" \
    "v_mfma_f32_16x16x32_bf16 v[188:191], v[92:95], a[12:15], v[188:191]\n\t" \
    "ds_read_b64_tr_b16 v[86:87], v37 offset:17408\n\t" \
    "s_waitcnt vmcnt(47) lgkmcnt(10)\n\t" \
    "v_mfma_f32_16x16x32_bf16 v[128:131], v[64:67], a[16:19], v[128:131]\n\t" \
    "ds_read_b64_tr_b16 v[88:89], v38 offset:16384\n\t" \
    "buffer_load_dwordx4 a[112:115], v25, s[8:11], s31 offen offset:0 sc1\n\t" \
    "s_waitcnt vmcnt(47)\n\t" \
    "v_mfma_f32_16x16x32_bf16 v[132:135], v[64:67], a[20:23], v[132:135]\n\t" \
    "ds_read_b64_tr_b16 v[90:91], v38 offset:17408\n\t" \
    "s_waitcnt lgkmcnt(10)\n\t" \
    "v_mfma_f32_16x16x32_bf16 v[136:139], v[68:71], a[16:19], v[136:139]\n\t" \
    "ds_read_b64_tr_b16 v[92:93], v39 offset:16384\n\t" \
    "v_mfma_f32_16x16x32_bf16 v[140:143], v[68:71], a[20:23], v[140:143]\n\t" \
    "ds_read_b64_tr_b16 v[94:95], v39 offset:17408\n\t" \
    "buffer_load_dwordx4 a[116:119], v25, s[8:11], s31 offen offset:256 sc1\n\t" \
    "s_waitcnt lgkmcnt(10)\n\t" \
    "v_mfma_f32_16x16x32_bf16 v[144:147], v[72:75], a[16:19], v[144:147]\n\t" \
    "ds_read_b64_tr_b16 v[64:65], v32 offset:24576\n\t" \
    "v_mfma_f32_16x16x32_bf16 v[148:151], v[72:75], a[20:23], v[148:151]\n\t" \
    "ds_read_b64_tr_b16 v[66:67], v32 offset:25600\n\t" \
    "s_waitcnt lgkmcnt(10)\n\t" \
    "v_mfma_f32_16x16x32_bf16 v[152:155], v[76:79], a[16:19], v[152:155]\n\t" \
    "ds_read_b64_tr_b16 v[68:69], v33 offset:24576\n\t" \
    "v_mfma_f32_16x16x32_bf16 v[156:159], v[76:79], a[20:23], v[156:159]\n\t" \
    "ds_read_b64_tr_b16 v[70:71], v33 offset:25600\n\t" \
    "buffer_load_dwordx4 a[120:123], v25, s[8:11], s31 offen offset:2048 sc1\n\t" \
    "s_waitcnt lgkmcnt(10)\n\t" \
    "v_mfma_f32_16x16x32_bf16 v[160:163], v[80:83], a[16:19], v[160:163]\n\t" \
    "ds_read_b64_tr_b16 v[72:73], v34 offset:24576\n\t" \
    "v_mfma_f32_16x16x32_bf16 v[164:167], v[80:83], a[20:23], v[164:167]\n\t" \
    "ds_read_b64_tr_b16 v[74:75], v34 offset:25600\n\t" \
    "s_waitcnt lgkmcnt(10)\n\t" \
    "v_mfma_f32_16x16x32_bf16 v[168:171], v[84:87], a[16:19], v[168:171]\n\t" \
    "ds_read_b64_tr_b16 v[76:77], v35 offset:24576\n\t" \
    "v_mfma_f32_16x16x32_bf16 v[172:175], v[84:87], a[20:23], v[172:175]\n\t" \
    "ds_read_b64_tr_b16 v[78:79], v35 offset:25600\n\t" \
    "buffer_load_dwordx4 a[124:127], v25, s[8:11], s31 offen offset:2304 sc1\n\t" \
    "s_add_u32 s31, s31, 4096\n\t" \
    "s_add_u32 s32, s32, 65536\n\t" \
    "s_waitcnt lgkmcnt(10)\n\t" \
    "v_mfma_f32_16x16x32_bf16 v[176:179], v[88:91], a[16:19], v[176:179]\n\t" \
    "ds_read_b64_tr_b16 v[80:81], v36 offset:24576\n\t" \
    "v_mfma_f32_16x16x32_bf16 v[180:183], v[88:91], a[20:23], v[180:183]\n\t" \
    "ds_read_b64_tr_b16 v[82:83], v36 offset:25600\n\t" \
    "s_waitcnt lgkmcnt(10)\n\t" \
    "v_mfma_f32_16x16x32_bf16 v[184:187], v[92:95], a[16:19], v[184:187]\n\t" \
    "ds_read_b64_tr_b16 v[84:85], v37 offset:24576\n\t" \
    "v_mfma_f32_16x16x32_bf16 v[188:191], v[92:95], a[20:23], v[188:191]\n\t" \
    "ds_read_b64_tr_b16 v[86:87], v37 offset:25600\n\t" \
    "s_waitcnt vmcnt(44)\n\t" \
    "s_barrier\n\t" \
    "s_waitcnt lgkmcnt(10)\n\t" \
    "v_mfma_f32_16x16x32_bf16 v[128:131], v[64:67], a[24:27], v[128:131]\n\t" \
    "ds_read_b64_tr_b16 v[88:89], v38 offset:24576\n\t" \
    "s_mul_i32 s28, %[wv], 44032\n\t" \
    "s_add_u32 s29, s28, 0\n\t" \
    "s_mul_i32 s28, %[wv], 3072\n\t" \
    "s_add_u32 m0, s28, 0\n\t" \
    "v_mfma_f32_16x16x32_bf16 v[132:135], v[64:67], a[28:31], v[132:135]\n\t" \
    "ds_read_b64_tr_b16 v[90:91], v38 offset:25600\n\t" \
    "buffer_load_dwordx4 v18, s[16:19], s29 offen offset:0 lds\n\t" \
    "s_waitcnt lgkmcnt(10)\n\t" \
    "v_mfma_f32_16x16x32_bf16 v[136:139], v[68:71], a[24:27], v[136:139]\n\t" \
    "ds_read_b64_tr_b16 v[92:93], v39 offset:24576\n\t" \
    "v_mfma_f32_16x16x32_bf16 v[140:143], v[68:71], a[28:31], v[140:143]\n\t" \
    "ds_read_b64_tr_b16 v[94:95], v39 offset:25600\n\t" \
    "s_waitcnt lgkmcnt(10)\n\t" \
    "v_mfma_f32_16x16x32_bf16 v[144:147], v[72:75], a[24:27], v[144:147]\n\t" \
    "ds_read_b64_tr_b16 v[64:65], v32 offset:32768\n\t" \
    "v_mfma_f32_16x16x32_bf16 v[148:151], v[72:75], a[28:31], v[148:151]\n\t" \
    "ds_read_b64_tr_b16 v[66:67], v32 offset:33792\n\t" \
    "s_waitcnt lgkmcnt(10)\n\t" \
    "v_mfma_f32_16x16x32_bf16 v[152:155], v[76:79], a[24:27], v[152:155]\n\t" \
    "ds_read_b64_tr_b16 v[68:69], v33 offset:32768\n\t" \
    "v_mfma_f32_16x16x32_bf16 v[156:159], v[76:79], a[28:31], v[156:159]\n\t" \
    "ds_read_b64_tr_b16 v[70:71], v33 offset:33792\n\t" \
    "s_waitcnt lgkmcnt(10)\n\t" \
    "v_mfma_f32_16x16x32_bf16 v[160:163], v[80:83], a[24:27], v[160:163]\n\t" \
    "ds_read_b64_tr_b16 v[72:73], v34 offset:32768\n\t" \
    "v_mfma_f32_16x16x32_bf16 v[164:167], v[80:83], a[28:31], v[164:167]\n\t" \
    "ds_read_b64_tr_b16 v[74:75], v34 offset:33792\n\t" \
    "s_waitcnt lgkmcnt(10)\n\t" \
    "v_mfma_f32_16x16x32_bf16 v[168:171], v[84:87], a[24:27], v[168:171]\n\t" \
    "ds_read_b64_tr_b16 v[76:77], v35 offset:32768\n\t" \
    "v_mfma_f32_16x16x32_bf16 v[172:175], v[84:87], a[28:31], v[172:175]\n\t" \
    "ds_read_b64_tr_b16 v[78:79], v35 offset:33792\n\t" \
    "buffer_load_dwordx4 v19, s[16:19], s29 offen offset:1024 lds\n\t" \
    "s_waitcnt lgkmcnt(10)\n\t" \
    "v_mfma_f32_16x16x32_bf16 v[176:179], v[88:91], a[24:27], v[176:179]\n\t" \
    "ds_read_b64_tr_b16 v[80:81], v36 offset:32768\n\t" \
    "v_mfma_f32_16x16x32_bf16 v[180:183], v[88:91], a[28:31], v[180:183]\n\t" \
    "ds_read_b64_tr_b16 v[82:83], v36 offset:33792\n\t" \
    "s_waitcnt lgkmcnt(10)\n\t" \
    "v_mfma_f32_16x16x32_bf16 v[184:187], v[92:95], a[24:27], v[184:187]\n\t" \
    "ds_read_b64_tr_b16 v[84:85], v37 offset:32768\n\t" \
    "v_mfma_f32_16x16x32_bf16 v[188:191], v[92:95], a[28:31], v[188:191]\n\t" \
    "ds_read_b64_tr_b16 v[86:87], v37 offset:33792\n\t" \
    "s_waitcnt vmcnt(45) lgkmcnt(10)\n\t" \
    "v_mfma_f32_16x16x32_bf16 v[128:131], v[64:67], a[32:35], v[128:131]\n\t" \
    "ds_read_b64_tr_b16 v[88:89], v38 offset:32768\n\t" \
    "s_waitcnt vmcnt(44)\n\t" \
    "v_mfma_f32_16x16x32_bf16 v[132:135], v[64:67], a[36:39], v[132:135]\n\t" \
    "ds_read_b64_tr_b16 v[90:91], v38 offset:33792\n\t" \
    "s_waitcnt lgkmcnt(10)\n\t" \
    "v_mfma_f32_16x16x32_bf16 v[136:139], v[68:71], a[32:35], v[136:139]\n\t" \
    "ds_read_b64_tr_b16 v[92:93], v39 offset:32768\n\t" \
    "v_mfma_f32_16x16x32_bf16 v[140:143], v[68:71], a[36:39], v[140:143]\n\t" \
    "ds_read_b64_tr_b16 v[94:95], v39 offset:33792\n\t" \
    "s_waitcnt lgkmcnt(10)\n\t" \
    "v_mfma_f32_16x16x32_bf16 v[144:147], v[72:75], a[32:35], v[144:147]\n\t" \
    "ds_read_b64_tr_b16 v[64:65], v32 offset:40960\n\t" \
    "v_mfma_f32_16x16x32_bf16 v[148:151], v[72:75], a[36:39], v[148:151]\n\t" \
    "ds_read_b64_tr_b16 v[66:67], v32 offset:41984\n\t" \
    "buffer_load_dwordx4 v20, s[16:19], s29 offen offset:2048 lds\n\t" \
    "s_waitcnt lgkmcnt(10)\n\t" \
    "v_mfma_f32_16x16x32_bf16 v[152:155], v[76:79], a[32:35], v[152:155]\n\t" \
    "ds_read_b64_tr_b16 v[68:69], v33 offset:40960\n\t" \
    "v_mfma_f32_16x16x32_bf16 v[156:159], v[76:79], a[36:39], v[156:159]\n\t" \
    "ds_read_b64_tr_b16 v[70:71], v33 offset:41984\n\t" \
    "s_waitcnt lgkmcnt(10)\n\t" \
    "v_mfma_f32_16x16x32_bf16 v[160:163], v[80:83], a[32:35], v[160:163]\n\t" \
    "ds_read_b64_tr_b16 v[72:73], v34 offset:40960\n\t" \
    "v_mfma_f32_16x16x32_bf16 v[164:167], v[80:83], a[36:39], v[164:167]\n\t" \
    "ds_read_b64_tr_b16 v[74:75], v34 offset:41984\n\t" \
    "s_waitcnt lgkmcnt(10)\n\t" \
    "v_mfma_f32_16x16x32_bf16 v[168:171], v[84:87], a[32:35], v[168:171]\n\t" \
    "ds_read_b64_tr_b16 v[76:77], v35 offset:40960\n\t" \
    "v_mfma_f32_16x16x32_bf16 v[172:175], v[84:87], a[36:39], v[172:175]\n\t" \
    "ds_read_b64_tr_b16 v[78:79], v35 offset:41984\n\t" \
    "s_waitcnt lgkmcnt(10)\n\t" \
    "v_mfma_f32_16x16x32_bf16 v[176:179], v[88:91], a[32:35], v[176:179]\n\t" \
    "ds_read_b64_tr_b16 v[80:81], v36 offset:40960\n\t" \
    "v_mfma_f32_16x16x32_bf16 v[180:183], v[88:91], a[36:39], v[180:183]\n\t" \
    "ds_read_b64_tr_b16 v[82:83], v36 offset:41984\n\t" \
    "s_waitcnt lgkmcnt(10)\n\t" \
    "v_mfma_f32_16x16x32_bf16 v[184:187], v[92:95], a[32:35], v[184:187]\n\t" \
    "ds_read_b64_tr_b16 v[84:85], v37 offset:40960\n\t" \
    "v_mfma_f32_16x16x32_bf16 v[188:191], v[92:95], a[36:39], v[188:191]\n\t" \
    "ds_read_b64_tr_b16 v[86:87], v37 offset:41984\n\t" \
    "s_waitcnt vmcnt(39)\n\t" \
    "s_barrier\n\t" \
    "s_waitcnt lgkmcnt(10)\n\t" \
    "v_mfma_f32_16x16x32_bf16 v[128:131], v[64:67], a[40:43], v[128:131]\n\t" \
    "ds_read_b64_tr_b16 v[88:89], v38 offset:40960\n\t" \
    "s_mul_i32 s28, %[wv], 44032\n\t" \
    "s_add_u32 s29, s28, 176128\n\t" \
    "s_mul_i32 s28, %[wv], 3072\n\t" \
    "s_add_u32 m0, s28, 12288\n\t" \
    "v_mfma_f32_16x16x32_bf16 v[132:135], v[64:67], a[44:47], v[132:135]\n\t" \
    "ds_read_b64_tr_b16 v[90:91], v38 offset:41984\n\t" \
    "buffer_load_dwordx4 v18, s[16:19], s29 offen offset:0 lds\n\t" \
    "s_waitcnt lgkmcnt(10)\n\t" \
    "v_mfma_f32_16x16x32_bf16 v[136:139], v[68:71], a[40:43], v[136:139]\n\t" \
    "ds_read_b64_tr_b16 v[92:93], v39 offset:40960\n\t" \
    "v_mfma_f32_16x16x32_bf16 v[140:143], v[68:71], a[44:47], v[140:143]\n\t" \
    "ds_read_b64_tr_b16 v[94:95], v39 offset:41984\n\t" \
    "s_waitcnt lgkmcnt(10)\n\t" \
    "v_mfma_f32_16x16x32_bf16 v[144:147], v[72:75], a[40:43], v[144:147]\n\t" \
    "ds_read_b64_tr_b16 v[64:65], v32 offset:49152\n\t" \
    "v_mfma_f32_16x16x32_bf16 v[148:151], v[72:75], a[44:47], v[148:151]\n\t" \
    "ds_read_b64_tr_b16 v[66:67], v32 offset:50176\n\t" \
    "s_waitcnt lgkmcnt(10)\n\t" \
    "v_mfma_f32_16x16x32_bf16 v[152:155], v[76:79], a[40:43], v[152:155]\n\t" \
    "ds_read_b64_tr_b16 v[68:69], v33 offset:49152\n\t" \
    "v_mfma_f32_16x16x32_bf16 v[156:159], v[76:79], a[44:47], v[156:159]\n\t" \
    "ds_read_b64_tr_b16 v[70:71], v33 offset:50176\n\t" \
    "s_waitcnt lgkmcnt(10)\n\t" \
    "v_mfma_f32_16x16x32_bf16 v[160:163], v[80:83], a[40:43], v[160:163]\n\t" \
    "ds_read_b64_tr_b16 v[72:73], v34 offset:49152\n\t" \
    "v_mfma_f32_16x16x32_bf16 v[164:167], v[80:83], a[44:47], v[164:167]\n\t" \
    "ds_read_b64_tr_b16 v[74:75], v34 offset:50176\n\t" \
    "s_waitcnt lgkmcnt(10)\n\t" \
    "v_mfma_f32_16x16x32_bf16 v[168:171], v[84:87], a[40:43], v[168:171]\n\t" \
    "ds_read_b64_tr_b16 v[76:77], v35 offset:49152\n\t" \
    "v_mfma_f32_16x16x32_bf16 v[172:175], v[84:87], a[44:47], v[172:175]\n\t" \
    "ds_read_b64_tr_b16 v[78:79], v35 offset:50176\n\t" \
    "buffer_load_dwordx4 v19, s[16:19], s29 offen offset:1024 lds\n\t" \
    "s_waitcnt lgkmcnt(10)\n\t" \
    "v_mfma_f32_16x16x32_bf16 v[176:179], v[88:91], a[40:43], v[176:179]\n\t" \
    "ds_read_b64_tr_b16 v[80:81], v36 offset:49152\n\t" \
    "v_mfma_f32_16x16x32_bf16 v[180:183], v[88:91], a[44:47], v[180:183]\n\t" \
    "ds_read_b64_tr_b16 v[82:83], v36 offset:50176\n\t" \
    "s_waitcnt lgkmcnt(10)\n\t" \
    "v_mfma_f32_16x16x32_bf16 v[184:187], v[92:95], a[40:43], v[184:187]\n\t" \
    "ds_read_b64_tr_b16 v[84:85], v37 offset:49152\n\t" \
    "v_mfma_f32_16x16x32_bf16 v[188:191], v[92:95], a[44:47], v[188:191]\n\t" \
    "ds_read_b64_tr_b16 v[86:87], v37 offset:50176\n\t" \
    "s_waitcnt vmcnt(40) lgkmcnt(10)\n\t" \
    "v_mfma_f32_16x16x32_bf16 v[128:131], v[64:67], a[48:51], v[128:131]\n\t" \
    "ds_read_b64_tr_b16 v[88:89], v38 offset:49152\n\t" \
    "s_waitcnt vmcnt(39)\n\t" \
    "v_mfma_f32_16x16x32_bf16 v[132:135], v[64:67], a[52:55], v[132:135]\n\t" \
    "ds_read_b64_tr_b16 v[90:91], v38 offset:50176\n\t" \
    "s_waitcnt lgkmcnt(10)\n\t" \
    "v_mfma_f32_16x16x32_bf16 v[136:139], v[68:71], a[48:51], v[136:139]\n\t" \
    "ds_read_b64_tr_b16 v[92:93], v39 offset:49152\n\t" \
    "v_mfma_f32_16x16x32_bf16 v[140:143], v[68:71], a[52:55], v[140:143]\n\t" \
    "ds_read_b64_tr_b16 v[94:95], v39 offset:50176\n\t" \
    "s_waitcnt lgkmcnt(10)\n\t" \
    "v_mfma_f32_16x16x32_bf16 v[144:147], v[72:75], a[48:51], v[144:147]\n\t" \
    "ds_read_b64_tr_b16 v[64:65], v32 offset:57344\n\t" \
    "v_mfma_f32_16x16x32_bf16 v[148:151], v[72:75], a[52:55], v[148:151]\n\t" \
    "ds_read_b64_tr_b16 v[66:67], v32 offset:58368\n\t" \
    "buffer_load_dwordx4 v20, s[16:19], s29 offen offset:2048 lds\n\t" \
    "s_waitcnt lgkmcnt(10)\n\t" \
    "v_mfma_f32_16x16x32_bf16 v[152:155], v[76:79], a[48:51], v[152:155]\n\t" \
    "ds_read_b64_tr_b16 v[68:69], v33 offset:57344\n\t" \
    "v_mfma_f32_16x16x32_bf16 v[156:159], v[76:79], a[52:55], v[156:159]\n\t" \
    "ds_read_b64_tr_b16 v[70:71], v33 offset:58368\n\t" \
    "s_waitcnt lgkmcnt(10)\n\t" \
    "v_mfma_f32_16x16x32_bf16 v[160:163], v[80:83], a[48:51], v[160:163]\n\t" \
    "ds_read_b64_tr_b16 v[72:73], v34 offset:57344\n\t" \
    "v_mfma_f32_16x16x32_bf16 v[164:167], v[80:83], a[52:55], v[164:167]\n\t" \
    "ds_read_b64_tr_b16 v[74:75], v34 offset:58368\n\t" \
    "s_waitcnt lgkmcnt(10)\n\t" \
    "v_mfma_f32_16x16x32_bf16 v[168:171], v[84:87], a[48:51], v[168:171]\n\t" \
    "ds_read_b64_tr_b16 v[76:77], v35 offset:57344\n\t" \
    "v_mfma_f32_16x16x32_bf16 v[172:175], v[84:87], a[52:55], v[172:175]\n\t" \
    "ds_read_b64_tr_b16 v[78:79], v35 offset:58368\n\t" \
    "s_waitcnt lgkmcnt(10)\n\t" \
    "v_mfma_f32_16x16x32_bf16 v[176:179], v[88:91], a[48:51], v[176:179]\n\t" \
    "ds_read_b64_tr_b16 v[80:81], v36 offset:57344\n\t" \
    "v_mfma_f32_16x16x32_bf16 v[180:183], v[88:91], a[52:55], v[180:183]\n\t" \
    "ds_read_b64_tr_b16 v[82:83], v36 offset:58368\n\t" \
    "s_waitcnt lgkmcnt(10)\n\t" \
    "v_mfma_f32_16x16x32_bf16 v[184:187], v[92:95], a[48:51], v[184:187]\n\t" \
    "ds_read_b64_tr_b16 v[84:85], v37 offset:57344\n\t" \
    "v_mfma_f32_16x16x32_bf16 v[188:191], v[92:95], a[52:55], v[188:191]\n\t" \
    "ds_read_b64_tr_b16 v[86:87], v37 offset:58368\n\t" \
    "s_waitcnt vmcnt(34)\n\t" \
    "s_barrier\n\t" \
    "s_waitcnt lgkmcnt(10)\n\t" \
    "v_mfma_f32_16x16x32_bf16 v[128:131], v[64:67], a[56:59], v[128:131]\n\t" \
    "ds_read_b64_tr_b16 v[88:89], v38 offset:57344\n\t" \
    "s_mul_i32 s28, %[wv], 44032\n\t" \
    "s_add_u32 s29, s28, 352256\n\t" \
    "s_mul_i32 s28, %[wv], 3072\n\t" \
    "s_add_u32 m0, s28, 24576\n\t" \
    "v_mfma_f32_16x16x32_bf16 v[132:135], v[64:67], a[60:63], v[132:135]\n\t" \
    "ds_read_b64_tr_b16 v[90:91], v38 offset:58368\n\t" \
    "buffer_load_dwordx4 v18, s[16:19], s29 offen offset:0 lds\n\t" \
    "s_waitcnt lgkmcnt(10)\n\t" \
    "v_mfma_f32_16x16x32_bf16 v[136:139], v[68:71], a[56:59], v[136:139]\n\t" \
    "ds_read_b64_tr_b16 v[92:93], v39 offset:57344\n\t" \
    "v_mfma_f32_16x16x32_bf16 v[140:143], v[68:71], a[60:63], v[140:143]\n\t" \
    "ds_read_b64_tr_b16 v[94:95], v39 offset:58368\n\t" \
    "s_waitcnt lgkmcnt(10)\n\t" \
    "v_mfma_f32_16x16x32_bf16 v[144:147], v[72:75], a[56:59], v[144:147]\n\t" \
    "ds_read_b64_tr_b16 v[64:65], v40 offset:0\n\t" \
    "v_mfma_f32_16x16x32_bf16 v[148:151], v[72:75], a[60:63], v[148:151]\n\t" \
    "ds_read_b64_tr_b16 v[66:67], v40 offset:1024\n\t" \
    "s_waitcnt lgkmcnt(10)\n\t" \
    "v_mfma_f32_16x16x32_bf16 v[152:155], v[76:79], a[56:59], v[152:155]\n\t" \
    "ds_read_b64_tr_b16 v[68:69], v41 offset:0\n\t" \
    "buffer_load_dwordx4 v19, s[16:19], s29 offen offset:1024 lds\n\t" \
    "v_mfma_f32_16x16x32_bf16 v[156:159], v[76:79], a[60:63], v[156:159]\n\t" \
    "ds_read_b64_tr_b16 v[70:71], v41 offset:1024\n\t" \
    "s_waitcnt lgkmcnt(10)\n\t" \
    "v_mfma_f32_16x16x32_bf16 v[160:163], v[80:83], a[56:59], v[160:163]\n\t" \
    "ds_read_b64_tr_b16 v[72:73], v42 offset:0\n\t" \
    "v_mfma_f32_16x16x32_bf16 v[164:167], v[80:83], a[60:63], v[164:167]\n\t" \
    "ds_read_b64_tr_b16 v[74:75], v42 offset:1024\n\t" \
    "s_waitcnt lgkmcnt(10)\n\t" \
    "v_mfma_f32_16x16x32_bf16 v[168:171], v[84:87], a[56:59], v[168:171]\n\t" \
    "ds_read_b64_tr_b16 v[76:77], v43 offset:0\n\t" \
    "v_mfma_f32_16x16x32_bf16 v[172:175], v[84:87], a[60:63], v[172:175]\n\t" \
    "ds_read_b64_tr_b16 v[78:79], v43 offset:1024\n\t" \
    "buffer_load_dwordx4 v20, s[16:19], s29 offen offset:2048 lds\n\t" \
    "s_waitcnt lgkmcnt(10)\n\t" \
    "v_mfma_f32_16x16x32_bf16 v[176:179], v[88:91], a[56:59], v[176:179]\n\t" \
    "ds_read_b64_tr_b16 v[80:81], v44 offset:0\n\t" \
    "v_mfma_f32_16x16x32_bf16 v[180:183], v[88:91], a[60:63], v[180:183]\n\t" \
    "ds_read_b64_tr_b16 v[82:83], v44 offset:1024\n\t" \
    "s_waitcnt lgkmcnt(10)\n\t" \
    "v_mfma_f32_16x16x32_bf16 v[184:187], v[92:95], a[56:59], v[184:187]\n\t" \
    "ds_read_b64_tr_b16 v[84:85], v45 offset:0\n\t" \
    "v_mfma_f32_16x16x32_bf16 v[188:191], v[92:95], a[60:63], v[188:191]\n\t" \
    "ds_read_b64_tr_b16 v[86:87], v45 offset:1024\n\t" \
    "s_mul_i32 s28, %[wv], 44032\n\t" \
    "s_add_u32 s29, s28, 528384\n\t" \
    "s_mul_i32 s28, %[wv], 3072\n\t" \
    "s_add_u32 m0, s28, 36864\n\t" \
    "s_waitcnt vmcnt(36) lgkmcnt(10)\n\t" \
    "v_mfma_f32_16x16x32_bf16 v[128:131], v[64:67], a[64:67], v[128:131]\n\t" \
    "ds_read_b64_tr_b16 v[88:89], v46 offset:0\n\t" \
    "buffer_load_dwordx4 v18, s[16:19], s29 offen offset:0 lds\n\t" \
    "s_waitcnt vmcnt(36)\n\t" \
    "v_mfma_f32_16x16x32_bf16 v[132:135], v[64:67], a[68:71], v[132:135]\n\t" \
    "ds_read_b64_tr_b16 v[90:91], v46 offset:1024\n\t" \
    "s_waitcnt lgkmcnt(10)\n\t" \
    "v_mfma_f32_16x16x32_bf16 v[136:139], v[68:71], a[64:67], v[136:139]\n\t" \
    "ds_read_b64_tr_b16 v[92:93], v47 offset:0\n\t" \
    "v_mfma_f32_16x16x32_bf16 v[140:143], v[68:71], a[68:71], v[140:143]\n\t" \
    "ds_read_b64_tr_b16 v[94:95], v47 offset:1024\n\t" \
    "s_waitcnt lgkmcnt(10)\n\t" \
    "v_mfma_f32_16x16x32_bf16 v[144:147], v[72:75], a[64:67], v[144:147]\n\t" \
    "ds_read_b64_tr_b16 v[64:65], v40 offset:8192\n\t" \
    "v_mfma_f32_16x16x32_bf16 v[148:151], v[72:75], a[68:71], v[148:151]\n\t" \
    "ds_read_b64_tr_b16 v[66:67], v40 offset:9216\n\t" \
    "buffer_load_dwordx4 v19, s[16:19], s29 offen offset:1024 lds\n\t" \
    "s_waitcnt lgkmcnt(10)\n\t" \
    "v_mfma_f32_16x16x32_bf16 v[152:155], v[76:79], a[64:67], v[152:155]\n\t" \
    "ds_read_b64_tr_b16 v[68:69], v41 offset:8192\n\t" \
    "v_mfma_f32_16x16x32_bf16 v[156:159], v[76:79], a[68:71], v[156:159]\n\t" \
    "ds_read_b64_tr_b16 v[70:71], v41 offset:9216\n\t" \
    "s_waitcnt lgkmcnt(10)\n\t" \
    "v_mfma_f32_16x16x32_bf16 v[160:163], v[80:83], a[64:67], v[160:163]\n\t" \
    "ds_read_b64_tr_b16 v[72:73], v42 offset:8192\n\t" \
    "v_mfma_f32_16x16x32_bf16 v[164:167], v[80:83], a[68:71], v[164:167]\n\t" \
    "ds_read_b64_tr_b16 v[74:75], v42 offset:9216\n\t" \
    "s_waitcnt lgkmcnt(10)\n\t" \
    "v_mfma_f32_16x16x32_bf16 v[168:171], v[84:87], a[64:67], v[168:171]\n\t" \
    "ds_read_b64_tr_b16 v[76:77], v43 offset:8192\n\t" \
    "buffer_load_dwordx4 v20, s[16:19], s29 offen offset:2048 lds\n\t" \
    "v_mfma_f32_16x16x32_bf16 v[172:175], v[84:87], a[68:71], v[172:175]\n\t" \
    "ds_read_b64_tr_b16 v[78:79], v43 offset:9216\n\t" \
    "s_waitcnt lgkmcnt(10)\n\t" \
    "v_mfma_f32_16x16x32_bf16 v[176:179], v[88:91], a[64:67], v[176:179]\n\t" \
    "ds_read_b64_tr_b16 v[80:81], v44 offset:8192\n\t" \
    "v_mfma_f32_16x16x32_bf16 v[180:183], v[88:91], a[68:71], v[180:183]\n\t" \
    "ds_read_b64_tr_b16 v[82:83], v44 offset:9216\n\t" \
    "s_waitcnt lgkmcnt(10)\n\t" \
    "v_mfma_f32_16x16x32_bf16 v[184:187], v[92:95], a[64:67], v[184:187]\n\t" \
    "ds_read_b64_tr_b16 v[84:85], v45 offset:8192\n\t" \
    "v_mfma_f32_16x16x32_bf16 v[188:191], v[92:95], a[68:71], v[188:191]\n\t" \
    "ds_read_b64_tr_b16 v[86:87], v45 offset:9216\n\t" \
    "s_waitcnt vmcnt(32)\n\t" \
    "s_barrier\n\t" \
    "s_waitcnt lgkmcnt(10)\n\t" \
    "v_mfma_f32_16x16x32_bf16 v[128:131], v[64:67], a[72:75], v[128:131]\n\t" \
    "ds_read_b64_tr_b16 v[88:89], v46 offset:8192\n\t" \
    "s_mul_i32 s28, %[wv], 44032\n\t" \
    "s_add_u32 s29, s28, 704512\n\t" \
    "s_mul_i32 s28, %[wv], 3072\n\t" \
    "s_add_u32 m0, s28, 49152\n\t" \
    "v_mfma_f32_16x16x32_bf16 v[132:135], v[64:67], a[76:79], v[132:135]\n\t" \
    "ds_read_b64_tr_b16 v[90:91], v46 offset:9216\n\t" \
    "buffer_load_dwordx4 v18, s[16:19], s29 offen offset:0 lds\n\t" \
    "s_waitcnt lgkmcnt(10)\n\t" \
    "v_mfma_f32_16x16x32_bf16 v[136:139], v[68:71], a[72:75], v[136:139]\n\t" \
    "ds_read_b64_tr_b16 v[92:93], v47 offset:8192\n\t" \
    "v_mfma_f32_16x16x32_bf16 v[140:143], v[68:71], a[76:79], v[140:143]\n\t" \
    "ds_read_b64_tr_b16 v[94:95], v47 offset:9216\n\t" \
    "s_waitcnt lgkmcnt(10)\n\t" \
    "v_mfma_f32_16x16x32_bf16 v[144:147], v[72:75], a[72:75], v[144:147]\n\t" \
    "ds_read_b64_tr_b16 v[64:65], v40 offset:16384\n\t" \
    "v_mfma_f32_16x16x32_bf16 v[148:151], v[72:75], a[76:79], v[148:151]\n\t" \
    "ds_read_b64_tr_b16 v[66:67], v40 offset:17408\n\t" \
    "s_waitcnt lgkmcnt(10)\n\t" \
    "v_mfma_f32_16x16x32_bf16 v[152:155], v[76:79], a[72:75], v[152:155]\n\t" \
    "ds_read_b64_tr_b16 v[68:69], v41 offset:16384\n\t" \
    "v_mfma_f32_16x16x32_bf16 v[156:159], v[76:79], a[76:79], v[156:159]\n\t" \
    "ds_read_b64_tr_b16 v[70:71], v41 offset:17408\n\t" \
    "s_waitcnt lgkmcnt(10)\n\t" \
    "v_mfma_f32_16x16x32_bf16 v[160:163], v[80:83], a[72:75], v[160:163]\n\t" \
    "ds_read_b64_tr_b16 v[72:73], v42 offset:16384\n\t" \
    "v_mfma_f32_16x16x32_bf16 v[164:167], v[80:83], a[76:79], v[164:167]\n\t" \
    "ds_read_b64_tr_b16 v[74:75], v42 offset:17408\n\t" \
    "s_waitcnt lgkmcnt(10)\n\t" \
    "v_mfma_f32_16x16x32_bf16 v[168:171], v[84:87], a[72:75], v[168:171]\n\t" \
    "ds_read_b64_tr_b16 v[76:77], v43 offset:16384\n\t" \
    "v_mfma_f32_16x16x32_bf16 v[172:175], v[84:87], a[76:79], v[172:175]\n\t" \
    "ds_read_b64_tr_b16 v[78:79], v43 offset:17408\n\t" \
    "buffer_load_dwordx4 v19, s[16:19], s29 offen offset:1024 lds\n\t" \
    "s_waitcnt lgkmcnt(10)\n\t" \
    "v_mfma_f32_16x16x32_bf16 v[176:179], v[88:91], a[72:75], v[176:179]\n\t" \
    "ds_read_b64_tr_b16 v[80:81], v44 offset:16384\n\t" \
    "v_mfma_f32_16x16x32_bf16 v[180:183], v[88:91], a[76:79], v[180:183]\n\t" \
    "ds_read_b64_tr_b16 v[82:83], v44 offset:17408\n\t" \
    "s_waitcnt lgkmcnt(10)\n\t" \
    "v_mfma_f32_16x16x32_bf16 v[184:187], v[92:95], a[72:75], v[184:187]\n\t" \
    "ds_read_b64_tr_b16 v[84:85], v45 offset:16384\n\t" \
    "v_mfma_f32_16x16x32_bf16 v[188:191], v[92:95], a[76:79], v[188:191]\n\t" \
    "ds_read_b64_tr_b16 v[86:87], v45 offset:17408\n\t" \
    "s_waitcnt vmcnt(33) lgkmcnt(10)\n\t" \
    "v_mfma_f32_16x16x32_bf16 v[128:131], v[64:67], a[80:83], v[128:131]\n\t" \
    "ds_read_b64_tr_b16 v[88:89], v46 offset:16384\n\t" \
    "s_waitcnt vmcnt(32)\n\t" \
    "v_mfma_f32_16x16x32_bf16 v[132:135], v[64:67], a[84:87], v[132:135]\n\t" \
    "ds_read_b64_tr_b16 v[90:91], v46 offset:17408\n\t" \
    "s_waitcnt lgkmcnt(10)\n\t" \
    "v_mfma_f32_16x16x32_bf16 v[136:139], v[68:71], a[80:83], v[136:139]\n\t" \
    "ds_read_b64_tr_b16 v[92:93], v47 offset:16384\n\t" \
    "v_mfma_f32_16x16x32_bf16 v[140:143], v[68:71], a[84:87], v[140:143]\n\t" \
    "ds_read_b64_tr_b16 v[94:95], v47 offset:17408\n\t" \
    "s_waitcnt lgkmcnt(10)\n\t" \
    "v_mfma_f32_16x16x32_bf16 v[144:147], v[72:75], a[80:83], v[144:147]\n\t" \
    "ds_read_b64_tr_b16 v[64:65], v40 offset:24576\n\t" \
    "v_mfma_f32_16x16x32_bf16 v[148:151], v[72:75], a[84:87], v[148:151]\n\t" \
    "ds_read_b64_tr_b16 v[66:67], v40 offset:25600\n\t" \
    "buffer_load_dwordx4 v20, s[16:19], s29 offen offset:2048 lds\n\t" \
    "s_waitcnt lgkmcnt(10)\n\t" \
    "v_mfma_f32_16x16x32_bf16 v[152:155], v[76:79], a[80:83], v[152:155]\n\t" \
    "ds_read_b64_tr_b16 v[68:69], v41 offset:24576\n\t" \
    "v_mfma_f32_16x16x32_bf16 v[156:159], v[76:79], a[84:87], v[156:159]\n\t" \
    "ds_read_b64_tr_b16 v[70:71], v41 offset:25600\n\t" \
    "s_waitcnt lgkmcnt(10)\n\t" \
    "v_mfma_f32_16x16x32_bf16 v[160:163], v[80:83], a[80:83], v[160:163]\n\t" \
    "ds_read_b64_tr_b16 v[72:73], v42 offset:24576\n\t" \
    "v_mfma_f32_16x16x32_bf16 v[164:167], v[80:83], a[84:87], v[164:167]\n\t" \
    "ds_read_b64_tr_b16 v[74:75], v42 offset:25600\n\t" \
    "s_waitcnt lgkmcnt(10)\n\t" \
    "v_mfma_f32_16x16x32_bf16 v[168:171], v[84:87], a[80:83], v[168:171]\n\t" \
    "ds_read_b64_tr_b16 v[76:77], v43 offset:24576\n\t" \
    "v_mfma_f32_16x16x32_bf16 v[172:175], v[84:87], a[84:87], v[172:175]\n\t" \
    "ds_read_b64_tr_b16 v[78:79], v43 offset:25600\n\t" \
    "s_waitcnt lgkmcnt(10)\n\t" \
    "v_mfma_f32_16x16x32_bf16 v[176:179], v[88:91], a[80:83], v[176:179]\n\t" \
    "ds_read_b64_tr_b16 v[80:81], v44 offset:24576\n\t" \
    "v_mfma_f32_16x16x32_bf16 v[180:183], v[88:91], a[84:87], v[180:183]\n\t" \
    "ds_read_b64_tr_b16 v[82:83], v44 offset:25600\n\t" \
    "s_waitcnt lgkmcnt(10)\n\t" \
    "v_mfma_f32_16x16x32_bf16 v[184:187], v[92:95], a[80:83], v[184:187]\n\t" \
    "ds_read_b64_tr_b16 v[84:85], v45 offset:24576\n\t" \
    "v_mfma_f32_16x16x32_bf16 v[188:191], v[92:95], a[84:87], v[188:191]\n\t" \
    "ds_read_b64_tr_b16 v[86:87], v45 offset:25600\n\t" \
    "s_waitcnt vmcnt(27)\n\t" \
    "s_barrier\n\t" \
    "s_waitcnt lgkmcnt(10)\n\t" \
    "v_mfma_f32_16x16x32_bf16 v[128:131], v[64:67], a[88:91], v[128:131]\n\t" \
    "ds_read_b64_tr_b16 v[88:89], v46 offset:24576\n\t" \
    "s_mul_i32 s28, %[wv], 44032\n\t" \
    "s_add_u32 s29, s28, 880640\n\t" \
    "s_mul_i32 s28, %[wv], 3072\n\t" \
    "s_add_u32 m0, s28, 61440\n\t" \
    "v_mfma_f32_16x16x32_bf16 v[132:135], v[64:67], a[92:95], v[132:135]\n\t" \
    "ds_read_b64_tr_b16 v[90:91], v46 offset:25600\n\t" \
    "buffer_load_dwordx4 v18, s[16:19], s29 offen offset:0 lds\n\t" \
    "s_waitcnt lgkmcnt(10)\n\t" \
    "v_mfma_f32_16x16x32_bf16 v[136:139], v[68:71], a[88:91], v[136:139]\n\t" \
    "ds_read_b64_tr_b16 v[92:93], v47 offset:24576\n\t" \
    "buffer_load_dwordx4 v19, s[16:19], s29 offen offset:1024 lds\n\t" \
    "v_mfma_f32_16x16x32_bf16 v[140:143], v[68:71], a[92:95], v[140:143]\n\t" \
    "ds_read_b64_tr_b16 v[94:95], v47 offset:25600\n\t" \
    "s_waitcnt lgkmcnt(10)\n\t" \
    "v_mfma_f32_16x16x32_bf16 v[144:147], v[72:75], a[88:91], v[144:147]\n\t" \
    "ds_read_b64_tr_b16 v[64:65], v40 offset:32768\n\t" \
    "buffer_load_dwordx4 v20, s[16:19], s29 offen offset:2048 lds\n\t" \
    "v_mfma_f32_16x16x32_bf16 v[148:151], v[72:75], a[92:95], v[148:151]\n\t" \
    "ds_read_b64_tr_b16 v[66:67], v40 offset:33792\n\t" \
    "s_waitcnt lgkmcnt(10)\n\t" \
    "v_mfma_f32_16x16x32_bf16 v[152:155], v[76:79], a[88:91], v[152:155]\n\t" \
    "ds_read_b64_tr_b16 v[68:69], v41 offset:32768\n\t" \
    "buffer_load_dwordx4 v[192:195], v49, s[24:27], 0 offen offset:0\n\t" \
    "v_mfma_f32_16x16x32_bf16 v[156:159], v[76:79], a[92:95], v[156:159]\n\t" \
    "ds_read_b64_tr_b16 v[70:71], v41 offset:33792\n\t" \
    "s_waitcnt lgkmcnt(10)\n\t" \
    "v_mfma_f32_16x16x32_bf16 v[160:163], v[80:83], a[88:91], v[160:163]\n\t" \
    "ds_read_b64_tr_b16 v[72:73], v42 offset:32768\n\t" \
    "buffer_load_dwordx4 v[196:199], v49, s[24:27], 0 offen offset:512\n\t" \
    "s_mov_b32 s36, 4096\n\t" \
    "v_mfma_f32_16x16x32_bf16 v[164:167], v[80:83], a[92:95], v[164:167]\n\t" \
    "ds_read_b64_tr_b16 v[74:75], v42 offset:33792\n\t" \
    "buffer_load_dwordx4 v[200:203], v50, s[8:11], s36 offen offset:0\n\t" \
    "s_waitcnt lgkmcnt(10)\n\t" \
    "v_mfma_f32_16x16x32_bf16 v[168:171], v[84:87], a[88:91], v[168:171]\n\t" \
    "ds_read_b64_tr_b16 v[76:77], v43 offset:32768\n\t" \
    "v_mfma_f32_16x16x32_bf16 v[172:175], v[84:87], a[92:95], v[172:175]\n\t" \
    "ds_read_b64_tr_b16 v[78:79], v43 offset:33792\n\t" \
    "buffer_load_dwordx4 v[204:207], v50, s[8:11], s36 offen offset:16\n\t" \
    "s_waitcnt lgkmcnt(10)\n\t" \
    "v_mfma_f32_16x16x32_bf16 v[176:179], v[88:91], a[88:91], v[176:179]\n\t" \
    "ds_read_b64_tr_b16 v[80:81], v44 offset:32768\n\t" \
    "v_mfma_f32_16x16x32_bf16 v[180:183], v[88:91], a[92:95], v[180:183]\n\t" \
    "ds_read_b64_tr_b16 v[82:83], v44 offset:33792\n\t" \
    "buffer_load_dwordx4 v[208:211], v50, s[8:11], s36 offen offset:1024\n\t" \
    "s_waitcnt lgkmcnt(10)\n\t" \
    "v_mfma_f32_16x16x32_bf16 v[184:187], v[92:95], a[88:91], v[184:187]\n\t" \
    "ds_read_b64_tr_b16 v[84:85], v45 offset:32768\n\t" \
    "v_mfma_f32_16x16x32_bf16 v[188:191], v[92:95], a[92:95], v[188:191]\n\t" \
    "ds_read_b64_tr_b16 v[86:87], v45 offset:33792\n\t" \
    "buffer_load_dwordx4 v[212:215], v50, s[8:11], s36 offen offset:1040\n\t" \
    "s_mov_b32 s36, 6144\n\t" \
    "s_waitcnt vmcnt(35) lgkmcnt(10)\n\t" \
    "v_mfma_f32_16x16x32_bf16 v[128:131], v[64:67], a[96:99], v[128:131]\n\t" \
    "ds_read_b64_tr_b16 v[88:89], v46 offset:32768\n\t" \
    "buffer_load_dwordx4 v[232:235], v50, s[8:11], s36 offen offset:0\n\t" \
    "s_waitcnt vmcnt(35)\n\t" \
    "v_mfma_f32_16x16x32_bf16 v[132:135], v[64:67], a[100:103], v[132:135]\n\t" \
    "ds_read_b64_tr_b16 v[90:91], v46 offset:33792\n\t" \
    "s_waitcnt lgkmcnt(10)\n\t" \
    "v_mfma_f32_16x16x32_bf16 v[136:139], v[68:71], a[96:99], v[136:139]\n\t" \
    "ds_read_b64_tr_b16 v[92:93], v47 offset:32768\n\t" \
    "buffer_load_dwordx4 v[236:239], v50, s[8:11], s36 offen offset:16\n\t" \
    "v_mfma_f32_16x16x32_bf16 v[140:143], v[68:71], a[100:103], v[140:143]\n\t" \
    "ds_read_b64_tr_b16 v[94:95], v47 offset:33792\n\t" \
    "s_waitcnt lgkmcnt(10)\n\t" \
    "v_mfma_f32_16x16x32_bf16 v[144:147], v[72:75], a[96:99], v[144:147]\n\t" \
    "ds_read_b64_tr_b16 v[64:65], v40 offset:40960\n\t" \
    "buffer_load_dwordx4 v[240:243], v50, s[8:11], s36 offen offset:1024\n\t" \
    "v_mfma_f32_16x16x32_bf16 v[148:151], v[72:75], a[100:103], v[148:151]\n\t" \
    "ds_read_b64_tr_b16 v[66:67], v40 offset:41984\n\t" \
    "s_waitcnt lgkmcnt(10)\n\t" \
    "v_mfma_f32_16x16x32_bf16 v[152:155], v[76:79], a[96:99], v[152:155]\n\t" \
    "ds_read_b64_tr_b16 v[68:69], v41 offset:40960\n\t" \
    "buffer_load_dwordx4 v[244:247], v50, s[8:11], s36 offen offset:1040\n\t" \
    "s_mov_b32 s36, 8192\n\t" \
    "v_mfma_f32_16x16x32_bf16 v[156:159], v[76:79], a[100:103], v[156:159]\n\t" \
    "ds_read_b64_tr_b16 v[70:71], v41 offset:41984\n\t" \
    "buffer_load_dwordx4 v[216:219], v50, s[8:11], s36 offen offset:0\n\t" \
    "s_waitcnt lgkmcnt(10)\n\t" \
    "v_mfma_f32_16x16x32_bf16 v[160:163], v[80:83], a[96:99], v[160:163]\n\t" \
    "ds_read_b64_tr_b16 v[72:73], v42 offset:40960\n\t" \
    "v_mfma_f32_16x16x32_bf16 v[164:167], v[80:83], a[100:103], v[164:167]\n\t" \
    "ds_read_b64_tr_b16 v[74:75], v42 offset:41984\n\t" \
    "buffer_load_dwordx4 v[220:223], v50, s[8:11], s36 offen offset:16\n\t" \
    "s_waitcnt lgkmcnt(10)\n\t" \
    "v_mfma_f32_16x16x32_bf16 v[168:171], v[84:87], a[96:99], v[168:171]\n\t" \
    "ds_read_b64_tr_b16 v[76:77], v43 offset:40960\n\t" \
    "v_mfma_f32_16x16x32_bf16 v[172:175], v[84:87], a[100:103], v[172:175]\n\t" \
    "ds_read_b64_tr_b16 v[78:79], v43 offset:41984\n\t" \
    "buffer_load_dwordx4 v[224:227], v50, s[8:11], s36 offen offset:1024\n\t" \
    "s_waitcnt lgkmcnt(10)\n\t" \
    "v_mfma_f32_16x16x32_bf16 v[176:179], v[88:91], a[96:99], v[176:179]\n\t" \
    "ds_read_b64_tr_b16 v[80:81], v44 offset:40960\n\t" \
    "v_mfma_f32_16x16x32_bf16 v[180:183], v[88:91], a[100:103], v[180:183]\n\t" \
    "ds_read_b64_tr_b16 v[82:83], v44 offset:41984\n\t" \
    "buffer_load_dwordx4 v[228:231], v50, s[8:11], s36 offen offset:1040\n\t" \
    "s_waitcnt lgkmcnt(10)\n\t" \
    "v_mfma_f32_16x16x32_bf16 v[184:187], v[92:95], a[96:99], v[184:187]\n\t" \
    "ds_read_b64_tr_b16 v[84:85], v45 offset:40960\n\t" \
    "v_mfma_f32_16x16x32_bf16 v[188:191], v[92:95], a[100:103], v[188:191]\n\t" \
    "ds_read_b64_tr_b16 v[86:87], v45 offset:41984\n\t" \
    "s_waitcnt vmcnt(36)\n\t" \
    "s_barrier\n\t" \
    "s_waitcnt lgkmcnt(10)\n\t" \
    "v_mfma_f32_16x16x32_bf16 v[128:131], v[64:67], a[104:107], v[128:131]\n\t" \
    "ds_read_b64_tr_b16 v[88:89], v46 offset:40960\n\t" \
    "s_mul_i32 s28, %[wv], 44032\n\t" \
    "s_add_u32 s29, s28, 1056768\n\t" \
    "s_mul_i32 s28, %[wv], 3072\n\t" \
    "s_add_u32 m0, s28, 73728\n\t" \
    "v_mfma_f32_16x16x32_bf16 v[132:135], v[64:67], a[108:111], v[132:135]\n\t" \
    "ds_read_b64_tr_b16 v[90:91], v46 offset:41984\n\t" \
    "buffer_load_dwordx4 v18, s[16:19], s29 offen offset:0 lds\n\t" \
    "s_waitcnt lgkmcnt(10)\n\t" \
    "v_mfma_f32_16x16x32_bf16 v[136:139], v[68:71], a[104:107], v[136:139]\n\t" \
    "ds_read_b64_tr_b16 v[92:93], v47 offset:40960\n\t" \
    "v_mfma_f32_16x16x32_bf16 v[140:143], v[68:71], a[108:111], v[140:143]\n\t" \
    "ds_read_b64_tr_b16 v[94:95], v47 offset:41984\n\t" \
    "s_waitcnt lgkmcnt(10)\n\t" \
    "v_mfma_f32_16x16x32_bf16 v[144:147], v[72:75], a[104:107], v[144:147]\n\t" \
    "ds_read_b64_tr_b16 v[64:65], v40 offset:49152\n\t" \
    "v_mfma_f32_16x16x32_bf16 v[148:151], v[72:75], a[108:111], v[148:151]\n\t" \
    "ds_read_b64_tr_b16 v[66:67], v40 offset:50176\n\t" \
    "s_waitcnt lgkmcnt(10)\n\t" \
    "v_mfma_f32_16x16x32_bf16 v[152:155], v[76:79], a[104:107], v[152:155]\n\t" \
    "ds_read_b64_tr_b16 v[68:69], v41 offset:49152\n\t" \
    "buffer_load_dwordx4 v19, s[16:19], s29 offen offset:1024 lds\n\t" \
    "v_mfma_f32_16x16x32_bf16 v[156:159], v[76:79], a[108:111], v[156:159]\n\t" \
    "ds_read_b64_tr_b16 v[70:71], v41 offset:50176\n\t" \
    "s_waitcnt lgkmcnt(10)\n\t" \
    "v_mfma_f32_16x16x32_bf16 v[160:163], v[80:83], a[104:107], v[160:163]\n\t" \
    "ds_read_b64_tr_b16 v[72:73], v42 offset:49152\n\t" \
    "v_mfma_f32_16x16x32_bf16 v[164:167], v[80:83], a[108:111], v[164:167]\n\t" \
    "ds_read_b64_tr_b16 v[74:75], v42 offset:50176\n\t" \
    "s_waitcnt lgkmcnt(10)\n\t" \
    "v_mfma_f32_16x16x32_bf16 v[168:171], v[84:87], a[104:107], v[168:171]\n\t" \
    "ds_read_b64_tr_b16 v[76:77], v43 offset:49152\n\t" \
    "v_mfma_f32_16x16x32_bf16 v[172:175], v[84:87], a[108:111], v[172:175]\n\t" \
    "ds_read_b64_tr_b16 v[78:79], v43 offset:50176\n\t" \
    "buffer_load_dwordx4 v20, s[16:19], s29 offen offset:2048 lds\n\t" \
    "s_waitcnt lgkmcnt(10)\n\t" \
    "v_mfma_f32_16x16x32_bf16 v[176:179], v[88:91], a[104:107], v[176:179]\n\t" \
    "ds_read_b64_tr_b16 v[80:81], v44 offset:49152\n\t" \
    "v_mfma_f32_16x16x32_bf16 v[180:183], v[88:91], a[108:111], v[180:183]\n\t" \
    "ds_read_b64_tr_b16 v[82:83], v44 offset:50176\n\t" \
    "s_waitcnt lgkmcnt(10)\n\t" \
    "v_mfma_f32_16x16x32_bf16 v[184:187], v[92:95], a[104:107], v[184:187]\n\t" \
    "ds_read_b64_tr_b16 v[84:85], v45 offset:49152\n\t" \
    "v_mfma_f32_16x16x32_bf16 v[188:191], v[92:95], a[108:111], v[188:191]\n\t" \
    "ds_read_b64_tr_b16 v[86:87], v45 offset:50176\n\t" \
    "s_mul_i32 s28, %[wv], 44032\n\t" \
    "s_add_u32 s29, s28, 1232896\n\t" \
    "s_mul_i32 s28, %[wv], 3072\n\t" \
    "s_add_u32 m0, s28, 86016\n\t" \
    "s_waitcnt vmcnt(38) lgkmcnt(10)\n\t" \
    "v_mfma_f32_16x16x32_bf16 v[128:131], v[64:67], a[112:115], v[128:131]\n\t" \
    "ds_read_b64_tr_b16 v[88:89], v46 offset:49152\n\t" \
    "buffer_load_dwordx4 v18, s[16:19], s29 offen offset:0 lds\n\t" \
    "s_waitcnt vmcnt(38)\n\t" \
    "v_mfma_f32_16x16x32_bf16 v[132:135], v[64:67], a[116:119], v[132:135]\n\t" \
    "ds_read_b64_tr_b16 v[90:91], v46 offset:50176\n\t" \
    "s_waitcnt lgkmcnt(10)\n\t" \
    "v_mfma_f32_16x16x32_bf16 v[136:139], v[68:71], a[112:115], v[136:139]\n\t" \
    "ds_read_b64_tr_b16 v[92:93], v47 offset:49152\n\t" \
    "v_mfma_f32_16x16x32_bf16 v[140:143], v[68:71], a[116:119], v[140:143]\n\t" \
    "ds_read_b64_tr_b16 v[94:95], v47 offset:50176\n\t" \
    "s_waitcnt lgkmcnt(10)\n\t" \
    "v_mfma_f32_16x16x32_bf16 v[144:147], v[72:75], a[112:115], v[144:147]\n\t" \
    "ds_read_b64_tr_b16 v[64:65], v40 offset:57344\n\t" \
    "v_mfma_f32_16x16x32_bf16 v[148:151], v[72:75], a[116:119], v[148:151]\n\t" \
    "ds_read_b64_tr_b16 v[66:67], v40 offset:58368\n\t" \
    "buffer_load_dwordx4 v19, s[16:19], s29 offen offset:1024 lds\n\t" \
    "s_waitcnt lgkmcnt(10)\n\t" \
    "v_mfma_f32_16x16x32_bf16 v[152:155], v[76:79], a[112:115], v[152:155]\n\t" \
    "ds_read_b64_tr_b16 v[68:69], v41 offset:57344\n\t" \
    "v_mfma_f32_16x16x32_bf16 v[156:159], v[76:79], a[116:119], v[156:159]\n\t" \
    "ds_read_b64_tr_b16 v[70:71], v41 offset:58368\n\t" \
    "s_waitcnt lgkmcnt(10)\n\t" \
    "v_mfma_f32_16x16x32_bf16 v[160:163], v[80:83], a[112:115], v[160:163]\n\t" \
    "ds_read_b64_tr_b16 v[72:73], v42 offset:57344\n\t" \
    "v_mfma_f32_16x16x32_bf16 v[164:167], v[80:83], a[116:119], v[164:167]\n\t" \
    "ds_read_b64_tr_b16 v[74:75], v42 offset:58368\n\t" \
    "s_waitcnt lgkmcnt(10)\n\t" \
    "v_mfma_f32_16x16x32_bf16 v[168:171], v[84:87], a[112:115], v[168:171]\n\t" \
    "ds_read_b64_tr_b16 v[76:77], v43 offset:57344\n\t" \
    "buffer_load_dwordx4 v20, s[16:19], s29 offen offset:2048 lds\n\t" \
    "v_mfma_f32_16x16x32_bf16 v[172:175], v[84:87], a[116:119], v[172:175]\n\t" \
    "ds_read_b64_tr_b16 v[78:79], v43 offset:58368\n\t" \
    "s_waitcnt lgkmcnt(10)\n\t" \
    "v_mfma_f32_16x16x32_bf16 v[176:179], v[88:91], a[112:115], v[176:179]\n\t" \
    "ds_read_b64_tr_b16 v[80:81], v44 offset:57344\n\t" \
    "v_mfma_f32_16x16x32_bf16 v[180:183], v[88:91], a[116:119], v[180:183]\n\t" \
    "ds_read_b64_tr_b16 v[82:83], v44 offset:58368\n\t" \
    "s_waitcnt lgkmcnt(10)\n\t" \
    "v_mfma_f32_16x16x32_bf16 v[184:187], v[92:95], a[112:115], v[184:187]\n\t" \
    "ds_read_b64_tr_b16 v[84:85], v45 offset:57344\n\t" \
    "v_mfma_f32_16x16x32_bf16 v[188:191], v[92:95], a[116:119], v[188:191]\n\t" \
    "ds_read_b64_tr_b16 v[86:87], v45 offset:58368\n\t" \
    "s_waitcnt vmcnt(39) lgkmcnt(10)\n\t" \
    "v_mfma_f32_16x16x32_bf16 v[128:131], v[64:67], a[120:123], v[128:131]\n\t" \
    "ds_read_b64_tr_b16 v[88:89], v46 offset:57344\n\t" \
    "s_waitcnt vmcnt(38)\n\t" \
    "v_mfma_f32_16x16x32_bf16 v[132:135], v[64:67], a[124:127], v[132:135]\n\t" \
    "ds_read_b64_tr_b16 v[90:91], v46 offset:58368\n\t" \
    "s_waitcnt lgkmcnt(10)\n\t" \
    "v_mfma_f32_16x16x32_bf16 v[136:139], v[68:71], a[120:123], v[136:139]\n\t" \
    "ds_read_b64_tr_b16 v[92:93], v47 offset:57344\n\t" \
    "v_mfma_f32_16x16x32_bf16 v[140:143], v[68:71], a[124:127], v[140:143]\n\t" \
    "ds_read_b64_tr_b16 v[94:95], v47 offset:58368\n\t" \
    "s_waitcnt lgkmcnt(10)\n\t" \
    "v_mfma_f32_16x16x32_bf16 v[144:147], v[72:75], a[120:123], v[144:147]\n\t" \
    "v_mfma_f32_16x16x32_bf16 v[148:151], v[72:75], a[124:127], v[148:151]\n\t" \
    "s_waitcnt lgkmcnt(8)\n\t" \
    "v_mfma_f32_16x16x32_bf16 v[152:155], v[76:79], a[120:123], v[152:155]\n\t" \
    "v_mfma_f32_16x16x32_bf16 v[156:159], v[76:79], a[124:127], v[156:159]\n\t" \
    "s_waitcnt lgkmcnt(6)\n\t" \
    "v_mfma_f32_16x16x32_bf16 v[160:163], v[80:83], a[120:123], v[160:163]\n\t" \
    "v_mfma_f32_16x16x32_bf16 v[164:167], v[80:83], a[124:127], v[164:167]\n\t" \
    "s_waitcnt lgkmcnt(4)\n\t" \
    "v_mfma_f32_16x16x32_bf16 v[168:171], v[84:87], a[120:123], v[168:171]\n\t" \
    "v_mfma_f32_16x16x32_bf16 v[172:175], v[84:87], a[124:127], v[172:175]\n\t" \
    "s_waitcnt lgkmcnt(2)\n\t" \
    "v_mfma_f32_16x16x32_bf16 v[176:179], v[88:91], a[120:123], v[176:179]\n\t" \
    "v_mfma_f32_16x16x32_bf16 v[180:183], v[88:91], a[124:127], v[180:183]\n\t" \
    "s_waitcnt lgkmcnt(0)\n\t" \
    "v_mfma_f32_16x16x32_bf16 v[184:187], v[92:95], a[120:123], v[184:187]\n\t" \
    "v_mfma_f32_16x16x32_bf16 v[188:191], v[92:95], a[124:127], v[188:191]\n\t" \
    "s_mov_b32 s31, %[sst]\n\t" \
    "s_mov_b32 s24, %[b1lo]\n\t" \
    "s_mov_b32 s25, %[b1hi]\n\t" \
    "s_mov_b32 s26, 0xac0\n\t" \
    "s_mov_b32 s27, 0x00020000\n\t" \
    "v_cvt_pk_bf16_f32 v64, v128, v129\n\t" \
    "v_cvt_pk_bf16_f32 v65, v130, v131\n\t" \
    "v_cvt_pk_bf16_f32 v66, v136, v137\n\t" \
    "v_cvt_pk_bf16_f32 v67, v138, v139\n\t" \
    "s_nop 0\n\t" \
    "v_permlane16_swap_b32 v64, v66\n\t" \
    "v_permlane16_swap_b32 v65, v67\n\t" \
    "buffer_store_dwordx4 v[64:67], v48, s[8:11], s31 offen offset:0 sc1\n\t" \
    "v_cvt_pk_bf16_f32 v68, v144, v145\n\t" \
    "v_cvt_pk_bf16_f32 v69, v146, v147\n\t" \
    "v_cvt_pk_bf16_f32 v70, v152, v153\n\t" \
    "v_cvt_pk_bf16_f32 v71, v154, v155\n\t" \
    "s_nop 0\n\t" \
    "v_permlane16_swap_b32 v68, v70\n\t" \
    "v_permlane16_swap_b32 v69, v71\n\t" \
    "buffer_store_dwordx4 v[68:71], v48, s[8:11], s31 offen offset:64 sc1\n\t" \
    "v_cvt_pk_bf16_f32 v72, v160, v161\n\t" \
    "v_cvt_pk_bf16_f32 v73, v162, v163\n\t" \
    "v_cvt_pk_bf16_f32 v74, v168, v169\n\t" \
    "v_cvt_pk_bf16_f32 v75, v170, v171\n\t" \
    "s_nop 0\n\t" \
    "v_permlane16_swap_b32 v72, v74\n\t" \
    "v_permlane16_swap_b32 v73, v75\n\t" \
    "buffer_store_dwordx4 v[72:75], v48, s[8:11], s31 offen offset:128 sc1\n\t" \
    "v_cvt_pk_bf16_f32 v76, v176, v177\n\t" \
    "v_cvt_pk_bf16_f32 v77, v178, v179\n\t" \
    "v_cvt_pk_bf16_f32 v78, v184, v185\n\t" \
    "v_cvt_pk_bf16_f32 v79, v186, v187\n\t" \
    "s_nop 0\n\t" \
    "v_permlane16_swap_b32 v76, v78\n\t" \
    "v_permlane16_swap_b32 v77, v79\n\t" \
    "buffer_store_dwordx4 v[76:79], v48, s[8:11], s31 offen offset:192 sc1\n\t" \
    "s_add_u32 s31, s31, 16384\n\t" \
    "v_cvt_pk_bf16_f32 v80, v132, v133\n\t" \
    "v_cvt_pk_bf16_f32 v81, v134, v135\n\t" \
    "v_cvt_pk_bf16_f32 v82, v140, v141\n\t" \
    "v_cvt_pk_bf16_f32 v83, v142, v143\n\t" \
    "s_nop 0\n\t" \
    "v_permlane16_swap_b32 v80, v82\n\t" \
    "v_permlane16_swap_b32 v81, v83\n\t" \
    "buffer_store_dwordx4 v[80:83], v48, s[8:11], s31 offen offset:0 sc1\n\t" \
    "v_cvt_pk_bf16_f32 v84, v148, v149\n\t" \
    "v_cvt_pk_bf16_f32 v85, v150, v151\n\t" \
    "v_cvt_pk_bf16_f32 v86, v156, v157\n\t" \
    "v_cvt_pk_bf16_f32 v87, v158, v159\n\t" \
    "s_nop 0\n\t" \
    "v_permlane16_swap_b32 v84, v86\n\t" \
    "v_permlane16_swap_b32 v85, v87\n\t" \
    "buffer_store_dwordx4 v[84:87], v48, s[8:11], s31 offen offset:64 sc1\n\t" \
    "v_cvt_pk_bf16_f32 v88, v164, v165\n\t" \
    "v_cvt_pk_bf16_f32 v89, v166, v167\n\t" \
    "v_cvt_pk_bf16_f32 v90, v172, v173\n\t" \
    "v_cvt_pk_bf16_f32 v91, v174, v175\n\t" \
    "s_nop 0\n\t" \
    "v_permlane16_swap_b32 v88, v90\n\t" \
    "v_permlane16_swap_b32 v89, v91\n\t" \
    "buffer_store_dwordx4 v[88:91], v48, s[8:11], s31 offen offset:128 sc1\n\t" \
    "v_cvt_pk_bf16_f32 v92, v180, v181\n\t" \
    "v_cvt_pk_bf16_f32 v93, v182, v183\n\t" \
    "v_cvt_pk_bf16_f32 v94, v188, v189\n\t" \
    "v_cvt_pk_bf16_f32 v95, v190, v191\n\t" \
    "s_nop 0\n\t" \
    "v_permlane16_swap_b32 v92, v94\n\t" \
    "v_permlane16_swap_b32 v93, v95\n\t" \
    "buffer_store_dwordx4 v[92:95], v48, s[8:11], s31 offen offset:192 sc1\n\t" \
    "s_add_u32 s31, s31, 16384\n\t" \
    "buffer_load_dwordx2 v[100:101], v51, s[24:27], 0 offen offset:0\n\t" \
    "buffer_load_dwordx2 v[102:103], v51, s[24:27], 0 offen offset:32\n\t" \
    "buffer_load_dwordx2 v[104:105], v51, s[24:27], 0 offen offset:64\n\t" \
    "buffer_load_dwordx2 v[106:107], v51, s[24:27], 0 offen offset:96\n\t" \
    "buffer_load_dwordx2 v[108:109], v51, s[24:27], 0 offen offset:128\n\t" \
    "buffer_load_dwordx2 v[110:111], v51, s[24:27], 0 offen offset:160\n\t" \
    "s_waitcnt vmcnt(0) lgkmcnt(0)\n\t" \
    "s_barrier\n\t" \
    "s_cmp_lg_u32 %[wv], 0\n\t" \
    "s_cbranch_scc1 S1_L_%=\n\t" \
    "v_mov_b32 v17, 1\n\t" \
    "s_mov_b64 exec, 1\n\t" \
    "buffer_atomic_add v17, off, s[8:11], %[co1] sc1\n\t" \
    "s_mov_b64 exec, -1\n\t" \
    "S1_L_%=:\n\t" \
    "s_lshr_b32 s28, %[bid], 3\n\t" \
    "s_and_b32 s28, s28, 3\n\t" \
    "s_mul_i32 s28, s28, 56\n\t" \
    "s_mul_i32 s29, %[wv], 352\n\t" \
    "s_add_u32 s28, s28, s29\n\t" \
    "s_add_u32 s28, s28, 128\n\t" \
    "v_lshrrev_b32 v115, 4, v16\n\t" \
    "v_add_u32 v115, s28, v115\n\t" \
    "v_min_u32 v115, 1375, v115\n\t" \
    "v_lshlrev_b32 v115, 10, v115\n\t" \
    "s_lshr_b32 s29, %[mm], 1\n\t" \
    "s_lshl_b32 s29, s29, 7\n\t" \
    "buffer_load_dword v112, v115, s[20:23], s29 offen\n\t" \
    "s_cmp_lg_u32 %[wv], 0\n\t" \
    "s_cbranch_scc1 S1_OK_%=\n\t" \
    "s_mov_b32 s30, 0\n\t" \
    "S1_P_%=:\n\t" \
    "buffer_load_dword v17, off, s[8:11], %[co2] sc1\n\t" \
    "s_waitcnt vmcnt(0)\n\t" \
    "v_readfirstlane_b32 s28, v17\n\t" \
    "s_nop 0\n\t" \
    "s_cmp_ge_u32 s28, 16\n\t" \
    "s_cbranch_scc1 S1_OK_%=\n\t" \
    "s_add_u32 s30, s30, 1\n\t" \
    "s_cmp_gt_u32 s30, 0x40000\n\t" \
    "s_cbranch_scc1 S1_OK_%=\n\t" \
    "s_sleep 1\n\t" \
    "s_branch S1_P_%=\n\t" \
    "S1_OK_%=:\n\t" \
    "s_barrier\n\t" \
    "s_mov_b32 s28, 0x4400000\n\t" \
    "buffer_load_dwordx4 v[128:131], v49, s[8:11], s28 offen offset:0 sc1\n\t" \
    "buffer_load_dwordx4 v[132:135], v49, s[8:11], s28 offen offset:512 sc1\n\t" \
    "s_mov_b32 s28, 0x4600000\n\t" \
    "buffer_load_dwordx4 v[136:139], v49, s[8:11], s28 offen offset:0 sc1\n\t" \
    "buffer_load_dwordx4 v[140:143], v49, s[8:11], s28 offen offset:512 sc1\n\t" \
    "s_mov_b32 s28, 0x4800000\n\t" \
    "buffer_load_dwordx4 v[144:147], v49, s[8:11], s28 offen offset:0 sc1\n\t" \
    "buffer_load_dwordx4 v[148:151], v49, s[8:11], s28 offen offset:512 sc1\n\t" \
    "s_mov_b32 s28, 0x4a00000\n\t" \
    "buffer_load_dwordx4 v[152:155], v49, s[8:11], s28 offen offset:0 sc1\n\t" \
    "buffer_load_dwordx4 v[156:159], v49, s[8:11], s28 offen offset:512 sc1\n\t" \
    "s_waitcnt vmcnt(7)\n\t" \
    "v_lshlrev_b32 v160, 16, v128\n\t" \
    "v_and_b32 v161, 0xffff0000, v128\n\t" \
    "v_lshlrev_b32 v162, 16, v129\n\t" \
    "v_and_b32 v163, 0xffff0000, v129\n\t" \
    "v_lshlrev_b32 v164, 16, v130\n\t" \
    "v_and_b32 v165, 0xffff0000, v130\n\t" \
    "v_lshlrev_b32 v166, 16, v131\n\t" \
    "v_and_b32 v167, 0xffff0000, v131\n\t" \
    "s_waitcnt vmcnt(6)\n\t" \
    "v_lshlrev_b32 v168, 16, v132\n\t" \
    "v_and_b32 v169, 0xffff0000, v132\n\t" \
    "v_lshlrev_b32 v170, 16, v133\n\t" \
    "v_and_b32 v171, 0xffff0000, v133\n\t" \
    "v_lshlrev_b32 v172, 16, v134\n\t" \
    "v_and_b32 v173, 0xffff0000, v134\n\t" \
    "v_lshlrev_b32 v174, 16, v135\n\t" \
    "v_and_b32 v175, 0xffff0000, v135\n\t" \
    "s_waitcnt vmcnt(5)\n\t" \
    "v_lshlrev_b32 v176, 16, v136\n\t" \
    "v_and_b32 v177, 0xffff0000, v136\n\t" \
    "v_lshlrev_b32 v178, 16, v137\n\t" \
    "v_and_b32 v179, 0xffff0000, v137\n\t" \
    "v_lshlrev_b32 v180, 16, v138\n\t" \
    "v_and_b32 v181, 0xffff0000, v138\n\t" \
    "v_lshlrev_b32 v182, 16, v139\n\t" \
    "v_and_b32 v183, 0xffff0000, v139\n\t" \
    "s_waitcnt vmcnt(4)\n\t" \
    "v_lshlrev_b32 v184, 16, v140\n\t" \
    "v_and_b32 v185, 0xffff0000, v140\n\t" \
    "v_lshlrev_b32 v186, 16, v141\n\t" \
    "v_and_b32 v187, 0xffff0000, v141\n\t" \
    "v_lshlrev_b32 v188, 16, v142\n\t" \
    "v_and_b32 v189, 0xffff0000, v142\n\t" \
    "v_lshlrev_b32 v190, 16, v143\n\t" \
    "v_and_b32 v191, 0xffff0000, v143\n\t" \
    "v_pk_add_f32 v[160:161], v[160:161], v[176:177]\n\t" \
    "v_pk_add_f32 v[162:163], v[162:163], v[178:179]\n\t" \
    "v_pk_add_f32 v[164:165], v[164:165], v[180:181]\n\t" \
    "v_pk_add_f32 v[166:167], v[166:167], v[182:183]\n\t" \
    "v_pk_add_f32 v[168:169], v[168:169], v[184:185]\n\t" \
    "v_pk_add_f32 v[170:171], v[170:171], v[186:187]\n\t" \
    "v_pk_add_f32 v[172:173], v[172:173], v[188:189]\n\t" \
    "v_pk_add_f32 v[174:175], v[174:175], v[190:191]\n\t" \
    "s_waitcnt vmcnt(3)\n\t" \
    "v_lshlrev_b32 v176, 16, v144\n\t" \
    "v_and_b32 v177, 0xffff0000, v144\n\t" \
    "v_lshlrev_b32 v178, 16, v145\n\t" \
    "v_and_b32 v179, 0xffff0000, v145\n\t" \
    "v_lshlrev_b32 v180, 16, v146\n\t" \
    "v_and_b32 v181, 0xffff0000, v146\n\t" \
    "v_lshlrev_b32 v182, 16, v147\n\t" \
    "v_and_b32 v183, 0xffff0000, v147\n\t" \
    "s_waitcnt vmcnt(2)\n\t" \
    "v_lshlrev_b32 v184, 16, v148\n\t" \
    "v_and_b32 v185, 0xffff0000, v148\n\t" \
    "v_lshlrev_b32 v186, 16, v149\n\t" \
    "v_and_b32 v187, 0xffff0000, v149\n\t" \
    "v_lshlrev_b32 v188, 16, v150\n\t" \
    "v_and_b32 v189, 0xffff0000, v150\n\t" \
    "v_lshlrev_b32 v190, 16, v151\n\t" \
    "v_and_b32 v191, 0xffff0000, v151\n\t" \
    "v_pk_add_f32 v[160:161], v[160:161], v[176:177]\n\t" \
    "v_pk_add_f32 v[162:163], v[162:163], v[178:179]\n\t" \
    "v_pk_add_f32 v[164:165], v[164:165], v[180:181]\n\t" \
    "v_pk_add_f32 v[166:167], v[166:167], v[182:183]\n\t" \
    "v_pk_add_f32 v[168:169], v[168:169], v[184:185]\n\t" \
    "v_pk_add_f32 v[170:171], v[170:171], v[186:187]\n\t" \
    "v_pk_add_f32 v[172:173], v[172:173], v[188:189]\n\t" \
    "v_pk_add_f32 v[174:175], v[174:175], v[190:191]\n\t" \
    "s_waitcnt vmcnt(1)\n\t" \
    "v_lshlrev_b32 v176, 16, v152\n\t" \
    "v_and_b32 v177, 0xffff0000, v152\n\t" \
    "v_lshlrev_b32 v178, 16, v153\n\t" \
    "v_and_b32 v179, 0xffff0000, v153\n\t" \
    "v_lshlrev_b32 v180, 16, v154\n\t" \
    "v_and_b32 v181, 0xffff0000, v154\n\t" \
    "v_lshlrev_b32 v182, 16, v155\n\t" \
    "v_and_b32 v183, 0xffff0000, v155\n\t" \
    "s_waitcnt vmcnt(0)\n\t" \
    "v_lshlrev_b32 v184, 16, v156\n\t" \
    "v_and_b32 v185, 0xffff0000, v156\n\t" \
    "v_lshlrev_b32 v186, 16, v157\n\t" \
    "v_and_b32 v187, 0xffff0000, v157\n\t" \
    "v_lshlrev_b32 v188, 16, v158\n\t" \
    "v_and_b32 v189, 0xffff0000, v158\n\t" \
    "v_lshlrev_b32 v190, 16, v159\n\t" \
    "v_and_b32 v191, 0xffff0000, v159\n\t" \
    "v_pk_add_f32 v[160:161], v[160:161], v[176:177]\n\t" \
    "v_pk_add_f32 v[162:163], v[162:163], v[178:179]\n\t" \
    "v_pk_add_f32 v[164:165], v[164:165], v[180:181]\n\t" \
    "v_pk_add_f32 v[166:167], v[166:167], v[182:183]\n\t" \
    "v_pk_add_f32 v[168:169], v[168:169], v[184:185]\n\t" \
    "v_pk_add_f32 v[170:171], v[170:171], v[186:187]\n\t" \
    "v_pk_add_f32 v[172:173], v[172:173], v[188:189]\n\t" \
    "v_pk_add_f32 v[174:175], v[174:175], v[190:191]\n\t" \
    "v_lshlrev_b32 v64, 16, v192\n\t" \
    "v_and_b32 v65, 0xffff0000, v192\n\t" \
    "v_lshlrev_b32 v66, 16, v193\n\t" \
    "v_and_b32 v67, 0xffff0000, v193\n\t" \
    "v_lshlrev_b32 v68, 16, v194\n\t" \
    "v_and_b32 v69, 0xffff0000, v194\n\t" \
    "v_lshlrev_b32 v70, 16, v195\n\t" \
    "v_and_b32 v71, 0xffff0000, v195\n\t" \
    "v_lshlrev_b32 v72, 16, v196\n\t" \
    "v_and_b32 v73, 0xffff0000, v196\n\t" \
    "v_lshlrev_b32 v74, 16, v197\n\t" \
    "v_and_b32 v75, 0xffff0000, v197\n\t" \
    "v_lshlrev_b32 v76, 16, v198\n\t" \
    "v_and_b32 v77, 0xffff0000, v198\n\t" \
    "v_lshlrev_b32 v78, 16, v199\n\t" \
    "v_and_b32 v79, 0xffff0000, v199\n\t" \
    "v_pk_fma_f32 v[64:65], v[200:201], v[160:161], v[64:65]\n\t" \
    "v_pk_fma_f32 v[66:67], v[202:203], v[162:163], v[66:67]\n\t" \
    "v_pk_fma_f32 v[68:69], v[204:205], v[164:165], v[68:69]\n\t" \
    "v_pk_fma_f32 v[70:71], v[206:207], v[166:167], v[70:71]\n\t" \
    "v_pk_fma_f32 v[72:73], v[208:209], v[168:169], v[72:73]\n\t" \
    "v_pk_fma_f32 v[74:75], v[210:211], v[170:171], v[74:75]\n\t" \
    "v_pk_fma_f32 v[76:77], v[212:213], v[172:173], v[76:77]\n\t" \
    "v_pk_fma_f32 v[78:79], v[214:215], v[174:175], v[78:79]\n\t" \
    "v_cvt_pk_bf16_f32 v128, v64, v65\n\t" \
    "v_cvt_pk_bf16_f32 v129, v66, v67\n\t" \
    "v_cvt_pk_bf16_f32 v130, v68, v69\n\t" \
    "v_cvt_pk_bf16_f32 v131, v70, v71\n\t" \
    "v_cvt_pk_bf16_f32 v132, v72, v73\n\t" \
    "v_cvt_pk_bf16_f32 v133, v74, v75\n\t" \
    "v_cvt_pk_bf16_f32 v134, v76, v77\n\t" \
    "v_cvt_pk_bf16_f32 v135, v78, v79\n\t" \
    "s_mov_b32 s28, 0x5400000\n\t" \
    "buffer_store_dwordx4 v[128:131], v49, s[8:11], s28 offen offset:0 sc1\n\t" \
    "buffer_store_dwordx4 v[132:135], v49, s[8:11], s28 offen offset:512 sc1\n\t" \
    "v_lshlrev_b32 v64, 16, v128\n\t" \
    "v_and_b32 v65, 0xffff0000, v128\n\t" \
    "v_lshlrev_b32 v66, 16, v129\n\t" \
    "v_and_b32 v67, 0xffff0000, v129\n\t" \
    "v_lshlrev_b32 v68, 16, v130\n\t" \
    "v_and_b32 v69, 0xffff0000, v130\n\t" \
    "v_lshlrev_b32 v70, 16, v131\n\t" \
    "v_and_b32 v71, 0xffff0000, v131\n\t" \
    "v_lshlrev_b32 v72, 16, v132\n\t" \
    "v_and_b32 v73, 0xffff0000, v132\n\t" \
    "v_lshlrev_b32 v74, 16, v133\n\t" \
    "v_and_b32 v75, 0xffff0000, v133\n\t" \
    "v_lshlrev_b32 v76, 16, v134\n\t" \
    "v_and_b32 v77, 0xffff0000, v134\n\t" \
    "v_lshlrev_b32 v78, 16, v135\n\t" \
    "v_and_b32 v79, 0xffff0000, v135\n\t" \
    "v_pk_add_f32 v[160:161], v[64:65], v[66:67]\n\t" \
    "v_pk_add_f32 v[160:161], v[160:161], v[68:69]\n\t" \
    "v_pk_add_f32 v[160:161], v[160:161], v[70:71]\n\t" \
    "v_pk_add_f32 v[160:161], v[160:161], v[72:73]\n\t" \
    "v_pk_add_f32 v[160:161], v[160:161], v[74:75]\n\t" \
    "v_pk_add_f32 v[160:161], v[160:161], v[76:77]\n\t" \
    "v_pk_add_f32 v[160:161], v[160:161], v[78:79]\n\t" \
    "v_add_f32 v160, v160, v161\n\t" \
    "s_nop 1\n\t" \
    "v_add_f32_dpp v160, v160, v160 quad_perm:[1,0,3,2] row_mask:0xf bank_mask:0xf\n\t" \
    "s_nop 1\n\t" \
    "v_add_f32_dpp v160, v160, v160 quad_perm:[2,3,0,1] row_mask:0xf bank_mask:0xf\n\t" \
    "s_nop 1\n\t" \
    "v_add_f32_dpp v160, v160, v160 row_half_mirror row_mask:0xf bank_mask:0xf\n\t" \
    "s_nop 1\n\t" \
    "v_add_f32_dpp v160, v160, v160 row_mirror row_mask:0xf bank_mask:0xf\n\t" \
    "v_mov_b32 v162, v160\n\t" \
    "s_nop 1\n\t" \
    "v_permlane16_swap_b32 v162, v160\n\t" \
    "v_add_f32 v160, v160, v162\n\t" \
    "v_mul_f32 v160, 0xbb000000, v160\n\t" \
    "v_mov_b32 v161, v160\n\t" \
    "v_pk_add_f32 v[64:65], v[64:65], v[160:161]\n\t" \
    "v_pk_add_f32 v[66:67], v[66:67], v[160:161]\n\t" \
    "v_pk_add_f32 v[68:69], v[68:69], v[160:161]\n\t" \
    "v_pk_add_f32 v[70:71], v[70:71], v[160:161]\n\t" \
    "v_pk_add_f32 v[72:73], v[72:73], v[160:161]\n\t" \
    "v_pk_add_f32 v[74:75], v[74:75], v[160:161]\n\t" \
    "v_pk_add_f32 v[76:77], v[76:77], v[160:161]\n\t" \
    "v_pk_add_f32 v[78:79], v[78:79], v[160:161]\n\t" \
    "v_pk_mul_f32 v[164:165], v[64:65], v[64:65]\n\t" \
    "v_pk_fma_f32 v[164:165], v[66:67], v[66:67], v[164:165]\n\t" \
    "v_pk_fma_f32 v[164:165], v[68:69], v[68:69], v[164:165]\n\t" \
    "v_pk_fma_f32 v[164:165], v[70:71], v[70:71], v[164:165]\n\t" \
    "v_pk_fma_f32 v[164:165], v[72:73], v[72:73], v[164:165]\n\t" \
    "v_pk_fma_f32 v[164:165], v[74:75], v[74:75], v[164:165]\n\t" \
    "v_pk_fma_f32 v[164:165], v[76:77], v[76:77], v[164:165]\n\t" \
    "v_pk_fma_f32 v[164:165], v[78:79], v[78:79], v[164:165]\n\t" \
    "v_add_f32 v164, v164, v165\n\t" \
    "s_nop 1\n\t" \
    "v_add_f32_dpp v164, v164, v164 quad_perm:[1,0,3,2] row_mask:0xf bank_mask:0xf\n\t" \
    "s_nop 1\n\t" \
    "v_add_f32_dpp v164, v164, v164 quad_perm:[2,3,0,1] row_mask:0xf bank_mask:0xf\n\t" \
    "s_nop 1\n\t" \
    "v_add_f32_dpp v164, v164, v164 row_half_mirror row_mask:0xf bank_mask:0xf\n\t" \
    "s_nop 1\n\t" \
    "v_add_f32_dpp v164, v164, v164 row_mirror row_mask:0xf bank_mask:0xf\n\t" \
    "v_mov_b32 v162, v164\n\t" \
    "s_nop 1\n\t" \
    "v_permlane16_swap_b32 v162, v164\n\t" \
    "v_add_f32 v164, v164, v162\n\t" \
    "v_mov_b32 v166, 0x3727c5ac\n\t" \
    "v_fmac_f32 v166, 0x3b000000, v164\n\t" \
    "v_rsq_f32 v166, v166\n\t" \
    "s_nop 0\n\t" \
    "v_mov_b32 v167, v166\n\t" \
    "v_pk_mul_f32 v[64:65], v[64:65], v[166:167]\n\t" \
    "v_pk_mul_f32 v[66:67], v[66:67], v[166:167]\n\t" \
    "v_pk_mul_f32 v[68:69], v[68:69], v[166:167]\n\t" \
    "v_pk_mul_f32 v[70:71], v[70:71], v[166:167]\n\t" \
    "v_pk_mul_f32 v[72:73], v[72:73], v[166:167]\n\t" \
    "v_pk_mul_f32 v[74:75], v[74:75], v[166:167]\n\t" \
    "v_pk_mul_f32 v[76:77], v[76:77], v[166:167]\n\t" \
    "v_pk_mul_f32 v[78:79], v[78:79], v[166:167]\n\t" \
    "v_pk_add_f32 v[216:217], v[216:217], 1.0 op_sel_hi:[1,0]\n\t" \
    "v_pk_add_f32 v[218:219], v[218:219], 1.0 op_sel_hi:[1,0]\n\t" \
    "v_pk_add_f32 v[220:221], v[220:221], 1.0 op_sel_hi:[1,0]\n\t" \
    "v_pk_add_f32 v[222:223], v[222:223], 1.0 op_sel_hi:[1,0]\n\t" \
    "v_pk_add_f32 v[224:225], v[224:225], 1.0 op_sel_hi:[1,0]\n\t" \
    "v_pk_add_f32 v[226:227], v[226:227], 1.0 op_sel_hi:[1,0]\n\t" \
    "v_pk_add_f32 v[228:229], v[228:229], 1.0 op_sel_hi:[1,0]\n\t" \
    "v_pk_add_f32 v[230:231], v[230:231], 1.0 op_sel_hi:[1,0]\n\t" \
    "v_pk_fma_f32 v[64:65], v[64:65], v[216:217], v[232:233]\n\t" \
    "v_pk_fma_f32 v[66:67], v[66:67], v[218:219], v[234:235]\n\t" \
    "v_pk_fma_f32 v[68:69], v[68:69], v[220:221], v[236:237]\n\t" \
    "v_pk_fma_f32 v[70:71], v[70:71], v[222:223], v[238:239]\n\t" \
    "v_pk_fma_f32 v[72:73], v[72:73], v[224:225], v[240:241]\n\t" \
    "v_pk_fma_f32 v[74:75], v[74:75], v[226:227], v[242:243]\n\t" \
    "v_pk_fma_f32 v[76:77], v[76:77], v[228:229], v[244:245]\n\t" \
    "v_pk_fma_f32 v[78:79], v[78:79], v[230:231], v[246:247]\n\t" \
    "v_pk_add_f32 v[160:161], v[64:65], v[66:67]\n\t" \
    "v_pk_add_f32 v[160:161], v[160:161], v[68:69]\n\t" \
    "v_pk_add_f32 v[160:161], v[160:161], v[70:71]\n\t" \
    "v_pk_add_f32 v[160:161], v[160:161], v[72:73]\n\t" \
    "v_pk_add_f32 v[160:161], v[160:161], v[74:75]\n\t" \
    "v_pk_add_f32 v[160:161], v[160:161], v[76:77]\n\t" \
    "v_pk_add_f32 v[160:161], v[160:161], v[78:79]\n\t" \
    "v_add_f32 v160, v160, v161\n\t" \
    "s_nop 1\n\t" \
    "v_add_f32_dpp v160, v160, v160 quad_perm:[1,0,3,2] row_mask:0xf bank_mask:0xf\n\t" \
    "s_nop 1\n\t" \
    "v_add_f32_dpp v160, v160, v160 quad_perm:[2,3,0,1] row_mask:0xf bank_mask:0xf\n\t" \
    "s_nop 1\n\t" \
    "v_add_f32_dpp v160, v160, v160 row_half_mirror row_mask:0xf bank_mask:0xf\n\t" \
    "s_nop 1\n\t" \
    "v_add_f32_dpp v160, v160, v160 row_mirror row_mask:0xf bank_mask:0xf\n\t" \
    "v_mov_b32 v162, v160\n\t" \
    "s_nop 1\n\t" \
    "v_permlane16_swap_b32 v162, v160\n\t" \
    "v_add_f32 v160, v160, v162\n\t" \
    "v_mul_f32 v160, 0xbb000000, v160\n\t" \
    "v_mov_b32 v161, v160\n\t" \
    "v_pk_add_f32 v[64:65], v[64:65], v[160:161]\n\t" \
    "v_pk_add_f32 v[66:67], v[66:67], v[160:161]\n\t" \
    "v_pk_add_f32 v[68:69], v[68:69], v[160:161]\n\t" \
    "v_pk_add_f32 v[70:71], v[70:71], v[160:161]\n\t" \
    "v_pk_add_f32 v[72:73], v[72:73], v[160:161]\n\t" \
    "v_pk_add_f32 v[74:75], v[74:75], v[160:161]\n\t" \
    "v_pk_add_f32 v[76:77], v[76:77], v[160:161]\n\t" \
    "v_pk_add_f32 v[78:79], v[78:79], v[160:161]\n\t" \
    "v_pk_mul_f32 v[164:165], v[64:65], v[64:65]\n\t" \
    "v_pk_fma_f32 v[164:165], v[66:67], v[66:67], v[164:165]\n\t" \
    "v_pk_fma_f32 v[164:165], v[68:69], v[68:69], v[164:165]\n\t" \
    "v_pk_fma_f32 v[164:165], v[70:71], v[70:71], v[164:165]\n\t" \
    "v_pk_fma_f32 v[164:165], v[72:73], v[72:73], v[164:165]\n\t" \
    "v_pk_fma_f32 v[164:165], v[74:75], v[74:75], v[164:165]\n\t" \
    "v_pk_fma_f32 v[164:165], v[76:77], v[76:77], v[164:165]\n\t" \
    "v_pk_fma_f32 v[164:165], v[78:79], v[78:79], v[164:165]\n\t" \
    "v_add_f32 v164, v164, v165\n\t" \
    "s_nop 1\n\t" \
    "v_add_f32_dpp v164, v164, v164 quad_perm:[1,0,3,2] row_mask:0xf bank_mask:0xf\n\t" \
    "s_nop 1\n\t" \
    "v_add_f32_dpp v164, v164, v164 quad_perm:[2,3,0,1] row_mask:0xf bank_mask:0xf\n\t" \
    "s_nop 1\n\t" \
    "v_add_f32_dpp v164, v164, v164 row_half_mirror row_mask:0xf bank_mask:0xf\n\t" \
    "s_nop 1\n\t" \
    "v_add_f32_dpp v164, v164, v164 row_mirror row_mask:0xf bank_mask:0xf\n\t" \
    "v_mov_b32 v162, v164\n\t" \
    "s_nop 1\n\t" \
    "v_permlane16_swap_b32 v162, v164\n\t" \
    "v_add_f32 v164, v164, v162\n\t" \
    "v_mov_b32 v166, 0x3727c5ac\n\t" \
    "v_fmac_f32 v166, 0x3b000000, v164\n\t" \
    "v_rsq_f32 v166, v166\n\t" \
    "s_nop 0\n\t" \
    "v_mov_b32 v167, v166\n\t" \
    "v_pk_mul_f32 v[64:65], v[64:65], v[166:167]\n\t" \
    "v_pk_mul_f32 v[66:67], v[66:67], v[166:167]\n\t" \
    "v_pk_mul_f32 v[68:69], v[68:69], v[166:167]\n\t" \
    "v_pk_mul_f32 v[70:71], v[70:71], v[166:167]\n\t" \
    "v_pk_mul_f32 v[72:73], v[72:73], v[166:167]\n\t" \
    "v_pk_mul_f32 v[74:75], v[74:75], v[166:167]\n\t" \
    "v_pk_mul_f32 v[76:77], v[76:77], v[166:167]\n\t" \
    "v_pk_mul_f32 v[78:79], v[78:79], v[166:167]\n\t" \
    "v_cvt_pk_bf16_f32 v136, v64, v65\n\t" \
    "v_cvt_pk_bf16_f32 v137, v66, v67\n\t" \
    "v_cvt_pk_bf16_f32 v138, v68, v69\n\t" \
    "v_cvt_pk_bf16_f32 v139, v70, v71\n\t" \
    "v_cvt_pk_bf16_f32 v140, v72, v73\n\t" \
    "v_cvt_pk_bf16_f32 v141, v74, v75\n\t" \
    "v_cvt_pk_bf16_f32 v142, v76, v77\n\t" \
    "v_cvt_pk_bf16_f32 v143, v78, v79\n\t" \
    "v_accvgpr_read_b32 v52, a238\n\t" \
    "s_mov_b32 s28, 0x5600000\n\t" \
    "s_mov_b32 s29, 0x5602000\n\t" \
    "buffer_store_dwordx4 v[136:139], v52, s[8:11], s28 offen sc1\n\t" \
    "buffer_store_dwordx4 v[140:143], v52, s[8:11], s29 offen sc1\n\t" \
    "s_waitcnt vmcnt(0) lgkmcnt(0)\n\t" \
    "s_barrier\n\t" \
    "s_cmp_lg_u32 %[wv], 0\n\t" \
    "s_cbranch_scc1 S2_L_%=\n\t" \
    "v_mov_b32 v17, 1\n\t" \
    "s_mov_b64 exec, 1\n\t" \
    "buffer_atomic_add v17, off, s[8:11], %[cy] sc1\n\t" \
    "s_mov_b64 exec, -1\n\t" \
    "S2_L_%=:\n\t" \
    "s_min_u32 s28, %[nl], 0\n\t" \
    "s_lshl_b32 s28, s28, 15\n\t" \
    "s_add_u32 s35, s28, %[sw2]\n\t" \
    "s_mul_i32 s28, %[wv], 8192\n\t" \
    "s_add_u32 m0, s28, 131072\n\t" \
    "s_nop 0\n\t" \
    "buffer_load_dwordx4 v21, s[20:23], s35 offen offset:0 lds\n\t" \
    "buffer_load_dwordx4 v22, s[20:23], s35 offen offset:1024 lds\n\t" \
    "buffer_load_dwordx4 v23, s[20:23], s35 offen offset:2048 lds\n\t" \
    "buffer_load_dwordx4 v24, s[20:23], s35 offen offset:3072 lds\n\t" \
    "s_min_u32 s28, %[nl], 1\n\t" \
    "s_lshl_b32 s28, s28, 15\n\t" \
    "s_add_u32 s35, s28, %[sw2]\n\t" \
    "s_mul_i32 s28, %[wv], 8192\n\t" \
    "s_add_u32 m0, s28, 135168\n\t" \
    "s_nop 0\n\t" \
    "buffer_load_dwordx4 v21, s[20:23], s35 offen offset:0 lds\n\t" \
    "buffer_load_dwordx4 v22, s[20:23], s35 offen offset:1024 lds\n\t" \
    "buffer_load_dwordx4 v23, s[20:23], s35 offen offset:2048 lds\n\t" \
    "buffer_load_dwordx4 v24, s[20:23], s35 offen offset:3072 lds\n\t" \
    "s_min_u32 s28, %[nl], 2\n\t" \
    "s_lshl_b32 s28, s28, 15\n\t" \
    "s_add_u32 s35, s28, %[sw2]\n\t" \
    "s_mul_i32 s28, %[wv], 8192\n\t" \
    "s_add_u32 m0, s28, 98304\n\t" \
    "s_nop 0\n\t" \
    "buffer_load_dwordx4 v21, s[20:23], s35 offen offset:0 lds\n\t" \
    "buffer_load_dwordx4 v22, s[20:23], s35 offen offset:1024 lds\n\t" \
    "buffer_load_dwordx4 v23, s[20:23], s35 offen offset:2048 lds\n\t" \
    "buffer_load_dwordx4 v24, s[20:23], s35 offen offset:3072 lds\n\t" \
    "s_min_u32 s28, %[nl], 3\n\t" \
    "s_lshl_b32 s28, s28, 15\n\t" \
    "s_add_u32 s35, s28, %[sw2]\n\t" \
    "s_mul_i32 s28, %[wv], 8192\n\t" \
    "s_add_u32 m0, s28, 102400\n\t" \
    "s_nop 0\n\t" \
    "buffer_load_dwordx4 v21, s[20:23], s35 offen offset:0 lds\n\t" \
    "buffer_load_dwordx4 v22, s[20:23], s35 offen offset:1024 lds\n\t" \
    "buffer_load_dwordx4 v23, s[20:23], s35 offen offset:2048 lds\n\t" \
    "buffer_load_dwordx4 v24, s[20:23], s35 offen offset:3072 lds\n\t" \
    "s_cmp_lg_u32 %[wv], 0\n\t" \
    "s_cbranch_scc1 S2_OK_%=\n\t" \
    "s_mov_b32 s30, 0\n\t" \
    "S2_P_%=:\n\t" \
    "buffer_load_dword v17, off, s[8:11], %[cy] sc1\n\t" \
    "s_waitcnt vmcnt(0)\n\t" \
    "v_readfirstlane_b32 s28, v17\n\t" \
    "s_nop 0\n\t" \
    "s_cmp_ge_u32 s28, 16\n\t" \
    "s_cbranch_scc1 S2_OK_%=\n\t" \
    "s_add_u32 s30, s30, 1\n\t" \
    "s_cmp_gt_u32 s30, 0x40000\n\t" \
    "s_cbranch_scc1 S2_OK_%=\n\t" \
    "s_sleep 1\n\t" \
    "s_branch S2_P_%=\n\t" \
    "S2_OK_%=:\n\t" \
    "s_barrier\n\t" \
    "s_cmp_ge_u32 %[mm], 15\n\t" \
    "s_cbranch_scc1 IDLE_%=\n\t" \
    "s_add_u32 s33, %[sy], 0\n\t" \
    "buffer_load_dwordx4 a[0:3], v16, s[8:11], s33 offen offset:0 sc1\n\t" \
    "s_add_u32 s34, %[sy], 16384\n\t" \
    "buffer_load_dwordx4 a[4:7], v16, s[8:11], s34 offen offset:0 sc1\n\t" \
    "buffer_load_dwordx4 a[8:11], v16, s[8:11], s33 offen offset:1024 sc1\n\t" \
    "buffer_load_dwordx4 a[12:15], v16, s[8:11], s34 offen offset:1024 sc1\n\t" \
    "buffer_load_dwordx4 a[16:19], v16, s[8:11], s33 offen offset:2048 sc1\n\t" \
    "buffer_load_dwordx4 a[20:23], v16, s[8:11], s34 offen offset:2048 sc1\n\t" \
    "buffer_load_dwordx4 a[24:27], v16, s[8:11], s33 offen offset:3072 sc1\n\t" \
    "buffer_load_dwordx4 a[28:31], v16, s[8:11], s34 offen offset:3072 sc1\n\t" \
    "s_add_u32 s33, %[sy], 4096\n\t" \
    "buffer_load_dwordx4 a[32:35], v16, s[8:11], s33 offen offset:0 sc1\n\t" \
    "s_add_u32 s34, %[sy], 20480\n\t" \
    "buffer_load_dwordx4 a[36:39], v16, s[8:11], s34 offen offset:0 sc1\n\t" \
    "buffer_load_dwordx4 a[40:43], v16, s[8:11], s33 offen offset:1024 sc1\n\t" \
    "buffer_load_dwordx4 a[44:47], v16, s[8:11], s34 offen offset:1024 sc1\n\t" \
    "v_accvgpr_read_b32 v252, a243\n\t" \
    "v_add_u32 v253, 49152, v252\n\t" \
    "v_accvgpr_read_b32 v254, a244\n\t" \
    "v_add_u32 v27, 49152, v254\n\t" \
    "v_lshlrev_b32 v28, 16, v100\n\t" \
    "v_and_b32 v29, 0xffff0000, v100\n\t" \
    "v_lshlrev_b32 v30, 16, v101\n\t" \
    "v_and_b32 v31, 0xffff0000, v101\n\t" \
    "v_lshlrev_b32 v32, 16, v102\n\t" \
    "v_and_b32 v33, 0xffff0000, v102\n\t" \
    "v_lshlrev_b32 v34, 16, v103\n\t" \
    "v_and_b32 v35, 0xffff0000, v103\n\t" \
    "v_lshlrev_b32 v36, 16, v104\n\t" \
    "v_and_b32 v37, 0xffff0000, v104\n\t" \
    "v_lshlrev_b32 v38, 16, v105\n\t" \
    "v_and_b32 v39, 0xffff0000, v105\n\t" \
    "v_lshlrev_b32 v40, 16, v106\n\t" \
    "v_and_b32 v41, 0xffff0000, v106\n\t" \
    "v_lshlrev_b32 v42, 16, v107\n\t" \
    "v_and_b32 v43, 0xffff0000, v107\n\t" \
    "v_lshlrev_b32 v44, 16, v108\n\t" \
    "v_and_b32 v45, 0xffff0000, v108\n\t" \
    "v_lshlrev_b32 v46, 16, v109\n\t" \
    "v_and_b32 v47, 0xffff0000, v109\n\t" \
    "v_lshlrev_b32 v48, 16, v110\n\t" \
    "v_and_b32 v49, 0xffff0000, v110\n\t" \
    "v_lshlrev_b32 v50, 16, v111\n\t" \
    "v_and_b32 v51, 0xffff0000, v111\n\t" \
    "ds_read_b64_tr_b16 v[52:53], v252 offset:0\n\t" \
    "ds_read_b64_tr_b16 v[54:55], v252 offset:768\n\t" \
    "ds_read_b64_tr_b16 v[56:57], v254 offset:32\n\t" \
    "ds_read_b64_tr_b16 v[58:59], v254 offset:800\n\t" \
    "ds_read_b64_tr_b16 v[60:61], v252 offset:64\n\t" \
    "ds_read_b64_tr_b16 v[62:63], v252 offset:832\n\t" \
    "ds_read_b64_tr_b16 v[64:65], v254 offset:96\n\t" \
    "ds_read_b64_tr_b16 v[66:67], v254 offset:864\n\t" \
    "ds_read_b64_tr_b16 v[68:69], v252 offset:128\n\t" \
    "ds_read_b64_tr_b16 v[70:71], v252 offset:896\n\t" \
    "ds_read_b64_tr_b16 v[72:73], v254 offset:160\n\t" \
    "ds_read_b64_tr_b16 v[74:75], v254 offset:928\n\t" \
    "s_waitcnt vmcnt(11) lgkmcnt(10)\n\t" \
    "v_mfma_f32_16x16x32_bf16 v[112:115], v[52:55], a[0:3], v[28:31]\n\t" \
    "ds_read_b64_tr_b16 v[76:77], v252 offset:6144\n\t" \
    "s_waitcnt vmcnt(10)\n\t" \
    "v_mfma_f32_16x16x32_bf16 v[116:119], v[52:55], a[4:7], v[28:31]\n\t" \
    "ds_read_b64_tr_b16 v[78:79], v252 offset:6912\n\t" \
    "s_waitcnt lgkmcnt(10)\n\t" \
    "v_mfma_f32_16x16x32_bf16 v[120:123], v[56:59], a[0:3], v[32:35]\n\t" \
    "ds_read_b64_tr_b16 v[80:81], v254 offset:6176\n\t" \
    "buffer_load_dwordx4 a[48:51], v16, s[8:11], s33 offen offset:2048 sc1\n\t" \
    "v_mfma_f32_16x16x32_bf16 v[124:127], v[56:59], a[4:7], v[32:35]\n\t" \
    "ds_read_b64_tr_b16 v[82:83], v254 offset:6944\n\t" \
    "s_waitcnt lgkmcnt(10)\n\t" \
    "v_mfma_f32_16x16x32_bf16 v[128:131], v[60:63], a[0:3], v[36:39]\n\t" \
    "ds_read_b64_tr_b16 v[84:85], v252 offset:6208\n\t" \
    "v_mfma_f32_16x16x32_bf16 v[132:135], v[60:63], a[4:7], v[36:39]\n\t" \
    "ds_read_b64_tr_b16 v[86:87], v252 offset:6976\n\t" \
    "s_waitcnt lgkmcnt(10)\n\t" \
    "v_mfma_f32_16x16x32_bf16 v[136:139], v[64:67], a[0:3], v[40:43]\n\t" \
    "ds_read_b64_tr_b16 v[88:89], v254 offset:6240\n\t" \
    "v_mfma_f32_16x16x32_bf16 v[140:143], v[64:67], a[4:7], v[40:43]\n\t" \
    "ds_read_b64_tr_b16 v[90:91], v254 offset:7008\n\t" \
    "s_waitcnt lgkmcnt(10)\n\t" \
    "v_mfma_f32_16x16x32_bf16 v[144:147], v[68:71], a[0:3], v[44:47]\n\t" \
    "ds_read_b64_tr_b16 v[92:93], v252 offset:6272\n\t" \
    "buffer_load_dwordx4 a[52:55], v16, s[8:11], s34 offen offset:2048 sc1\n\t" \
    "v_mfma_f32_16x16x32_bf16 v[148:151], v[68:71], a[4:7], v[44:47]\n\t" \
    "ds_read_b64_tr_b16 v[94:95], v252 offset:7040\n\t" \
    "s_waitcnt lgkmcnt(10)\n\t" \
    "v_mfma_f32_16x16x32_bf16 v[152:155], v[72:75], a[0:3], v[48:51]\n\t" \
    "ds_read_b64_tr_b16 v[96:97], v254 offset:6304\n\t" \
    "v_mfma_f32_16x16x32_bf16 v[156:159], v[72:75], a[4:7], v[48:51]\n\t" \
    "ds_read_b64_tr_b16 v[98:99], v254 offset:7072\n\t" \
    "s_waitcnt vmcnt(11) lgkmcnt(10)\n\t" \
    "v_mfma_f32_16x16x32_bf16 v[112:115], v[76:79], a[8:11], v[112:115]\n\t" \
    "ds_read_b64_tr_b16 v[52:53], v252 offset:12288\n\t" \
    "s_waitcnt vmcnt(10)\n\t" \
    "v_mfma_f32_16x16x32_bf16 v[116:119], v[76:79], a[12:15], v[116:119]\n\t" \
    "ds_read_b64_tr_b16 v[54:55], v252 offset:13056\n\t" \
    "s_waitcnt lgkmcnt(10)\n\t" \
    "v_mfma_f32_16x16x32_bf16 v[120:123], v[80:83], a[8:11], v[120:123]\n\t" \
    "ds_read_b64_tr_b16 v[56:57], v254 offset:12320\n\t" \
    "buffer_load_dwordx4 a[56:59], v16, s[8:11], s33 offen offset:3072 sc1\n\t" \
    "v_mfma_f32_16x16x32_bf16 v[124:127], v[80:83], a[12:15], v[124:127]\n\t" \
    "ds_read_b64_tr_b16 v[58:59], v254 offset:13088\n\t" \
    "s_waitcnt lgkmcnt(10)\n\t" \
    "v_mfma_f32_16x16x32_bf16 v[128:131], v[84:87], a[8:11], v[128:131]\n\t" \
    "ds_read_b64_tr_b16 v[60:61], v252 offset:12352\n\t" \
    "v_mfma_f32_16x16x32_bf16 v[132:135], v[84:87], a[12:15], v[132:135]\n\t" \
    "ds_read_b64_tr_b16 v[62:63], v252 offset:13120\n\t" \
    "s_waitcnt lgkmcnt(10)\n\t" \
    "v_mfma_f32_16x16x32_bf16 v[136:139], v[88:91], a[8:11], v[136:139]\n\t" \
    "ds_read_b64_tr_b16 v[64:65], v254 offset:12384\n\t" \
    "v_mfma_f32_16x16x32_bf16 v[140:143], v[88:91], a[12:15], v[140:143]\n\t" \
    "ds_read_b64_tr_b16 v[66:67], v254 offset:13152\n\t" \
    "s_waitcnt lgkmcnt(10)\n\t" \
    "v_mfma_f32_16x16x32_bf16 v[144:147], v[92:95], a[8:11], v[144:147]\n\t" \
    "ds_read_b64_tr_b16 v[68:69], v252 offset:12416\n\t" \
    "buffer_load_dwordx4 a[60:63], v16, s[8:11], s34 offen offset:3072 sc1\n\t" \
    "v_mfma_f32_16x16x32_bf16 v[148:151], v[92:95], a[12:15], v[148:151]\n\t" \
    "ds_read_b64_tr_b16 v[70:71], v252 offset:13184\n\t" \
    "s_waitcnt lgkmcnt(10)\n\t" \
    "v_mfma_f32_16x16x32_bf16 v[152:155], v[96:99], a[8:11], v[152:155]\n\t" \
    "ds_read_b64_tr_b16 v[72:73], v254 offset:12448\n\t" \
    "v_mfma_f32_16x16x32_bf16 v[156:159], v[96:99], a[12:15], v[156:159]\n\t" \
    "ds_read_b64_tr_b16 v[74:75], v254 offset:13216\n\t" \
    "s_waitcnt vmcnt(11) lgkmcnt(10)\n\t" \
    "v_mfma_f32_16x16x32_bf16 v[112:115], v[52:55], a[16:19], v[112:115]\n\t" \
    "ds_read_b64_tr_b16 v[76:77], v252 offset:18432\n\t" \
    "s_waitcnt vmcnt(10)\n\t" \
    "v_mfma_f32_16x16x32_bf16 v[116:119], v[52:55], a[20:23], v[116:119]\n\t" \
    "ds_read_b64_tr_b16 v[78:79], v252 offset:19200\n\t" \
    "s_add_u32 s33, %[sy], 8192\n\t" \
    "s_waitcnt lgkmcnt(10)\n\t" \
    "v_mfma_f32_16x16x32_bf16 v[120:123], v[56:59], a[16:19], v[120:123]\n\t" \
    "ds_read_b64_tr_b16 v[80:81], v254 offset:18464\n\t" \
    "buffer_load_dwordx4 a[64:67], v16, s[8:11], s33 offen offset:0 sc1\n\t" \
    "v_mfma_f32_16x16x32_bf16 v[124:127], v[56:59], a[20:23], v[124:127]\n\t" \
    "ds_read_b64_tr_b16 v[82:83], v254 offset:19232\n\t" \
    "s_waitcnt lgkmcnt(10)\n\t" \
    "v_mfma_f32_16x16x32_bf16 v[128:131], v[60:63], a[16:19], v[128:131]\n\t" \
    "ds_read_b64_tr_b16 v[84:85], v252 offset:18496\n\t" \
    "v_mfma_f32_16x16x32_bf16 v[132:135], v[60:63], a[20:23], v[132:135]\n\t" \
    "ds_read_b64_tr_b16 v[86:87], v252 offset:19264\n\t" \
    "s_waitcnt lgkmcnt(10)\n\t" \
    "v_mfma_f32_16x16x32_bf16 v[136:139], v[64:67], a[16:19], v[136:139]\n\t" \
    "ds_read_b64_tr_b16 v[88:89], v254 offset:18528\n\t" \
    "v_mfma_f32_16x16x32_bf16 v[140:143], v[64:67], a[20:23], v[140:143]\n\t" \
    "ds_read_b64_tr_b16 v[90:91], v254 offset:19296\n\t" \
    "s_add_u32 s34, %[sy], 24576\n\t" \
    "s_waitcnt lgkmcnt(10)\n\t" \
    "v_mfma_f32_16x16x32_bf16 v[144:147], v[68:71], a[16:19], v[144:147]\n\t" \
    "ds_read_b64_tr_b16 v[92:93], v252 offset:18560\n\t" \
    "buffer_load_dwordx4 a[68:71], v16, s[8:11], s34 offen offset:0 sc1\n\t" \
    "v_mfma_f32_16x16x32_bf16 v[148:151], v[68:71], a[20:23], v[148:151]\n\t" \
    "ds_read_b64_tr_b16 v[94:95], v252 offset:19328\n\t" \
    "s_waitcnt lgkmcnt(10)\n\t" \
    "v_mfma_f32_16x16x32_bf16 v[152:155], v[72:75], a[16:19], v[152:155]\n\t" \
    "ds_read_b64_tr_b16 v[96:97], v254 offset:18592\n\t" \
    "v_mfma_f32_16x16x32_bf16 v[156:159], v[72:75], a[20:23], v[156:159]\n\t" \
    "ds_read_b64_tr_b16 v[98:99], v254 offset:19360\n\t" \
    "s_waitcnt vmcnt(11) lgkmcnt(10)\n\t" \
    "v_mfma_f32_16x16x32_bf16 v[112:115], v[76:79], a[24:27], v[112:115]\n\t" \
    "ds_read_b64_tr_b16 v[52:53], v252 offset:24576\n\t" \
    "s_waitcnt vmcnt(10)\n\t" \
    "v_mfma_f32_16x16x32_bf16 v[116:119], v[76:79], a[28:31], v[116:119]\n\t" \
    "ds_read_b64_tr_b16 v[54:55], v252 offset:25344\n\t" \
    "s_waitcnt lgkmcnt(10)\n\t" \
    "v_mfma_f32_16x16x32_bf16 v[120:123], v[80:83], a[24:27], v[120:123]\n\t" \
    "ds_read_b64_tr_b16 v[56:57], v254 offset:24608\n\t" \
    "buffer_load_dwordx4 a[72:75], v16, s[8:11], s33 offen offset:1024 sc1\n\t" \
    "v_mfma_f32_16x16x32_bf16 v[124:127], v[80:83], a[28:31], v[124:127]\n\t" \
    "ds_read_b64_tr_b16 v[58:59], v254 offset:25376\n\t" \
    "s_waitcnt lgkmcnt(10)\n\t" \
    "v_mfma_f32_16x16x32_bf16 v[128:131], v[84:87], a[24:27], v[128:131]\n\t" \
    "ds_read_b64_tr_b16 v[60:61], v252 offset:24640\n\t" \
    "v_mfma_f32_16x16x32_bf16 v[132:135], v[84:87], a[28:31], v[132:135]\n\t" \
    "ds_read_b64_tr_b16 v[62:63], v252 offset:25408\n\t" \
    "s_waitcnt lgkmcnt(10)\n\t" \
    "v_mfma_f32_16x16x32_bf16 v[136:139], v[88:91], a[24:27], v[136:139]\n\t" \
    "ds_read_b64_tr_b16 v[64:65], v254 offset:24672\n\t" \
    "v_mfma_f32_16x16x32_bf16 v[140:143], v[88:91], a[28:31], v[140:143]\n\t" \
    "ds_read_b64_tr_b16 v[66:67], v254 offset:25440\n\t" \
    "s_waitcnt lgkmcnt(10)\n\t" \
    "v_mfma_f32_16x16x32_bf16 v[144:147], v[92:95], a[24:27], v[144:147]\n\t" \
    "ds_read_b64_tr_b16 v[68:69], v252 offset:24704\n\t" \
    "buffer_load_dwordx4 a[76:79], v16, s[8:11], s34 offen offset:1024 sc1\n\t" \
    "v_mfma_f32_16x16x32_bf16 v[148:151], v[92:95], a[28:31], v[148:151]\n\t" \
    "ds_read_b64_tr_b16 v[70:71], v252 offset:25472\n\t" \
    "s_waitcnt lgkmcnt(10)\n\t" \
    "v_mfma_f32_16x16x32_bf16 v[152:155], v[96:99], a[24:27], v[152:155]\n\t" \
    "ds_read_b64_tr_b16 v[72:73], v254 offset:24736\n\t" \
    "v_mfma_f32_16x16x32_bf16 v[156:159], v[96:99], a[28:31], v[156:159]\n\t" \
    "ds_read_b64_tr_b16 v[74:75], v254 offset:25504\n\t" \
    "s_waitcnt vmcnt(11) lgkmcnt(10)\n\t" \
    "v_mfma_f32_16x16x32_bf16 v[112:115], v[52:55], a[32:35], v[112:115]\n\t" \
    "ds_read_b64_tr_b16 v[76:77], v252 offset:30720\n\t" \
    "s_waitcnt vmcnt(10)\n\t" \
    "v_mfma_f32_16x16x32_bf16 v[116:119], v[52:55], a[36:39], v[116:119]\n\t" \
    "ds_read_b64_tr_b16 v[78:79], v252 offset:31488\n\t" \
    "s_waitcnt lgkmcnt(10)\n\t" \
    "v_mfma_f32_16x16x32_bf16 v[120:123], v[56:59], a[32:35], v[120:123]\n\t" \
    "ds_read_b64_tr_b16 v[80:81], v254 offset:30752\n\t" \
    "buffer_load_dwordx4 a[80:83], v16, s[8:11], s33 offen offset:2048 sc1\n\t" \
    "v_mfma_f32_16x16x32_bf16 v[124:127], v[56:59], a[36:39], v[124:127]\n\t" \
    "ds_read_b64_tr_b16 v[82:83], v254 offset:31520\n\t" \
    "s_waitcnt lgkmcnt(10)\n\t" \
    "v_mfma_f32_16x16x32_bf16 v[128:131], v[60:63], a[32:35], v[128:131]\n\t" \
    "ds_read_b64_tr_b16 v[84:85], v252 offset:30784\n\t" \
    "v_mfma_f32_16x16x32_bf16 v[132:135], v[60:63], a[36:39], v[132:135]\n\t" \
    "ds_read_b64_tr_b16 v[86:87], v252 offset:31552\n\t" \
    "s_waitcnt lgkmcnt(10)\n\t" \
    "v_mfma_f32_16x16x32_bf16 v[136:139], v[64:67], a[32:35], v[136:139]\n\t" \
    "ds_read_b64_tr_b16 v[88:89], v254 offset:30816\n\t" \
    "v_mfma_f32_16x16x32_bf16 v[140:143], v[64:67], a[36:39], v[140:143]\n\t" \
    "ds_read_b64_tr_b16 v[90:91], v254 offset:31584\n\t" \
    "s_waitcnt lgkmcnt(10)\n\t" \
    "v_mfma_f32_16x16x32_bf16 v[144:147], v[68:71], a[32:35], v[144:147]\n\t" \
    "ds_read_b64_tr_b16 v[92:93], v252 offset:30848\n\t" \
    "buffer_load_dwordx4 a[84:87], v16, s[8:11], s34 offen offset:2048 sc1\n\t" \
    "v_mfma_f32_16x16x32_bf16 v[148:151], v[68:71], a[36:39], v[148:151]\n\t" \
    "ds_read_b64_tr_b16 v[94:95], v252 offset:31616\n\t" \
    "s_waitcnt lgkmcnt(10)\n\t" \
    "v_mfma_f32_16x16x32_bf16 v[152:155], v[72:75], a[32:35], v[152:155]\n\t" \
    "ds_read_b64_tr_b16 v[96:97], v254 offset:30880\n\t" \
    "v_mfma_f32_16x16x32_bf16 v[156:159], v[72:75], a[36:39], v[156:159]\n\t" \
    "ds_read_b64_tr_b16 v[98:99], v254 offset:31648\n\t" \
    "s_waitcnt vmcnt(11) lgkmcnt(10)\n\t" \
    "v_mfma_f32_16x16x32_bf16 v[112:115], v[76:79], a[40:43], v[112:115]\n\t" \
    "ds_read_b64_tr_b16 v[52:53], v252 offset:36864\n\t" \
    "s_waitcnt vmcnt(10)\n\t" \
    "v_mfma_f32_16x16x32_bf16 v[116:119], v[76:79], a[44:47], v[116:119]\n\t" \
    "ds_read_b64_tr_b16 v[54:55], v252 offset:37632\n\t" \
    "s_waitcnt lgkmcnt(10)\n\t" \
    "v_mfma_f32_16x16x32_bf16 v[120:123], v[80:83], a[40:43], v[120:123]\n\t" \
    "ds_read_b64_tr_b16 v[56:57], v254 offset:36896\n\t" \
    "buffer_load_dwordx4 a[88:91], v16, s[8:11], s33 offen offset:3072 sc1\n\t" \
    "v_mfma_f32_16x16x32_bf16 v[124:127], v[80:83], a[44:47], v[124:127]\n\t" \
    "ds_read_b64_tr_b16 v[58:59], v254 offset:37664\n\t" \
    "s_waitcnt lgkmcnt(10)\n\t" \
    "v_mfma_f32_16x16x32_bf16 v[128:131], v[84:87], a[40:43], v[128:131]\n\t" \
    "ds_read_b64_tr_b16 v[60:61], v252 offset:36928\n\t" \
    "v_mfma_f32_16x16x32_bf16 v[132:135], v[84:87], a[44:47], v[132:135]\n\t" \
    "ds_read_b64_tr_b16 v[62:63], v252 offset:37696\n\t" \
    "s_waitcnt lgkmcnt(10)\n\t" \
    "v_mfma_f32_16x16x32_bf16 v[136:139], v[88:91], a[40:43], v[136:139]\n\t" \
    "ds_read_b64_tr_b16 v[64:65], v254 offset:36960\n\t" \
    "v_mfma_f32_16x16x32_bf16 v[140:143], v[88:91], a[44:47], v[140:143]\n\t" \
    "ds_read_b64_tr_b16 v[66:67], v254 offset:37728\n\t" \
    "s_waitcnt lgkmcnt(10)\n\t" \
    "v_mfma_f32_16x16x32_bf16 v[144:147], v[92:95], a[40:43], v[144:147]\n\t" \
    "ds_read_b64_tr_b16 v[68:69], v252 offset:36992\n\t" \
    "buffer_load_dwordx4 a[92:95], v16, s[8:11], s34 offen offset:3072 sc1\n\t" \
    "v_mfma_f32_16x16x32_bf16 v[148:151], v[92:95], a[44:47], v[148:151]\n\t" \
    "ds_read_b64_tr_b16 v[70:71], v252 offset:37760\n\t" \
    "s_waitcnt lgkmcnt(10)\n\t" \
    "v_mfma_f32_16x16x32_bf16 v[152:155], v[96:99], a[40:43], v[152:155]\n\t" \
    "ds_read_b64_tr_b16 v[72:73], v254 offset:37024\n\t" \
    "v_mfma_f32_16x16x32_bf16 v[156:159], v[96:99], a[44:47], v[156:159]\n\t" \
    "ds_read_b64_tr_b16 v[74:75], v254 offset:37792\n\t" \
    "s_waitcnt vmcnt(11) lgkmcnt(10)\n\t" \
    "v_mfma_f32_16x16x32_bf16 v[112:115], v[52:55], a[48:51], v[112:115]\n\t" \
    "ds_read_b64_tr_b16 v[76:77], v252 offset:43008\n\t" \
    "s_waitcnt vmcnt(10)\n\t" \
    "v_mfma_f32_16x16x32_bf16 v[116:119], v[52:55], a[52:55], v[116:119]\n\t" \
    "ds_read_b64_tr_b16 v[78:79], v252 offset:43776\n\t" \
    "s_add_u32 s33, %[sy], 12288\n\t" \
    "s_waitcnt lgkmcnt(10)\n\t" \
    "v_mfma_f32_16x16x32_bf16 v[120:123], v[56:59], a[48:51], v[120:123]\n\t" \
    "ds_read_b64_tr_b16 v[80:81], v254 offset:43040\n\t" \
    "buffer_load_dwordx4 a[96:99], v16, s[8:11], s33 offen offset:0 sc1\n\t" \
    "v_mfma_f32_16x16x32_bf16 v[124:127], v[56:59], a[52:55], v[124:127]\n\t" \
    "ds_read_b64_tr_b16 v[82:83], v254 offset:43808\n\t" \
    "s_waitcnt lgkmcnt(10)\n\t" \
    "v_mfma_f32_16x16x32_bf16 v[128:131], v[60:63], a[48:51], v[128:131]\n\t" \
    "ds_read_b64_tr_b16 v[84:85], v252 offset:43072\n\t" \
    "v_mfma_f32_16x16x32_bf16 v[132:135], v[60:63], a[52:55], v[132:135]\n\t" \
    "ds_read_b64_tr_b16 v[86:87], v252 offset:43840\n\t" \
    "s_waitcnt lgkmcnt(10)\n\t" \
    "v_mfma_f32_16x16x32_bf16 v[136:139], v[64:67], a[48:51], v[136:139]\n\t" \
    "ds_read_b64_tr_b16 v[88:89], v254 offset:43104\n\t" \
    "v_mfma_f32_16x16x32_bf16 v[140:143], v[64:67], a[52:55], v[140:143]\n\t" \
    "ds_read_b64_tr_b16 v[90:91], v254 offset:43872\n\t" \
    "s_add_u32 s34, %[sy], 28672\n\t" \
    "s_waitcnt lgkmcnt(10)\n\t" \
    "v_mfma_f32_16x16x32_bf16 v[144:147], v[68:71], a[48:51], v[144:147]\n\t" \
    "ds_read_b64_tr_b16 v[92:93], v252 offset:43136\n\t" \
    "buffer_load_dwordx4 a[100:103], v16, s[8:11], s34 offen offset:0 sc1\n\t" \
    "v_mfma_f32_16x16x32_bf16 v[148:151], v[68:71], a[52:55], v[148:151]\n\t" \
    "ds_read_b64_tr_b16 v[94:95], v252 offset:43904\n\t" \
    "s_waitcnt lgkmcnt(10)\n\t" \
    "v_mfma_f32_16x16x32_bf16 v[152:155], v[72:75], a[48:51], v[152:155]\n\t" \
    "ds_read_b64_tr_b16 v[96:97], v254 offset:43168\n\t" \
    "v_mfma_f32_16x16x32_bf16 v[156:159], v[72:75], a[52:55], v[156:159]\n\t" \
    "ds_read_b64_tr_b16 v[98:99], v254 offset:43936\n\t" \
    "s_waitcnt vmcnt(11) lgkmcnt(10)\n\t" \
    "v_mfma_f32_16x16x32_bf16 v[112:115], v[76:79], a[56:59], v[112:115]\n\t" \
    "ds_read_b64_tr_b16 v[52:53], v253 offset:0\n\t" \
    "s_waitcnt vmcnt(10)\n\t" \
    "v_mfma_f32_16x16x32_bf16 v[116:119], v[76:79], a[60:63], v[116:119]\n\t" \
    "ds_read_b64_tr_b16 v[54:55], v253 offset:768\n\t" \
    "s_waitcnt lgkmcnt(10)\n\t" \
    "v_mfma_f32_16x16x32_bf16 v[120:123], v[80:83], a[56:59], v[120:123]\n\t" \
    "ds_read_b64_tr_b16 v[56:57], v27 offset:32\n\t" \
    "buffer_load_dwordx4 a[104:107], v16, s[8:11], s33 offen offset:1024 sc1\n\t" \
    "v_mfma_f32_16x16x32_bf16 v[124:127], v[80:83], a[60:63], v[124:127]\n\t" \
    "ds_read_b64_tr_b16 v[58:59], v27 offset:800\n\t" \
    "s_waitcnt lgkmcnt(10)\n\t" \
    "v_mfma_f32_16x16x32_bf16 v[128:131], v[84:87], a[56:59], v[128:131]\n\t" \
    "ds_read_b64_tr_b16 v[60:61], v253 offset:64\n\t" \
    "v_mfma_f32_16x16x32_bf16 v[132:135], v[84:87], a[60:63], v[132:135]\n\t" \
    "ds_read_b64_tr_b16 v[62:63], v253 offset:832\n\t" \
    "s_waitcnt lgkmcnt(10)\n\t" \
    "v_mfma_f32_16x16x32_bf16 v[136:139], v[88:91], a[56:59], v[136:139]\n\t" \
    "ds_read_b64_tr_b16 v[64:65], v27 offset:96\n\t" \
    "v_mfma_f32_16x16x32_bf16 v[140:143], v[88:91], a[60:63], v[140:143]\n\t" \
    "ds_read_b64_tr_b16 v[66:67], v27 offset:864\n\t" \
    "s_waitcnt lgkmcnt(10)\n\t" \
    "v_mfma_f32_16x16x32_bf16 v[144:147], v[92:95], a[56:59], v[144:147]\n\t" \
    "ds_read_b64_tr_b16 v[68:69], v253 offset:128\n\t" \
    "buffer_load_dwordx4 a[108:111], v16, s[8:11], s34 offen offset:1024 sc1\n\t" \
    "v_mfma_f32_16x16x32_bf16 v[148:151], v[92:95], a[60:63], v[148:151]\n\t" \
    "ds_read_b64_tr_b16 v[70:71], v253 offset:896\n\t" \
    "s_waitcnt lgkmcnt(10)\n\t" \
    "v_mfma_f32_16x16x32_bf16 v[152:155], v[96:99], a[56:59], v[152:155]\n\t" \
    "ds_read_b64_tr_b16 v[72:73], v27 offset:160\n\t" \
    "v_mfma_f32_16x16x32_bf16 v[156:159], v[96:99], a[60:63], v[156:159]\n\t" \
    "ds_read_b64_tr_b16 v[74:75], v27 offset:928\n\t" \
    "s_waitcnt vmcnt(11) lgkmcnt(10)\n\t" \
    "v_mfma_f32_16x16x32_bf16 v[112:115], v[52:55], a[64:67], v[112:115]\n\t" \
    "ds_read_b64_tr_b16 v[76:77], v253 offset:6144\n\t" \
    "s_waitcnt vmcnt(10)\n\t" \
    "v_mfma_f32_16x16x32_bf16 v[116:119], v[52:55], a[68:71], v[116:119]\n\t" \
    "ds_read_b64_tr_b16 v[78:79], v253 offset:6912\n\t" \
    "s_waitcnt lgkmcnt(10)\n\t" \
    "v_mfma_f32_16x16x32_bf16 v[120:123], v[56:59], a[64:67], v[120:123]\n\t" \
    "ds_read_b64_tr_b16 v[80:81], v27 offset:6176\n\t" \
    "buffer_load_dwordx4 a[112:115], v16, s[8:11], s33 offen offset:2048 sc1\n\t" \
    "v_mfma_f32_16x16x32_bf16 v[124:127], v[56:59], a[68:71], v[124:127]\n\t" \
    "ds_read_b64_tr_b16 v[82:83], v27 offset:6944\n\t" \
    "s_waitcnt lgkmcnt(10)\n\t" \
    "v_mfma_f32_16x16x32_bf16 v[128:131], v[60:63], a[64:67], v[128:131]\n\t" \
    "ds_read_b64_tr_b16 v[84:85], v253 offset:6208\n\t" \
    "v_mfma_f32_16x16x32_bf16 v[132:135], v[60:63], a[68:71], v[132:135]\n\t" \
    "ds_read_b64_tr_b16 v[86:87], v253 offset:6976\n\t" \
    "s_waitcnt lgkmcnt(10)\n\t" \
    "v_mfma_f32_16x16x32_bf16 v[136:139], v[64:67], a[64:67], v[136:139]\n\t" \
    "ds_read_b64_tr_b16 v[88:89], v27 offset:6240\n\t" \
    "v_mfma_f32_16x16x32_bf16 v[140:143], v[64:67], a[68:71], v[140:143]\n\t" \
    "ds_read_b64_tr_b16 v[90:91], v27 offset:7008\n\t" \
    "s_waitcnt lgkmcnt(10)\n\t" \
    "v_mfma_f32_16x16x32_bf16 v[144:147], v[68:71], a[64:67], v[144:147]\n\t" \
    "ds_read_b64_tr_b16 v[92:93], v253 offset:6272\n\t" \
    "buffer_load_dwordx4 a[116:119], v16, s[8:11], s34 offen offset:2048 sc1\n\t" \
    "v_mfma_f32_16x16x32_bf16 v[148:151], v[68:71], a[68:71], v[148:151]\n\t" \
    "ds_read_b64_tr_b16 v[94:95], v253 offset:7040\n\t" \
    "s_waitcnt lgkmcnt(10)\n\t" \
    "v_mfma_f32_16x16x32_bf16 v[152:155], v[72:75], a[64:67], v[152:155]\n\t" \
    "ds_read_b64_tr_b16 v[96:97], v27 offset:6304\n\t" \
    "v_mfma_f32_16x16x32_bf16 v[156:159], v[72:75], a[68:71], v[156:159]\n\t" \
    "ds_read_b64_tr_b16 v[98:99], v27 offset:7072\n\t" \
    "s_waitcnt vmcnt(11) lgkmcnt(10)\n\t" \
    "v_mfma_f32_16x16x32_bf16 v[112:115], v[76:79], a[72:75], v[112:115]\n\t" \
    "ds_read_b64_tr_b16 v[52:53], v253 offset:12288\n\t" \
    "s_waitcnt vmcnt(10)\n\t" \
    "v_mfma_f32_16x16x32_bf16 v[116:119], v[76:79], a[76:79], v[116:119]\n\t" \
    "ds_read_b64_tr_b16 v[54:55], v253 offset:13056\n\t" \
    "s_waitcnt lgkmcnt(10)\n\t" \
    "v_mfma_f32_16x16x32_bf16 v[120:123], v[80:83], a[72:75], v[120:123]\n\t" \
    "ds_read_b64_tr_b16 v[56:57], v27 offset:12320\n\t" \
    "buffer_load_dwordx4 a[120:123], v16, s[8:11], s33 offen offset:3072 sc1\n\t" \
    "v_mfma_f32_16x16x32_bf16 v[124:127], v[80:83], a[76:79], v[124:127]\n\t" \
    "ds_read_b64_tr_b16 v[58:59], v27 offset:13088\n\t" \
    "s_waitcnt lgkmcnt(10)\n\t" \
    "v_mfma_f32_16x16x32_bf16 v[128:131], v[84:87], a[72:75], v[128:131]\n\t" \
    "ds_read_b64_tr_b16 v[60:61], v253 offset:12352\n\t" \
    "v_mfma_f32_16x16x32_bf16 v[132:135], v[84:87], a[76:79], v[132:135]\n\t" \
    "ds_read_b64_tr_b16 v[62:63], v253 offset:13120\n\t" \
    "s_waitcnt lgkmcnt(10)\n\t" \
    "v_mfma_f32_16x16x32_bf16 v[136:139], v[88:91], a[72:75], v[136:139]\n\t" \
    "ds_read_b64_tr_b16 v[64:65], v27 offset:12384\n\t" \
    "v_mfma_f32_16x16x32_bf16 v[140:143], v[88:91], a[76:79], v[140:143]\n\t" \
    "ds_read_b64_tr_b16 v[66:67], v27 offset:13152\n\t" \
    "s_waitcnt lgkmcnt(10)\n\t" \
    "v_mfma_f32_16x16x32_bf16 v[144:147], v[92:95], a[72:75], v[144:147]\n\t" \
    "ds_read_b64_tr_b16 v[68:69], v253 offset:12416\n\t" \
    "buffer_load_dwordx4 a[124:127], v16, s[8:11], s34 offen offset:3072 sc1\n\t" \
    "v_mfma_f32_16x16x32_bf16 v[148:151], v[92:95], a[76:79], v[148:151]\n\t" \
    "ds_read_b64_tr_b16 v[70:71], v253 offset:13184\n\t" \
    "s_waitcnt lgkmcnt(10)\n\t" \
    "v_mfma_f32_16x16x32_bf16 v[152:155], v[96:99], a[72:75], v[152:155]\n\t" \
    "ds_read_b64_tr_b16 v[72:73], v27 offset:12448\n\t" \
    "v_mfma_f32_16x16x32_bf16 v[156:159], v[96:99], a[76:79], v[156:159]\n\t" \
    "ds_read_b64_tr_b16 v[74:75], v27 offset:13216\n\t" \
    "s_waitcnt vmcnt(11) lgkmcnt(10)\n\t" \
    "v_mfma_f32_16x16x32_bf16 v[112:115], v[52:55], a[80:83], v[112:115]\n\t" \
    "ds_read_b64_tr_b16 v[76:77], v253 offset:18432\n\t" \
    "s_waitcnt vmcnt(10)\n\t" \
    "v_mfma_f32_16x16x32_bf16 v[116:119], v[52:55], a[84:87], v[116:119]\n\t" \
    "ds_read_b64_tr_b16 v[78:79], v253 offset:19200\n\t" \
    "s_waitcnt lgkmcnt(10)\n\t" \
    "v_mfma_f32_16x16x32_bf16 v[120:123], v[56:59], a[80:83], v[120:123]\n\t" \
    "ds_read_b64_tr_b16 v[80:81], v27 offset:18464\n\t" \
    "v_mfma_f32_16x16x32_bf16 v[124:127], v[56:59], a[84:87], v[124:127]\n\t" \
    "ds_read_b64_tr_b16 v[82:83], v27 offset:19232\n\t" \
    "s_waitcnt lgkmcnt(10)\n\t" \
    "v_mfma_f32_16x16x32_bf16 v[128:131], v[60:63], a[80:83], v[128:131]\n\t" \
    "ds_read_b64_tr_b16 v[84:85], v253 offset:18496\n\t" \
    "v_mfma_f32_16x16x32_bf16 v[132:135], v[60:63], a[84:87], v[132:135]\n\t" \
    "ds_read_b64_tr_b16 v[86:87], v253 offset:19264\n\t" \
    "s_waitcnt lgkmcnt(10)\n\t" \
    "v_mfma_f32_16x16x32_bf16 v[136:139], v[64:67], a[80:83], v[136:139]\n\t" \
    "ds_read_b64_tr_b16 v[88:89], v27 offset:18528\n\t" \
    "v_mfma_f32_16x16x32_bf16 v[140:143], v[64:67], a[84:87], v[140:143]\n\t" \
    "ds_read_b64_tr_b16 v[90:91], v27 offset:19296\n\t" \
    "s_waitcnt lgkmcnt(10)\n\t" \
    "v_mfma_f32_16x16x32_bf16 v[144:147], v[68:71], a[80:83], v[144:147]\n\t" \
    "ds_read_b64_tr_b16 v[92:93], v253 offset:18560\n\t" \
    "v_mfma_f32_16x16x32_bf16 v[148:151], v[68:71], a[84:87], v[148:151]\n\t" \
    "ds_read_b64_tr_b16 v[94:95], v253 offset:19328\n\t" \
    "s_waitcnt lgkmcnt(10)\n\t" \
    "v_mfma_f32_16x16x32_bf16 v[152:155], v[72:75], a[80:83], v[152:155]\n\t" \
    "ds_read_b64_tr_b16 v[96:97], v27 offset:18592\n\t" \
    "v_mfma_f32_16x16x32_bf16 v[156:159], v[72:75], a[84:87], v[156:159]\n\t" \
    "ds_read_b64_tr_b16 v[98:99], v27 offset:19360\n\t" \
    "s_waitcnt vmcnt(9) lgkmcnt(10)\n\t" \
    "v_mfma_f32_16x16x32_bf16 v[112:115], v[76:79], a[88:91], v[112:115]\n\t" \
    "ds_read_b64_tr_b16 v[52:53], v253 offset:24576\n\t" \
    "s_waitcnt vmcnt(8)\n\t" \
    "v_mfma_f32_16x16x32_bf16 v[116:119], v[76:79], a[92:95], v[116:119]\n\t" \
    "ds_read_b64_tr_b16 v[54:55], v253 offset:25344\n\t" \
    "s_waitcnt lgkmcnt(10)\n\t" \
    "v_mfma_f32_16x16x32_bf16 v[120:123], v[80:83], a[88:91], v[120:123]\n\t" \
    "ds_read_b64_tr_b16 v[56:57], v27 offset:24608\n\t" \
    "v_mfma_f32_16x16x32_bf16 v[124:127], v[80:83], a[92:95], v[124:127]\n\t" \
    "ds_read_b64_tr_b16 v[58:59], v27 offset:25376\n\t" \
    "s_waitcnt lgkmcnt(10)\n\t" \
    "v_mfma_f32_16x16x32_bf16 v[128:131], v[84:87], a[88:91], v[128:131]\n\t" \
    "ds_read_b64_tr_b16 v[60:61], v253 offset:24640\n\t" \
    "v_mfma_f32_16x16x32_bf16 v[132:135], v[84:87], a[92:95], v[132:135]\n\t" \
    "ds_read_b64_tr_b16 v[62:63], v253 offset:25408\n\t" \
    "s_waitcnt lgkmcnt(10)\n\t" \
    "v_mfma_f32_16x16x32_bf16 v[136:139], v[88:91], a[88:91], v[136:139]\n\t" \
    "ds_read_b64_tr_b16 v[64:65], v27 offset:24672\n\t" \
    "v_mfma_f32_16x16x32_bf16 v[140:143], v[88:91], a[92:95], v[140:143]\n\t" \
    "ds_read_b64_tr_b16 v[66:67], v27 offset:25440\n\t" \
    "s_waitcnt lgkmcnt(10)\n\t" \
    "v_mfma_f32_16x16x32_bf16 v[144:147], v[92:95], a[88:91], v[144:147]\n\t" \
    "ds_read_b64_tr_b16 v[68:69], v253 offset:24704\n\t" \
    "v_mfma_f32_16x16x32_bf16 v[148:151], v[92:95], a[92:95], v[148:151]\n\t" \
    "ds_read_b64_tr_b16 v[70:71], v253 offset:25472\n\t" \
    "s_waitcnt lgkmcnt(10)\n\t" \
    "v_mfma_f32_16x16x32_bf16 v[152:155], v[96:99], a[88:91], v[152:155]\n\t" \
    "ds_read_b64_tr_b16 v[72:73], v27 offset:24736\n\t" \
    "v_mfma_f32_16x16x32_bf16 v[156:159], v[96:99], a[92:95], v[156:159]\n\t" \
    "ds_read_b64_tr_b16 v[74:75], v27 offset:25504\n\t" \
    "s_waitcnt vmcnt(7) lgkmcnt(10)\n\t" \
    "v_mfma_f32_16x16x32_bf16 v[112:115], v[52:55], a[96:99], v[112:115]\n\t" \
    "ds_read_b64_tr_b16 v[76:77], v253 offset:30720\n\t" \
    "s_waitcnt vmcnt(6)\n\t" \
    "v_mfma_f32_16x16x32_bf16 v[116:119], v[52:55], a[100:103], v[116:119]\n\t" \
    "ds_read_b64_tr_b16 v[78:79], v253 offset:31488\n\t" \
    "s_waitcnt lgkmcnt(10)\n\t" \
    "v_mfma_f32_16x16x32_bf16 v[120:123], v[56:59], a[96:99], v[120:123]\n\t" \
    "ds_read_b64_tr_b16 v[80:81], v27 offset:30752\n\t" \
    "v_mfma_f32_16x16x32_bf16 v[124:127], v[56:59], a[100:103], v[124:127]\n\t" \
    "ds_read_b64_tr_b16 v[82:83], v27 offset:31520\n\t" \
    "s_waitcnt lgkmcnt(10)\n\t" \
    "v_mfma_f32_16x16x32_bf16 v[128:131], v[60:63], a[96:99], v[128:131]\n\t" \
    "ds_read_b64_tr_b16 v[84:85], v253 offset:30784\n\t" \
    "v_mfma_f32_16x16x32_bf16 v[132:135], v[60:63], a[100:103], v[132:135]\n\t" \
    "ds_read_b64_tr_b16 v[86:87], v253 offset:31552\n\t" \
    "s_waitcnt lgkmcnt(10)\n\t" \
    "v_mfma_f32_16x16x32_bf16 v[136:139], v[64:67], a[96:99], v[136:139]\n\t" \
    "ds_read_b64_tr_b16 v[88:89], v27 offset:30816\n\t" \
    "v_mfma_f32_16x16x32_bf16 v[140:143], v[64:67], a[100:103], v[140:143]\n\t" \
    "ds_read_b64_tr_b16 v[90:91], v27 offset:31584\n\t" \
    "s_waitcnt lgkmcnt(10)\n\t" \
    "v_mfma_f32_16x16x32_bf16 v[144:147], v[68:71], a[96:99], v[144:147]\n\t" \
    "ds_read_b64_tr_b16 v[92:93], v253 offset:30848\n\t" \
    "v_mfma_f32_16x16x32_bf16 v[148:151], v[68:71], a[100:103], v[148:151]\n\t" \
    "ds_read_b64_tr_b16 v[94:95], v253 offset:31616\n\t" \
    "s_waitcnt lgkmcnt(10)\n\t" \
    "v_mfma_f32_16x16x32_bf16 v[152:155], v[72:75], a[96:99], v[152:155]\n\t" \
    "ds_read_b64_tr_b16 v[96:97], v27 offset:30880\n\t" \
    "v_mfma_f32_16x16x32_bf16 v[156:159], v[72:75], a[100:103], v[156:159]\n\t" \
    "ds_read_b64_tr_b16 v[98:99], v27 offset:31648\n\t" \
    "s_waitcnt vmcnt(5) lgkmcnt(10)\n\t" \
    "v_mfma_f32_16x16x32_bf16 v[112:115], v[76:79], a[104:107], v[112:115]\n\t" \
    "ds_read_b64_tr_b16 v[52:53], v253 offset:36864\n\t" \
    "s_waitcnt vmcnt(4)\n\t" \
    "v_mfma_f32_16x16x32_bf16 v[116:119], v[76:79], a[108:111], v[116:119]\n\t" \
    "ds_read_b64_tr_b16 v[54:55], v253 offset:37632\n\t" \
    "s_waitcnt lgkmcnt(10)\n\t" \
    "v_mfma_f32_16x16x32_bf16 v[120:123], v[80:83], a[104:107], v[120:123]\n\t" \
    "ds_read_b64_tr_b16 v[56:57], v27 offset:36896\n\t" \
    "v_mfma_f32_16x16x32_bf16 v[124:127], v[80:83], a[108:111], v[124:127]\n\t" \
    "ds_read_b64_tr_b16 v[58:59], v27 offset:37664\n\t" \
    "s_waitcnt lgkmcnt(10)\n\t" \
    "v_mfma_f32_16x16x32_bf16 v[128:131], v[84:87], a[104:107], v[128:131]\n\t" \
    "ds_read_b64_tr_b16 v[60:61], v253 offset:36928\n\t" \
    "v_mfma_f32_16x16x32_bf16 v[132:135], v[84:87], a[108:111], v[132:135]\n\t" \
    "ds_read_b64_tr_b16 v[62:63], v253 offset:37696\n\t" \
    "s_waitcnt lgkmcnt(10)\n\t" \
    "v_mfma_f32_16x16x32_bf16 v[136:139], v[88:91], a[104:107], v[136:139]\n\t" \
    "ds_read_b64_tr_b16 v[64:65], v27 offset:36960\n\t" \
    "v_mfma_f32_16x16x32_bf16 v[140:143], v[88:91], a[108:111], v[140:143]\n\t" \
    "ds_read_b64_tr_b16 v[66:67], v27 offset:37728\n\t" \
    "s_waitcnt lgkmcnt(10)\n\t" \
    "v_mfma_f32_16x16x32_bf16 v[144:147], v[92:95], a[104:107], v[144:147]\n\t" \
    "ds_read_b64_tr_b16 v[68:69], v253 offset:36992\n\t" \
    "v_mfma_f32_16x16x32_bf16 v[148:151], v[92:95], a[108:111], v[148:151]\n\t" \
    "ds_read_b64_tr_b16 v[70:71], v253 offset:37760\n\t" \
    "s_waitcnt lgkmcnt(10)\n\t" \
    "v_mfma_f32_16x16x32_bf16 v[152:155], v[96:99], a[104:107], v[152:155]\n\t" \
    "ds_read_b64_tr_b16 v[72:73], v27 offset:37024\n\t" \
    "v_mfma_f32_16x16x32_bf16 v[156:159], v[96:99], a[108:111], v[156:159]\n\t" \
    "ds_read_b64_tr_b16 v[74:75], v27 offset:37792\n\t" \
    "s_waitcnt vmcnt(3) lgkmcnt(10)\n\t" \
    "v_mfma_f32_16x16x32_bf16 v[112:115], v[52:55], a[112:115], v[112:115]\n\t" \
    "ds_read_b64_tr_b16 v[76:77], v253 offset:43008\n\t" \
    "s_waitcnt vmcnt(2)\n\t" \
    "v_mfma_f32_16x16x32_bf16 v[116:119], v[52:55], a[116:119], v[116:119]\n\t" \
    "ds_read_b64_tr_b16 v[78:79], v253 offset:43776\n\t" \
    "s_waitcnt lgkmcnt(10)\n\t" \
    "v_mfma_f32_16x16x32_bf16 v[120:123], v[56:59], a[112:115], v[120:123]\n\t" \
    "ds_read_b64_tr_b16 v[80:81], v27 offset:43040\n\t" \
    "v_mfma_f32_16x16x32_bf16 v[124:127], v[56:59], a[116:119], v[124:127]\n\t" \
    "ds_read_b64_tr_b16 v[82:83], v27 offset:43808\n\t" \
    "s_waitcnt lgkmcnt(10)\n\t" \
    "v_mfma_f32_16x16x32_bf16 v[128:131], v[60:63], a[112:115], v[128:131]\n\t" \
    "ds_read_b64_tr_b16 v[84:85], v253 offset:43072\n\t" \
    "v_mfma_f32_16x16x32_bf16 v[132:135], v[60:63], a[116:119], v[132:135]\n\t" \
    "ds_read_b64_tr_b16 v[86:87], v253 offset:43840\n\t" \
    "s_waitcnt lgkmcnt(10)\n\t" \
    "v_mfma_f32_16x16x32_bf16 v[136:139], v[64:67], a[112:115], v[136:139]\n\t" \
    "ds_read_b64_tr_b16 v[88:89], v27 offset:43104\n\t" \
    "v_mfma_f32_16x16x32_bf16 v[140:143], v[64:67], a[116:119], v[140:143]\n\t" \
    "ds_read_b64_tr_b16 v[90:91], v27 offset:43872\n\t" \
    "s_waitcnt lgkmcnt(10)\n\t" \
    "v_mfma_f32_16x16x32_bf16 v[144:147], v[68:71], a[112:115], v[144:147]\n\t" \
    "ds_read_b64_tr_b16 v[92:93], v253 offset:43136\n\t" \
    "v_mfma_f32_16x16x32_bf16 v[148:151], v[68:71], a[116:119], v[148:151]\n\t" \
    "ds_read_b64_tr_b16 v[94:95], v253 offset:43904\n\t" \
    "s_waitcnt lgkmcnt(10)\n\t" \
    "v_mfma_f32_16x16x32_bf16 v[152:155], v[72:75], a[112:115], v[152:155]\n\t" \
    "ds_read_b64_tr_b16 v[96:97], v27 offset:43168\n\t" \
    "v_mfma_f32_16x16x32_bf16 v[156:159], v[72:75], a[116:119], v[156:159]\n\t" \
    "ds_read_b64_tr_b16 v[98:99], v27 offset:43936\n\t" \
    "s_waitcnt vmcnt(1) lgkmcnt(10)\n\t" \
    "v_mfma_f32_16x16x32_bf16 v[112:115], v[76:79], a[120:123], v[112:115]\n\t" \
    "s_waitcnt vmcnt(0)\n\t" \
    "v_mfma_f32_16x16x32_bf16 v[116:119], v[76:79], a[124:127], v[116:119]\n\t" \
    "s_waitcnt lgkmcnt(8)\n\t" \
    "v_mfma_f32_16x16x32_bf16 v[120:123], v[80:83], a[120:123], v[120:123]\n\t" \
    "v_mfma_f32_16x16x32_bf16 v[124:127], v[80:83], a[124:127], v[124:127]\n\t" \
    "s_waitcnt lgkmcnt(6)\n\t" \
    "v_mfma_f32_16x16x32_bf16 v[128:131], v[84:87], a[120:123], v[128:131]\n\t" \
    "v_mfma_f32_16x16x32_bf16 v[132:135], v[84:87], a[124:127], v[132:135]\n\t" \
    "s_waitcnt lgkmcnt(4)\n\t" \
    "v_mfma_f32_16x16x32_bf16 v[136:139], v[88:91], a[120:123], v[136:139]\n\t" \
    "v_mfma_f32_16x16x32_bf16 v[140:143], v[88:91], a[124:127], v[140:143]\n\t" \
    "s_waitcnt lgkmcnt(2)\n\t" \
    "v_mfma_f32_16x16x32_bf16 v[144:147], v[92:95], a[120:123], v[144:147]\n\t" \
    "v_mfma_f32_16x16x32_bf16 v[148:151], v[92:95], a[124:127], v[148:151]\n\t" \
    "s_waitcnt lgkmcnt(0)\n\t" \
    "v_mfma_f32_16x16x32_bf16 v[152:155], v[96:99], a[120:123], v[152:155]\n\t" \
    "v_mfma_f32_16x16x32_bf16 v[156:159], v[96:99], a[124:127], v[156:159]\n\t" \
    "s_waitcnt lgkmcnt(0)\n\t" \
    "s_barrier\n\t" \
    "v_mov_b32 v18, 0xc0135761\n\t" \
    "v_mov_b32 v19, 0xc0135761\n\t" \
    "v_mov_b32 v64, 0xbdd2d3e8\n\t" \
    "v_mov_b32 v65, 0xbdd2d3e8\n\t" \
    "v_mov_b32 v66, 1.0\n\t" \
    "v_mov_b32 v67, 1.0\n\t" \
    "v_pk_mul_f32 v[160:161], v[112:113], v[112:113]\n\t" \
    "v_pk_mul_f32 v[162:163], v[114:115], v[114:115]\n\t" \
    "v_pk_mul_f32 v[164:165], v[120:121], v[120:121]\n\t" \
    "v_pk_mul_f32 v[166:167], v[122:123], v[122:123]\n\t" \
    "v_pk_fma_f32 v[160:161], v[160:161], v[64:65], v[18:19]\n\t" \
    "s_min_u32 s28, %[nl], 4\n\t" \
    "s_lshl_b32 s28, s28, 15\n\t" \
    "s_add_u32 s35, s28, %[sw2]\n\t" \
    "s_mul_i32 s28, %[wv], 24576\n\t" \
    "s_add_u32 m0, s28, 0\n\t" \
    "s_nop 0\n\t" \
    "buffer_load_dwordx4 v21, s[20:23], s35 offen offset:0 lds\n\t" \
    "v_pk_fma_f32 v[162:163], v[162:163], v[64:65], v[18:19]\n\t" \
    "v_pk_fma_f32 v[164:165], v[164:165], v[64:65], v[18:19]\n\t" \
    "v_pk_fma_f32 v[166:167], v[166:167], v[64:65], v[18:19]\n\t" \
    "v_pk_mul_f32 v[160:161], v[160:161], v[112:113]\n\t" \
    "v_pk_mul_f32 v[162:163], v[162:163], v[114:115]\n\t" \
    "buffer_load_dwordx4 v22, s[20:23], s35 offen offset:1024 lds\n\t" \
    "v_pk_mul_f32 v[164:165], v[164:165], v[120:121]\n\t" \
    "v_pk_mul_f32 v[166:167], v[166:167], v[122:123]\n\t" \
    "v_exp_f32 v160, v160\n\t" \
    "v_exp_f32 v161, v161\n\t" \
    "buffer_load_dwordx4 v23, s[20:23], s35 offen offset:2048 lds\n\t" \
    "v_exp_f32 v162, v162\n\t" \
    "v_exp_f32 v163, v163\n\t" \
    "v_exp_f32 v164, v164\n\t" \
    "v_exp_f32 v165, v165\n\t" \
    "v_exp_f32 v166, v166\n\t" \
    "buffer_load_dwordx4 v24, s[20:23], s35 offen offset:3072 lds\n\t" \
    "v_exp_f32 v167, v167\n\t" \
    "v_pk_add_f32 v[160:161], v[160:161], v[66:67]\n\t" \
    "v_pk_add_f32 v[162:163], v[162:163], v[66:67]\n\t" \
    "v_pk_add_f32 v[164:165], v[164:165], v[66:67]\n\t" \
    "v_pk_add_f32 v[166:167], v[166:167], v[66:67]\n\t" \
    "s_min_u32 s28, %[nl], 5\n\t" \
    "s_lshl_b32 s28, s28, 15\n\t" \
    "s_add_u32 s35, s28, %[sw2]\n\t" \
    "s_mul_i32 s28, %[wv], 24576\n\t" \
    "s_add_u32 m0, s28, 4096\n\t" \
    "s_nop 0\n\t" \
    "buffer_load_dwordx4 v21, s[20:23], s35 offen offset:0 lds\n\t" \
    "v_pk_mul_f32 v[176:177], v[128:129], v[128:129]\n\t" \
    "v_rcp_f32 v160, v160\n\t" \
    "v_pk_mul_f32 v[178:179], v[130:131], v[130:131]\n\t" \
    "v_rcp_f32 v161, v161\n\t" \
    "buffer_load_dwordx4 v22, s[20:23], s35 offen offset:1024 lds\n\t" \
    "v_pk_mul_f32 v[180:181], v[136:137], v[136:137]\n\t" \
    "v_pk_mul_f32 v[182:183], v[138:139], v[138:139]\n\t" \
    "v_rcp_f32 v162, v162\n\t" \
    "v_pk_fma_f32 v[176:177], v[176:177], v[64:65], v[18:19]\n\t" \
    "v_rcp_f32 v163, v163\n\t" \
    "buffer_load_dwordx4 v23, s[20:23], s35 offen offset:2048 lds\n\t" \
    "v_pk_fma_f32 v[178:179], v[178:179], v[64:65], v[18:19]\n\t" \
    "v_pk_fma_f32 v[180:181], v[180:181], v[64:65], v[18:19]\n\t" \
    "v_rcp_f32 v164, v164\n\t" \
    "v_pk_fma_f32 v[182:183], v[182:183], v[64:65], v[18:19]\n\t" \
    "v_rcp_f32 v165, v165\n\t" \
    "buffer_load_dwordx4 v24, s[20:23], s35 offen offset:3072 lds\n\t" \
    "v_pk_mul_f32 v[176:177], v[176:177], v[128:129]\n\t" \
    "v_pk_mul_f32 v[178:179], v[178:179], v[130:131]\n\t" \
    "v_rcp_f32 v166, v166\n\t" \
    "v_pk_mul_f32 v[180:181], v[180:181], v[136:137]\n\t" \
    "s_min_u32 s28, %[nl], 6\n\t" \
    "s_lshl_b32 s28, s28, 15\n\t" \
    "s_add_u32 s35, s28, %[sw2]\n\t" \
    "s_mul_i32 s28, %[wv], 24576\n\t" \
    "s_add_u32 m0, s28, 8192\n\t" \
    "s_nop 0\n\t" \
    "buffer_load_dwordx4 v21, s[20:23], s35 offen offset:0 lds\n\t" \
    "v_rcp_f32 v167, v167\n\t" \
    "v_pk_mul_f32 v[182:183], v[182:183], v[138:139]\n\t" \
    "v_pk_mul_f32 v[160:161], v[160:161], v[112:113]\n\t" \
    "v_exp_f32 v176, v176\n\t" \
    "v_pk_mul_f32 v[162:163], v[162:163], v[114:115]\n\t" \
    "buffer_load_dwordx4 v22, s[20:23], s35 offen offset:1024 lds\n\t" \
    "v_exp_f32 v177, v177\n\t" \
    "v_pk_mul_f32 v[164:165], v[164:165], v[120:121]\n\t" \
    "v_exp_f32 v178, v178\n\t" \
    "v_pk_mul_f32 v[166:167], v[166:167], v[122:123]\n\t" \
    "v_exp_f32 v179, v179\n\t" \
    "buffer_load_dwordx4 v23, s[20:23], s35 offen offset:2048 lds\n\t" \
    "v_cvt_pk_bf16_f32 v168, v160, v161\n\t" \
    "v_cvt_pk_bf16_f32 v169, v162, v163\n\t" \
    "v_exp_f32 v180, v180\n\t" \
    "v_cvt_pk_bf16_f32 v170, v164, v165\n\t" \
    "buffer_load_dwordx4 v24, s[20:23], s35 offen offset:3072 lds\n\t" \
    "v_exp_f32 v181, v181\n\t" \
    "v_cvt_pk_bf16_f32 v171, v166, v167\n\t" \
    "v_exp_f32 v182, v182\n\t" \
    "s_add_u32 s31, %[sh], 0\n\t" \
    "v_exp_f32 v183, v183\n\t" \
    "s_min_u32 s28, %[nl], 7\n\t" \
    "s_lshl_b32 s28, s28, 15\n\t" \
    "s_add_u32 s35, s28, %[sw2]\n\t" \
    "s_mul_i32 s28, %[wv], 24576\n\t" \
    "s_add_u32 m0, s28, 12288\n\t" \
    "s_nop 0\n\t" \
    "buffer_load_dwordx4 v21, s[20:23], s35 offen offset:0 lds\n\t" \
    "buffer_store_dwordx4 v[168:171], v16, s[8:11], s31 offen sc1\n\t" \
    "v_pk_add_f32 v[176:177], v[176:177], v[66:67]\n\t" \
    "v_pk_add_f32 v[178:179], v[178:179], v[66:67]\n\t" \
    "v_pk_add_f32 v[180:181], v[180:181], v[66:67]\n\t" \
    "v_pk_add_f32 v[182:183], v[182:183], v[66:67]\n\t" \
    "buffer_load_dwordx4 v22, s[20:23], s35 offen offset:1024 lds\n\t" \
    "v_pk_mul_f32 v[192:193], v[144:145], v[144:145]\n\t" \
    "v_rcp_f32 v176, v176\n\t" \
    "v_pk_mul_f32 v[194:195], v[146:147], v[146:147]\n\t" \
    "v_rcp_f32 v177, v177\n\t" \
    "buffer_load_dwordx4 v23, s[20:23], s35 offen offset:2048 lds\n\t" \
    "v_pk_mul_f32 v[196:197], v[152:153], v[152:153]\n\t" \
    "v_pk_mul_f32 v[198:199], v[154:155], v[154:155]\n\t" \
    "v_rcp_f32 v178, v178\n\t" \
    "v_pk_fma_f32 v[192:193], v[192:193], v[64:65], v[18:19]\n\t" \
    "v_rcp_f32 v179, v179\n\t" \
    "buffer_load_dwordx4 v24, s[20:23], s35 offen offset:3072 lds\n\t" \
    "v_pk_fma_f32 v[194:195], v[194:195], v[64:65], v[18:19]\n\t" \
    "v_pk_fma_f32 v[196:197], v[196:197], v[64:65], v[18:19]\n\t" \
    "v_rcp_f32 v180, v180\n\t" \
    "v_pk_fma_f32 v[198:199], v[198:199], v[64:65], v[18:19]\n\t" \
    "v_rcp_f32 v181, v181\n\t" \
    "s_min_u32 s28, %[nl], 8\n\t" \
    "s_lshl_b32 s28, s28, 15\n\t" \
    "s_add_u32 s35, s28, %[sw2]\n\t" \
    "s_mul_i32 s28, %[wv], 24576\n\t" \
    "s_add_u32 m0, s28, 16384\n\t" \
    "s_nop 0\n\t" \
    "buffer_load_dwordx4 v21, s[20:23], s35 offen offset:0 lds\n\t" \
    "v_pk_mul_f32 v[192:193], v[192:193], v[144:145]\n\t" \
    "v_pk_mul_f32 v[194:195], v[194:195], v[146:147]\n\t" \
    "v_rcp_f32 v182, v182\n\t" \
    "v_pk_mul_f32 v[196:197], v[196:197], v[152:153]\n\t" \
    "buffer_load_dwordx4 v22, s[20:23], s35 offen offset:1024 lds\n\t" \
    "v_rcp_f32 v183, v183\n\t" \
    "v_pk_mul_f32 v[198:199], v[198:199], v[154:155]\n\t" \
    "v_pk_mul_f32 v[176:177], v[176:177], v[128:129]\n\t" \
    "v_exp_f32 v192, v192\n\t" \
    "v_pk_mul_f32 v[178:179], v[178:179], v[130:131]\n\t" \
    "buffer_load_dwordx4 v23, s[20:23], s35 offen offset:2048 lds\n\t" \
    "v_exp_f32 v193, v193\n\t" \
    "v_pk_mul_f32 v[180:181], v[180:181], v[136:137]\n\t" \
    "v_exp_f32 v194, v194\n\t" \
    "v_pk_mul_f32 v[182:183], v[182:183], v[138:139]\n\t" \
    "v_exp_f32 v195, v195\n\t" \
    "buffer_load_dwordx4 v24, s[20:23], s35 offen offset:3072 lds\n\t" \
    "v_cvt_pk_bf16_f32 v184, v176, v177\n\t" \
    "v_cvt_pk_bf16_f32 v185, v178, v179\n\t" \
    "v_exp_f32 v196, v196\n\t" \
    "v_cvt_pk_bf16_f32 v186, v180, v181\n\t" \
    "s_min_u32 s28, %[nl], 9\n\t" \
    "s_lshl_b32 s28, s28, 15\n\t" \
    "s_add_u32 s35, s28, %[sw2]\n\t" \
    "s_mul_i32 s28, %[wv], 24576\n\t" \
    "s_add_u32 m0, s28, 20480\n\t" \
    "s_nop 0\n\t" \
    "buffer_load_dwordx4 v21, s[20:23], s35 offen offset:0 lds\n\t" \
    "v_exp_f32 v197, v197\n\t" \
    "v_cvt_pk_bf16_f32 v187, v182, v183\n\t" \
    "v_exp_f32 v198, v198\n\t" \
    "s_add_u32 s31, %[sh], 1024\n\t" \
    "v_exp_f32 v199, v199\n\t" \
    "buffer_load_dwordx4 v22, s[20:23], s35 offen offset:1024 lds\n\t" \
    "buffer_store_dwordx4 v[184:187], v16, s[8:11], s31 offen sc1\n\t" \
    "v_pk_add_f32 v[192:193], v[192:193], v[66:67]\n\t" \
    "v_pk_add_f32 v[194:195], v[194:195], v[66:67]\n\t" \
    "v_pk_add_f32 v[196:197], v[196:197], v[66:67]\n\t" \
    "v_pk_add_f32 v[198:199], v[198:199], v[66:67]\n\t" \
    "buffer_load_dwordx4 v23, s[20:23], s35 offen offset:2048 lds\n\t" \
    "v_pk_mul_f32 v[208:209], v[116:117], v[116:117]\n\t" \
    "v_rcp_f32 v192, v192\n\t" \
    "v_pk_mul_f32 v[210:211], v[118:119], v[118:119]\n\t" \
    "v_rcp_f32 v193, v193\n\t" \
    "buffer_load_dwordx4 v24, s[20:23], s35 offen offset:3072 lds\n\t" \
    "v_pk_mul_f32 v[212:213], v[124:125], v[124:125]\n\t" \
    "v_pk_mul_f32 v[214:215], v[126:127], v[126:127]\n\t" \
    "v_rcp_f32 v194, v194\n\t" \
    "v_pk_fma_f32 v[208:209], v[208:209], v[64:65], v[18:19]\n\t" \
    "v_rcp_f32 v195, v195\n\t" \
    "v_accvgpr_read_b32 v60, a247\n\t" \
    "v_accvgpr_read_b32 v61, a248\n\t" \
    "v_lshlrev_b32 v62, 1, v61\n\t" \
    "s_mov_b32 s36, 10240\n\t" \
    "s_mov_b32 s34, 0x5400000\n\t" \
    "s_mov_b32 s24, %[b2lo]\n\t" \
    "s_mov_b32 s25, %[b2hi]\n\t" \
    "s_mov_b32 s26, 0x400\n\t" \
    "s_mov_b32 s27, 0x00020000\n\t" \
    "s_nop 0\n\t" \
    "buffer_load_dwordx2 v[28:29], v60, s[8:11], s34 offen offset:0 sc1\n\t" \
    "v_pk_fma_f32 v[210:211], v[210:211], v[64:65], v[18:19]\n\t" \
    "v_pk_fma_f32 v[212:213], v[212:213], v[64:65], v[18:19]\n\t" \
    "v_rcp_f32 v196, v196\n\t" \
    "v_pk_fma_f32 v[214:215], v[214:215], v[64:65], v[18:19]\n\t" \
    "v_rcp_f32 v197, v197\n\t" \
    "buffer_load_dwordx2 v[36:37], v61, s[24:27], 0 offen offset:0\n\t" \
    "v_pk_mul_f32 v[208:209], v[208:209], v[116:117]\n\t" \
    "v_pk_mul_f32 v[210:211], v[210:211], v[118:119]\n\t" \
    "v_rcp_f32 v198, v198\n\t" \
    "v_pk_mul_f32 v[212:213], v[212:213], v[124:125]\n\t" \
    "buffer_load_dwordx4 v[44:47], v62, s[8:11], s36 offen offset:0\n\t" \
    "v_rcp_f32 v199, v199\n\t" \
    "v_pk_mul_f32 v[214:215], v[214:215], v[126:127]\n\t" \
    "v_pk_mul_f32 v[192:193], v[192:193], v[144:145]\n\t" \
    "v_exp_f32 v208, v208\n\t" \
    "v_pk_mul_f32 v[194:195], v[194:195], v[146:147]\n\t" \
    "buffer_load_dwordx2 v[30:31], v60, s[8:11], s34 offen offset:32 sc1\n\t" \
    "v_exp_f32 v209, v209\n\t" \
    "v_pk_mul_f32 v[196:197], v[196:197], v[152:153]\n\t" \
    "v_exp_f32 v210, v210\n\t" \
    "v_pk_mul_f32 v[198:199], v[198:199], v[154:155]\n\t" \
    "v_exp_f32 v211, v211\n\t" \
    "buffer_load_dwordx2 v[38:39], v61, s[24:27], 0 offen offset:32\n\t" \
    "v_cvt_pk_bf16_f32 v200, v192, v193\n\t" \
    "v_cvt_pk_bf16_f32 v201, v194, v195\n\t" \
    "v_exp_f32 v212, v212\n\t" \
    "v_cvt_pk_bf16_f32 v202, v196, v197\n\t" \
    "buffer_load_dwordx4 v[48:51], v62, s[8:11], s36 offen offset:64\n\t" \
    "v_exp_f32 v213, v213\n\t" \
    "v_cvt_pk_bf16_f32 v203, v198, v199\n\t" \
    "v_exp_f32 v214, v214\n\t" \
    "s_add_u32 s31, %[sh], 2048\n\t" \
    "v_exp_f32 v215, v215\n\t" \
    "buffer_load_dwordx2 v[32:33], v60, s[8:11], s34 offen offset:64 sc1\n\t" \
    "buffer_store_dwordx4 v[200:203], v16, s[8:11], s31 offen sc1\n\t" \
    "v_pk_add_f32 v[208:209], v[208:209], v[66:67]\n\t" \
    "v_pk_add_f32 v[210:211], v[210:211], v[66:67]\n\t" \
    "v_pk_add_f32 v[212:213], v[212:213], v[66:67]\n\t" \
    "v_pk_add_f32 v[214:215], v[214:215], v[66:67]\n\t" \
    "buffer_load_dwordx2 v[40:41], v61, s[24:27], 0 offen offset:64\n\t" \
    "v_pk_mul_f32 v[160:161], v[132:133], v[132:133]\n\t" \
    "v_rcp_f32 v208, v208\n\t" \
    "v_pk_mul_f32 v[162:163], v[134:135], v[134:135]\n\t" \
    "v_rcp_f32 v209, v209\n\t" \
    "buffer_load_dwordx4 v[52:55], v62, s[8:11], s36 offen offset:128\n\t" \
    "v_pk_mul_f32 v[164:165], v[140:141], v[140:141]\n\t" \
    "v_pk_mul_f32 v[166:167], v[142:143], v[142:143]\n\t" \
    "v_rcp_f32 v210, v210\n\t" \
    "v_pk_fma_f32 v[160:161], v[160:161], v[64:65], v[18:19]\n\t" \
    "v_rcp_f32 v211, v211\n\t" \
    "buffer_load_dwordx2 v[34:35], v60, s[8:11], s34 offen offset:96 sc1\n\t" \
    "v_pk_fma_f32 v[162:163], v[162:163], v[64:65], v[18:19]\n\t" \
    "v_pk_fma_f32 v[164:165], v[164:165], v[64:65], v[18:19]\n\t" \
    "v_rcp_f32 v212, v212\n\t" \
    "v_pk_fma_f32 v[166:167], v[166:167], v[64:65], v[18:19]\n\t" \
    "v_rcp_f32 v213, v213\n\t" \
    "buffer_load_dwordx2 v[42:43], v61, s[24:27], 0 offen offset:96\n\t" \
    "v_pk_mul_f32 v[160:161], v[160:161], v[132:133]\n\t" \
    "v_pk_mul_f32 v[162:163], v[162:163], v[134:135]\n\t" \
    "v_rcp_f32 v214, v214\n\t" \
    "v_pk_mul_f32 v[164:165], v[164:165], v[140:141]\n\t" \
    "buffer_load_dwordx4 v[56:59], v62, s[8:11], s36 offen offset:192\n\t" \
    "v_rcp_f32 v215, v215\n\t" \
    "v_pk_mul_f32 v[166:167], v[166:167], v[142:143]\n\t" \
    "v_pk_mul_f32 v[208:209], v[208:209], v[116:117]\n\t" \
    "v_exp_f32 v160, v160\n\t" \
    "v_pk_mul_f32 v[210:211], v[210:211], v[118:119]\n\t" \
    "v_exp_f32 v161, v161\n\t" \
    "v_pk_mul_f32 v[212:213], v[212:213], v[124:125]\n\t" \
    "v_exp_f32 v162, v162\n\t" \
    "v_pk_mul_f32 v[214:215], v[214:215], v[126:127]\n\t" \
    "v_exp_f32 v163, v163\n\t" \
    "v_cvt_pk_bf16_f32 v216, v208, v209\n\t" \
    "v_cvt_pk_bf16_f32 v217, v210, v211\n\t" \
    "v_exp_f32 v164, v164\n\t" \
    "v_cvt_pk_bf16_f32 v218, v212, v213\n\t" \
    "v_exp_f32 v165, v165\n\t" \
    "v_cvt_pk_bf16_f32 v219, v214, v215\n\t" \
    "v_exp_f32 v166, v166\n\t" \
    "s_add_u32 s31, %[sh], 44032\n\t" \
    "v_exp_f32 v167, v167\n\t" \
    "buffer_store_dwordx4 v[216:219], v16, s[8:11], s31 offen sc1\n\t" \
    "v_pk_add_f32 v[160:161], v[160:161], v[66:67]\n\t" \
    "v_pk_add_f32 v[162:163], v[162:163], v[66:67]\n\t" \
    "v_pk_add_f32 v[164:165], v[164:165], v[66:67]\n\t" \
    "v_pk_add_f32 v[166:167], v[166:167], v[66:67]\n\t" \
    "v_pk_mul_f32 v[176:177], v[148:149], v[148:149]\n\t" \
    "v_rcp_f32 v160, v160\n\t" \
    "v_pk_mul_f32 v[178:179], v[150:151], v[150:151]\n\t" \
    "v_rcp_f32 v161, v161\n\t" \
    "v_pk_mul_f32 v[180:181], v[156:157], v[156:157]\n\t" \
    "v_pk_mul_f32 v[182:183], v[158:159], v[158:159]\n\t" \
    "v_rcp_f32 v162, v162\n\t" \
    "v_pk_fma_f32 v[176:177], v[176:177], v[64:65], v[18:19]\n\t" \
    "v_rcp_f32 v163, v163\n\t" \
    "v_pk_fma_f32 v[178:179], v[178:179], v[64:65], v[18:19]\n\t" \
    "v_pk_fma_f32 v[180:181], v[180:181], v[64:65], v[18:19]\n\t" \
    "v_rcp_f32 v164, v164\n\t" \
    "v_pk_fma_f32 v[182:183], v[182:183], v[64:65], v[18:19]\n\t" \
    "v_rcp_f32 v165, v165\n\t" \
    "v_pk_mul_f32 v[176:177], v[176:177], v[148:149]\n\t" \
    "v_pk_mul_f32 v[178:179], v[178:179], v[150:151]\n\t" \
    "v_rcp_f32 v166, v166\n\t" \
    "v_pk_mul_f32 v[180:181], v[180:181], v[156:157]\n\t" \
    "v_rcp_f32 v167, v167\n\t" \
    "v_pk_mul_f32 v[182:183], v[182:183], v[158:159]\n\t" \
    "v_pk_mul_f32 v[160:161], v[160:161], v[132:133]\n\t" \
    "v_exp_f32 v176, v176\n\t" \
    "v_pk_mul_f32 v[162:163], v[162:163], v[134:135]\n\t" \
    "v_exp_f32 v177, v177\n\t" \
    "v_pk_mul_f32 v[164:165], v[164:165], v[140:141]\n\t" \
    "v_exp_f32 v178, v178\n\t" \
    "v_pk_mul_f32 v[166:167], v[166:167], v[142:143]\n\t" \
    "v_exp_f32 v179, v179\n\t" \
    "v_cvt_pk_bf16_f32 v168, v160, v161\n\t" \
    "v_cvt_pk_bf16_f32 v169, v162, v163\n\t" \
    "v_exp_f32 v180, v180\n\t" \
    "v_cvt_pk_bf16_f32 v170, v164, v165\n\t" \
    "v_exp_f32 v181, v181\n\t" \
    "v_cvt_pk_bf16_f32 v171, v166, v167\n\t" \
    "v_exp_f32 v182, v182\n\t" \
    "s_add_u32 s31, %[sh], 45056\n\t" \
    "v_exp_f32 v183, v183\n\t" \
    "buffer_store_dwordx4 v[168:171], v16, s[8:11], s31 offen sc1\n\t" \
    "v_pk_add_f32 v[176:177], v[176:177], v[66:67]\n\t" \
    "v_pk_add_f32 v[178:179], v[178:179], v[66:67]\n\t" \
    "v_pk_add_f32 v[180:181], v[180:181], v[66:67]\n\t" \
    "v_pk_add_f32 v[182:183], v[182:183], v[66:67]\n\t" \
    "v_rcp_f32 v176, v176\n\t" \
    "v_rcp_f32 v177, v177\n\t" \
    "v_rcp_f32 v178, v178\n\t" \
    "v_rcp_f32 v179, v179\n\t" \
    "v_rcp_f32 v180, v180\n\t" \
    "v_rcp_f32 v181, v181\n\t" \
    "v_rcp_f32 v182, v182\n\t" \
    "v_rcp_f32 v183, v183\n\t" \
    "v_pk_mul_f32 v[176:177], v[176:177], v[148:149]\n\t" \
    "v_pk_mul_f32 v[178:179], v[178:179], v[150:151]\n\t" \
    "v_pk_mul_f32 v[180:181], v[180:181], v[156:157]\n\t" \
    "v_pk_mul_f32 v[182:183], v[182:183], v[158:159]\n\t" \
    "v_cvt_pk_bf16_f32 v184, v176, v177\n\t" \
    "v_cvt_pk_bf16_f32 v185, v178, v179\n\t" \
    "v_cvt_pk_bf16_f32 v186, v180, v181\n\t" \
    "v_cvt_pk_bf16_f32 v187, v182, v183\n\t" \
    "s_add_u32 s31, %[sh], 46080\n\t" \
    "buffer_store_dwordx4 v[184:187], v16, s[8:11], s31 offen sc1\n\t" \
    "s_branch J3_%=\n\t" \
    "IDLE_%=:\n\t" \
    "s_barrier\n\t" \
    "s_min_u32 s28, %[nl], 4\n\t" \
    "s_lshl_b32 s28, s28, 15\n\t" \
    "s_add_u32 s35, s28, %[sw2]\n\t" \
    "s_mul_i32 s28, %[wv], 24576\n\t" \
    "s_add_u32 m0, s28, 0\n\t" \
    "s_nop 0\n\t" \
    "buffer_load_dwordx4 v21, s[20:23], s35 offen offset:0 lds\n\t" \
    "buffer_load_dwordx4 v22, s[20:23], s35 offen offset:1024 lds\n\t" \
    "buffer_load_dwordx4 v23, s[20:23], s35 offen offset:2048 lds\n\t" \
    "buffer_load_dwordx4 v24, s[20:23], s35 offen offset:3072 lds\n\t" \
    "s_min_u32 s28, %[nl], 5\n\t" \
    "s_lshl_b32 s28, s28, 15\n\t" \
    "s_add_u32 s35, s28, %[sw2]\n\t" \
    "s_mul_i32 s28, %[wv], 24576\n\t" \
    "s_add_u32 m0, s28, 4096\n\t" \
    "s_nop 0\n\t" \
    "buffer_load_dwordx4 v21, s[20:23], s35 offen offset:0 lds\n\t" \
    "buffer_load_dwordx4 v22, s[20:23], s35 offen offset:1024 lds\n\t" \
    "buffer_load_dwordx4 v23, s[20:23], s35 offen offset:2048 lds\n\t" \
    "buffer_load_dwordx4 v24, s[20:23], s35 offen offset:3072 lds\n\t" \
    "s_min_u32 s28, %[nl], 6\n\t" \
    "s_lshl_b32 s28, s28, 15\n\t" \
    "s_add_u32 s35, s28, %[sw2]\n\t" \
    "s_mul_i32 s28, %[wv], 24576\n\t" \
    "s_add_u32 m0, s28, 8192\n\t" \
    "s_nop 0\n\t" \
    "buffer_load_dwordx4 v21, s[20:23], s35 offen offset:0 lds\n\t" \
    "buffer_load_dwordx4 v22, s[20:23], s35 offen offset:1024 lds\n\t" \
    "buffer_load_dwordx4 v23, s[20:23], s35 offen offset:2048 lds\n\t" \
    "buffer_load_dwordx4 v24, s[20:23], s35 offen offset:3072 lds\n\t" \
    "s_min_u32 s28, %[nl], 7\n\t" \
    "s_lshl_b32 s28, s28, 15\n\t" \
    "s_add_u32 s35, s28, %[sw2]\n\t" \
    "s_mul_i32 s28, %[wv], 24576\n\t" \
    "s_add_u32 m0, s28, 12288\n\t" \
    "s_nop 0\n\t" \
    "buffer_load_dwordx4 v21, s[20:23], s35 offen offset:0 lds\n\t" \
    "buffer_load_dwordx4 v22, s[20:23], s35 offen offset:1024 lds\n\t" \
    "buffer_load_dwordx4 v23, s[20:23], s35 offen offset:2048 lds\n\t" \
    "buffer_load_dwordx4 v24, s[20:23], s35 offen offset:3072 lds\n\t" \
    "s_min_u32 s28, %[nl], 8\n\t" \
    "s_lshl_b32 s28, s28, 15\n\t" \
    "s_add_u32 s35, s28, %[sw2]\n\t" \
    "s_mul_i32 s28, %[wv], 24576\n\t" \
    "s_add_u32 m0, s28, 16384\n\t" \
    "s_nop 0\n\t" \
    "buffer_load_dwordx4 v21, s[20:23], s35 offen offset:0 lds\n\t" \
    "buffer_load_dwordx4 v22, s[20:23], s35 offen offset:1024 lds\n\t" \
    "buffer_load_dwordx4 v23, s[20:23], s35 offen offset:2048 lds\n\t" \
    "buffer_load_dwordx4 v24, s[20:23], s35 offen offset:3072 lds\n\t" \
    "s_min_u32 s28, %[nl], 9\n\t" \
    "s_lshl_b32 s28, s28, 15\n\t" \
    "s_add_u32 s35, s28, %[sw2]\n\t" \
    "s_mul_i32 s28, %[wv], 24576\n\t" \
    "s_add_u32 m0, s28, 20480\n\t" \
    "s_nop 0\n\t" \
    "buffer_load_dwordx4 v21, s[20:23], s35 offen offset:0 lds\n\t" \
    "buffer_load_dwordx4 v22, s[20:23], s35 offen offset:1024 lds\n\t" \
    "buffer_load_dwordx4 v23, s[20:23], s35 offen offset:2048 lds\n\t" \
    "buffer_load_dwordx4 v24, s[20:23], s35 offen offset:3072 lds\n\t" \
    "v_accvgpr_read_b32 v60, a247\n\t" \
    "v_accvgpr_read_b32 v61, a248\n\t" \
    "v_lshlrev_b32 v62, 1, v61\n\t" \
    "s_mov_b32 s36, 10240\n\t" \
    "s_mov_b32 s34, 0x5400000\n\t" \
    "s_mov_b32 s24, %[b2lo]\n\t" \
    "s_mov_b32 s25, %[b2hi]\n\t" \
    "s_mov_b32 s26, 0x400\n\t" \
    "s_mov_b32 s27, 0x00020000\n\t" \
    "s_nop 0\n\t" \
    "buffer_load_dwordx2 v[28:29], v60, s[8:11], s34 offen offset:0 sc1\n\t" \
    "buffer_load_dwordx2 v[36:37], v61, s[24:27], 0 offen offset:0\n\t" \
    "buffer_load_dwordx4 v[44:47], v62, s[8:11], s36 offen offset:0\n\t" \
    "buffer_load_dwordx2 v[30:31], v60, s[8:11], s34 offen offset:32 sc1\n\t" \
    "buffer_load_dwordx2 v[38:39], v61, s[24:27], 0 offen offset:32\n\t" \
    "buffer_load_dwordx4 v[48:51], v62, s[8:11], s36 offen offset:64\n\t" \
    "buffer_load_dwordx2 v[32:33], v60, s[8:11], s34 offen offset:64 sc1\n\t" \
    "buffer_load_dwordx2 v[40:41], v61, s[24:27], 0 offen offset:64\n\t" \
    "buffer_load_dwordx4 v[52:55], v62, s[8:11], s36 offen offset:128\n\t" \
    "buffer_load_dwordx2 v[34:35], v60, s[8:11], s34 offen offset:96 sc1\n\t" \
    "buffer_load_dwordx2 v[42:43], v61, s[24:27], 0 offen offset:96\n\t" \
    "buffer_load_dwordx4 v[56:59], v62, s[8:11], s36 offen offset:192\n\t" \
    "J3_%=:\n\t" \
    "s_waitcnt vmcnt(0) lgkmcnt(0)\n\t" \
    "s_barrier\n\t" \
    "s_cmp_lg_u32 %[wv], 0\n\t" \
    "s_cbranch_scc1 S3_L_%=\n\t" \
    "s_cmp_ge_u32 %[mm], 15\n\t" \
    "s_cbranch_scc1 S3_L_%=\n\t" \
    "v_mov_b32 v17, 1\n\t" \
    "s_mov_b64 exec, 1\n\t" \
    "buffer_atomic_add v17, off, s[8:11], %[ch] sc1\n\t" \
    "s_mov_b64 exec, -1\n\t" \
    "S3_L_%=:\n\t" \
    "s_cmp_lg_u32 %[wv], 0\n\t" \
    "s_cbranch_scc1 S3_OK_%=\n\t" \
    "s_mov_b32 s30, 0\n\t" \
    "S3_P_%=:\n\t" \
    "buffer_load_dword v17, off, s[8:11], %[ch] sc1\n\t" \
    "s_waitcnt vmcnt(0)\n\t" \
    "v_readfirstlane_b32 s28, v17\n\t" \
    "s_nop 0\n\t" \
    "s_cmp_ge_u32 s28, 15\n\t" \
    "s_cbranch_scc1 S3_OK_%=\n\t" \
    "s_add_u32 s30, s30, 1\n\t" \
    "s_cmp_gt_u32 s30, 0x40000\n\t" \
    "s_cbranch_scc1 S3_OK_%=\n\t" \
    "s_sleep 1\n\t" \
    "s_branch S3_P_%=\n\t" \
    "S3_OK_%=:\n\t" \
    "s_barrier\n\t" \
    "s_min_u32 s28, %[nl], 0\n\t" \
    "s_lshl_b32 s28, s28, 10\n\t" \
    "s_add_u32 s34, s28, %[sh2]\n\t" \
    "buffer_load_dwordx4 a[0:3], v16, s[8:11], s34 offen sc1\n\t" \
    "s_add_u32 s34, s34, 44032\n\t" \
    "buffer_load_dwordx4 a[4:7], v16, s[8:11], s34 offen sc1\n\t" \
    "s_add_u32 s34, s34, 44032\n\t" \
    "buffer_load_dwordx4 a[8:11], v16, s[8:11], s34 offen sc1\n\t" \
    "s_add_u32 s34, s34, 44032\n\t" \
    "buffer_load_dwordx4 a[12:15], v16, s[8:11], s34 offen sc1\n\t" \
    "s_min_u32 s28, %[nl], 1\n\t" \
    "s_lshl_b32 s28, s28, 10\n\t" \
    "s_add_u32 s34, s28, %[sh2]\n\t" \
    "buffer_load_dwordx4 a[16:19], v16, s[8:11], s34 offen sc1\n\t" \
    "s_add_u32 s34, s34, 44032\n\t" \
    "buffer_load_dwordx4 a[20:23], v16, s[8:11], s34 offen sc1\n\t" \
    "s_add_u32 s34, s34, 44032\n\t" \
    "buffer_load_dwordx4 a[24:27], v16, s[8:11], s34 offen sc1\n\t" \
    "s_add_u32 s34, s34, 44032\n\t" \
    "buffer_load_dwordx4 a[28:31], v16, s[8:11], s34 offen sc1\n\t" \
    "s_min_u32 s28, %[nl], 2\n\t" \
    "s_lshl_b32 s28, s28, 10\n\t" \
    "s_add_u32 s34, s28, %[sh2]\n\t" \
    "buffer_load_dwordx4 a[32:35], v16, s[8:11], s34 offen sc1\n\t" \
    "s_add_u32 s34, s34, 44032\n\t" \
    "buffer_load_dwordx4 a[36:39], v16, s[8:11], s34 offen sc1\n\t" \
    "s_add_u32 s34, s34, 44032\n\t" \
    "buffer_load_dwordx4 a[40:43], v16, s[8:11], s34 offen sc1\n\t" \
    "s_add_u32 s34, s34, 44032\n\t" \
    "buffer_load_dwordx4 a[44:47], v16, s[8:11], s34 offen sc1\n\t" \
    "s_min_u32 s28, %[nl], 3\n\t" \
    "s_lshl_b32 s28, s28, 10\n\t" \
    "s_add_u32 s34, s28, %[sh2]\n\t" \
    "buffer_load_dwordx4 a[48:51], v16, s[8:11], s34 offen sc1\n\t" \
    "s_add_u32 s34, s34, 44032\n\t" \
    "buffer_load_dwordx4 a[52:55], v16, s[8:11], s34 offen sc1\n\t" \
    "s_add_u32 s34, s34, 44032\n\t" \
    "buffer_load_dwordx4 a[56:59], v16, s[8:11], s34 offen sc1\n\t" \
    "s_add_u32 s34, s34, 44032\n\t" \
    "buffer_load_dwordx4 a[60:63], v16, s[8:11], s34 offen sc1\n\t" \
    "v_accvgpr_read_b32 v108, a246\n\t" \
    "s_mul_i32 s28, %[wv], 8192\n\t" \
    "s_add_u32 s29, s28, 131072\n\t" \
    "v_add_u32 v96, s29, v108\n\t" \
    "s_add_u32 s29, s28, 98304\n\t" \
    "v_add_u32 v100, s29, v108\n\t" \
    "s_mul_i32 s28, %[wv], 24576\n\t" \
    "v_add_u32 v104, s28, v108\n\t" \
    "v_xor_b32 v97, 32, v96\n\t" \
    "v_xor_b32 v98, 64, v96\n\t" \
    "v_xor_b32 v99, 96, v96\n\t" \
    "v_xor_b32 v101, 32, v100\n\t" \
    "v_xor_b32 v102, 64, v100\n\t" \
    "v_xor_b32 v103, 96, v100\n\t" \
    "v_xor_b32 v105, 32, v104\n\t" \
    "v_xor_b32 v106, 64, v104\n\t" \
    "v_xor_b32 v107, 96, v104\n\t" \
    "ds_read_b64_tr_b16 v[64:65], v96 offset:0\n\t" \
    "ds_read_b64_tr_b16 v[66:67], v96 offset:2048\n\t" \
    "ds_read_b64_tr_b16 v[68:69], v97 offset:0\n\t" \
    "ds_read_b64_tr_b16 v[70:71], v97 offset:2048\n\t" \
    "ds_read_b64_tr_b16 v[72:73], v98 offset:0\n\t" \
    "ds_read_b64_tr_b16 v[74:75], v98 offset:2048\n\t" \
    "ds_read_b64_tr_b16 v[76:77], v99 offset:0\n\t" \
    "ds_read_b64_tr_b16 v[78:79], v99 offset:2048\n\t" \
    "s_waitcnt vmcnt(15) lgkmcnt(6)\n\t" \
    "v_mfma_f32_16x16x32_bf16 v[128:131], v[64:67], a[0:3], 0\n\t" \
    "ds_read_b64_tr_b16 v[80:81], v96 offset:4096\n\t" \
    "s_min_u32 s28, %[nl], 4\n\t" \
    "s_lshl_b32 s28, s28, 10\n\t" \
    "s_add_u32 s34, s28, %[sh2]\n\t" \
    "s_waitcnt vmcnt(14)\n\t" \
    "v_mfma_f32_16x16x32_bf16 v[144:147], v[64:67], a[4:7], 0\n\t" \
    "ds_read_b64_tr_b16 v[82:83], v96 offset:6144\n\t" \
    "buffer_load_dwordx4 a[64:67], v16, s[8:11], s34 offen sc1\n\t" \
    "s_waitcnt vmcnt(14)\n\t" \
    "v_mfma_f32_16x16x32_bf16 v[160:163], v[64:67], a[8:11], 0\n\t" \
    "ds_read_b64_tr_b16 v[84:85], v97 offset:4096\n\t" \
    "s_add_u32 s34, s34, 44032\n\t" \
    "s_waitcnt vmcnt(13)\n\t" \
    "v_mfma_f32_16x16x32_bf16 v[176:179], v[64:67], a[12:15], 0\n\t" \
    "ds_read_b64_tr_b16 v[86:87], v97 offset:6144\n\t" \
    "buffer_load_dwordx4 a[68:71], v16, s[8:11], s34 offen sc1\n\t" \
    "s_waitcnt lgkmcnt(8)\n\t" \
    "v_mfma_f32_16x16x32_bf16 v[132:135], v[68:71], a[0:3], 0\n\t" \
    "ds_read_b64_tr_b16 v[88:89], v98 offset:4096\n\t" \
    "s_add_u32 s34, s34, 44032\n\t" \
    "v_mfma_f32_16x16x32_bf16 v[148:151], v[68:71], a[4:7], 0\n\t" \
    "ds_read_b64_tr_b16 v[90:91], v98 offset:6144\n\t" \
    "buffer_load_dwordx4 a[72:75], v16, s[8:11], s34 offen sc1\n\t" \
    "v_mfma_f32_16x16x32_bf16 v[164:167], v[68:71], a[8:11], 0\n\t" \
    "ds_read_b64_tr_b16 v[92:93], v99 offset:4096\n\t" \
    "s_add_u32 s34, s34, 44032\n\t" \
    "v_mfma_f32_16x16x32_bf16 v[180:183], v[68:71], a[12:15], 0\n\t" \
    "ds_read_b64_tr_b16 v[94:95], v99 offset:6144\n\t" \
    "buffer_load_dwordx4 a[76:79], v16, s[8:11], s34 offen sc1\n\t" \
    "s_waitcnt lgkmcnt(10)\n\t" \
    "v_mfma_f32_16x16x32_bf16 v[136:139], v[72:75], a[0:3], 0\n\t" \
    "s_waitcnt lgkmcnt(8)\n\t" \
    "s_min_u32 s28, %[nl], 10\n\t" \
    "s_lshl_b32 s28, s28, 15\n\t" \
    "s_add_u32 s35, s28, %[sw2]\n\t" \
    "s_mul_i32 s28, %[wv], 8192\n\t" \
    "s_add_u32 m0, s28, 131072\n\t" \
    "v_mfma_f32_16x16x32_bf16 v[152:155], v[72:75], a[4:7], 0\n\t" \
    "buffer_load_dwordx4 v21, s[20:23], s35 offen offset:0 lds\n\t" \
    "v_mfma_f32_16x16x32_bf16 v[168:171], v[72:75], a[8:11], 0\n\t" \
    "buffer_load_dwordx4 v22, s[20:23], s35 offen offset:1024 lds\n\t" \
    "v_mfma_f32_16x16x32_bf16 v[184:187], v[72:75], a[12:15], 0\n\t" \
    "buffer_load_dwordx4 v23, s[20:23], s35 offen offset:2048 lds\n\t" \
    "v_mfma_f32_16x16x32_bf16 v[140:143], v[76:79], a[0:3], 0\n\t" \
    "buffer_load_dwordx4 v24, s[20:23], s35 offen offset:3072 lds\n\t" \
    "v_mfma_f32_16x16x32_bf16 v[156:159], v[76:79], a[4:7], 0\n\t" \
    "v_mfma_f32_16x16x32_bf16 v[172:175], v[76:79], a[8:11], 0\n\t" \
    "v_mfma_f32_16x16x32_bf16 v[188:191], v[76:79], a[12:15], 0\n\t" \
    "s_waitcnt vmcnt(19) lgkmcnt(6)\n\t" \
    "v_mfma_f32_16x16x32_bf16 v[128:131], v[80:83], a[16:19], v[128:131]\n\t" \
    "ds_read_b64_tr_b16 v[64:65], v100 offset:0\n\t" \
    "s_min_u32 s28, %[nl], 5\n\t" \
    "s_lshl_b32 s28, s28, 10\n\t" \
    "s_add_u32 s34, s28, %[sh2]\n\t" \
    "s_waitcnt vmcnt(18)\n\t" \
    "v_mfma_f32_16x16x32_bf16 v[144:147], v[80:83], a[20:23], v[144:147]\n\t" \
    "ds_read_b64_tr_b16 v[66:67], v100 offset:2048\n\t" \
    "buffer_load_dwordx4 a[80:83], v16, s[8:11], s34 offen sc1\n\t" \
    "s_waitcnt vmcnt(18)\n\t" \
    "v_mfma_f32_16x16x32_bf16 v[160:163], v[80:83], a[24:27], v[160:163]\n\t" \
    "ds_read_b64_tr_b16 v[68:69], v101 offset:0\n\t" \
    "s_add_u32 s34, s34, 44032\n\t" \
    "s_waitcnt vmcnt(17)\n\t" \
    "v_mfma_f32_16x16x32_bf16 v[176:179], v[80:83], a[28:31], v[176:179]\n\t" \
    "ds_read_b64_tr_b16 v[70:71], v101 offset:2048\n\t" \
    "buffer_load_dwordx4 a[84:87], v16, s[8:11], s34 offen sc1\n\t" \
    "s_waitcnt lgkmcnt(8)\n\t" \
    "v_mfma_f32_16x16x32_bf16 v[132:135], v[84:87], a[16:19], v[132:135]\n\t" \
    "ds_read_b64_tr_b16 v[72:73], v102 offset:0\n\t" \
    "s_add_u32 s34, s34, 44032\n\t" \
    "v_mfma_f32_16x16x32_bf16 v[148:151], v[84:87], a[20:23], v[148:151]\n\t" \
    "ds_read_b64_tr_b16 v[74:75], v102 offset:2048\n\t" \
    "buffer_load_dwordx4 a[88:91], v16, s[8:11], s34 offen sc1\n\t" \
    "v_mfma_f32_16x16x32_bf16 v[164:167], v[84:87], a[24:27], v[164:167]\n\t" \
    "ds_read_b64_tr_b16 v[76:77], v103 offset:0\n\t" \
    "s_add_u32 s34, s34, 44032\n\t" \
    "v_mfma_f32_16x16x32_bf16 v[180:183], v[84:87], a[28:31], v[180:183]\n\t" \
    "ds_read_b64_tr_b16 v[78:79], v103 offset:2048\n\t" \
    "buffer_load_dwordx4 a[92:95], v16, s[8:11], s34 offen sc1\n\t" \
    "s_waitcnt lgkmcnt(10)\n\t" \
    "v_mfma_f32_16x16x32_bf16 v[136:139], v[88:91], a[16:19], v[136:139]\n\t" \
    "v_mfma_f32_16x16x32_bf16 v[152:155], v[88:91], a[20:23], v[152:155]\n\t" \
    "v_mfma_f32_16x16x32_bf16 v[168:171], v[88:91], a[24:27], v[168:171]\n\t" \
    "v_mfma_f32_16x16x32_bf16 v[184:187], v[88:91], a[28:31], v[184:187]\n\t" \
    "s_waitcnt lgkmcnt(8)\n\t" \
    "v_mfma_f32_16x16x32_bf16 v[140:143], v[92:95], a[16:19], v[140:143]\n\t" \
    "v_mfma_f32_16x16x32_bf16 v[156:159], v[92:95], a[20:23], v[156:159]\n\t" \
    "v_mfma_f32_16x16x32_bf16 v[172:175], v[92:95], a[24:27], v[172:175]\n\t" \
    "v_mfma_f32_16x16x32_bf16 v[188:191], v[92:95], a[28:31], v[188:191]\n\t" \
    "s_waitcnt vmcnt(19) lgkmcnt(6)\n\t" \
    "v_mfma_f32_16x16x32_bf16 v[128:131], v[64:67], a[32:35], v[128:131]\n\t" \
    "ds_read_b64_tr_b16 v[80:81], v100 offset:4096\n\t" \
    "s_min_u32 s28, %[nl], 6\n\t" \
    "s_lshl_b32 s28, s28, 10\n\t" \
    "s_add_u32 s34, s28, %[sh2]\n\t" \
    "s_waitcnt vmcnt(18)\n\t" \
    "v_mfma_f32_16x16x32_bf16 v[144:147], v[64:67], a[36:39], v[144:147]\n\t" \
    "ds_read_b64_tr_b16 v[82:83], v100 offset:6144\n\t" \
    "buffer_load_dwordx4 a[96:99], v16, s[8:11], s34 offen sc1\n\t" \
    "s_waitcnt vmcnt(18)\n\t" \
    "v_mfma_f32_16x16x32_bf16 v[160:163], v[64:67], a[40:43], v[160:163]\n\t" \
    "ds_read_b64_tr_b16 v[84:85], v101 offset:4096\n\t" \
    "s_add_u32 s34, s34, 44032\n\t" \
    "s_waitcnt vmcnt(17)\n\t" \
    "v_mfma_f32_16x16x32_bf16 v[176:179], v[64:67], a[44:47], v[176:179]\n\t" \
    "ds_read_b64_tr_b16 v[86:87], v101 offset:6144\n\t" \
    "buffer_load_dwordx4 a[100:103], v16, s[8:11], s34 offen sc1\n\t" \
    "s_waitcnt lgkmcnt(8)\n\t" \
    "v_mfma_f32_16x16x32_bf16 v[132:135], v[68:71], a[32:35], v[132:135]\n\t" \
    "ds_read_b64_tr_b16 v[88:89], v102 offset:4096\n\t" \
    "s_add_u32 s34, s34, 44032\n\t" \
    "v_mfma_f32_16x16x32_bf16 v[148:151], v[68:71], a[36:39], v[148:151]\n\t" \
    "ds_read_b64_tr_b16 v[90:91], v102 offset:6144\n\t" \
    "buffer_load_dwordx4 a[104:107], v16, s[8:11], s34 offen sc1\n\t" \
    "v_mfma_f32_16x16x32_bf16 v[164:167], v[68:71], a[40:43], v[164:167]\n\t" \
    "ds_read_b64_tr_b16 v[92:93], v103 offset:4096\n\t" \
    "s_add_u32 s34, s34, 44032\n\t" \
    "v_mfma_f32_16x16x32_bf16 v[180:183], v[68:71], a[44:47], v[180:183]\n\t" \
    "ds_read_b64_tr_b16 v[94:95], v103 offset:6144\n\t" \
    "buffer_load_dwordx4 a[108:111], v16, s[8:11], s34 offen sc1\n\t" \
    "s_waitcnt lgkmcnt(10)\n\t" \
    "v_mfma_f32_16x16x32_bf16 v[136:139], v[72:75], a[32:35], v[136:139]\n\t" \
    "v_mfma_f32_16x16x32_bf16 v[152:155], v[72:75], a[36:39], v[152:155]\n\t" \
    "v_mfma_f32_16x16x32_bf16 v[168:171], v[72:75], a[40:43], v[168:171]\n\t" \
    "v_mfma_f32_16x16x32_bf16 v[184:187], v[72:75], a[44:47], v[184:187]\n\t" \
    "s_waitcnt lgkmcnt(8)\n\t" \
    "v_mfma_f32_16x16x32_bf16 v[140:143], v[76:79], a[32:35], v[140:143]\n\t" \
    "v_mfma_f32_16x16x32_bf16 v[156:159], v[76:79], a[36:39], v[156:159]\n\t" \
    "v_mfma_f32_16x16x32_bf16 v[172:175], v[76:79], a[40:43], v[172:175]\n\t" \
    "v_mfma_f32_16x16x32_bf16 v[188:191], v[76:79], a[44:47], v[188:191]\n\t" \
    "s_waitcnt vmcnt(19) lgkmcnt(6)\n\t" \
    "v_mfma_f32_16x16x32_bf16 v[128:131], v[80:83], a[48:51], v[128:131]\n\t" \
    "ds_read_b64_tr_b16 v[64:65], v104 offset:0\n\t" \
    "s_min_u32 s28, %[nl], 7\n\t" \
    "s_lshl_b32 s28, s28, 10\n\t" \
    "s_add_u32 s34, s28, %[sh2]\n\t" \
    "s_waitcnt vmcnt(18)\n\t" \
    "v_mfma_f32_16x16x32_bf16 v[144:147], v[80:83], a[52:55], v[144:147]\n\t" \
    "ds_read_b64_tr_b16 v[66:67], v104 offset:2048\n\t" \
    "buffer_load_dwordx4 a[112:115], v16, s[8:11], s34 offen sc1\n\t" \
    "s_waitcnt vmcnt(18)\n\t" \
    "v_mfma_f32_16x16x32_bf16 v[160:163], v[80:83], a[56:59], v[160:163]\n\t" \
    "ds_read_b64_tr_b16 v[68:69], v105 offset:0\n\t" \
    "s_add_u32 s34, s34, 44032\n\t" \
    "s_waitcnt vmcnt(17)\n\t" \
    "v_mfma_f32_16x16x32_bf16 v[176:179], v[80:83], a[60:63], v[176:179]\n\t" \
    "ds_read_b64_tr_b16 v[70:71], v105 offset:2048\n\t" \
    "buffer_load_dwordx4 a[116:119], v16, s[8:11], s34 offen sc1\n\t" \
    "s_waitcnt lgkmcnt(8)\n\t" \
    "v_mfma_f32_16x16x32_bf16 v[132:135], v[84:87], a[48:51], v[132:135]\n\t" \
    "ds_read_b64_tr_b16 v[72:73], v106 offset:0\n\t" \
    "s_add_u32 s34, s34, 44032\n\t" \
    "v_mfma_f32_16x16x32_bf16 v[148:151], v[84:87], a[52:55], v[148:151]\n\t" \
    "ds_read_b64_tr_b16 v[74:75], v106 offset:2048\n\t" \
    "buffer_load_dwordx4 a[120:123], v16, s[8:11], s34 offen sc1\n\t" \
    "v_mfma_f32_16x16x32_bf16 v[164:167], v[84:87], a[56:59], v[164:167]\n\t" \
    "ds_read_b64_tr_b16 v[76:77], v107 offset:0\n\t" \
    "s_add_u32 s34, s34, 44032\n\t" \
    "v_mfma_f32_16x16x32_bf16 v[180:183], v[84:87], a[60:63], v[180:183]\n\t" \
    "ds_read_b64_tr_b16 v[78:79], v107 offset:2048\n\t" \
    "buffer_load_dwordx4 a[124:127], v16, s[8:11], s34 offen sc1\n\t" \
    "s_waitcnt lgkmcnt(10)\n\t" \
    "v_mfma_f32_16x16x32_bf16 v[136:139], v[88:91], a[48:51], v[136:139]\n\t" \
    "v_mfma_f32_16x16x32_bf16 v[152:155], v[88:91], a[52:55], v[152:155]\n\t" \
    "v_mfma_f32_16x16x32_bf16 v[168:171], v[88:91], a[56:59], v[168:171]\n\t" \
    "v_mfma_f32_16x16x32_bf16 v[184:187], v[88:91], a[60:63], v[184:187]\n\t" \
    "s_waitcnt lgkmcnt(8)\n\t" \
    "v_mfma_f32_16x16x32_bf16 v[140:143], v[92:95], a[48:51], v[140:143]\n\t" \
    "v_mfma_f32_16x16x32_bf16 v[156:159], v[92:95], a[52:55], v[156:159]\n\t" \
    "v_mfma_f32_16x16x32_bf16 v[172:175], v[92:95], a[56:59], v[172:175]\n\t" \
    "v_mfma_f32_16x16x32_bf16 v[188:191], v[92:95], a[60:63], v[188:191]\n\t" \
    "s_waitcnt vmcnt(19) lgkmcnt(6)\n\t" \
    "v_mfma_f32_16x16x32_bf16 v[128:131], v[64:67], a[64:67], v[128:131]\n\t" \
    "ds_read_b64_tr_b16 v[80:81], v104 offset:4096\n\t" \
    "s_min_u32 s28, %[nl], 8\n\t" \
    "s_lshl_b32 s28, s28, 10\n\t" \
    "s_add_u32 s34, s28, %[sh2]\n\t" \
    "s_waitcnt vmcnt(18)\n\t" \
    "v_mfma_f32_16x16x32_bf16 v[144:147], v[64:67], a[68:71], v[144:147]\n\t" \
    "ds_read_b64_tr_b16 v[82:83], v104 offset:6144\n\t" \
    "buffer_load_dwordx4 a[128:131], v16, s[8:11], s34 offen sc1\n\t" \
    "s_waitcnt vmcnt(18)\n\t" \
    "v_mfma_f32_16x16x32_bf16 v[160:163], v[64:67], a[72:75], v[160:163]\n\t" \
    "ds_read_b64_tr_b16 v[84:85], v105 offset:4096\n\t" \
    "s_add_u32 s34, s34, 44032\n\t" \
    "s_waitcnt vmcnt(17)\n\t" \
    "v_mfma_f32_16x16x32_bf16 v[176:179], v[64:67], a[76:79], v[176:179]\n\t" \
    "ds_read_b64_tr_b16 v[86:87], v105 offset:6144\n\t" \
    "buffer_load_dwordx4 a[132:135], v16, s[8:11], s34 offen sc1\n\t" \
    "s_waitcnt lgkmcnt(8)\n\t" \
    "v_mfma_f32_16x16x32_bf16 v[132:135], v[68:71], a[64:67], v[132:135]\n\t" \
    "ds_read_b64_tr_b16 v[88:89], v106 offset:4096\n\t" \
    "s_add_u32 s34, s34, 44032\n\t" \
    "v_mfma_f32_16x16x32_bf16 v[148:151], v[68:71], a[68:71], v[148:151]\n\t" \
    "ds_read_b64_tr_b16 v[90:91], v106 offset:6144\n\t" \
    "buffer_load_dwordx4 a[136:139], v16, s[8:11], s34 offen sc1\n\t" \
    "v_mfma_f32_16x16x32_bf16 v[164:167], v[68:71], a[72:75], v[164:167]\n\t" \
    "ds_read_b64_tr_b16 v[92:93], v107 offset:4096\n\t" \
    "s_add_u32 s34, s34, 44032\n\t" \
    "v_mfma_f32_16x16x32_bf16 v[180:183], v[68:71], a[76:79], v[180:183]\n\t" \
    "ds_read_b64_tr_b16 v[94:95], v107 offset:6144\n\t" \
    "buffer_load_dwordx4 a[140:143], v16, s[8:11], s34 offen sc1\n\t" \
    "s_waitcnt lgkmcnt(10)\n\t" \
    "v_mfma_f32_16x16x32_bf16 v[136:139], v[72:75], a[64:67], v[136:139]\n\t" \
    "v_mfma_f32_16x16x32_bf16 v[152:155], v[72:75], a[68:71], v[152:155]\n\t" \
    "v_mfma_f32_16x16x32_bf16 v[168:171], v[72:75], a[72:75], v[168:171]\n\t" \
    "v_mfma_f32_16x16x32_bf16 v[184:187], v[72:75], a[76:79], v[184:187]\n\t" \
    "s_waitcnt lgkmcnt(8)\n\t" \
    "v_mfma_f32_16x16x32_bf16 v[140:143], v[76:79], a[64:67], v[140:143]\n\t" \
    "v_mfma_f32_16x16x32_bf16 v[156:159], v[76:79], a[68:71], v[156:159]\n\t" \
    "v_mfma_f32_16x16x32_bf16 v[172:175], v[76:79], a[72:75], v[172:175]\n\t" \
    "v_mfma_f32_16x16x32_bf16 v[188:191], v[76:79], a[76:79], v[188:191]\n\t" \
    "s_waitcnt vmcnt(15) lgkmcnt(6)\n\t" \
    "v_mfma_f32_16x16x32_bf16 v[128:131], v[80:83], a[80:83], v[128:131]\n\t" \
    "ds_read_b64_tr_b16 v[64:65], v104 offset:8192\n\t" \
    "s_min_u32 s28, %[nl], 9\n\t" \
    "s_lshl_b32 s28, s28, 10\n\t" \
    "s_add_u32 s34, s28, %[sh2]\n\t" \
    "s_waitcnt vmcnt(14)\n\t" \
    "v_mfma_f32_16x16x32_bf16 v[144:147], v[80:83], a[84:87], v[144:147]\n\t" \
    "ds_read_b64_tr_b16 v[66:67], v104 offset:10240\n\t" \
    "buffer_load_dwordx4 a[144:147], v16, s[8:11], s34 offen sc1\n\t" \
    "s_waitcnt vmcnt(14)\n\t" \
    "v_mfma_f32_16x16x32_bf16 v[160:163], v[80:83], a[88:91], v[160:163]\n\t" \
    "ds_read_b64_tr_b16 v[68:69], v105 offset:8192\n\t" \
    "s_add_u32 s34, s34, 44032\n\t" \
    "s_waitcnt vmcnt(13)\n\t" \
    "v_mfma_f32_16x16x32_bf16 v[176:179], v[80:83], a[92:95], v[176:179]\n\t" \
    "ds_read_b64_tr_b16 v[70:71], v105 offset:10240\n\t" \
    "buffer_load_dwordx4 a[148:151], v16, s[8:11], s34 offen sc1\n\t" \
    "s_waitcnt lgkmcnt(8)\n\t" \
    "v_mfma_f32_16x16x32_bf16 v[132:135], v[84:87], a[80:83], v[132:135]\n\t" \
    "ds_read_b64_tr_b16 v[72:73], v106 offset:8192\n\t" \
    "s_add_u32 s34, s34, 44032\n\t" \
    "v_mfma_f32_16x16x32_bf16 v[148:151], v[84:87], a[84:87], v[148:151]\n\t" \
    "ds_read_b64_tr_b16 v[74:75], v106 offset:10240\n\t" \
    "buffer_load_dwordx4 a[152:155], v16, s[8:11], s34 offen sc1\n\t" \
    "v_mfma_f32_16x16x32_bf16 v[164:167], v[84:87], a[88:91], v[164:167]\n\t" \
    "ds_read_b64_tr_b16 v[76:77], v107 offset:8192\n\t" \
    "s_add_u32 s34, s34, 44032\n\t" \
    "v_mfma_f32_16x16x32_bf16 v[180:183], v[84:87], a[92:95], v[180:183]\n\t" \
    "ds_read_b64_tr_b16 v[78:79], v107 offset:10240\n\t" \
    "buffer_load_dwordx4 a[156:159], v16, s[8:11], s34 offen sc1\n\t" \
    "s_waitcnt lgkmcnt(10)\n\t" \
    "v_mfma_f32_16x16x32_bf16 v[136:139], v[88:91], a[80:83], v[136:139]\n\t" \
    "v_mfma_f32_16x16x32_bf16 v[152:155], v[88:91], a[84:87], v[152:155]\n\t" \
    "v_mfma_f32_16x16x32_bf16 v[168:171], v[88:91], a[88:91], v[168:171]\n\t" \
    "v_mfma_f32_16x16x32_bf16 v[184:187], v[88:91], a[92:95], v[184:187]\n\t" \
    "s_waitcnt lgkmcnt(8)\n\t" \
    "v_mfma_f32_16x16x32_bf16 v[140:143], v[92:95], a[80:83], v[140:143]\n\t" \
    "v_mfma_f32_16x16x32_bf16 v[156:159], v[92:95], a[84:87], v[156:159]\n\t" \
    "v_mfma_f32_16x16x32_bf16 v[172:175], v[92:95], a[88:91], v[172:175]\n\t" \
    "v_mfma_f32_16x16x32_bf16 v[188:191], v[92:95], a[92:95], v[188:191]\n\t" \
    "s_waitcnt vmcnt(15) lgkmcnt(6)\n\t" \
    "v_mfma_f32_16x16x32_bf16 v[128:131], v[64:67], a[96:99], v[128:131]\n\t" \
    "ds_read_b64_tr_b16 v[80:81], v104 offset:12288\n\t" \
    "s_min_u32 s28, %[nl], 10\n\t" \
    "s_lshl_b32 s28, s28, 10\n\t" \
    "s_add_u32 s34, s28, %[sh2]\n\t" \
    "s_waitcnt vmcnt(14)\n\t" \
    "v_mfma_f32_16x16x32_bf16 v[144:147], v[64:67], a[100:103], v[144:147]\n\t" \
    "ds_read_b64_tr_b16 v[82:83], v104 offset:14336\n\t" \
    "buffer_load_dwordx4 a[160:163], v16, s[8:11], s34 offen sc1\n\t" \
    "s_waitcnt vmcnt(14)\n\t" \
    "v_mfma_f32_16x16x32_bf16 v[160:163], v[64:67], a[104:107], v[160:163]\n\t" \
    "ds_read_b64_tr_b16 v[84:85], v105 offset:12288\n\t" \
    "s_add_u32 s34, s34, 44032\n\t" \
    "s_waitcnt vmcnt(13)\n\t" \
    "v_mfma_f32_16x16x32_bf16 v[176:179], v[64:67], a[108:111], v[176:179]\n\t" \
    "ds_read_b64_tr_b16 v[86:87], v105 offset:14336\n\t" \
    "buffer_load_dwordx4 a[164:167], v16, s[8:11], s34 offen sc1\n\t" \
    "s_waitcnt lgkmcnt(8)\n\t" \
    "v_mfma_f32_16x16x32_bf16 v[132:135], v[68:71], a[96:99], v[132:135]\n\t" \
    "ds_read_b64_tr_b16 v[88:89], v106 offset:12288\n\t" \
    "s_add_u32 s34, s34, 44032\n\t" \
    "v_mfma_f32_16x16x32_bf16 v[148:151], v[68:71], a[100:103], v[148:151]\n\t" \
    "ds_read_b64_tr_b16 v[90:91], v106 offset:14336\n\t" \
    "buffer_load_dwordx4 a[168:171], v16, s[8:11], s34 offen sc1\n\t" \
    "v_mfma_f32_16x16x32_bf16 v[164:167], v[68:71], a[104:107], v[164:167]\n\t" \
    "ds_read_b64_tr_b16 v[92:93], v107 offset:12288\n\t" \
    "s_add_u32 s34, s34, 44032\n\t" \
    "v_mfma_f32_16x16x32_bf16 v[180:183], v[68:71], a[108:111], v[180:183]\n\t" \
    "ds_read_b64_tr_b16 v[94:95], v107 offset:14336\n\t" \
    "buffer_load_dwordx4 a[172:175], v16, s[8:11], s34 offen sc1\n\t" \
    "s_waitcnt lgkmcnt(10)\n\t" \
    "v_mfma_f32_16x16x32_bf16 v[136:139], v[72:75], a[96:99], v[136:139]\n\t" \
    "v_mfma_f32_16x16x32_bf16 v[152:155], v[72:75], a[100:103], v[152:155]\n\t" \
    "v_mfma_f32_16x16x32_bf16 v[168:171], v[72:75], a[104:107], v[168:171]\n\t" \
    "v_mfma_f32_16x16x32_bf16 v[184:187], v[72:75], a[108:111], v[184:187]\n\t" \
    "s_waitcnt lgkmcnt(8)\n\t" \
    "v_mfma_f32_16x16x32_bf16 v[140:143], v[76:79], a[96:99], v[140:143]\n\t" \
    "v_mfma_f32_16x16x32_bf16 v[156:159], v[76:79], a[100:103], v[156:159]\n\t" \
    "v_mfma_f32_16x16x32_bf16 v[172:175], v[76:79], a[104:107], v[172:175]\n\t" \
    "v_mfma_f32_16x16x32_bf16 v[188:191], v[76:79], a[108:111], v[188:191]\n\t" \
    "s_waitcnt vmcnt(15) lgkmcnt(6)\n\t" \
    "v_mfma_f32_16x16x32_bf16 v[128:131], v[80:83], a[112:115], v[128:131]\n\t" \
    "ds_read_b64_tr_b16 v[64:65], v104 offset:16384\n\t" \
    "s_waitcnt vmcnt(14)\n\t" \
    "v_mfma_f32_16x16x32_bf16 v[144:147], v[80:83], a[116:119], v[144:147]\n\t" \
    "ds_read_b64_tr_b16 v[66:67], v104 offset:18432\n\t" \
    "s_waitcnt vmcnt(13)\n\t" \
    "v_mfma_f32_16x16x32_bf16 v[160:163], v[80:83], a[120:123], v[160:163]\n\t" \
    "ds_read_b64_tr_b16 v[68:69], v105 offset:16384\n\t" \
    "s_waitcnt vmcnt(12)\n\t" \
    "v_mfma_f32_16x16x32_bf16 v[176:179], v[80:83], a[124:127], v[176:179]\n\t" \
    "ds_read_b64_tr_b16 v[70:71], v105 offset:18432\n\t" \
    "s_waitcnt lgkmcnt(8)\n\t" \
    "v_mfma_f32_16x16x32_bf16 v[132:135], v[84:87], a[112:115], v[132:135]\n\t" \
    "ds_read_b64_tr_b16 v[72:73], v106 offset:16384\n\t" \
    "v_mfma_f32_16x16x32_bf16 v[148:151], v[84:87], a[116:119], v[148:151]\n\t" \
    "ds_read_b64_tr_b16 v[74:75], v106 offset:18432\n\t" \
    "v_mfma_f32_16x16x32_bf16 v[164:167], v[84:87], a[120:123], v[164:167]\n\t" \
    "ds_read_b64_tr_b16 v[76:77], v107 offset:16384\n\t" \
    "v_mfma_f32_16x16x32_bf16 v[180:183], v[84:87], a[124:127], v[180:183]\n\t" \
    "ds_read_b64_tr_b16 v[78:79], v107 offset:18432\n\t" \
    "s_waitcnt lgkmcnt(10)\n\t" \
    "v_mfma_f32_16x16x32_bf16 v[136:139], v[88:91], a[112:115], v[136:139]\n\t" \
    "v_mfma_f32_16x16x32_bf16 v[152:155], v[88:91], a[116:119], v[152:155]\n\t" \
    "v_mfma_f32_16x16x32_bf16 v[168:171], v[88:91], a[120:123], v[168:171]\n\t" \
    "v_mfma_f32_16x16x32_bf16 v[184:187], v[88:91], a[124:127], v[184:187]\n\t" \
    "s_waitcnt lgkmcnt(8)\n\t" \
    "v_mfma_f32_16x16x32_bf16 v[140:143], v[92:95], a[112:115], v[140:143]\n\t" \
    "v_mfma_f32_16x16x32_bf16 v[156:159], v[92:95], a[116:119], v[156:159]\n\t" \
    "v_mfma_f32_16x16x32_bf16 v[172:175], v[92:95], a[120:123], v[172:175]\n\t" \
    "v_mfma_f32_16x16x32_bf16 v[188:191], v[92:95], a[124:127], v[188:191]\n\t" \
    "s_waitcnt vmcnt(11) lgkmcnt(6)\n\t" \
    "v_mfma_f32_16x16x32_bf16 v[128:131], v[64:67], a[128:131], v[128:131]\n\t" \
    "ds_read_b64_tr_b16 v[80:81], v104 offset:20480\n\t" \
    "s_waitcnt vmcnt(10)\n\t" \
    "v_mfma_f32_16x16x32_bf16 v[144:147], v[64:67], a[132:135], v[144:147]\n\t" \
    "ds_read_b64_tr_b16 v[82:83], v104 offset:22528\n\t" \
    "s_waitcnt vmcnt(9)\n\t" \
    "v_mfma_f32_16x16x32_bf16 v[160:163], v[64:67], a[136:139], v[160:163]\n\t" \
    "ds_read_b64_tr_b16 v[84:85], v105 offset:20480\n\t" \
    "s_waitcnt vmcnt(8)\n\t" \
    "v_mfma_f32_16x16x32_bf16 v[176:179], v[64:67], a[140:143], v[176:179]\n\t" \
    "ds_read_b64_tr_b16 v[86:87], v105 offset:22528\n\t" \
    "s_waitcnt lgkmcnt(8)\n\t" \
    "v_mfma_f32_16x16x32_bf16 v[132:135], v[68:71], a[128:131], v[132:135]\n\t" \
    "ds_read_b64_tr_b16 v[88:89], v106 offset:20480\n\t" \
    "v_mfma_f32_16x16x32_bf16 v[148:151], v[68:71], a[132:135], v[148:151]\n\t" \
    "ds_read_b64_tr_b16 v[90:91], v106 offset:22528\n\t" \
    "v_mfma_f32_16x16x32_bf16 v[164:167], v[68:71], a[136:139], v[164:167]\n\t" \
    "ds_read_b64_tr_b16 v[92:93], v107 offset:20480\n\t" \
    "v_mfma_f32_16x16x32_bf16 v[180:183], v[68:71], a[140:143], v[180:183]\n\t" \
    "ds_read_b64_tr_b16 v[94:95], v107 offset:22528\n\t" \
    "s_waitcnt lgkmcnt(10)\n\t" \
    "v_mfma_f32_16x16x32_bf16 v[136:139], v[72:75], a[128:131], v[136:139]\n\t" \
    "v_mfma_f32_16x16x32_bf16 v[152:155], v[72:75], a[132:135], v[152:155]\n\t" \
    "v_mfma_f32_16x16x32_bf16 v[168:171], v[72:75], a[136:139], v[168:171]\n\t" \
    "v_mfma_f32_16x16x32_bf16 v[184:187], v[72:75], a[140:143], v[184:187]\n\t" \
    "s_waitcnt lgkmcnt(8)\n\t" \
    "v_mfma_f32_16x16x32_bf16 v[140:143], v[76:79], a[128:131], v[140:143]\n\t" \
    "v_mfma_f32_16x16x32_bf16 v[156:159], v[76:79], a[132:135], v[156:159]\n\t" \
    "v_mfma_f32_16x16x32_bf16 v[172:175], v[76:79], a[136:139], v[172:175]\n\t" \
    "v_mfma_f32_16x16x32_bf16 v[188:191], v[76:79], a[140:143], v[188:191]\n\t" \
    "s_waitcnt vmcnt(0)\n\t" \
    "s_waitcnt lgkmcnt(6)\n\t" \
    "v_mfma_f32_16x16x32_bf16 v[128:131], v[80:83], a[144:147], v[128:131]\n\t" \
    "ds_read_b64_tr_b16 v[64:65], v96 offset:0\n\t" \
    "v_mfma_f32_16x16x32_bf16 v[144:147], v[80:83], a[148:151], v[144:147]\n\t" \
    "ds_read_b64_tr_b16 v[66:67], v96 offset:2048\n\t" \
    "v_mfma_f32_16x16x32_bf16 v[160:163], v[80:83], a[152:155], v[160:163]\n\t" \
    "ds_read_b64_tr_b16 v[68:69], v97 offset:0\n\t" \
    "v_mfma_f32_16x16x32_bf16 v[176:179], v[80:83], a[156:159], v[176:179]\n\t" \
    "ds_read_b64_tr_b16 v[70:71], v97 offset:2048\n\t" \
    "s_waitcnt lgkmcnt(8)\n\t" \
    "v_mfma_f32_16x16x32_bf16 v[132:135], v[84:87], a[144:147], v[132:135]\n\t" \
    "ds_read_b64_tr_b16 v[72:73], v98 offset:0\n\t" \
    "v_mfma_f32_16x16x32_bf16 v[148:151], v[84:87], a[148:151], v[148:151]\n\t" \
    "ds_read_b64_tr_b16 v[74:75], v98 offset:2048\n\t" \
    "v_mfma_f32_16x16x32_bf16 v[164:167], v[84:87], a[152:155], v[164:167]\n\t" \
    "ds_read_b64_tr_b16 v[76:77], v99 offset:0\n\t" \
    "v_mfma_f32_16x16x32_bf16 v[180:183], v[84:87], a[156:159], v[180:183]\n\t" \
    "ds_read_b64_tr_b16 v[78:79], v99 offset:2048\n\t" \
    "s_waitcnt lgkmcnt(10)\n\t" \
    "v_mfma_f32_16x16x32_bf16 v[136:139], v[88:91], a[144:147], v[136:139]\n\t" \
    "v_mfma_f32_16x16x32_bf16 v[152:155], v[88:91], a[148:151], v[152:155]\n\t" \
    "v_mfma_f32_16x16x32_bf16 v[168:171], v[88:91], a[152:155], v[168:171]\n\t" \
    "v_mfma_f32_16x16x32_bf16 v[184:187], v[88:91], a[156:159], v[184:187]\n\t" \
    "s_waitcnt lgkmcnt(8)\n\t" \
    "v_mfma_f32_16x16x32_bf16 v[140:143], v[92:95], a[144:147], v[140:143]\n\t" \
    "v_mfma_f32_16x16x32_bf16 v[156:159], v[92:95], a[148:151], v[156:159]\n\t" \
    "v_mfma_f32_16x16x32_bf16 v[172:175], v[92:95], a[152:155], v[172:175]\n\t" \
    "v_mfma_f32_16x16x32_bf16 v[188:191], v[92:95], a[156:159], v[188:191]\n\t" \
    "s_cmp_lt_u32 %[nl], 10\n\t" \
    "s_cbranch_scc1 SKIP_%=\n\t" \
    "s_waitcnt lgkmcnt(6)\n\t" \
    "v_mfma_f32_16x16x32_bf16 v[128:131], v[64:67], a[160:163], v[128:131]\n\t" \
    "v_mfma_f32_16x16x32_bf16 v[144:147], v[64:67], a[164:167], v[144:147]\n\t" \
    "v_mfma_f32_16x16x32_bf16 v[160:163], v[64:67], a[168:171], v[160:163]\n\t" \
    "v_mfma_f32_16x16x32_bf16 v[176:179], v[64:67], a[172:175], v[176:179]\n\t" \
    "s_waitcnt lgkmcnt(4)\n\t" \
    "v_mfma_f32_16x16x32_bf16 v[132:135], v[68:71], a[160:163], v[132:135]\n\t" \
    "v_mfma_f32_16x16x32_bf16 v[148:151], v[68:71], a[164:167], v[148:151]\n\t" \
    "v_mfma_f32_16x16x32_bf16 v[164:167], v[68:71], a[168:171], v[164:167]\n\t" \
    "v_mfma_f32_16x16x32_bf16 v[180:183], v[68:71], a[172:175], v[180:183]\n\t" \
    "s_waitcnt lgkmcnt(2)\n\t" \
    "v_mfma_f32_16x16x32_bf16 v[136:139], v[72:75], a[160:163], v[136:139]\n\t" \
    "v_mfma_f32_16x16x32_bf16 v[152:155], v[72:75], a[164:167], v[152:155]\n\t" \
    "v_mfma_f32_16x16x32_bf16 v[168:171], v[72:75], a[168:171], v[168:171]\n\t" \
    "v_mfma_f32_16x16x32_bf16 v[184:187], v[72:75], a[172:175], v[184:187]\n\t" \
    "s_waitcnt lgkmcnt(0)\n\t" \
    "v_mfma_f32_16x16x32_bf16 v[140:143], v[76:79], a[160:163], v[140:143]\n\t" \
    "v_mfma_f32_16x16x32_bf16 v[156:159], v[76:79], a[164:167], v[156:159]\n\t" \
    "v_mfma_f32_16x16x32_bf16 v[172:175], v[76:79], a[168:171], v[172:175]\n\t" \
    "v_mfma_f32_16x16x32_bf16 v[188:191], v[76:79], a[172:175], v[188:191]\n\t" \
    "SKIP_%=:\n\t" \
    "s_waitcnt vmcnt(0) lgkmcnt(0)\n\t" \
    "s_barrier\n\t" \
    "s_lshl_b32 s28, %[wv], 12\n\t" \
    "v_add_u32 v96, s28, v16\n\t" \
    "ds_write_b128 v96, v[128:131] offset:0\n\t" \
    "ds_write_b128 v96, v[132:135] offset:1024\n\t" \
    "ds_write_b128 v96, v[136:139] offset:2048\n\t" \
    "s_nop 1\n\t" \
    "ds_write_b128 v96, v[140:143] offset:3072\n\t" \
    "ds_write_b128 v96, v[144:147] offset:16384\n\t" \
    "ds_write_b128 v96, v[148:151] offset:17408\n\t" \
    "ds_write_b128 v96, v[152:155] offset:18432\n\t" \
    "ds_write_b128 v96, v[156:159] offset:19456\n\t" \
    "ds_write_b128 v96, v[160:163] offset:32768\n\t" \
    "ds_write_b128 v96, v[164:167] offset:33792\n\t" \
    "ds_write_b128 v96, v[168:171] offset:34816\n\t" \
    "ds_write_b128 v96, v[172:175] offset:35840\n\t" \
    "ds_write_b128 v96, v[176:179] offset:49152\n\t" \
    "ds_write_b128 v96, v[180:183] offset:50176\n\t" \
    "ds_write_b128 v96, v[184:187] offset:51200\n\t" \
    "ds_write_b128 v96, v[188:191] offset:52224\n\t" \
    "s_lshl_b32 s28, %[wv], 14\n\t" \
    "v_add_u32 v96, s28, v16\n\t" \
    "s_waitcnt lgkmcnt(0)\n\t" \
    "s_barrier\n\t" \
    "ds_read_b128 v[128:131], v96 offset:0\n\t" \
    "ds_read_b128 v[132:135], v96 offset:1024\n\t" \
    "ds_read_b128 v[136:139], v96 offset:2048\n\t" \
    "ds_read_b128 v[140:143], v96 offset:3072\n\t" \
    "ds_read_b128 v[144:147], v96 offset:4096\n\t" \
    "ds_read_b128 v[148:151], v96 offset:5120\n\t" \
    "ds_read_b128 v[152:155], v96 offset:6144\n\t" \
    "ds_read_b128 v[156:159], v96 offset:7168\n\t" \
    "ds_read_b128 v[160:163], v96 offset:8192\n\t" \
    "ds_read_b128 v[164:167], v96 offset:9216\n\t" \
    "ds_read_b128 v[168:171], v96 offset:10240\n\t" \
    "ds_read_b128 v[172:175], v96 offset:11264\n\t" \
    "ds_read_b128 v[176:179], v96 offset:12288\n\t" \
    "ds_read_b128 v[180:183], v96 offset:13312\n\t" \
    "ds_read_b128 v[184:187], v96 offset:14336\n\t" \
    "ds_read_b128 v[188:191], v96 offset:15360\n\t" \
    "s_waitcnt lgkmcnt(11)\n\t" \
    "v_pk_add_f32 v[128:129], v[128:129], v[144:145]\n\t" \
    "s_waitcnt lgkmcnt(3)\n\t" \
    "v_pk_add_f32 v[160:161], v[160:161], v[176:177]\n\t" \
    "v_pk_add_f32 v[128:129], v[128:129], v[160:161]\n\t" \
    "v_pk_add_f32 v[130:131], v[130:131], v[146:147]\n\t" \
    "v_pk_add_f32 v[162:163], v[162:163], v[178:179]\n\t" \
    "v_pk_add_f32 v[130:131], v[130:131], v[162:163]\n\t" \
    "v_pk_add_f32 v[132:133], v[132:133], v[148:149]\n\t" \
    "s_waitcnt lgkmcnt(2)\n\t" \
    "v_pk_add_f32 v[164:165], v[164:165], v[180:181]\n\t" \
    "v_pk_add_f32 v[132:133], v[132:133], v[164:165]\n\t" \
    "v_pk_add_f32 v[134:135], v[134:135], v[150:151]\n\t" \
    "v_pk_add_f32 v[166:167], v[166:167], v[182:183]\n\t" \
    "v_pk_add_f32 v[134:135], v[134:135], v[166:167]\n\t" \
    "v_pk_add_f32 v[136:137], v[136:137], v[152:153]\n\t" \
    "s_waitcnt lgkmcnt(1)\n\t" \
    "v_pk_add_f32 v[168:169], v[168:169], v[184:185]\n\t" \
    "v_pk_add_f32 v[136:137], v[136:137], v[168:169]\n\t" \
    "v_pk_add_f32 v[138:139], v[138:139], v[154:155]\n\t" \
    "v_pk_add_f32 v[170:171], v[170:171], v[186:187]\n\t" \
    "v_pk_add_f32 v[138:139], v[138:139], v[170:171]\n\t" \
    "v_pk_add_f32 v[140:141], v[140:141], v[156:157]\n\t" \
    "s_waitcnt lgkmcnt(0)\n\t" \
    "v_pk_add_f32 v[172:173], v[172:173], v[188:189]\n\t" \
    "v_pk_add_f32 v[140:141], v[140:141], v[172:173]\n\t" \
    "v_pk_add_f32 v[142:143], v[142:143], v[158:159]\n\t" \
    "v_pk_add_f32 v[174:175], v[174:175], v[190:191]\n\t" \
    "v_pk_add_f32 v[142:143], v[142:143], v[174:175]\n\t" \
    "v_lshlrev_b32 v200, 16, v36\n\t" \
    "v_and_b32 v201, 0xffff0000, v36\n\t" \
    "v_lshlrev_b32 v204, 16, v28\n\t" \
    "v_and_b32 v205, 0xffff0000, v28\n\t" \
    "v_lshlrev_b32 v202, 16, v37\n\t" \
    "v_and_b32 v203, 0xffff0000, v37\n\t" \
    "v_lshlrev_b32 v206, 16, v29\n\t" \
    "v_and_b32 v207, 0xffff0000, v29\n\t" \
    "v_pk_add_f32 v[128:129], v[128:129], v[200:201]\n\t" \
    "v_pk_fma_f32 v[128:129], v[128:129], v[44:45], v[204:205]\n\t" \
    "v_pk_add_f32 v[130:131], v[130:131], v[202:203]\n\t" \
    "v_pk_fma_f32 v[130:131], v[130:131], v[46:47], v[206:207]\n\t" \
    "v_lshlrev_b32 v208, 16, v38\n\t" \
    "v_and_b32 v209, 0xffff0000, v38\n\t" \
    "v_lshlrev_b32 v212, 16, v30\n\t" \
    "v_and_b32 v213, 0xffff0000, v30\n\t" \
    "v_lshlrev_b32 v210, 16, v39\n\t" \
    "v_and_b32 v211, 0xffff0000, v39\n\t" \
    "v_lshlrev_b32 v214, 16, v31\n\t" \
    "v_and_b32 v215, 0xffff0000, v31\n\t" \
    "v_pk_add_f32 v[132:133], v[132:133], v[208:209]\n\t" \
    "v_pk_fma_f32 v[132:133], v[132:133], v[48:49], v[212:213]\n\t" \
    "v_pk_add_f32 v[134:135], v[134:135], v[210:211]\n\t" \
    "v_pk_fma_f32 v[134:135], v[134:135], v[50:51], v[214:215]\n\t" \
    "v_lshlrev_b32 v200, 16, v40\n\t" \
    "v_and_b32 v201, 0xffff0000, v40\n\t" \
    "v_lshlrev_b32 v204, 16, v32\n\t" \
    "v_and_b32 v205, 0xffff0000, v32\n\t" \
    "v_lshlrev_b32 v202, 16, v41\n\t" \
    "v_and_b32 v203, 0xffff0000, v41\n\t" \
    "v_lshlrev_b32 v206, 16, v33\n\t" \
    "v_and_b32 v207, 0xffff0000, v33\n\t" \
    "v_pk_add_f32 v[136:137], v[136:137], v[200:201]\n\t" \
    "v_pk_fma_f32 v[136:137], v[136:137], v[52:53], v[204:205]\n\t" \
    "v_pk_add_f32 v[138:139], v[138:139], v[202:203]\n\t" \
    "v_pk_fma_f32 v[138:139], v[138:139], v[54:55], v[206:207]\n\t" \
    "v_lshlrev_b32 v208, 16, v42\n\t" \
    "v_and_b32 v209, 0xffff0000, v42\n\t" \
    "v_lshlrev_b32 v212, 16, v34\n\t" \
    "v_and_b32 v213, 0xffff0000, v34\n\t" \
    "v_lshlrev_b32 v210, 16, v43\n\t" \
    "v_and_b32 v211, 0xffff0000, v43\n\t" \
    "v_lshlrev_b32 v214, 16, v35\n\t" \
    "v_and_b32 v215, 0xffff0000, v35\n\t" \
    "v_pk_add_f32 v[140:141], v[140:141], v[208:209]\n\t" \
    "v_pk_fma_f32 v[140:141], v[140:141], v[56:57], v[212:213]\n\t" \
    "v_pk_add_f32 v[142:143], v[142:143], v[210:211]\n\t" \
    "v_pk_fma_f32 v[142:143], v[142:143], v[58:59], v[214:215]\n\t" \
    "v_accvgpr_read_b32 v97, a249\n\t" \
    "s_mov_b32 s24, %[olo]\n\t" \
    "s_mov_b32 s25, %[ohi]\n\t" \
    "s_mov_b32 s26, 0x200000\n\t" \
    "s_mov_b32 s27, 0x00020000\n\t" \
    "v_cvt_pk_bf16_f32 v100, v128, v129\n\t" \
    "v_cvt_pk_bf16_f32 v101, v130, v131\n\t" \
    "v_cvt_pk_bf16_f32 v102, v132, v133\n\t" \
    "v_cvt_pk_bf16_f32 v103, v134, v135\n\t" \
    "s_nop 0\n\t" \
    "v_permlane16_swap_b32 v100, v102\n\t" \
    "v_permlane16_swap_b32 v101, v103\n\t" \
    "buffer_store_dwordx4 v[100:103], v97, s[24:27], 0 offen offset:0\n\t" \
    "v_cvt_pk_bf16_f32 v104, v136, v137\n\t" \
    "v_cvt_pk_bf16_f32 v105, v138, v139\n\t" \
    "v_cvt_pk_bf16_f32 v106, v140, v141\n\t" \
    "v_cvt_pk_bf16_f32 v107, v142, v143\n\t" \
    "s_nop 0\n\t" \
    "v_permlane16_swap_b32 v104, v106\n\t" \
    "v_permlane16_swap_b32 v105, v107\n\t" \
    "buffer_store_dwordx4 v[104:107], v97, s[24:27], 0 offen offset:64\n\t" \
    "s_waitcnt vmcnt(0)\n\t" \
    ".Lkt_end:\n\t"
#define F_CLOB "v24", "v25", "v26", "v27", "v28", "v29", "v30", "v31", "v32", "v33", "v34", "v35", "v36", "v37", "v38", "v39", "v40", "v41", "v42", "v43", "v44", "v45", "v46", "v47", "v48", "v49", "v50", "v51", "v52", "v53", "v54", "v55", "v56", "v57", "v58", "v59", "v60", "v61", "v62", "v63", "v64", "v65", "v66", "v67", "v68", "v69", "v70", "v71", "v72", "v73", "v74", "v75", "v76", "v77", "v78", "v79", "v80", "v81", "v82", "v83", "v84", "v85", "v86", "v87", "v88", "v89", "v90", "v91", "v92", "v93", "v94", "v95", "v96", "v97", "v98", "v99", "v100", "v101", "v102", "v103", "v104", "v105", "v106", "v107", "v108", "v109", "v110", "v111", "v112", "v113", "v114", "v115", "v116", "v117", "v118", "v119", "v120", "v121", "v122", "v123", "v124", "v125", "v126", "v127", "v128", "v129", "v130", "v131", "v132", "v133", "v134", "v135", "v136", "v137", "v138", "v139", "v140", "v141", "v142", "v143", "v144", "v145", "v146", "v147", "v148", "v149", "v150", "v151", "v152", "v153", "v154", "v155", "v156", "v157", "v158", "v159", "v160", "v161", "v162", "v163", "v164", "v165", "v166", "v167", "v168", "v169", "v170", "v171", "v172", "v173", "v174", "v175", "v176", "v177", "v178", "v179", "v180", "v181", "v182", "v183", "v184", "v185", "v186", "v187", "v188", "v189", "v190", "v191", "v192", "v193", "v194", "v195", "v196", "v197", "v198", "v199", "v200", "v201", "v202", "v203", "v204", "v205", "v206", "v207", "v208", "v209", "v210", "v211", "v212", "v213", "v214", "v215", "v216", "v217", "v218", "v219", "v220", "v221", "v222", "v223", "v224", "v225", "v226", "v227", "v228", "v229", "v230", "v231", "v232", "v233", "v234", "v235", "v236", "v237", "v238", "v239", "v240", "v241", "v242", "v243", "v244", "v245", "v246", "v247", "v248", "v249", "v250", "v251", "v252", "v253", "v254", "v255", "a0", "a1", "a2", "a3", "a4", "a5", "a6", "a7", "a8", "a9", "a10", "a11", "a12", "a13", "a14", "a15", "a16", "a17", "a18", "a19", "a20", "a21", "a22", "a23", "a24", "a25", "a26", "a27", "a28", "a29", "a30", "a31", "a32", "a33", "a34", "a35", "a36", "a37", "a38", "a39", "a40", "a41", "a42", "a43", "a44", "a45", "a46", "a47", "a48", "a49", "a50", "a51", "a52", "a53", "a54", "a55", "a56", "a57", "a58", "a59", "a60", "a61", "a62", "a63", "a64", "a65", "a66", "a67", "a68", "a69", "a70", "a71", "a72", "a73", "a74", "a75", "a76", "a77", "a78", "a79", "a80", "a81", "a82", "a83", "a84", "a85", "a86", "a87", "a88", "a89", "a90", "a91", "a92", "a93", "a94", "a95", "a96", "a97", "a98", "a99", "a100", "a101", "a102", "a103", "a104", "a105", "a106", "a107", "a108", "a109", "a110", "a111", "a112", "a113", "a114", "a115", "a116", "a117", "a118", "a119", "a120", "a121", "a122", "a123", "a124", "a125", "a126", "a127", "a128", "a129", "a130", "a131", "a132", "a133", "a134", "a135", "a136", "a137", "a138", "a139", "a140", "a141", "a142", "a143", "a144", "a145", "a146", "a147", "a148", "a149", "a150", "a151", "a152", "a153", "a154", "a155", "a156", "a157", "a158", "a159", "a160", "a161", "a162", "a163", "a164", "a165", "a166", "a167", "a168", "a169", "a170", "a171", "a172", "a173", "a174", "a175", "a176", "a177", "a178", "a179", "a180", "a181", "a182", "a183", "a184", "a185", "a186", "a187", "a188", "a189", "a190", "a191", "a192", "a193", "a194", "a195", "a196", "a197", "a198", "a199", "a200", "a201", "a202", "a203", "a204", "a205", "a206", "a207", "a208", "a209", "a210", "a211", "a212", "a213", "a214", "a215", "a216", "a217", "a218", "a219", "a220", "a221", "a222", "a223", "a224", "a225", "a226", "a227", "a228", "a229", "a230", "a231", "a232", "a233", "a234", "a235", "a236", "a237", "a238", "a239", "a240", "a241", "a242", "a243", "a244", "a245", "a246", "a247", "a248", "a249", "a250", "a251", "a252", "a253", "a254", "a255", "s8", "s9", "s10", "s11", "s12", "s13", "s14", "s15", "s16", "s17", "s18", "s19", "s20", "s21", "s22", "s23", "s24", "s25", "s26", "s27", "s28", "s29", "s30", "s31", "s32", "s33", "s34", "s35", "s36", "s37", "s38", "s39", "s40", "s41", "s42", "s43", "s44", "s45", "m0", "vcc", "scc", "memory"
#define LO(p) ((unsigned)(unsigned long long)(p))
#define HI(p) ((unsigned)((unsigned long long)(p) >> 32))
#define MBY (1u << 20)

__global__ __launch_bounds__(256, 1) void k_a(const u16* x, const u16* ckv, const u16* wq, const u16* wk, const u16* wvv, char* ws,
                                               const u16* wo, const u16* w1, const u16* w2, const u16* b1, const u16* b2, u16* out) {
  unsigned b = blockIdx.x;
  unsigned h = (b & 7) * 4 + ((b >> 3) & 3), g = b >> 5;
  unsigned wv = __builtin_amdgcn_readfirstlane(threadIdx.x >> 6);
  unsigned long long pa = (unsigned long long)x, px = (unsigned long long)ckv, pw = (unsigned long long)ws;
  unsigned long long pq = (unsigned long long)wq, pk = (unsigned long long)wk, pv = (unsigned long long)wvv;
  unsigned wg = b * 4 + wv;
  unsigned long long pfq = pq + wg * 4096u, pfa = (unsigned long long)wo + wg * 4096u;
  unsigned long long pfb = wg < 344 ? (unsigned long long)w1 + wg * 4096u : (unsigned long long)w2 + min(wg - 344, 343u) * 4096u;
  unsigned bad;
  asm volatile(A_ASM : [bad] "=s"(bad)
               : [alo] "s"((unsigned)pa), [ahi] "s"((unsigned)(pa >> 32)), [xlo] "s"((unsigned)px), [xhi] "s"((unsigned)(px >> 32)),
                 [wslo] "s"((unsigned)pw), [wshi] "s"((unsigned)(pw >> 32)), [qlo] "s"((unsigned)pq), [qhi] "s"((unsigned)(pq >> 32)),
                 [klo] "s"((unsigned)pk), [khi] "s"((unsigned)(pk >> 32)), [vlo] "s"((unsigned)pv), [vhi] "s"((unsigned)(pv >> 32)),
                 [hh] "s"(h), [jj] "s"(g), [gg] "s"(g), [wv] "s"(wv), [bid] "s"(b),
                 [pfq] "s"(pfq), [pfa] "s"(pfa), [pfb] "s"(pfb) : A_CLOB);
  if (bad) {
    const size_t MB = 1u << 20;
    int lane = __builtin_amdgcn_mbcnt_hi(~0u, __builtin_amdgcn_mbcnt_lo(~0u, 0u));
    for (int k = 0; k < 2; k++)
      attn_exact((const u16*)(ws + 1 * MB), wq, (const u16*)(ws + 4 * MB), (const u16*)(ws + 20 * MB), (u16*)(ws + 52 * MB), h, 4 * (k ? g : 15 - g) + wv, lane);
    __threadfence();
  }
  unsigned xc = b & 7, i = b >> 3, w = wv;
  unsigned l = __builtin_amdgcn_mbcnt_hi(~0u, __builtin_amdgcn_mbcnt_lo(~0u, 0u));
  asm volatile("" : "+v"(l));
  unsigned gl = l >> 4, q = (l >> 2) & 3, p = l & 3, i16 = l & 15;
  unsigned ks = xc & 3, rb1 = (xc >> 2) * 8 + (i & 7), cg = i >> 3;
  unsigned vx = (gl >> 1) * 1024 + (gl & 1) * 512 + i16 * 16;
  unsigned vw = (16 * w + gl) * 1024 + cg * 256 + (2 * ((i16 >> 1) ^ gl) + (l & 1)) * 16;
  unsigned vwa = (8 * gl + q) * 256 + ((q | ((gl & 1) << 2)) * 32) + 8 * p;
  unsigned vst = i16 * 1024 + ((gl & 1) * 16 + (gl >> 1) * 8) * 2;
  unsigned sa = 52 * MBY + ((4 * rb1 + w) * 256 + 64 * ks) * 1024, sw = ks * MBY;
  unsigned sst = 68 * MBY + ks * 2 * MBY + (128 * rb1 + 32 * w) * 1024 + 128 * cg * 2;
  unsigned rb = 2 * xc + (i & 1), mm = i >> 1;
  unsigned row = 128 * rb + 8 * mm + 2 * w + (l >> 5), t = l & 31;
  unsigned vp = row * 1024 + t * 16, vm = t * 32;
  unsigned vy = ((row >> 4) * 16 + (t >> 2)) * 1024 + (t & 3) * 256 + (row & 15) * 16;
  unsigned c0 = mm < 14 ? 96 * mm : 1280;
  unsigned w1o[3];
#pragma unroll
  for (unsigned a = 0; a < 3; a++) {
    unsigned n = 64 * a + l, r = n / 12, c16 = n % 12;
    w1o[a] = r * 2752 + c0 * 2 + ((((c16 >> 1) ^ ((r >> 3) & 1)) * 2) + (c16 & 1)) * 16 - 1024 * a;
  }
  unsigned vbo = (c0 + 4 * gl) * 2;
  unsigned vre = (8 * gl + q) * 192 + 8 * p + 32 * (gl & 1), vro = (8 * gl + q) * 192 + 8 * p - 32 * (gl & 1);
  unsigned sy = 86 * MBY + (8 * rb + 2 * w) * 16384, sh = 88 * MBY + ((8 * rb + 2 * w) * 43 + c0 / 32) * 1024;
  unsigned rb64 = 2 * rb + (mm & 1), cg2 = mm >> 1;
  unsigned w2o = (l >> 3) * 1024 + cg2 * 128 + (((((l & 7) >> 1) ^ (gl & 3)) * 2) + (l & 1)) * 16;
  unsigned vr2 = (4 * gl + q) * 128 + (((2 * gl + (q >> 1)) & 3) * 32) + 8 * p;
  unsigned row2 = 64 * rb64 + 16 * w + i16;
  unsigned vb2 = (64 * cg2 + 4 * gl) * 2, vx1 = row2 * 1024 + vb2;
  unsigned vo = row2 * 1024 + (64 * cg2 + (gl & 1) * 16 + (gl >> 1) * 8) * 2;
  unsigned sw2 = 11 * w * 32768, sh2 = 88 * MBY + (4 * rb64 * 43 + 11 * w) * 1024, nl = w < 3 ? 10 : 9;
  unsigned co1 = 512 * 1024 + 8192 + rb1 * 128, co2 = 512 * 1024 + 8192 + rb * 128, cy = 512 * 1024 + 10240 + rb * 128, ch = 512 * 1024 + 12288 + rb * 128;
  unsigned cs1 = 512 * 1024 + 14336 + ((15 - g) * 4 + (h >> 3)) * 32, cs2 = 512 * 1024 + 14336 + (g * 4 + (h >> 3)) * 32;
  unsigned cp = 512 * 1024 + 14336 + (rb1 * 4 + ks) * 32;
  asm volatile(F_ASM :: [wslo] "s"(LO(ws)), [wshi] "s"(HI(ws)), [wolo] "s"(LO(wo)), [wohi] "s"(HI(wo)), [w1lo] "s"(LO(w1)), [w1hi] "s"(HI(w1)),
               [w2lo] "s"(LO(w2)), [w2hi] "s"(HI(w2)), [xlo] "s"(LO(x)), [xhi] "s"(HI(x)), [b1lo] "s"(LO(b1)), [b1hi] "s"(HI(b1)),
               [b2lo] "s"(LO(b2)), [b2hi] "s"(HI(b2)), [olo] "s"(LO(out)), [ohi] "s"(HI(out)), [wv] "s"(w), [bid] "s"(b),
               [sa] "s"(sa), [sw] "s"(sw), [sst] "s"(sst), [co1] "s"(co1), [co2] "s"(co2), [cy] "s"(cy), [ch] "s"(ch), [mm] "s"(mm),
               [sy] "s"(sy), [sh] "s"(sh), [sh2] "s"(sh2), [sw2] "s"(sw2), [nl] "s"(nl), [cs1] "s"(cs1), [cs2] "s"(cs2), [cp] "s"(cp),
               [vx] "v"(vx), [vw] "v"(vw), [vwa] "v"(vwa), [vst] "v"(vst), [vp] "v"(vp), [vm] "v"(vm), [vy] "v"(vy),
               [w10] "v"(w1o[0]), [w11] "v"(w1o[1]), [w12] "v"(w1o[2]), [vbo] "v"(vbo), [vre] "v"(vre), [vro] "v"(vro),
               [w2] "v"(w2o), [vr2] "v"(vr2), [vx1] "v"(vx1), [vb2] "v"(vb2), [vo] "v"(vo) : F_CLOB);
}

extern "C" void kernel_launch(void* const* d_in, const int* in_sizes, int n_in, void* d_out, int out_size,
                              void* d_ws, size_t ws_size, hipStream_t stream) {
  const u16* x = (const u16*)d_in[0]; const u16* ckv = (const u16*)d_in[1];
  const u16* wq = (const u16*)d_in[2]; const u16* wk = (const u16*)d_in[3]; const u16* wv = (const u16*)d_in[4];
  const u16* wo = (const u16*)d_in[5]; const u16* w1 = (const u16*)d_in[7]; const u16* b1 = (const u16*)d_in[8];
  const u16* w2 = (const u16*)d_in[9]; const u16* b2 = (const u16*)d_in[10];
  const u16* c = (const u16*)d_in[11]; const u16* wmod = (const u16*)d_in[12];
  char* ws = (char*)d_ws;
  float* mod = (float*)(ws);
  hipFuncSetAttribute((const void*)k_a, hipFuncAttributeMaxDynamicSharedMemorySize, 163840);
  k_mod<<<PFALL ? 256 : 96, 256, 0, stream>>>(c, wmod, mod, wo, w1, w2, ckv, wk, wv, wq, x, b1, b2, (const u16*)d_out);
  k_a<<<256, 256, 163840, stream>>>(x, ckv, wq, wk, wv, ws, wo, w1, w2, b1, b2, (u16*)d_out);
}
```
